```python
import torch

def _softmax(x, axis=-1):
    return torch.softmax(x, dim=axis)
def _gelu(x):
    return torch.nn.functional.gelu(x, approximate="tanh")
def _silu(x):
    return torch.nn.functional.silu(x)
def _sigmoid(x):
    return torch.sigmoid(x)
def _layernorm(x, axis=-1, eps=1e-5):
    m = x.mean(dim=axis, keepdim=True)
    v = x.var(dim=axis, keepdim=True, unbiased=False)
    return (x - m) / torch.sqrt(v + eps)
def _scatter_add(tgt, idx, src, axis=0):
    return tgt.clone().index_add_(axis, idx, src)
def _segment_sum(src, seg, n):
    out = torch.zeros((n,) + src.shape[1:], dtype=src.dtype)
    return out.index_add_(0, seg, src)
def _pad(x, pw):
    flat = tuple(v for pair in reversed(pw) for v in pair)
    return torch.nn.functional.pad(x, flat)

kernel_name = 'recurrent_scan__linear_kernel_hedgehog__glu_gated_glu__prompt'

def setup_inputs():

    _w = (lambda a: (lambda t: t.to(torch.bfloat16) if t.is_floating_point() else t)(torch.as_tensor(a)))
    def _A_setup():
        B, S, D, H = 1, 2048, 1024, 32
        Dh = D // H
        return {
            "x": _w((lambda *s: torch.randn(*s, dtype=torch.bfloat16) * 0.05)(B, S, D)),
            "wqkv": _w((lambda *s: torch.randn(*s, dtype=torch.bfloat16) * 0.2)(D, 3 * D)),
            "w_phi": _w((lambda *s: torch.randn(*s, dtype=torch.bfloat16) * 0.2)(H, Dh, Dh) * (Dh ** -0.5)),
            "wo": _w((lambda *s: torch.randn(*s, dtype=torch.bfloat16) * 0.05)(D, D)),
        }
    _A_in = _A_setup()

    def _B_setup():
        B, S, D, F = 1, 2048, 1024, 3584
        return {
            "x": _w((lambda *s: torch.randn(*s, dtype=torch.bfloat16) * 0.05)(B, S, D)),
            "w_gate": _w((lambda *s: torch.randn(*s, dtype=torch.bfloat16) * 0.05)(D, F)),
            "w_up": _w((lambda *s: torch.randn(*s, dtype=torch.bfloat16) * 0.05)(D, F)),
            "w_down": _w((lambda *s: torch.randn(*s, dtype=torch.bfloat16) * 0.05)(F, D)),
        }
    _B_in = _B_setup()

    return {
        "A_x": _A_in["x"],
        "A_wqkv": _A_in["wqkv"],
        "A_w_phi": _A_in["w_phi"],
        "A_wo": _A_in["wo"],
        "B_x": _B_in["x"],
        "B_w_gate": _B_in["w_gate"],
        "B_w_up": _B_in["w_up"],
        "B_w_down": _B_in["w_down"],
    }

def reference(A_x, A_wqkv, A_w_phi, A_wo, B_x, B_w_gate, B_w_up, B_w_down):
    def _A_ref(x, wqkv, w_phi, wo):
        B, S, D = x.shape
        H = 32; Dh = D // H
        qkv = (lambda x, *d: x.permute(*d))((x @ wqkv).reshape(B, S, 3, H, Dh), 2, 0, 3, 1, 4)
        q, k, v = qkv[0], qkv[1], qkv[2]
        pq = torch.einsum("bhsd,hdf->bhsf", q, w_phi)
        pk = torch.einsum("bhsd,hdf->bhsf", k, w_phi)
        phi_q = torch.concatenate([_softmax(pq, axis=-1), _softmax(-pq, axis=-1)], axis=-1)
        phi_k = torch.concatenate([_softmax(pk, axis=-1), _softmax(-pk, axis=-1)], axis=-1)
        kv = torch.einsum("bhsf,bhsd->bhfd", phi_k, v)
        z  = torch.einsum("bhsf->bhf", phi_k)
        num = torch.einsum("bhqf,bhfd->bhqd", phi_q, kv)
        den = torch.einsum("bhqf,bhf->bhq", phi_q, z)[..., None] + 1e-6
        o = num / den
        return (lambda x, *d: x.permute(*d))(o, 0, 2, 1, 3).reshape(B, S, D) @ wo

    def _B_ref(x, w_gate, w_up, w_down):
        g = torch.einsum("bsd,df->bsf", x, w_gate)
        u = torch.einsum("bsd,df->bsf", x, w_up)
        h = _sigmoid(g) * u
        return torch.einsum("bsf,fd->bsd", h, w_down)

    x = A_x
    xn = _layernorm(x, axis=-1)
    s = _A_ref(xn, A_wqkv, A_w_phi, A_wo)
    g = _silu(_B_ref(xn, B_w_gate, B_w_up, B_w_down))
    return x + s * g
```

```cpp
#include <hip/hip_runtime.h>
#include <hip/hip_bf16.h>
#include <stdint.h>
typedef unsigned short u16;
typedef __attribute__((ext_vector_type(8))) short bf16x8;
typedef __attribute__((ext_vector_type(4))) float f32x4;
constexpr int S=2048, D=1024, H=32, DH=32, F=3584;
__device__ __forceinline__ float bf2f(u16 v){ return __uint_as_float(((unsigned)v)<<16); }
__device__ __forceinline__ u16 f2bf(float f){ __hip_bfloat16 b=__float2bfloat16(f); return *reinterpret_cast<u16*>(&b); }
__device__ __forceinline__ float rbf(float f){ return bf2f(f2bf(f)); }

__global__ void ln_kernel(const u16* x, u16* xn){
  int row=blockIdx.x; int t=threadIdx.x;
  float v[16]; float s=0;
  for(int i=0;i<16;i++){ v[i]=bf2f(x[row*D+t*16+i]); s+=v[i]; }
  for(int o=32;o>0;o>>=1) s+=__shfl_xor(s,o);
  float m=s/D; float q=0;
  for(int i=0;i<16;i++){ float d=v[i]-m; q+=d*d; }
  for(int o=32;o>0;o>>=1) q+=__shfl_xor(q,o);
  float var=rbf(q/D); float mb=rbf(m);
  float den=rbf(sqrtf(rbf(var+1e-5f)));
  for(int i=0;i<16;i++){ xn[row*D+t*16+i]=f2bf(rbf(v[i]-mb)/den); }
}
__global__ void gemm_kernel(const u16* A, int lda, const u16* B, int ldb, u16* C, int ldc, int K){
  int lane=threadIdx.x&63; int m0=blockIdx.y*64, n0=blockIdx.x*64;
  f32x4 acc[4][4]={};
  for(int k0=0;k0<K;k0+=32){
    bf16x8 a[4], b[4];
    for(int i=0;i<4;i++) a[i]=*(const bf16x8*)(A+(long)(m0+i*16+(lane&15))*lda+k0+(lane>>4)*8);
    for(int j=0;j<4;j++){ for(int e=0;e<8;e++) b[j][e]=(short)B[(long)(k0+(lane>>4)*8+e)*ldb+n0+j*16+(lane&15)]; }
    for(int i=0;i<4;i++)for(int j=0;j<4;j++) acc[i][j]=__builtin_amdgcn_mfma_f32_16x16x32_bf16(a[i],b[j],acc[i][j],0,0,0);
  }
  for(int i=0;i<4;i++)for(int j=0;j<4;j++)for(int r=0;r<4;r++)
    C[(long)(m0+i*16+(lane>>4)*4+r)*ldc+n0+j*16+(lane&15)]=f2bf(acc[i][j][r]);
}
__global__ void phi_kernel(const u16* qkv, const u16* wphi, u16* phiq, u16* phik){
  int t=blockIdx.x; int h=threadIdx.x>>5, f=threadIdx.x&31;
  for(int w=0;w<2;w++){
    float p=0; for(int d=0;d<32;d++) p+=bf2f(qkv[t*3*D+w*D+h*32+d])*bf2f(wphi[h*1024+d*32+f]);
    p=rbf(p);
    float mx=p, mn=p; for(int o=16;o>0;o>>=1){ mx=fmaxf(mx,__shfl_xor(mx,o)); mn=fminf(mn,__shfl_xor(mn,o)); }
    float e1=__expf(p-mx), e2=__expf(mn-p); float s1=e1,s2=e2;
    for(int o=16;o>0;o>>=1){ s1+=__shfl_xor(s1,o); s2+=__shfl_xor(s2,o); }
    u16* dst=w?phik:phiq;
    dst[(t*H+h)*64+f]=f2bf(e1/s1); dst[(t*H+h)*64+32+f]=f2bf(e2/s2);
  }
}
__global__ void kv_kernel(const u16* qkv, const u16* phik, float* kv, float* z){
  int h=blockIdx.x>>1; int f=(blockIdx.x&1)*32+(threadIdx.x>>5), d=threadIdx.x&31;
  float a=0, zz=0;
  for(int t=0;t<S;t++){ float p=bf2f(phik[(t*H+h)*64+f]); a+=p*bf2f(qkv[t*3*D+2*D+h*32+d]); zz+=p; }
  kv[(h*64+f)*32+d]=rbf(a); if(d==0) z[h*64+f]=rbf(zz);
}
__global__ void o_kernel(const u16* phiq, const float* kv, const float* z, u16* o){
  int t=blockIdx.x; int h=threadIdx.x>>5, d=threadIdx.x&31;
  float n=0, de=0; for(int f=0;f<64;f++){ float p=bf2f(phiq[(t*H+h)*64+f]); n+=p*kv[(h*64+f)*32+d]; de+=p*z[h*64+f]; }
  o[t*D+h*32+d]=f2bf(rbf(n)/rbf(rbf(de)+1e-6f));
}
__global__ void h_kernel(const u16* g, const u16* u, u16* hh){
  long i=(long)blockIdx.x*256+threadIdx.x; float gg=bf2f(g[i]); hh[i]=f2bf(rbf(1.f/(1.f+__expf(-gg)))*bf2f(u[i]));
}
__global__ void fin_kernel(const u16* x, const u16* s, const u16* d, u16* out){
  long i=(long)blockIdx.x*256+threadIdx.x; float dd=bf2f(d[i]); float g=rbf(dd/(1.f+__expf(-dd)));
  out[i]=f2bf(bf2f(x[i])+rbf(bf2f(s[i])*g));
}
extern "C" void kernel_launch(void* const* d_in, const int* in_sizes, int n_in, void* d_out, int out_size, void* d_ws, size_t ws_size, hipStream_t stream){
  const u16* x=(const u16*)d_in[0]; const u16* wqkv=(const u16*)d_in[1]; const u16* wphi=(const u16*)d_in[2]; const u16* wo=(const u16*)d_in[3];
  const u16* wg=(const u16*)d_in[5]; const u16* wu=(const u16*)d_in[6]; const u16* wd=(const u16*)d_in[7];
  char* ws=(char*)d_ws; size_t off=0; auto alloc=[&](size_t b){ void* p=ws+off; off+=(b+255)/256*256; return p; };
  u16* xn=(u16*)alloc(S*D*2); u16* qkv=(u16*)alloc(S*3*D*2); u16* g=(u16*)alloc(S*F*2); u16* u=(u16*)alloc(S*F*2); u16* hh=(u16*)alloc(S*F*2);
  u16* phiq=(u16*)alloc(S*H*64*2); u16* phik=(u16*)alloc(S*H*64*2); float* kv=(float*)alloc(H*64*32*4); float* z=(float*)alloc(H*64*4);
  u16* o=(u16*)alloc(S*D*2); u16* s=(u16*)alloc(S*D*2); u16* d=(u16*)alloc(S*D*2);
  ln_kernel<<<S,64,0,stream>>>(x,xn);
  gemm_kernel<<<dim3(3*D/64,S/64),64,0,stream>>>(xn,D,wqkv,3*D,qkv,3*D,D);
  gemm_kernel<<<dim3(F/64,S/64),64,0,stream>>>(xn,D,wg,F,g,F,D);
  gemm_kernel<<<dim3(F/64,S/64),64,0,stream>>>(xn,D,wu,F,u,F,D);
  phi_kernel<<<S,1024,0,stream>>>(qkv,wphi,phiq,phik);
  kv_kernel<<<H*2,1024,0,stream>>>(qkv,phik,kv,z);
  o_kernel<<<S,1024,0,stream>>>(phiq,kv,z,o);
  h_kernel<<<S*F/256,256,0,stream>>>(g,u,hh);
  gemm_kernel<<<dim3(D/64,S/64),64,0,stream>>>(o,D,wo,D,s,D,D);
  gemm_kernel<<<dim3(D/64,S/64),64,0,stream>>>(hh,F,wd,D,d,D,F);
  fin_kernel<<<S*D/256,256,0,stream>>>(x,s,d,(u16*)d_out);
}
```

```cpp
#include <hip/hip_runtime.h>
#include <hip/hip_bf16.h>
#include <stdint.h>
typedef unsigned u32; typedef unsigned long long u64;
typedef __attribute__((ext_vector_type(16))) u32 u32x16;
typedef __attribute__((ext_vector_type(8))) u32 u32x8;
typedef __attribute__((ext_vector_type(4))) u32 u32x4;
struct D { const void* p; u32 n, f; };
struct P { D d[9]; };
#define RFL(x) __builtin_amdgcn_readfirstlane(x)
#define LNF 4096u
#define HF 12288u
#define OF 20480u
#define KVPF 24576u
#define KVFF 28672u
#define XF 32768u
#define TSO 65536u
#define CSF (STA+851968u)
#define STA (1u<<20)
#define STV (STA+65536u)
#define CSB (STA+131072u)
#define HX (8u<<20)
#define KVP (32u<<20)
#define KVF (40u<<20)
#define XCH (48u<<20)
#define WSZ (64u<<20)
#define MISC 147456u
#define GEO const int xcd=b&7, j=b>>3, pp=j&7, pr=xcd*2+(j>>4), ty=(j>>3)&1, wm=w>>1, wn=w&1; \
  const int head=2*pr+wn, tt1=pp*2+wm, rot=(0*xcd+(pp&4))&7, cg=pr*2+ty, tt=(xcd&1)*8+(j&7), nt=(xcd&2)*2+(j>>3), hf=xcd>>2;
struct Tab { u32 v[1024*52]; };
constexpr Tab mktab(){ Tab t={}; for(int b=0;b<256;b++) for(int w=0;w<4;w++){ GEO u32* sn=t.v+(b*4+w)*52; u32* sd=sn+16; u32* se=sn+32;
  sd[0]=LNF+(b*4+w)*8; sd[1]=LNF+pp*1024; sd[2]=head*2048; sd[3]=MISC+w*4096;
  sd[4]=HF+(tt1*64+ty*32+pr*2+wn)*8; const int k0=0? (ty? 26*hf : 28-30*hf) : 0; sd[5]=HF+(tt*64+32+hf*(16+0*(ty?2:-2)))*8; sn[50]=(ty? 4-26*hf : 30*hf-29)*16384;
  if(ty){ sd[6]=KVFF+head*128; sd[7]=KVF+head*8192; } else { sd[6]=KVP+(head*16+tt1)*8448; sd[7]=KVPF+(head*16+tt1)*8; }
  sd[8]=HX+(tt*72+28*hf+k0)*16384+w*4096; sd[9]=HX+(tt*72+56)*16384+wm*8192+hf*4096+wn*2048;
  sd[10]=(28*hf+k0)*(64*2048); sd[11]=HF+(tt*64+hf*16)*8; sd[12]=(w^hf)*4096; sd[13]=65536+w*4096;
  sd[14]=KVPF+head*128; sd[15]=KVP+head*(16*8448)+((tt1>>3)*4+((tt1>>2)&1))*1024+(tt1&3)*256;
  se[0]=KVF+head*8192+(((tt1>>2)&1)*2+(tt1>>3))*1024+(tt1&3)*256; se[1]=KVP+head*(16*8448)+8192; se[2]=KVFF+(head*16+tt1)*8; se[3]=OF+(tt1*32+pr*2+wn)*8;
  se[4]=OF+tt*32*8; se[5]=XCH+(b*4+w)*8192; se[6]=XCH+((b^4)*4+w)*8192; se[7]=XF+(b*4+w)*8; se[8]=XF+((b^4)*4+w)*8; se[9]=b+w;
  se[10]=((b>>3)*4+w)<<10; { int i=b*4+w, id=(b&63)+(b>>7)*64, c0=ty? id*4+w : 512+(id*2+wm)*2, c1=ty? c0 : c0+1;
    se[11]=((i&1)?640:128)*6144+(i>>1)*4096+4096; sn[48]=((c0&1)?640:128)*7168+(c0>>1)*4096+4096; sn[49]=((c1&1)?640:128)*7168+(c1>>1)*4096+4096; }
  se[12]=se[13]=0x3fb8aa3bu; se[14]=(b*4+w)*2048+4096; se[15]=(b*4+w)*7168+4096;
  sn[0]=1|(80>>(pp&3)); sn[1]=4-(pp&3); sn[2]=rot; sn[3]=CSB+(cg*2+wn)*10240+wm*5120+(pp>>1)*256+(pp&1)*8; sn[4]=CSF+(cg*32+pp*4+w)*32; sn[5]=CSF+cg*1024;
  sn[6]=CSB+(cg*2+wn)*10240+wm*5120; sn[7]=STA+(pp*256+64*w)*32; sn[8]=STV+(pp*256+64*w)*4; sn[9]=STA+(8*b+2*w)*32; sn[10]=STV+(8*b+2*w)*4;
  sn[11]=wn*10240+wm*5120; sn[12]=20480+w*2048; sn[13]=28672+w*256; sn[14]=wm; sn[15]=wn*10240;
  (void)nt; } return t; }
static __device__ const Tab tab=mktab();
__global__ __launch_bounds__(256,1) void fused(P p){
  const int b=blockIdx.x, w=RFL(threadIdx.x>>6), l=threadIdx.x&63;
  u32x16 va, vb, sd, se, ka, kb; u32x4 vc, kc; u32x8 sb; u32 pw;
  GEO
  const int g=l>>4, qq=(l>>2)&3, pq=l&3, row=l&15, sw=row>>1;
  const int gbase=pr*224+((pr&1)? (ty?96:0) : (ty?0:128));
  for(int i=0;i<16;i++){ va[i]=0; vb[i]=0; }
  va[0]=l*16;
  { int r1=8*w+(l>>3); int lc=(l&7)^((((r1>>1)&1)<<2)|(3*((r1>>3)&1))); u32 a=r1*6144+pr*128+lc*16+4096;
    if(ty){ va[1]=a;
      for(int jj=0;jj<2;jj++){ int r2=8*w+4*jj+(l>>4); int lc2=(l&15)^(((r2&3)<<2)|(3*((r2>>3)&1))); u32 a2=r2*7168+gbase*2+lc2*16+4096; va[2+jj]=a2-(1+jj)*1024; va[4+jj]=a2-(jj?0:3072); }
    } else { va[1]=a+2048; va[2]=a+4096-1024;
      for(int jj=0;jj<3;jj++){ int o=jj*1024+l*16; int lr=o/192, pc=(o%192)>>4; int r2=16*(w>>1)+lr; int lc2=pc^(3*((r2>>3)&1)); va[3+jj]=r2*7168+gbase*2+lc2*16+4096-(jj==0?2048:jj==1?3072:0); }
    } }
  vc[1]=0 ? (l>>2)*2048+(((l&3)^g)<<4) : (l>>3)*2048+(((l&7)^g)<<4);
  sb[0]=(pp*256+64*w)*2048+rot*256; sb[1]=rot*(128*6144); sb[2]=rot*(128*7168); sb[3]=w*(0?4096:8192); sb[4]=65536+w*5120; sb[5]=(8*b+2*w)*2048; sb[6]=ty; sb[7]=TSO+(b*4+w)*256;
  u64 tb=(u64)(tab.v+(b*4+w)*52);
  asm volatile(
      "s_load_dwordx16 s[20:35], %[kp], 0\n"
      "s_load_dwordx16 s[44:59], %[kp], 64\n"
      "s_load_dwordx4 s[60:63], %[kp], 128\n"
      "s_waitcnt lgkmcnt(0)\n"
      "s_add_u32 m0, s39, 0\n"
      "s_nop 0\n"
      "buffer_load_dwordx4 v33, s[44:47], s36 offen offset:0 lds\n"
      "s_add_u32 s36, s36, 15360\n"
      "s_cmp_lg_u32 %[sel], 0\n"
      "s_cselect_b64 s[28:29], s[32:33], s[28:29]\n"
      "buffer_load_dwordx4 v36, s[44:47], s36 offen offset:1024 lds\n"
      "s_add_u32 s36, s36, 15360\n"
      "s_sub_u32 s24, s24, 4096\n"
      "s_subb_u32 s25, s25, 0\n"
      "buffer_load_dwordx4 v33, s[44:47], s36 offen offset:2048 lds\n"
      "s_add_u32 s36, s36, 15360\n"
      "s_sub_u32 s28, s28, 4096\n"
      "s_subb_u32 s29, s29, 0\n"
      "buffer_load_dwordx4 v36, s[44:47], s36 offen offset:3072 lds\n"
      "s_add_u32 s36, s36, 19456\n"
      "s_sub_u32 s32, s32, 4096\n"
      "s_subb_u32 s33, s33, 0\n"
      "s_add_u32 m0, s39, 4096\n"
      "s_nop 0\n"
      "buffer_load_dwordx4 v33, s[44:47], s36 offen offset:0 lds\n"
      "s_add_u32 s36, s36, 15360\n"
      "s_sub_u32 s52, s52, 4096\n"
      "s_subb_u32 s53, s53, 0\n"
      "buffer_load_dwordx4 v36, s[44:47], s36 offen offset:1024 lds\n"
      "s_add_u32 s36, s36, 15360\n"
      "s_sub_u32 s56, s56, 4096\n"
      "s_subb_u32 s57, s57, 0\n"
      "buffer_load_dwordx4 v33, s[44:47], s36 offen offset:2048 lds\n"
      "s_add_u32 s36, s36, 15360\n"
      "buffer_load_dwordx4 v36, s[44:47], s36 offen offset:3072 lds\n"
      "s_sub_u32 s36, s36, 111488\n"
      "s_cmp_eq_u32 s42, 0\n"
      "s_cbranch_scc1 EK_%=\n"
      "s_add_u32 m0, s40, 0\n"
      "s_nop 0\n"
      "buffer_load_dwordx4 v1, s[24:27], s37 offen offset:0 lds\n"
      "s_add_u32 s37, s37, 196608\n"
      "buffer_load_dwordx4 v2, s[28:31], s38 offen offset:1024 lds\n"
      "buffer_load_dwordx4 v3, s[28:31], s38 offen offset:2048 lds\n"
      "buffer_load_dwordx4 v4, s[32:35], s38 offen offset:3072 lds\n"
      "s_add_u32 m0, s40, 4096\n"
      "s_nop 0\n"
      "buffer_load_dwordx4 v5, s[32:35], s38 offen offset:0 lds\n"
      "s_add_u32 s38, s38, 229376\n"
      "s_branch EE_%=\n"
      "EK_%=:\n"
      "s_add_u32 m0, s40, 0\n"
      "s_nop 0\n"
      "buffer_load_dwordx4 v1, s[24:27], s37 offen offset:0 lds\n"
      "buffer_load_dwordx4 v2, s[24:27], s37 offen offset:1024 lds\n"
      "s_add_u32 s37, s37, 196608\n"
      "buffer_load_dwordx4 v3, s[28:31], s38 offen offset:2048 lds\n"
      "buffer_load_dwordx4 v4, s[28:31], s38 offen offset:3072 lds\n"
      "s_add_u32 m0, s40, 4096\n"
      "s_nop 0\n"
      "buffer_load_dwordx4 v5, s[28:31], s38 offen offset:0 lds\n"
      "s_add_u32 s38, s38, 229376\n"
      "EE_%=:\n"
      : "=&{s[20:35]}"(ka),"=&{s[44:59]}"(kb),"=&{s[60:63]}"(kc),"+{s[36:43]}"(sb)
      : "{v33}"(vc[1]),"{v36}"(vc[1]^64),"{v1}"(va[1]),"{v2}"(va[2]),"{v3}"(va[3]),"{v4}"(va[4]),"{v5}"(va[5]),[sel]"s"(wn&~ty),[kp]"s"(__builtin_amdgcn_kernarg_segment_ptr()) : "memory","scc");
  for(int kh=0;kh<2;kh++) va[6+kh]=0 ? wm*8192+row*64+((g^(row>>2))<<4) : wm*16384+row*128+(((4*kh+g)^sw)<<4);
  { for(int bb=0;bb<2;bb++) va[8+bb]=65536+g*5120+qq*128+(((4*wn+pq)^(((qq>>1)<<2)|(3*(g&1))))<<4)+8*((pq&1)^bb);
    if(ty){ for(int m=0;m<4;m++) va[10+m]=65536+g*5120+1024+qq*256+(((8*wn+4*(m>>1)+pq)^((qq<<2)|(3*(g&1))))<<4)+8*((pq&1)^(m&1)); }
    else { u32 base=65536+(2*(g>>1))*5120+2048+(8*(g&1)+qq)*192;
      for(int bb=0;bb<2;bb++) va[10+bb]=base+((((wn?8:0)+pq)^(3*(g&1)))<<4)+8*((pq&1)^bb);
      va[12]=base+((((wn?6:4)+(pq>>1))^(3*(g&1)))<<4)+8*((pq&1)^(pq>>1)); for(int m=0;m<3;m++) va[13+m]=va[10+m]+5120; } }
  const u32 TB=HX+tt1*72*16384+row*128;
  vb[0]=MISC+w*4096+(8*g+qq)*64+pq*8;
  vb[1]=131072+w*4096+row*128+((g^(l&7))<<4);
  vb[2]=131072+w*4096+(4*g+qq)*128+((pq^(4*(g&1)+qq))<<4);
  { int c0=gbase+64*wn, c1=gbase+32;
    vb[3]=TB+(c0>>6)*16384+(((((c0>>3)&7)+g)^sw)<<4);
    vb[4]=ty? (vb[3]^64) : TB+(c1>>6)*16384+wn*2048+(((((c1>>3)&4)+2*(g&1)+(g>>1))^sw)<<4); }
  vb[5]=TB+(56+pr)*16384+(((4*wn+g)^sw)<<4);
  vb[6]=(tt*128+64*wm+32*hf+row)*2048+(nt*128+64*wn+8*g)*2;
  vb[7]=wm*8192+row*128+((g^sw)<<4);
  vb[8]=65536+(8*g+qq)*256+(((8*wn+pq)^((qq<<2)|(3*(g&1))))<<4)+8*(pq&1);
  for(int jj=0;jj<4;jj++) vb[9+jj]=(16*w+8*(jj>>1)+4*((jj&1)^(jj>>1))+g)*2048+nt*256+((row^((g<<2)|(3*(jj>>1))))<<4)+4096-jj*1024;
  { int h=l>>5;
    vc[2]=(8*h+4*((g&1)^h)+qq)*64+pq*16; vc[0]=0; vc[3]=0; }
  asm volatile(
"CB_%=:\n"
".Lfz_cb:\n"
"s_load_dwordx16 s[0:15], s[18:19], 0\n"
"s_load_dwordx16 s[64:79], s[18:19], 64\n"
"s_load_dwordx16 s[80:95], s[18:19], 128\n"
"s_cmp_eq_u32 s42, 0\n"
"s_cbranch_scc1 KVF_%=\n"
"v_xor_b32 v36, 64, v33\n"
"s_add_u32 m0, s40, 20480\n"
"s_nop 0\n"
"buffer_load_dwordx4 v1, s[24:27], s37 offen offset:0 lds\n"
"s_add_u32 s37, s37, 196608\n"
"buffer_load_dwordx4 v2, s[28:31], s38 offen offset:1024 lds\n"
"v_accvgpr_write_b32 a241, v20\n"
"buffer_load_dwordx4 v3, s[28:31], s38 offen offset:2048 lds\n"
"v_accvgpr_write_b32 a242, v21\n"
"buffer_load_dwordx4 v4, s[32:35], s38 offen offset:3072 lds\n"
"v_accvgpr_write_b32 a243, v22\n"
"s_add_u32 m0, s40, 24576\n"
"s_nop 0\n"
"buffer_load_dwordx4 v5, s[32:35], s38 offen offset:0 lds\n"
"s_add_u32 s38, s38, 229376\n"
"v_accvgpr_write_b32 a244, v23\n"
"s_add_u32 m0, s39, 32768\n"
"s_nop 0\n"
"buffer_load_dwordx4 v33, s[44:47], s36 offen offset:0 lds\n"
"s_add_u32 s36, s36, 15360\n"
"v_accvgpr_write_b32 a245, v24\n"
"buffer_load_dwordx4 v36, s[44:47], s36 offen offset:1024 lds\n"
"s_add_u32 s36, s36, 15360\n"
"v_accvgpr_write_b32 a250, v25\n"
"buffer_load_dwordx4 v33, s[44:47], s36 offen offset:2048 lds\n"
"s_add_u32 s36, s36, 15360\n"
"v_accvgpr_write_b32 a251, v27\n"
"buffer_load_dwordx4 v36, s[44:47], s36 offen offset:3072 lds\n"
"s_add_u32 s36, s36, 19456\n"
"v_accvgpr_write_b32 a252, v16\n"
"s_add_u32 m0, s39, 36864\n"
"s_nop 0\n"
"buffer_load_dwordx4 v33, s[44:47], s36 offen offset:0 lds\n"
"s_add_u32 s36, s36, 15360\n"
"buffer_load_dwordx4 v36, s[44:47], s36 offen offset:1024 lds\n"
"s_add_u32 s36, s36, 15360\n"
"v_accvgpr_write_b32 a253, v17\n"
"buffer_load_dwordx4 v33, s[44:47], s36 offen offset:2048 lds\n"
"s_add_u32 s36, s36, 15360\n"
"v_accvgpr_write_b32 a254, v18\n"
"buffer_load_dwordx4 v36, s[44:47], s36 offen offset:3072 lds\n"
"s_sub_u32 s36, s36, 111488\n"
"v_accvgpr_write_b32 a255, v19\n"
"s_add_u32 m0, s40, 40960\n"
"s_nop 0\n"
"buffer_load_dwordx4 v1, s[24:27], s37 offen offset:0 lds\n"
"s_add_u32 s37, s37, 196608\n"
"v_accvgpr_write_b32 a240, v28\n"
"buffer_load_dwordx4 v2, s[28:31], s38 offen offset:1024 lds\n"
"v_mov_b32 v20, v33\n"
"buffer_load_dwordx4 v3, s[28:31], s38 offen offset:2048 lds\n"
"v_mov_b32 v21, v36\n"
"buffer_load_dwordx4 v4, s[32:35], s38 offen offset:3072 lds\n"
"v_mov_b32 v30, v34\n"
"s_add_u32 m0, s40, 45056\n"
"s_nop 0\n"
"buffer_load_dwordx4 v5, s[32:35], s38 offen offset:0 lds\n"
"s_add_u32 s38, s38, 229376\n"
"s_getpc_b64 s[18:19]\n"
"PCQ_%=:\n"
"s_sub_u32 s18, s18, PCQ_%=-CB_%=\n"
"s_subb_u32 s19, s19, 0\n"
"s_waitcnt vmcnt(18)\n"
"s_barrier\n"
"ds_read_b128 a[0:3], v6 offset:0\n"
"ds_read_b64_tr_b16 a[32:33], v8 offset:0\n"
"ds_read_b64_tr_b16 a[34:35], v8 offset:512\n"
"ds_read_b128 a[4:7], v6 offset:2048\n"
"ds_read_b64_tr_b16 a[36:37], v9 offset:0\n"
"ds_read_b64_tr_b16 a[38:39], v9 offset:512\n"
"ds_read_b64_tr_b16 a[40:41], v10 offset:0\n"
"ds_read_b128 a[8:11], v6 offset:4096\n"
"ds_read_b64_tr_b16 a[42:43], v10 offset:1024\n"
"ds_read_b64_tr_b16 a[44:45], v11 offset:0\n"
"ds_read_b128 a[12:15], v6 offset:6144\n"
"ds_read_b64_tr_b16 a[46:47], v11 offset:1024\n"
"ds_read_b64_tr_b16 a[48:49], v12 offset:0\n"
"ds_read_b64_tr_b16 a[50:51], v12 offset:1024\n"
"ds_read_b128 a[16:19], v6 offset:8192\n"
"ds_read_b64_tr_b16 a[52:53], v13 offset:0\n"
"ds_read_b64_tr_b16 a[54:55], v13 offset:1024\n"
"ds_read_b128 a[20:23], v6 offset:10240\n"
"ds_read_b64_tr_b16 a[56:57], v10 offset:2048\n"
"ds_read_b64_tr_b16 a[58:59], v10 offset:3072\n"
"ds_read_b64_tr_b16 a[60:61], v11 offset:2048\n"
"ds_read_b128 a[24:27], v6 offset:12288\n"
"ds_read_b64_tr_b16 a[62:63], v11 offset:3072\n"
"ds_read_b64_tr_b16 a[64:65], v12 offset:2048\n"
"ds_read_b128 a[28:31], v6 offset:14336\n"
"ds_read_b64_tr_b16 a[66:67], v12 offset:3072\n"
"ds_read_b64_tr_b16 a[68:69], v13 offset:2048\n"
"ds_read_b64_tr_b16 a[70:71], v13 offset:3072\n"
"s_waitcnt vmcnt(13) lgkmcnt(0)\n"
"s_barrier\n"
"v_mfma_f32_16x16x32_bf16 v[32:35], a[32:35], a[0:3], 0\n"
"ds_read_b128 a[72:75], v7 offset:0\n"
"s_add_u32 m0, s40, 61440\n"
"v_mfma_f32_16x16x32_bf16 v[64:67], a[36:39], a[0:3], 0\n"
"buffer_load_dwordx4 v1, s[24:27], s37 offen offset:0 lds\n"
"s_add_u32 s37, s37, 196608\n"
"v_mfma_f32_16x16x32_bf16 v[96:99], a[40:43], a[0:3], 0\n"
"ds_read_b64_tr_b16 a[104:105], v8 offset:20480\n"
"v_mfma_f32_16x16x32_bf16 v[128:131], a[44:47], a[0:3], 0\n"
"v_mfma_f32_16x16x32_bf16 v[160:163], a[48:51], a[0:3], 0\n"
"ds_read_b64_tr_b16 a[106:107], v8 offset:20992\n"
"v_mfma_f32_16x16x32_bf16 v[192:195], a[52:55], a[0:3], 0\n"
"v_mfma_f32_16x16x32_bf16 v[224:227], a[56:59], a[0:3], 0\n"
"buffer_load_dwordx4 v2, s[28:31], s38 offen offset:1024 lds\n"
"v_mfma_f32_16x16x32_bf16 a[144:147], a[60:63], a[0:3], 0\n"
"ds_read_b128 a[76:79], v7 offset:2048\n"
"v_mfma_f32_16x16x32_bf16 a[176:179], a[64:67], a[0:3], 0\n"
"v_mfma_f32_16x16x32_bf16 a[208:211], a[68:71], a[0:3], 0\n"
"ds_read_b64_tr_b16 a[108:109], v9 offset:20480\n"
"v_mfma_f32_16x16x32_bf16 a[212:215], a[68:71], a[4:7], 0\n"
"v_mfma_f32_16x16x32_bf16 a[180:183], a[64:67], a[4:7], 0\n"
"ds_read_b64_tr_b16 a[110:111], v9 offset:20992\n"
"buffer_load_dwordx4 v3, s[28:31], s38 offen offset:2048 lds\n"
"v_mfma_f32_16x16x32_bf16 a[148:151], a[60:63], a[4:7], 0\n"
"v_mfma_f32_16x16x32_bf16 v[228:231], a[56:59], a[4:7], 0\n"
"v_mfma_f32_16x16x32_bf16 v[196:199], a[52:55], a[4:7], 0\n"
"ds_read_b64_tr_b16 a[112:113], v10 offset:20480\n"
"v_mfma_f32_16x16x32_bf16 v[164:167], a[48:51], a[4:7], 0\n"
"v_mfma_f32_16x16x32_bf16 v[132:135], a[44:47], a[4:7], 0\n"
"ds_read_b128 a[80:83], v7 offset:4096\n"
"buffer_load_dwordx4 v4, s[32:35], s38 offen offset:3072 lds\n"
"v_mfma_f32_16x16x32_bf16 v[100:103], a[40:43], a[4:7], 0\n"
"v_mfma_f32_16x16x32_bf16 v[68:71], a[36:39], a[4:7], 0\n"
"ds_read_b64_tr_b16 a[114:115], v10 offset:21504\n"
"v_mfma_f32_16x16x32_bf16 v[36:39], a[32:35], a[4:7], 0\n"
"v_mfma_f32_16x16x32_bf16 v[40:43], a[32:35], a[8:11], 0\n"
"s_add_u32 m0, s40, 65536\n"
"v_mfma_f32_16x16x32_bf16 v[72:75], a[36:39], a[8:11], 0\n"
"ds_read_b64_tr_b16 a[116:117], v11 offset:20480\n"
"buffer_load_dwordx4 v5, s[32:35], s38 offen offset:0 lds\n"
"s_add_u32 s38, s38, 229376\n"
"v_mfma_f32_16x16x32_bf16 v[104:107], a[40:43], a[8:11], 0\n"
"v_mfma_f32_16x16x32_bf16 v[136:139], a[44:47], a[8:11], 0\n"
"ds_read_b128 a[84:87], v7 offset:6144\n"
"v_mfma_f32_16x16x32_bf16 v[168:171], a[48:51], a[8:11], 0\n"
"v_mfma_f32_16x16x32_bf16 v[200:203], a[52:55], a[8:11], 0\n"
"ds_read_b64_tr_b16 a[118:119], v11 offset:21504\n"
"v_mfma_f32_16x16x32_bf16 v[232:235], a[56:59], a[8:11], 0\n"
"buffer_load_dwordx2 v[28:29], off, s[20:23], 0 sc1\n"
"s_cmp_eq_u32 s2, 7\n"
"s_cselect_b32 s96, 6291456, 0\n"
"s_cselect_b32 s97, 7340032, 0\n"
"s_cselect_b32 s16, 2048, 0\n"
"v_mfma_f32_16x16x32_bf16 a[152:155], a[60:63], a[8:11], 0\n"
"v_mfma_f32_16x16x32_bf16 a[184:187], a[64:67], a[8:11], 0\n"
"ds_read_b64_tr_b16 a[120:121], v12 offset:20480\n"
"v_mfma_f32_16x16x32_bf16 a[216:219], a[68:71], a[8:11], 0\n"
"v_mfma_f32_16x16x32_bf16 a[220:223], a[68:71], a[12:15], 0\n"
"ds_read_b64_tr_b16 a[122:123], v12 offset:21504\n"
"v_mfma_f32_16x16x32_bf16 a[188:191], a[64:67], a[12:15], 0\n"
"buffer_load_dwordx4 v[16:19], v0, s[44:47], s41 offen offset:0\n"
"s_sub_u32 s37, s37, s96\n"
"s_sub_u32 s38, s38, s97\n"
"v_mfma_f32_16x16x32_bf16 a[156:159], a[60:63], a[12:15], 0\n"
"v_mfma_f32_16x16x32_bf16 v[236:239], a[56:59], a[12:15], 0\n"
"ds_read_b128 a[88:91], v7 offset:8192\n"
"v_mfma_f32_16x16x32_bf16 v[204:207], a[52:55], a[12:15], 0\n"
"v_mfma_f32_16x16x32_bf16 v[172:175], a[48:51], a[12:15], 0\n"
"ds_read_b64_tr_b16 a[124:125], v13 offset:20480\n"
"v_mfma_f32_16x16x32_bf16 v[140:143], a[44:47], a[12:15], 0\n"
"buffer_load_dwordx4 v[24:27], v0, s[44:47], s41 offen offset:1024\n"
"s_sub_u32 s36, s36, s16\n"
"v_mfma_f32_16x16x32_bf16 v[108:111], a[40:43], a[12:15], 0\n"
"ds_read_b64_tr_b16 a[126:127], v13 offset:21504\n"
"v_mfma_f32_16x16x32_bf16 v[76:79], a[36:39], a[12:15], 0\n"
"v_mfma_f32_16x16x32_bf16 v[44:47], a[32:35], a[12:15], 0\n"
"v_mfma_f32_16x16x32_bf16 v[48:51], a[32:35], a[16:19], 0\n"
"ds_read_b128 a[92:95], v7 offset:10240\n"
"v_mfma_f32_16x16x32_bf16 v[80:83], a[36:39], a[16:19], 0\n"
"s_add_u32 m0, s40, 0\n"
"v_mfma_f32_16x16x32_bf16 v[112:115], a[40:43], a[16:19], 0\n"
"ds_read_b64_tr_b16 a[128:129], v10 offset:22528\n"
"buffer_load_dwordx4 v1, s[24:27], s37 offen offset:0 lds\n"
"s_add_u32 s37, s37, 196608\n"
"v_mfma_f32_16x16x32_bf16 v[144:147], a[44:47], a[16:19], 0\n"
"v_mfma_f32_16x16x32_bf16 v[176:179], a[48:51], a[16:19], 0\n"
"ds_read_b64_tr_b16 a[130:131], v10 offset:23552\n"
"v_mfma_f32_16x16x32_bf16 v[208:211], a[52:55], a[16:19], 0\n"
"v_mfma_f32_16x16x32_bf16 v[240:243], a[56:59], a[16:19], 0\n"
"v_mfma_f32_16x16x32_bf16 a[160:163], a[60:63], a[16:19], 0\n"
"ds_read_b64_tr_b16 a[132:133], v11 offset:22528\n"
"buffer_load_dwordx4 v2, s[28:31], s38 offen offset:1024 lds\n"
"v_mfma_f32_16x16x32_bf16 a[192:195], a[64:67], a[16:19], 0\n"
"v_mfma_f32_16x16x32_bf16 a[224:227], a[68:71], a[16:19], 0\n"
"ds_read_b128 a[96:99], v7 offset:12288\n"
"v_mfma_f32_16x16x32_bf16 a[228:231], a[68:71], a[20:23], 0\n"
"v_mfma_f32_16x16x32_bf16 a[196:199], a[64:67], a[20:23], 0\n"
"ds_read_b64_tr_b16 a[134:135], v11 offset:23552\n"
"v_mfma_f32_16x16x32_bf16 a[164:167], a[60:63], a[20:23], 0\n"
"buffer_load_dwordx4 v3, s[28:31], s38 offen offset:2048 lds\n"
"v_mfma_f32_16x16x32_bf16 v[244:247], a[56:59], a[20:23], 0\n"
"v_mfma_f32_16x16x32_bf16 v[212:215], a[52:55], a[20:23], 0\n"
"ds_read_b64_tr_b16 a[136:137], v12 offset:22528\n"
"v_mfma_f32_16x16x32_bf16 v[180:183], a[48:51], a[20:23], 0\n"
"v_mfma_f32_16x16x32_bf16 v[148:151], a[44:47], a[20:23], 0\n"
"ds_read_b128 a[100:103], v7 offset:14336\n"
"v_mfma_f32_16x16x32_bf16 v[116:119], a[40:43], a[20:23], 0\n"
"buffer_load_dwordx4 v4, s[32:35], s38 offen offset:3072 lds\n"
"v_mfma_f32_16x16x32_bf16 v[84:87], a[36:39], a[20:23], 0\n"
"ds_read_b64_tr_b16 a[138:139], v12 offset:23552\n"
"v_mfma_f32_16x16x32_bf16 v[52:55], a[32:35], a[20:23], 0\n"
"v_mfma_f32_16x16x32_bf16 v[56:59], a[32:35], a[24:27], 0\n"
"v_mfma_f32_16x16x32_bf16 v[88:91], a[36:39], a[24:27], 0\n"
"ds_read_b64_tr_b16 a[140:141], v13 offset:22528\n"
"s_add_u32 m0, s40, 4096\n"
"v_mfma_f32_16x16x32_bf16 v[120:123], a[40:43], a[24:27], 0\n"
"buffer_load_dwordx4 v5, s[32:35], s38 offen offset:0 lds\n"
"s_add_u32 s38, s38, 229376\n"
"v_mfma_f32_16x16x32_bf16 v[152:155], a[44:47], a[24:27], 0\n"
"ds_read_b64_tr_b16 a[142:143], v13 offset:23552\n"
"v_mfma_f32_16x16x32_bf16 v[184:187], a[48:51], a[24:27], 0\n"
"v_mfma_f32_16x16x32_bf16 v[216:219], a[52:55], a[24:27], 0\n"
"v_mfma_f32_16x16x32_bf16 v[248:251], a[56:59], a[24:27], 0\n"
"s_add_u32 m0, s67, 0\n"
"v_mfma_f32_16x16x32_bf16 a[168:171], a[60:63], a[24:27], 0\n"
"buffer_load_dwordx4 v30, s[60:63], s66 offen offset:0 lds\n"
"v_mfma_f32_16x16x32_bf16 a[200:203], a[64:67], a[24:27], 0\n"
"v_mfma_f32_16x16x32_bf16 a[232:235], a[68:71], a[24:27], 0\n"
"v_mfma_f32_16x16x32_bf16 a[236:239], a[68:71], a[28:31], 0\n"
"v_mfma_f32_16x16x32_bf16 a[204:207], a[64:67], a[28:31], 0\n"
"v_mfma_f32_16x16x32_bf16 a[172:175], a[60:63], a[28:31], 0\n"
"buffer_load_dwordx4 v30, s[60:63], s66 offen offset:1024 lds\n"
"v_mfma_f32_16x16x32_bf16 v[252:255], a[56:59], a[28:31], 0\n"
"v_mfma_f32_16x16x32_bf16 v[220:223], a[52:55], a[28:31], 0\n"
"v_mfma_f32_16x16x32_bf16 v[188:191], a[48:51], a[28:31], 0\n"
"v_mfma_f32_16x16x32_bf16 v[156:159], a[44:47], a[28:31], 0\n"
"v_mfma_f32_16x16x32_bf16 v[124:127], a[40:43], a[28:31], 0\n"
"v_mfma_f32_16x16x32_bf16 v[92:95], a[36:39], a[28:31], 0\n"
"v_mfma_f32_16x16x32_bf16 v[60:63], a[32:35], a[28:31], 0\n"
"s_waitcnt vmcnt(15) lgkmcnt(0)\n"
"s_barrier\n"
"v_mfma_f32_16x16x32_bf16 v[32:35], a[104:107], a[72:75], v[32:35]\n"
"ds_read_b128 a[0:3], v6 offset:32768\n"
"s_add_u32 m0, s39, 0\n"
"v_mfma_f32_16x16x32_bf16 v[64:67], a[108:111], a[72:75], v[64:67]\n"
"buffer_load_dwordx4 v20, s[44:47], s36 offen offset:0 lds\n"
"s_add_u32 s36, s36, 15360\n"
"v_mfma_f32_16x16x32_bf16 v[96:99], a[112:115], a[72:75], v[96:99]\n"
"ds_read_b64_tr_b16 a[32:33], v8 offset:40960\n"
"v_mfma_f32_16x16x32_bf16 v[128:131], a[116:119], a[72:75], v[128:131]\n"
"v_mfma_f32_16x16x32_bf16 v[160:163], a[120:123], a[72:75], v[160:163]\n"
"ds_read_b64_tr_b16 a[34:35], v8 offset:41472\n"
"v_mfma_f32_16x16x32_bf16 v[192:195], a[124:127], a[72:75], v[192:195]\n"
"v_mfma_f32_16x16x32_bf16 v[224:227], a[128:131], a[72:75], v[224:227]\n"
"buffer_load_dwordx4 v21, s[44:47], s36 offen offset:1024 lds\n"
"s_add_u32 s36, s36, 15360\n"
"v_mfma_f32_16x16x32_bf16 a[144:147], a[132:135], a[72:75], a[144:147]\n"
"ds_read_b128 a[4:7], v6 offset:34816\n"
"v_mfma_f32_16x16x32_bf16 a[176:179], a[136:139], a[72:75], a[176:179]\n"
"v_mfma_f32_16x16x32_bf16 a[208:211], a[140:143], a[72:75], a[208:211]\n"
"ds_read_b64_tr_b16 a[36:37], v9 offset:40960\n"
"v_mfma_f32_16x16x32_bf16 a[212:215], a[140:143], a[76:79], a[212:215]\n"
"v_mfma_f32_16x16x32_bf16 a[180:183], a[136:139], a[76:79], a[180:183]\n"
"ds_read_b64_tr_b16 a[38:39], v9 offset:41472\n"
"v_mfma_f32_16x16x32_bf16 a[148:151], a[132:135], a[76:79], a[148:151]\n"
"buffer_load_dwordx4 v20, s[44:47], s36 offen offset:2048 lds\n"
"s_add_u32 s36, s36, 15360\n"
"v_mfma_f32_16x16x32_bf16 v[228:231], a[128:131], a[76:79], v[228:231]\n"
"v_mfma_f32_16x16x32_bf16 v[196:199], a[124:127], a[76:79], v[196:199]\n"
"ds_read_b64_tr_b16 a[40:41], v10 offset:40960\n"
"v_mfma_f32_16x16x32_bf16 v[164:167], a[120:123], a[76:79], v[164:167]\n"
"v_mfma_f32_16x16x32_bf16 v[132:135], a[116:119], a[76:79], v[132:135]\n"
"ds_read_b128 a[8:11], v6 offset:36864\n"
"v_mfma_f32_16x16x32_bf16 v[100:103], a[112:115], a[76:79], v[100:103]\n"
"v_mfma_f32_16x16x32_bf16 v[68:71], a[108:111], a[76:79], v[68:71]\n"
"ds_read_b64_tr_b16 a[42:43], v10 offset:41984\n"
"buffer_load_dwordx4 v21, s[44:47], s36 offen offset:3072 lds\n"
"s_add_u32 s36, s36, 19456\n"
"v_mfma_f32_16x16x32_bf16 v[36:39], a[104:107], a[76:79], v[36:39]\n"
"v_mfma_f32_16x16x32_bf16 v[40:43], a[104:107], a[80:83], v[40:43]\n"
"v_mfma_f32_16x16x32_bf16 v[72:75], a[108:111], a[80:83], v[72:75]\n"
"ds_read_b64_tr_b16 a[44:45], v11 offset:40960\n"
"v_mfma_f32_16x16x32_bf16 v[104:107], a[112:115], a[80:83], v[104:107]\n"
"v_mfma_f32_16x16x32_bf16 v[136:139], a[116:119], a[80:83], v[136:139]\n"
"ds_read_b128 a[12:15], v6 offset:38912\n"
"s_add_u32 m0, s39, 4096\n"
"v_mfma_f32_16x16x32_bf16 v[168:171], a[120:123], a[80:83], v[168:171]\n"
"buffer_load_dwordx4 v20, s[44:47], s36 offen offset:0 lds\n"
"s_add_u32 s36, s36, 15360\n"
"v_mfma_f32_16x16x32_bf16 v[200:203], a[124:127], a[80:83], v[200:203]\n"
"ds_read_b64_tr_b16 a[46:47], v11 offset:41984\n"
"v_mfma_f32_16x16x32_bf16 v[232:235], a[128:131], a[80:83], v[232:235]\n"
"v_mfma_f32_16x16x32_bf16 a[152:155], a[132:135], a[80:83], a[152:155]\n"
"v_mfma_f32_16x16x32_bf16 a[184:187], a[136:139], a[80:83], a[184:187]\n"
"ds_read_b64_tr_b16 a[48:49], v12 offset:40960\n"
"v_mfma_f32_16x16x32_bf16 a[216:219], a[140:143], a[80:83], a[216:219]\n"
"v_mfma_f32_16x16x32_bf16 a[220:223], a[140:143], a[84:87], a[220:223]\n"
"ds_read_b64_tr_b16 a[50:51], v12 offset:41984\n"
"s_waitcnt vmcnt(12)\n"
"v_lshlrev_b32 v30, 16, v16\n"
"buffer_load_dwordx4 v21, s[44:47], s36 offen offset:1024 lds\n"
"s_add_u32 s36, s36, 15360\n"
"v_mfma_f32_16x16x32_bf16 a[188:191], a[136:139], a[84:87], a[188:191]\n"
"v_and_b32 v16, 0xffff0000, v16\n"
"v_mfma_f32_16x16x32_bf16 a[156:159], a[132:135], a[84:87], a[156:159]\n"
"v_add_f32 v22, v30, v16\n"
"v_mfma_f32_16x16x32_bf16 v[236:239], a[128:131], a[84:87], v[236:239]\n"
"ds_read_b128 a[16:19], v6 offset:40960\n"
"v_mul_f32 v23, v30, v30\n"
"v_mfma_f32_16x16x32_bf16 v[204:207], a[124:127], a[84:87], v[204:207]\n"
"v_fmac_f32 v23, v16, v16\n"
"v_mfma_f32_16x16x32_bf16 v[172:175], a[120:123], a[84:87], v[172:175]\n"
"ds_read_b64_tr_b16 a[52:53], v13 offset:40960\n"
"v_lshlrev_b32 v30, 16, v17\n"
"v_mfma_f32_16x16x32_bf16 v[140:143], a[116:119], a[84:87], v[140:143]\n"
"v_and_b32 v17, 0xffff0000, v17\n"
"buffer_load_dwordx4 v20, s[44:47], s36 offen offset:2048 lds\n"
"s_add_u32 s36, s36, 15360\n"
"v_mfma_f32_16x16x32_bf16 v[108:111], a[112:115], a[84:87], v[108:111]\n"
"ds_read_b64_tr_b16 a[54:55], v13 offset:41984\n"
"v_add_f32 v22, v22, v30\n"
"v_mfma_f32_16x16x32_bf16 v[76:79], a[108:111], a[84:87], v[76:79]\n"
"v_fmac_f32 v23, v30, v30\n"
"v_mfma_f32_16x16x32_bf16 v[44:47], a[104:107], a[84:87], v[44:47]\n"
"v_add_f32 v22, v22, v17\n"
"v_mfma_f32_16x16x32_bf16 v[48:51], a[104:107], a[88:91], v[48:51]\n"
"ds_read_b128 a[20:23], v6 offset:43008\n"
"v_fmac_f32 v23, v17, v17\n"
"v_mfma_f32_16x16x32_bf16 v[80:83], a[108:111], a[88:91], v[80:83]\n"
"v_lshlrev_b32 v30, 16, v18\n"
"v_mfma_f32_16x16x32_bf16 v[112:115], a[112:115], a[88:91], v[112:115]\n"
"ds_read_b64_tr_b16 a[56:57], v10 offset:43008\n"
"v_and_b32 v18, 0xffff0000, v18\n"
"buffer_load_dwordx4 v21, s[44:47], s36 offen offset:3072 lds\n"
"s_sub_u32 s36, s36, 111488\n"
"v_mfma_f32_16x16x32_bf16 v[144:147], a[116:119], a[88:91], v[144:147]\n"
"v_add_f32 v22, v22, v30\n"
"v_mfma_f32_16x16x32_bf16 v[176:179], a[120:123], a[88:91], v[176:179]\n"
"ds_read_b64_tr_b16 a[58:59], v10 offset:44032\n"
"v_fmac_f32 v23, v30, v30\n"
"v_mfma_f32_16x16x32_bf16 v[208:211], a[124:127], a[88:91], v[208:211]\n"
"v_add_f32 v22, v22, v18\n"
"v_mfma_f32_16x16x32_bf16 v[240:243], a[128:131], a[88:91], v[240:243]\n"
"v_fmac_f32 v23, v18, v18\n"
"v_mfma_f32_16x16x32_bf16 a[160:163], a[132:135], a[88:91], a[160:163]\n"
"ds_read_b64_tr_b16 a[60:61], v11 offset:43008\n"
"v_lshlrev_b32 v30, 16, v19\n"
"s_add_u32 m0, s40, 20480\n"
"v_mfma_f32_16x16x32_bf16 a[192:195], a[136:139], a[88:91], a[192:195]\n"
"v_and_b32 v19, 0xffff0000, v19\n"
"buffer_load_dwordx4 v1, s[24:27], s37 offen offset:0 lds\n"
"s_add_u32 s37, s37, 196608\n"
"v_mfma_f32_16x16x32_bf16 a[224:227], a[140:143], a[88:91], a[224:227]\n"
"ds_read_b128 a[24:27], v6 offset:45056\n"
"v_add_f32 v22, v22, v30\n"
"v_mfma_f32_16x16x32_bf16 a[228:231], a[140:143], a[92:95], a[228:231]\n"
"v_fmac_f32 v23, v30, v30\n"
"v_mfma_f32_16x16x32_bf16 a[196:199], a[136:139], a[92:95], a[196:199]\n"
"ds_read_b64_tr_b16 a[62:63], v11 offset:44032\n"
"v_add_f32 v22, v22, v19\n"
"v_mfma_f32_16x16x32_bf16 a[164:167], a[132:135], a[92:95], a[164:167]\n"
"v_fmac_f32 v23, v19, v19\n"
"v_mfma_f32_16x16x32_bf16 v[244:247], a[128:131], a[92:95], v[244:247]\n"
"buffer_load_dwordx4 v[16:19], v0, s[44:47], s41 offen offset:2048\n"
"v_mfma_f32_16x16x32_bf16 v[212:215], a[124:127], a[92:95], v[212:215]\n"
"ds_read_b64_tr_b16 a[64:65], v12 offset:43008\n"
"v_lshlrev_b32 v30, 16, v24\n"
"v_and_b32 v24, 0xffff0000, v24\n"
"buffer_load_dwordx4 v2, s[28:31], s38 offen offset:1024 lds\n"
"v_mfma_f32_16x16x32_bf16 v[180:183], a[120:123], a[92:95], v[180:183]\n"
"v_add_f32 v22, v22, v30\n"
"v_mfma_f32_16x16x32_bf16 v[148:151], a[116:119], a[92:95], v[148:151]\n"
"ds_read_b128 a[28:31], v6 offset:47104\n"
"v_fmac_f32 v23, v30, v30\n"
"v_mfma_f32_16x16x32_bf16 v[116:119], a[112:115], a[92:95], v[116:119]\n"
"v_add_f32 v22, v22, v24\n"
"v_mfma_f32_16x16x32_bf16 v[84:87], a[108:111], a[92:95], v[84:87]\n"
"ds_read_b64_tr_b16 a[66:67], v12 offset:44032\n"
"v_fmac_f32 v23, v24, v24\n"
"v_mfma_f32_16x16x32_bf16 v[52:55], a[104:107], a[92:95], v[52:55]\n"
"v_lshlrev_b32 v30, 16, v25\n"
"v_mfma_f32_16x16x32_bf16 v[56:59], a[104:107], a[96:99], v[56:59]\n"
"v_and_b32 v25, 0xffff0000, v25\n"
"buffer_load_dwordx4 v3, s[28:31], s38 offen offset:2048 lds\n"
"v_mfma_f32_16x16x32_bf16 v[88:91], a[108:111], a[96:99], v[88:91]\n"
"ds_read_b64_tr_b16 a[68:69], v13 offset:43008\n"
"v_add_f32 v22, v22, v30\n"
"v_mfma_f32_16x16x32_bf16 v[120:123], a[112:115], a[96:99], v[120:123]\n"
"v_fmac_f32 v23, v30, v30\n"
"v_mfma_f32_16x16x32_bf16 v[152:155], a[116:119], a[96:99], v[152:155]\n"
"ds_read_b64_tr_b16 a[70:71], v13 offset:44032\n"
"v_add_f32 v22, v22, v25\n"
"v_mfma_f32_16x16x32_bf16 v[184:187], a[120:123], a[96:99], v[184:187]\n"
"v_fmac_f32 v23, v25, v25\n"
"v_mfma_f32_16x16x32_bf16 v[216:219], a[124:127], a[96:99], v[216:219]\n"
"v_lshlrev_b32 v30, 16, v26\n"
"v_mfma_f32_16x16x32_bf16 v[248:251], a[128:131], a[96:99], v[248:251]\n"
"v_and_b32 v26, 0xffff0000, v26\n"
"buffer_load_dwordx4 v4, s[32:35], s38 offen offset:3072 lds\n"
"v_mfma_f32_16x16x32_bf16 a[168:171], a[132:135], a[96:99], a[168:171]\n"
"v_add_f32 v22, v22, v30\n"
"v_mfma_f32_16x16x32_bf16 a[200:203], a[136:139], a[96:99], a[200:203]\n"
"v_fmac_f32 v23, v30, v30\n"
"v_mfma_f32_16x16x32_bf16 a[232:235], a[140:143], a[96:99], a[232:235]\n"
"v_add_f32 v22, v22, v26\n"
"v_mfma_f32_16x16x32_bf16 a[236:239], a[140:143], a[100:103], a[236:239]\n"
"v_fmac_f32 v23, v26, v26\n"
"v_mfma_f32_16x16x32_bf16 a[204:207], a[136:139], a[100:103], a[204:207]\n"
"v_lshlrev_b32 v30, 16, v27\n"
"s_add_u32 m0, s40, 24576\n"
"v_mfma_f32_16x16x32_bf16 a[172:175], a[132:135], a[100:103], a[172:175]\n"
"v_and_b32 v27, 0xffff0000, v27\n"
"buffer_load_dwordx4 v5, s[32:35], s38 offen offset:0 lds\n"
"s_add_u32 s38, s38, 229376\n"
"v_mfma_f32_16x16x32_bf16 v[252:255], a[128:131], a[100:103], v[252:255]\n"
"v_add_f32 v22, v22, v30\n"
"v_mfma_f32_16x16x32_bf16 v[220:223], a[124:127], a[100:103], v[220:223]\n"
"v_fmac_f32 v23, v30, v30\n"
"v_mfma_f32_16x16x32_bf16 v[188:191], a[120:123], a[100:103], v[188:191]\n"
"v_add_f32 v22, v22, v27\n"
"v_mfma_f32_16x16x32_bf16 v[156:159], a[116:119], a[100:103], v[156:159]\n"
"v_fmac_f32 v23, v27, v27\n"
"v_mfma_f32_16x16x32_bf16 v[124:127], a[112:115], a[100:103], v[124:127]\n"
"buffer_load_dwordx4 v[24:27], v0, s[44:47], s41 offen offset:3072\n"
"v_mfma_f32_16x16x32_bf16 v[92:95], a[108:111], a[100:103], v[92:95]\n"
"v_mfma_f32_16x16x32_bf16 v[60:63], a[104:107], a[100:103], v[60:63]\n"
"s_waitcnt vmcnt(25) lgkmcnt(0)\n"
"s_barrier\n"
"v_mfma_f32_16x16x32_bf16 v[32:35], a[32:35], a[0:3], v[32:35]\n"
"ds_read_b128 a[72:75], v7 offset:32768\n"
"s_add_u32 m0, s40, 40960\n"
"v_mfma_f32_16x16x32_bf16 v[64:67], a[36:39], a[0:3], v[64:67]\n"
"buffer_load_dwordx4 v1, s[24:27], s37 offen offset:0 lds\n"
"s_add_u32 s37, s37, 196608\n"
"v_mfma_f32_16x16x32_bf16 v[96:99], a[40:43], a[0:3], v[96:99]\n"
"ds_read_b64_tr_b16 a[104:105], v8 offset:61440\n"
"v_mfma_f32_16x16x32_bf16 v[128:131], a[44:47], a[0:3], v[128:131]\n"
"v_mfma_f32_16x16x32_bf16 v[160:163], a[48:51], a[0:3], v[160:163]\n"
"ds_read_b64_tr_b16 a[106:107], v8 offset:61952\n"
"v_mfma_f32_16x16x32_bf16 v[192:195], a[52:55], a[0:3], v[192:195]\n"
"v_mfma_f32_16x16x32_bf16 v[224:227], a[56:59], a[0:3], v[224:227]\n"
"v_mfma_f32_16x16x32_bf16 a[144:147], a[60:63], a[0:3], a[144:147]\n"
"ds_read_b128 a[76:79], v7 offset:34816\n"
"v_mfma_f32_16x16x32_bf16 a[176:179], a[64:67], a[0:3], a[176:179]\n"
"v_mfma_f32_16x16x32_bf16 a[208:211], a[68:71], a[0:3], a[208:211]\n"
"ds_read_b64_tr_b16 a[108:109], v9 offset:61440\n"
"v_mfma_f32_16x16x32_bf16 a[212:215], a[68:71], a[4:7], a[212:215]\n"
"s_min_u32 s90, s90, END_%=-CB_%=-1024\n"
"v_mfma_f32_16x16x32_bf16 a[180:183], a[64:67], a[4:7], a[180:183]\n"
"ds_read_b64_tr_b16 a[110:111], v9 offset:61952\n"
"v_mfma_f32_16x16x32_bf16 a[148:151], a[60:63], a[4:7], a[148:151]\n"
"s_add_u32 s18, s18, s90\n"
"s_addc_u32 s19, s19, 0\n"
"v_mfma_f32_16x16x32_bf16 v[228:231], a[56:59], a[4:7], v[228:231]\n"
"v_mfma_f32_16x16x32_bf16 v[196:199], a[52:55], a[4:7], v[196:199]\n"
"ds_read_b64_tr_b16 a[112:113], v10 offset:61440\n"
"v_mfma_f32_16x16x32_bf16 v[164:167], a[48:51], a[4:7], v[164:167]\n"
"s_sub_u32 s95, s95, 7296\n"
"v_mfma_f32_16x16x32_bf16 v[132:135], a[44:47], a[4:7], v[132:135]\n"
"ds_read_b128 a[80:83], v7 offset:36864\n"
"buffer_load_dwordx4 v2, s[28:31], s38 offen offset:1024 lds\n"
"v_mfma_f32_16x16x32_bf16 v[100:103], a[40:43], a[4:7], v[100:103]\n"
"s_sub_u32 s94, s94, 2112\n"
"v_mfma_f32_16x16x32_bf16 v[68:71], a[36:39], a[4:7], v[68:71]\n"
"ds_read_b64_tr_b16 a[114:115], v10 offset:62464\n"
"v_mfma_f32_16x16x32_bf16 v[36:39], a[32:35], a[4:7], v[36:39]\n"
"v_mfma_f32_16x16x32_bf16 v[40:43], a[32:35], a[8:11], v[40:43]\n"
"v_readfirstlane_b32 s90, v28\n"
"v_mfma_f32_16x16x32_bf16 v[72:75], a[36:39], a[8:11], v[72:75]\n"
"ds_read_b64_tr_b16 a[116:117], v11 offset:61440\n"
"v_mfma_f32_16x16x32_bf16 v[104:107], a[40:43], a[8:11], v[104:107]\n"
"v_readfirstlane_b32 s91, v29\n"
"v_mfma_f32_16x16x32_bf16 v[136:139], a[44:47], a[8:11], v[136:139]\n"
"ds_read_b128 a[84:87], v7 offset:38912\n"
"v_mfma_f32_16x16x32_bf16 v[168:171], a[48:51], a[8:11], v[168:171]\n"
"v_mfma_f32_16x16x32_bf16 v[200:203], a[52:55], a[8:11], v[200:203]\n"
"ds_read_b64_tr_b16 a[118:119], v11 offset:62464\n"
"v_mfma_f32_16x16x32_bf16 v[232:235], a[56:59], a[8:11], v[232:235]\n"
"v_mfma_f32_16x16x32_bf16 a[152:155], a[60:63], a[8:11], a[152:155]\n"
"s_add_u32 s90, s90, 1\n"
"v_mfma_f32_16x16x32_bf16 a[184:187], a[64:67], a[8:11], a[184:187]\n"
"ds_read_b64_tr_b16 a[120:121], v12 offset:61440\n"
"v_mfma_f32_16x16x32_bf16 a[216:219], a[68:71], a[8:11], a[216:219]\n"
"v_mfma_f32_16x16x32_bf16 a[220:223], a[68:71], a[12:15], a[220:223]\n"
"ds_read_b64_tr_b16 a[122:123], v12 offset:62464\n"
"v_accvgpr_write_b32 a246, s90\n"
"v_mfma_f32_16x16x32_bf16 a[188:191], a[64:67], a[12:15], a[188:191]\n"
"buffer_load_dwordx4 v3, s[28:31], s38 offen offset:2048 lds\n"
"v_mfma_f32_16x16x32_bf16 a[156:159], a[60:63], a[12:15], a[156:159]\n"
"v_accvgpr_write_b32 a247, s91\n"
"v_mfma_f32_16x16x32_bf16 v[236:239], a[56:59], a[12:15], v[236:239]\n"
"ds_read_b128 a[88:91], v7 offset:40960\n"
"v_mfma_f32_16x16x32_bf16 v[204:207], a[52:55], a[12:15], v[204:207]\n"
"v_mfma_f32_16x16x32_bf16 v[172:175], a[48:51], a[12:15], v[172:175]\n"
"ds_read_b64_tr_b16 a[124:125], v13 offset:61440\n"
"v_accvgpr_write_b32 a248, s90\n"
"v_mfma_f32_16x16x32_bf16 v[140:143], a[44:47], a[12:15], v[140:143]\n"
"v_mfma_f32_16x16x32_bf16 v[108:111], a[40:43], a[12:15], v[108:111]\n"
"ds_read_b64_tr_b16 a[126:127], v13 offset:62464\n"
"v_accvgpr_write_b32 a249, s91\n"
"v_mfma_f32_16x16x32_bf16 v[76:79], a[36:39], a[12:15], v[76:79]\n"
"v_mfma_f32_16x16x32_bf16 v[44:47], a[32:35], a[12:15], v[44:47]\n"
"v_mfma_f32_16x16x32_bf16 v[48:51], a[32:35], a[16:19], v[48:51]\n"
"ds_read_b128 a[92:95], v7 offset:43008\n"
"v_mfma_f32_16x16x32_bf16 v[80:83], a[36:39], a[16:19], v[80:83]\n"
"v_mfma_f32_16x16x32_bf16 v[112:115], a[40:43], a[16:19], v[112:115]\n"
"ds_read_b64_tr_b16 a[128:129], v10 offset:63488\n"
"v_mfma_f32_16x16x32_bf16 v[144:147], a[44:47], a[16:19], v[144:147]\n"
"v_mfma_f32_16x16x32_bf16 v[176:179], a[48:51], a[16:19], v[176:179]\n"
"ds_read_b64_tr_b16 a[130:131], v10 offset:64512\n"
"v_mfma_f32_16x16x32_bf16 v[208:211], a[52:55], a[16:19], v[208:211]\n"
"v_mfma_f32_16x16x32_bf16 v[240:243], a[56:59], a[16:19], v[240:243]\n"
"v_mfma_f32_16x16x32_bf16 a[160:163], a[60:63], a[16:19], a[160:163]\n"
"ds_read_b64_tr_b16 a[132:133], v11 offset:63488\n"
"buffer_load_dwordx4 v4, s[32:35], s38 offen offset:3072 lds\n"
"v_mfma_f32_16x16x32_bf16 a[192:195], a[64:67], a[16:19], a[192:195]\n"
"v_mfma_f32_16x16x32_bf16 a[224:227], a[68:71], a[16:19], a[224:227]\n"
"ds_read_b128 a[96:99], v7 offset:45056\n"
"v_mfma_f32_16x16x32_bf16 a[228:231], a[68:71], a[20:23], a[228:231]\n"
"v_mfma_f32_16x16x32_bf16 a[196:199], a[64:67], a[20:23], a[196:199]\n"
"ds_read_b64_tr_b16 a[134:135], v11 offset:64512\n"
"v_mfma_f32_16x16x32_bf16 a[164:167], a[60:63], a[20:23], a[164:167]\n"
"v_mfma_f32_16x16x32_bf16 v[244:247], a[56:59], a[20:23], v[244:247]\n"
"v_mfma_f32_16x16x32_bf16 v[212:215], a[52:55], a[20:23], v[212:215]\n"
"ds_read_b64_tr_b16 a[136:137], v12 offset:63488\n"
"v_mfma_f32_16x16x32_bf16 v[180:183], a[48:51], a[20:23], v[180:183]\n"
"v_mfma_f32_16x16x32_bf16 v[148:151], a[44:47], a[20:23], v[148:151]\n"
"ds_read_b128 a[100:103], v7 offset:47104\n"
"v_mfma_f32_16x16x32_bf16 v[116:119], a[40:43], a[20:23], v[116:119]\n"
"v_mfma_f32_16x16x32_bf16 v[84:87], a[36:39], a[20:23], v[84:87]\n"
"ds_read_b64_tr_b16 a[138:139], v12 offset:64512\n"
"v_mfma_f32_16x16x32_bf16 v[52:55], a[32:35], a[20:23], v[52:55]\n"
"v_mfma_f32_16x16x32_bf16 v[56:59], a[32:35], a[24:27], v[56:59]\n"
"v_mfma_f32_16x16x32_bf16 v[88:91], a[36:39], a[24:27], v[88:91]\n"
"ds_read_b64_tr_b16 a[140:141], v13 offset:63488\n"
"s_add_u32 m0, s40, 45056\n"
"v_mfma_f32_16x16x32_bf16 v[120:123], a[40:43], a[24:27], v[120:123]\n"
"buffer_load_dwordx4 v5, s[32:35], s38 offen offset:0 lds\n"
"s_add_u32 s38, s38, 229376\n"
"v_mfma_f32_16x16x32_bf16 v[152:155], a[44:47], a[24:27], v[152:155]\n"
"ds_read_b64_tr_b16 a[142:143], v13 offset:64512\n"
"v_mfma_f32_16x16x32_bf16 v[184:187], a[48:51], a[24:27], v[184:187]\n"
"v_mfma_f32_16x16x32_bf16 v[216:219], a[52:55], a[24:27], v[216:219]\n"
"v_mfma_f32_16x16x32_bf16 v[248:251], a[56:59], a[24:27], v[248:251]\n"
"v_mfma_f32_16x16x32_bf16 a[168:171], a[60:63], a[24:27], a[168:171]\n"
"v_mfma_f32_16x16x32_bf16 a[200:203], a[64:67], a[24:27], a[200:203]\n"
"v_mfma_f32_16x16x32_bf16 a[232:235], a[68:71], a[24:27], a[232:235]\n"
"v_mfma_f32_16x16x32_bf16 a[236:239], a[68:71], a[28:31], a[236:239]\n"
"v_mfma_f32_16x16x32_bf16 a[204:207], a[64:67], a[28:31], a[204:207]\n"
"v_mfma_f32_16x16x32_bf16 a[172:175], a[60:63], a[28:31], a[172:175]\n"
"v_mfma_f32_16x16x32_bf16 v[252:255], a[56:59], a[28:31], v[252:255]\n"
"v_mfma_f32_16x16x32_bf16 v[220:223], a[52:55], a[28:31], v[220:223]\n"
"v_mfma_f32_16x16x32_bf16 v[188:191], a[48:51], a[28:31], v[188:191]\n"
"v_mfma_f32_16x16x32_bf16 v[156:159], a[44:47], a[28:31], v[156:159]\n"
"v_mfma_f32_16x16x32_bf16 v[124:127], a[40:43], a[28:31], v[124:127]\n"
"v_mfma_f32_16x16x32_bf16 v[92:95], a[36:39], a[28:31], v[92:95]\n"
"v_mfma_f32_16x16x32_bf16 v[60:63], a[32:35], a[28:31], v[60:63]\n"
"s_waitcnt vmcnt(12) lgkmcnt(0)\n"
"s_barrier\n"
"v_mfma_f32_16x16x32_bf16 v[32:35], a[104:107], a[72:75], v[32:35]\n"
"ds_read_b128 a[0:3], v6 offset:0\n"
"s_add_u32 m0, s39, 32768\n"
"v_mfma_f32_16x16x32_bf16 v[64:67], a[108:111], a[72:75], v[64:67]\n"
"buffer_load_dwordx4 v20, s[44:47], s36 offen offset:0 lds\n"
"s_add_u32 s36, s36, 15360\n"
"v_mfma_f32_16x16x32_bf16 v[96:99], a[112:115], a[72:75], v[96:99]\n"
"ds_read_b64_tr_b16 a[32:33], v8 offset:0\n"
"v_mfma_f32_16x16x32_bf16 v[128:131], a[116:119], a[72:75], v[128:131]\n"
"v_mfma_f32_16x16x32_bf16 v[160:163], a[120:123], a[72:75], v[160:163]\n"
"ds_read_b64_tr_b16 a[34:35], v8 offset:512\n"
"v_mfma_f32_16x16x32_bf16 v[192:195], a[124:127], a[72:75], v[192:195]\n"
"v_mfma_f32_16x16x32_bf16 v[224:227], a[128:131], a[72:75], v[224:227]\n"
"s_waitcnt vmcnt(6)\n"
"v_lshlrev_b32 v30, 16, v16\n"
"buffer_load_dwordx4 v21, s[44:47], s36 offen offset:1024 lds\n"
"s_add_u32 s36, s36, 15360\n"
"v_mfma_f32_16x16x32_bf16 a[144:147], a[132:135], a[72:75], a[144:147]\n"
"ds_read_b128 a[4:7], v6 offset:2048\n"
"v_and_b32 v16, 0xffff0000, v16\n"
"v_mfma_f32_16x16x32_bf16 a[176:179], a[136:139], a[72:75], a[176:179]\n"
"v_add_f32 v28, v30, v16\n"
"v_mul_f32 v29, v30, v30\n"
"v_mfma_f32_16x16x32_bf16 a[208:211], a[140:143], a[72:75], a[208:211]\n"
"ds_read_b64_tr_b16 a[36:37], v9 offset:0\n"
"v_fmac_f32 v29, v16, v16\n"
"v_mfma_f32_16x16x32_bf16 a[212:215], a[140:143], a[76:79], a[212:215]\n"
"v_lshlrev_b32 v30, 16, v17\n"
"v_and_b32 v17, 0xffff0000, v17\n"
"v_mfma_f32_16x16x32_bf16 a[180:183], a[136:139], a[76:79], a[180:183]\n"
"ds_read_b64_tr_b16 a[38:39], v9 offset:512\n"
"v_add_f32 v28, v28, v30\n"
"v_mfma_f32_16x16x32_bf16 a[148:151], a[132:135], a[76:79], a[148:151]\n"
"v_fmac_f32 v29, v30, v30\n"
"buffer_load_dwordx4 v20, s[44:47], s36 offen offset:2048 lds\n"
"s_add_u32 s36, s36, 15360\n"
"v_mfma_f32_16x16x32_bf16 v[228:231], a[128:131], a[76:79], v[228:231]\n"
"v_add_f32 v28, v28, v17\n"
"v_fmac_f32 v29, v17, v17\n"
"v_mfma_f32_16x16x32_bf16 v[196:199], a[124:127], a[76:79], v[196:199]\n"
"ds_read_b64_tr_b16 a[40:41], v10 offset:0\n"
"v_lshlrev_b32 v30, 16, v18\n"
"v_mfma_f32_16x16x32_bf16 v[164:167], a[120:123], a[76:79], v[164:167]\n"
"v_and_b32 v18, 0xffff0000, v18\n"
"v_add_f32 v28, v28, v30\n"
"v_mfma_f32_16x16x32_bf16 v[132:135], a[116:119], a[76:79], v[132:135]\n"
"ds_read_b128 a[8:11], v6 offset:4096\n"
"v_fmac_f32 v29, v30, v30\n"
"v_mfma_f32_16x16x32_bf16 v[100:103], a[112:115], a[76:79], v[100:103]\n"
"v_add_f32 v28, v28, v18\n"
"v_mfma_f32_16x16x32_bf16 v[68:71], a[108:111], a[76:79], v[68:71]\n"
"ds_read_b64_tr_b16 a[42:43], v10 offset:1024\n"
"v_fmac_f32 v29, v18, v18\n"
"v_lshlrev_b32 v30, 16, v19\n"
"buffer_load_dwordx4 v21, s[44:47], s36 offen offset:3072 lds\n"
"s_add_u32 s36, s36, 19456\n"
"v_mfma_f32_16x16x32_bf16 v[36:39], a[104:107], a[76:79], v[36:39]\n"
"v_and_b32 v19, 0xffff0000, v19\n"
"v_mfma_f32_16x16x32_bf16 v[40:43], a[104:107], a[80:83], v[40:43]\n"
"v_add_f32 v28, v28, v30\n"
"v_fmac_f32 v29, v30, v30\n"
"v_mfma_f32_16x16x32_bf16 v[72:75], a[108:111], a[80:83], v[72:75]\n"
"ds_read_b64_tr_b16 a[44:45], v11 offset:0\n"
"v_add_f32 v28, v28, v19\n"
"v_mfma_f32_16x16x32_bf16 v[104:107], a[112:115], a[80:83], v[104:107]\n"
"v_fmac_f32 v29, v19, v19\n"
"v_mfma_f32_16x16x32_bf16 v[136:139], a[116:119], a[80:83], v[136:139]\n"
"ds_read_b128 a[12:15], v6 offset:6144\n"
"v_lshlrev_b32 v30, 16, v24\n"
"v_and_b32 v24, 0xffff0000, v24\n"
"s_add_u32 m0, s39, 36864\n"
"v_mfma_f32_16x16x32_bf16 v[168:171], a[120:123], a[80:83], v[168:171]\n"
"v_add_f32 v28, v28, v30\n"
"buffer_load_dwordx4 v20, s[44:47], s36 offen offset:0 lds\n"
"s_add_u32 s36, s36, 15360\n"
"v_mfma_f32_16x16x32_bf16 v[200:203], a[124:127], a[80:83], v[200:203]\n"
"ds_read_b64_tr_b16 a[46:47], v11 offset:1024\n"
"v_fmac_f32 v29, v30, v30\n"
"v_add_f32 v28, v28, v24\n"
"v_mfma_f32_16x16x32_bf16 v[232:235], a[128:131], a[80:83], v[232:235]\n"
"v_fmac_f32 v29, v24, v24\n"
"v_mfma_f32_16x16x32_bf16 a[152:155], a[132:135], a[80:83], a[152:155]\n"
"v_lshlrev_b32 v30, 16, v25\n"
"v_and_b32 v25, 0xffff0000, v25\n"
"v_mfma_f32_16x16x32_bf16 a[184:187], a[136:139], a[80:83], a[184:187]\n"
"ds_read_b64_tr_b16 a[48:49], v12 offset:0\n"
"v_add_f32 v28, v28, v30\n"
"v_mfma_f32_16x16x32_bf16 a[216:219], a[140:143], a[80:83], a[216:219]\n"
"v_fmac_f32 v29, v30, v30\n"
"v_mfma_f32_16x16x32_bf16 a[220:223], a[140:143], a[84:87], a[220:223]\n"
"ds_read_b64_tr_b16 a[50:51], v12 offset:1024\n"
"v_add_f32 v28, v28, v25\n"
"v_fmac_f32 v29, v25, v25\n"
"buffer_load_dwordx4 v21, s[44:47], s36 offen offset:1024 lds\n"
"s_add_u32 s36, s36, 15360\n"
"v_mfma_f32_16x16x32_bf16 a[188:191], a[136:139], a[84:87], a[188:191]\n"
"v_lshlrev_b32 v30, 16, v26\n"
"v_mfma_f32_16x16x32_bf16 a[156:159], a[132:135], a[84:87], a[156:159]\n"
"v_and_b32 v26, 0xffff0000, v26\n"
"v_add_f32 v28, v28, v30\n"
"v_mfma_f32_16x16x32_bf16 v[236:239], a[128:131], a[84:87], v[236:239]\n"
"ds_read_b128 a[16:19], v6 offset:8192\n"
"v_fmac_f32 v29, v30, v30\n"
"v_mfma_f32_16x16x32_bf16 v[204:207], a[124:127], a[84:87], v[204:207]\n"
"v_add_f32 v28, v28, v26\n"
"v_mfma_f32_16x16x32_bf16 v[172:175], a[120:123], a[84:87], v[172:175]\n"
"ds_read_b64_tr_b16 a[52:53], v13 offset:0\n"
"v_fmac_f32 v29, v26, v26\n"
"v_lshlrev_b32 v30, 16, v27\n"
"v_mfma_f32_16x16x32_bf16 v[140:143], a[116:119], a[84:87], v[140:143]\n"
"v_and_b32 v27, 0xffff0000, v27\n"
"buffer_load_dwordx4 v20, s[44:47], s36 offen offset:2048 lds\n"
"s_add_u32 s36, s36, 15360\n"
"v_mfma_f32_16x16x32_bf16 v[108:111], a[112:115], a[84:87], v[108:111]\n"
"ds_read_b64_tr_b16 a[54:55], v13 offset:1024\n"
"v_add_f32 v28, v28, v30\n"
"v_fmac_f32 v29, v30, v30\n"
"v_mfma_f32_16x16x32_bf16 v[76:79], a[108:111], a[84:87], v[76:79]\n"
"v_add_f32 v28, v28, v27\n"
"v_mfma_f32_16x16x32_bf16 v[44:47], a[104:107], a[84:87], v[44:47]\n"
"v_fmac_f32 v29, v27, v27\n"
"v_mfma_f32_16x16x32_bf16 v[48:51], a[104:107], a[88:91], v[48:51]\n"
"ds_read_b128 a[20:23], v6 offset:10240\n"
"v_permlane32_swap_b32 v22, v28\n"
"v_permlane32_swap_b32 v23, v29\n"
"v_mfma_f32_16x16x32_bf16 v[80:83], a[108:111], a[88:91], v[80:83]\n"
"v_add_f32 v22, v22, v28\n"
"v_mfma_f32_16x16x32_bf16 v[112:115], a[112:115], a[88:91], v[112:115]\n"
"ds_read_b64_tr_b16 a[56:57], v10 offset:2048\n"
"v_add_f32 v23, v23, v29\n"
"buffer_load_dwordx4 v21, s[44:47], s36 offen offset:3072 lds\n"
"s_sub_u32 s36, s36, 111488\n"
"v_mfma_f32_16x16x32_bf16 v[144:147], a[116:119], a[88:91], v[144:147]\n"
"v_add_f32_dpp v22, v22, v22 row_ror:8 row_mask:0xf bank_mask:0xf\n"
"v_add_f32_dpp v23, v23, v23 row_ror:8 row_mask:0xf bank_mask:0xf\n"
"v_mfma_f32_16x16x32_bf16 v[176:179], a[120:123], a[88:91], v[176:179]\n"
"ds_read_b64_tr_b16 a[58:59], v10 offset:3072\n"
"v_add_f32_dpp v22, v22, v22 row_ror:4 row_mask:0xf bank_mask:0xf\n"
"v_mfma_f32_16x16x32_bf16 v[208:211], a[124:127], a[88:91], v[208:211]\n"
"v_add_f32_dpp v23, v23, v23 row_ror:4 row_mask:0xf bank_mask:0xf\n"
"v_mfma_f32_16x16x32_bf16 v[240:243], a[128:131], a[88:91], v[240:243]\n"
"v_add_f32_dpp v22, v22, v22 row_ror:2 row_mask:0xf bank_mask:0xf\n"
"v_mfma_f32_16x16x32_bf16 a[160:163], a[132:135], a[88:91], a[160:163]\n"
"ds_read_b64_tr_b16 a[60:61], v11 offset:2048\n"
"v_add_f32_dpp v23, v23, v23 row_ror:2 row_mask:0xf bank_mask:0xf\n"
"v_add_f32_dpp v22, v22, v22 row_ror:1 row_mask:0xf bank_mask:0xf\n"
"s_add_u32 m0, s40, 61440\n"
"v_mfma_f32_16x16x32_bf16 a[192:195], a[136:139], a[88:91], a[192:195]\n"
"v_add_f32_dpp v23, v23, v23 row_ror:1 row_mask:0xf bank_mask:0xf\n"
"buffer_load_dwordx4 v1, s[24:27], s37 offen offset:0 lds\n"
"s_add_u32 s37, s37, 196608\n"
"v_mfma_f32_16x16x32_bf16 a[224:227], a[140:143], a[88:91], a[224:227]\n"
"ds_read_b128 a[24:27], v6 offset:12288\n"
"v_mov_b32 v28, v22\n"
"v_mfma_f32_16x16x32_bf16 a[228:231], a[140:143], a[92:95], a[228:231]\n"
"v_mov_b32 v29, v23\n"
"v_mfma_f32_16x16x32_bf16 a[196:199], a[136:139], a[92:95], a[196:199]\n"
"ds_read_b64_tr_b16 a[62:63], v11 offset:3072\n"
"v_lshrrev_b32 v30, 4, v0\n"
"v_permlane16_swap_b32 v28, v22\n"
"v_mfma_f32_16x16x32_bf16 a[164:167], a[132:135], a[92:95], a[164:167]\n"
"v_permlane16_swap_b32 v29, v23\n"
"v_mfma_f32_16x16x32_bf16 v[244:247], a[128:131], a[92:95], v[244:247]\n"
"v_add_f32 v22, v22, v28\n"
"v_mfma_f32_16x16x32_bf16 v[212:215], a[124:127], a[92:95], v[212:215]\n"
"ds_read_b64_tr_b16 a[64:65], v12 offset:2048\n"
"v_add_f32 v23, v23, v29\n"
"buffer_load_dwordx4 v2, s[28:31], s38 offen offset:1024 lds\n"
"v_mfma_f32_16x16x32_bf16 v[180:183], a[120:123], a[92:95], v[180:183]\n"
"v_mul_f32 v22, 0xba800000, v22\n"
"v_mul_f32 v23, 0x3a800000, v23\n"
"v_mfma_f32_16x16x32_bf16 v[148:151], a[116:119], a[92:95], v[148:151]\n"
"ds_read_b128 a[28:31], v6 offset:14336\n"
"v_cvt_pk_bf16_f32 v28, v22, v22\n"
"v_mfma_f32_16x16x32_bf16 v[116:119], a[112:115], a[92:95], v[116:119]\n"
"v_fma_f32 v23, -v22, v22, v23\n"
"v_mfma_f32_16x16x32_bf16 v[84:87], a[108:111], a[92:95], v[84:87]\n"
"ds_read_b64_tr_b16 a[66:67], v12 offset:3072\n"
"v_and_b32 v28, 0xffff0000, v28\n"
"v_max_f32 v23, 0, v23\n"
"v_mfma_f32_16x16x32_bf16 v[52:55], a[104:107], a[92:95], v[52:55]\n"
"v_sub_f32 v28, v22, v28\n"
"v_mfma_f32_16x16x32_bf16 v[56:59], a[104:107], a[96:99], v[56:59]\n"
"v_add_f32 v23, 0x3727c5ac, v23\n"
"buffer_load_dwordx4 v3, s[28:31], s38 offen offset:2048 lds\n"
"v_mfma_f32_16x16x32_bf16 v[88:91], a[108:111], a[96:99], v[88:91]\n"
"ds_read_b64_tr_b16 a[68:69], v13 offset:2048\n"
"v_and_b32 v30, 32, v30\n"
"v_mfma_f32_16x16x32_bf16 v[120:123], a[112:115], a[96:99], v[120:123]\n"
"v_rsq_f32 v26, v23\n"
"v_cvt_pk_bf16_f32 v16, v22, v28\n"
"v_mfma_f32_16x16x32_bf16 v[152:155], a[116:119], a[96:99], v[152:155]\n"
"ds_read_b64_tr_b16 a[70:71], v13 offset:3072\n"
"v_cvt_pk_bf16_f32 v17, v22, v28\n"
"v_mfma_f32_16x16x32_bf16 v[184:187], a[120:123], a[96:99], v[184:187]\n"
"v_cvt_pk_bf16_f32 v18, v22, v28\n"
"v_mfma_f32_16x16x32_bf16 v[216:219], a[124:127], a[96:99], v[216:219]\n"
"v_cvt_pk_bf16_f32 v19, v22, v28\n"
"v_mfma_f32_16x16x32_bf16 v[248:251], a[128:131], a[96:99], v[248:251]\n"
"v_mul_f32 v24, s92, v26\n"
"v_mul_f32 v25, v23, v26\n"
"buffer_load_dwordx4 v4, s[32:35], s38 offen offset:3072 lds\n"
"v_mfma_f32_16x16x32_bf16 a[168:171], a[132:135], a[96:99], a[168:171]\n"
"v_mov_b32 v27, v26\n"
"v_mfma_f32_16x16x32_bf16 a[200:203], a[136:139], a[96:99], a[200:203]\n"
"v_lshrrev_b32 v29, 3, v30\n"
"v_mfma_f32_16x16x32_bf16 a[232:235], a[140:143], a[96:99], a[232:235]\n"
"s_mov_b32 exec_lo, 1\n"
"s_mov_b32 exec_hi, 1\n"
"buffer_store_dwordx4 v[16:19], v30, s[20:23], s9 offen sc1\n"
"buffer_store_dwordx4 v[24:27], v30, s[20:23], s9 offen offset:16 sc1\n"
"buffer_store_dword v26, v29, s[20:23], s10 offen sc1\n"
"s_mov_b64 exec, -1\n"
"v_mfma_f32_16x16x32_bf16 a[236:239], a[140:143], a[100:103], a[236:239]\n"
"v_lshl_add_u32 v29, v0, 2, s95\n"
"v_lshl_add_u32 v30, v0, 2, s94\n"
"v_mfma_f32_16x16x32_bf16 a[204:207], a[136:139], a[100:103], a[204:207]\n"
"v_cmp_gt_u32 vcc, 608, v0\n"
"s_add_u32 m0, s40, 65536\n"
"v_mfma_f32_16x16x32_bf16 a[172:175], a[132:135], a[100:103], a[172:175]\n"
"v_bfrev_b32 v23, 3\n"
"buffer_load_dwordx4 v5, s[32:35], s38 offen offset:0 lds\n"
"s_add_u32 s38, s38, 229376\n"
"v_mfma_f32_16x16x32_bf16 v[252:255], a[128:131], a[100:103], v[252:255]\n"
"v_cndmask_b32 v29, v23, v29, vcc\n"
"v_cmp_gt_u32 vcc, 176, v0\n"
"s_cmp_eq_u32 s2, 6\n"
"v_mfma_f32_16x16x32_bf16 v[220:223], a[124:127], a[100:103], v[220:223]\n"
"s_cselect_b32 s96, 6291456, 0\n"
"v_mfma_f32_16x16x32_bf16 v[188:191], a[120:123], a[100:103], v[188:191]\n"
"v_cndmask_b32 v30, v23, v30, vcc\n"
"s_cselect_b32 s97, 7340032, 0\n"
"v_mfma_f32_16x16x32_bf16 v[156:159], a[116:119], a[100:103], v[156:159]\n"
"v_accvgpr_read_b32 v16, a252\n"
"v_accvgpr_read_b32 v17, a253\n"
"s_cselect_b32 s16, 2048, 0\n"
"v_mfma_f32_16x16x32_bf16 v[124:127], a[112:115], a[100:103], v[124:127]\n"
"v_accvgpr_read_b32 v18, a254\n"
"s_sub_u32 s37, s37, s96\n"
"v_mfma_f32_16x16x32_bf16 v[92:95], a[108:111], a[100:103], v[92:95]\n"
"v_accvgpr_read_b32 v19, a255\n"
"s_sub_u32 s38, s38, s97\n"
"v_mfma_f32_16x16x32_bf16 v[60:63], a[104:107], a[100:103], v[60:63]\n"
"v_accvgpr_read_b32 v28, a240\n"
"s_sub_u32 s36, s36, s16\n"
"s_mov_b32 s41, 6\n"
"s_bitcmp1_b32 s0, 6\n"
"s_cbranch_scc1 SDQ2_%=\n"
"G1Q1_%=:\n"
"s_waitcnt vmcnt(18) lgkmcnt(0)\n"
"s_barrier\n"
"v_mfma_f32_16x16x32_bf16 v[32:35], a[32:35], a[0:3], v[32:35]\n"
"ds_read_b128 a[72:75], v7 offset:0\n"
"s_add_u32 m0, s40, 0\n"
"v_mfma_f32_16x16x32_bf16 v[64:67], a[36:39], a[0:3], v[64:67]\n"
"buffer_load_dwordx4 v1, s[24:27], s37 offen offset:0 lds\n"
"s_add_u32 s37, s37, 196608\n"
"v_mfma_f32_16x16x32_bf16 v[96:99], a[40:43], a[0:3], v[96:99]\n"
"ds_read_b64_tr_b16 a[104:105], v8 offset:20480\n"
"s_cmp_le_u32 s41, 3\n"
"s_cselect_b32 s54, 7344128, 0\n"
"s_cselect_b32 s58, 2101248, 0\n"
"v_mfma_f32_16x16x32_bf16 v[128:131], a[44:47], a[0:3], v[128:131]\n"
"buffer_load_dword a240, v29, s[52:55], 0 offen\n"
"v_mfma_f32_16x16x32_bf16 v[160:163], a[48:51], a[0:3], v[160:163]\n"
"ds_read_b64_tr_b16 a[106:107], v8 offset:20992\n"
"v_mfma_f32_16x16x32_bf16 v[192:195], a[52:55], a[0:3], v[192:195]\n"
"v_add_u32 v29, 2432, v29\n"
"v_mfma_f32_16x16x32_bf16 v[224:227], a[56:59], a[0:3], v[224:227]\n"
"v_mfma_f32_16x16x32_bf16 a[144:147], a[60:63], a[0:3], a[144:147]\n"
"ds_read_b128 a[76:79], v7 offset:2048\n"
"v_mfma_f32_16x16x32_bf16 a[176:179], a[64:67], a[0:3], a[176:179]\n"
"v_mfma_f32_16x16x32_bf16 a[208:211], a[68:71], a[0:3], a[208:211]\n"
"ds_read_b64_tr_b16 a[108:109], v9 offset:20480\n"
"v_mfma_f32_16x16x32_bf16 a[212:215], a[68:71], a[4:7], a[212:215]\n"
"v_mfma_f32_16x16x32_bf16 a[180:183], a[64:67], a[4:7], a[180:183]\n"
"ds_read_b64_tr_b16 a[110:111], v9 offset:20992\n"
"v_mfma_f32_16x16x32_bf16 a[148:151], a[60:63], a[4:7], a[148:151]\n"
"v_mfma_f32_16x16x32_bf16 v[228:231], a[56:59], a[4:7], v[228:231]\n"
"v_mfma_f32_16x16x32_bf16 v[196:199], a[52:55], a[4:7], v[196:199]\n"
"ds_read_b64_tr_b16 a[112:113], v10 offset:20480\n"
"v_mfma_f32_16x16x32_bf16 v[164:167], a[48:51], a[4:7], v[164:167]\n"
"v_mfma_f32_16x16x32_bf16 v[132:135], a[44:47], a[4:7], v[132:135]\n"
"ds_read_b128 a[80:83], v7 offset:4096\n"
"buffer_load_dwordx4 v2, s[28:31], s38 offen offset:1024 lds\n"
"v_mfma_f32_16x16x32_bf16 v[100:103], a[40:43], a[4:7], v[100:103]\n"
"v_mfma_f32_16x16x32_bf16 v[68:71], a[36:39], a[4:7], v[68:71]\n"
"ds_read_b64_tr_b16 a[114:115], v10 offset:21504\n"
"v_mfma_f32_16x16x32_bf16 v[36:39], a[32:35], a[4:7], v[36:39]\n"
"v_mfma_f32_16x16x32_bf16 v[40:43], a[32:35], a[8:11], v[40:43]\n"
"v_mfma_f32_16x16x32_bf16 v[72:75], a[36:39], a[8:11], v[72:75]\n"
"ds_read_b64_tr_b16 a[116:117], v11 offset:20480\n"
"v_mfma_f32_16x16x32_bf16 v[104:107], a[40:43], a[8:11], v[104:107]\n"
"v_mfma_f32_16x16x32_bf16 v[136:139], a[44:47], a[8:11], v[136:139]\n"
"ds_read_b128 a[84:87], v7 offset:6144\n"
"v_mfma_f32_16x16x32_bf16 v[168:171], a[48:51], a[8:11], v[168:171]\n"
"v_mfma_f32_16x16x32_bf16 v[200:203], a[52:55], a[8:11], v[200:203]\n"
"ds_read_b64_tr_b16 a[118:119], v11 offset:21504\n"
"v_mfma_f32_16x16x32_bf16 v[232:235], a[56:59], a[8:11], v[232:235]\n"
"v_mfma_f32_16x16x32_bf16 a[152:155], a[60:63], a[8:11], a[152:155]\n"
"v_mfma_f32_16x16x32_bf16 a[184:187], a[64:67], a[8:11], a[184:187]\n"
"ds_read_b64_tr_b16 a[120:121], v12 offset:20480\n"
"v_mfma_f32_16x16x32_bf16 a[216:219], a[68:71], a[8:11], a[216:219]\n"
"v_mfma_f32_16x16x32_bf16 a[220:223], a[68:71], a[12:15], a[220:223]\n"
"ds_read_b64_tr_b16 a[122:123], v12 offset:21504\n"
"v_mfma_f32_16x16x32_bf16 a[188:191], a[64:67], a[12:15], a[188:191]\n"
"buffer_load_dwordx4 v3, s[28:31], s38 offen offset:2048 lds\n"
"v_mfma_f32_16x16x32_bf16 a[156:159], a[60:63], a[12:15], a[156:159]\n"
"v_mfma_f32_16x16x32_bf16 v[236:239], a[56:59], a[12:15], v[236:239]\n"
"ds_read_b128 a[88:91], v7 offset:8192\n"
"v_mfma_f32_16x16x32_bf16 v[204:207], a[52:55], a[12:15], v[204:207]\n"
"v_mfma_f32_16x16x32_bf16 v[172:175], a[48:51], a[12:15], v[172:175]\n"
"ds_read_b64_tr_b16 a[124:125], v13 offset:20480\n"
"v_mfma_f32_16x16x32_bf16 v[140:143], a[44:47], a[12:15], v[140:143]\n"
"v_mfma_f32_16x16x32_bf16 v[108:111], a[40:43], a[12:15], v[108:111]\n"
"ds_read_b64_tr_b16 a[126:127], v13 offset:21504\n"
"v_mfma_f32_16x16x32_bf16 v[76:79], a[36:39], a[12:15], v[76:79]\n"
"v_mfma_f32_16x16x32_bf16 v[44:47], a[32:35], a[12:15], v[44:47]\n"
"v_mfma_f32_16x16x32_bf16 v[48:51], a[32:35], a[16:19], v[48:51]\n"
"ds_read_b128 a[92:95], v7 offset:10240\n"
"buffer_load_dword a240, v30, s[56:59], 0 offen\n"
"v_mfma_f32_16x16x32_bf16 v[80:83], a[36:39], a[16:19], v[80:83]\n"
"v_mfma_f32_16x16x32_bf16 v[112:115], a[40:43], a[16:19], v[112:115]\n"
"ds_read_b64_tr_b16 a[128:129], v10 offset:22528\n"
"v_add_u32 v30, 704, v30\n"
"v_mfma_f32_16x16x32_bf16 v[144:147], a[44:47], a[16:19], v[144:147]\n"
"v_mfma_f32_16x16x32_bf16 v[176:179], a[48:51], a[16:19], v[176:179]\n"
"ds_read_b64_tr_b16 a[130:131], v10 offset:23552\n"
"v_mfma_f32_16x16x32_bf16 v[208:211], a[52:55], a[16:19], v[208:211]\n"
"v_mfma_f32_16x16x32_bf16 v[240:243], a[56:59], a[16:19], v[240:243]\n"
"v_mfma_f32_16x16x32_bf16 a[160:163], a[60:63], a[16:19], a[160:163]\n"
"ds_read_b64_tr_b16 a[132:133], v11 offset:22528\n"
"buffer_load_dwordx4 v4, s[32:35], s38 offen offset:3072 lds\n"
"v_mfma_f32_16x16x32_bf16 a[192:195], a[64:67], a[16:19], a[192:195]\n"
"v_mfma_f32_16x16x32_bf16 a[224:227], a[68:71], a[16:19], a[224:227]\n"
"ds_read_b128 a[96:99], v7 offset:12288\n"
"v_mfma_f32_16x16x32_bf16 a[228:231], a[68:71], a[20:23], a[228:231]\n"
"v_mfma_f32_16x16x32_bf16 a[196:199], a[64:67], a[20:23], a[196:199]\n"
"ds_read_b64_tr_b16 a[134:135], v11 offset:23552\n"
"v_mfma_f32_16x16x32_bf16 a[164:167], a[60:63], a[20:23], a[164:167]\n"
"v_mfma_f32_16x16x32_bf16 v[244:247], a[56:59], a[20:23], v[244:247]\n"
"v_mfma_f32_16x16x32_bf16 v[212:215], a[52:55], a[20:23], v[212:215]\n"
"ds_read_b64_tr_b16 a[136:137], v12 offset:22528\n"
"v_mfma_f32_16x16x32_bf16 v[180:183], a[48:51], a[20:23], v[180:183]\n"
"v_mfma_f32_16x16x32_bf16 v[148:151], a[44:47], a[20:23], v[148:151]\n"
"ds_read_b128 a[100:103], v7 offset:14336\n"
"v_mfma_f32_16x16x32_bf16 v[116:119], a[40:43], a[20:23], v[116:119]\n"
"v_mfma_f32_16x16x32_bf16 v[84:87], a[36:39], a[20:23], v[84:87]\n"
"ds_read_b64_tr_b16 a[138:139], v12 offset:23552\n"
"v_mfma_f32_16x16x32_bf16 v[52:55], a[32:35], a[20:23], v[52:55]\n"
"v_mfma_f32_16x16x32_bf16 v[56:59], a[32:35], a[24:27], v[56:59]\n"
"v_mfma_f32_16x16x32_bf16 v[88:91], a[36:39], a[24:27], v[88:91]\n"
"ds_read_b64_tr_b16 a[140:141], v13 offset:22528\n"
"s_add_u32 m0, s40, 4096\n"
"v_mfma_f32_16x16x32_bf16 v[120:123], a[40:43], a[24:27], v[120:123]\n"
"buffer_load_dwordx4 v5, s[32:35], s38 offen offset:0 lds\n"
"s_add_u32 s38, s38, 229376\n"
"v_mfma_f32_16x16x32_bf16 v[152:155], a[44:47], a[24:27], v[152:155]\n"
"ds_read_b64_tr_b16 a[142:143], v13 offset:23552\n"
"v_mfma_f32_16x16x32_bf16 v[184:187], a[48:51], a[24:27], v[184:187]\n"
"v_mfma_f32_16x16x32_bf16 v[216:219], a[52:55], a[24:27], v[216:219]\n"
"v_mfma_f32_16x16x32_bf16 v[248:251], a[56:59], a[24:27], v[248:251]\n"
"v_mfma_f32_16x16x32_bf16 a[168:171], a[60:63], a[24:27], a[168:171]\n"
"v_mfma_f32_16x16x32_bf16 a[200:203], a[64:67], a[24:27], a[200:203]\n"
"v_mfma_f32_16x16x32_bf16 a[232:235], a[68:71], a[24:27], a[232:235]\n"
"v_mfma_f32_16x16x32_bf16 a[236:239], a[68:71], a[28:31], a[236:239]\n"
"v_mfma_f32_16x16x32_bf16 a[204:207], a[64:67], a[28:31], a[204:207]\n"
"v_mfma_f32_16x16x32_bf16 a[172:175], a[60:63], a[28:31], a[172:175]\n"
"v_mfma_f32_16x16x32_bf16 v[252:255], a[56:59], a[28:31], v[252:255]\n"
"v_mfma_f32_16x16x32_bf16 v[220:223], a[52:55], a[28:31], v[220:223]\n"
"v_mfma_f32_16x16x32_bf16 v[188:191], a[48:51], a[28:31], v[188:191]\n"
"v_mfma_f32_16x16x32_bf16 v[156:159], a[44:47], a[28:31], v[156:159]\n"
"v_mfma_f32_16x16x32_bf16 v[124:127], a[40:43], a[28:31], v[124:127]\n"
"v_mfma_f32_16x16x32_bf16 v[92:95], a[36:39], a[28:31], v[92:95]\n"
"v_mfma_f32_16x16x32_bf16 v[60:63], a[32:35], a[28:31], v[60:63]\n"
"s_waitcnt vmcnt(12) lgkmcnt(0)\n"
"s_barrier\n"
"v_mfma_f32_16x16x32_bf16 v[32:35], a[104:107], a[72:75], v[32:35]\n"
"ds_read_b128 a[0:3], v6 offset:32768\n"
"s_add_u32 m0, s39, 0\n"
"v_mfma_f32_16x16x32_bf16 v[64:67], a[108:111], a[72:75], v[64:67]\n"
"buffer_load_dwordx4 v20, s[44:47], s36 offen offset:0 lds\n"
"s_add_u32 s36, s36, 15360\n"
"v_mfma_f32_16x16x32_bf16 v[96:99], a[112:115], a[72:75], v[96:99]\n"
"ds_read_b64_tr_b16 a[32:33], v8 offset:40960\n"
"v_mfma_f32_16x16x32_bf16 v[128:131], a[116:119], a[72:75], v[128:131]\n"
"v_mfma_f32_16x16x32_bf16 v[160:163], a[120:123], a[72:75], v[160:163]\n"
"ds_read_b64_tr_b16 a[34:35], v8 offset:41472\n"
"v_mfma_f32_16x16x32_bf16 v[192:195], a[124:127], a[72:75], v[192:195]\n"
"v_mfma_f32_16x16x32_bf16 v[224:227], a[128:131], a[72:75], v[224:227]\n"
"buffer_load_dwordx4 v21, s[44:47], s36 offen offset:1024 lds\n"
"s_add_u32 s36, s36, 15360\n"
"v_mfma_f32_16x16x32_bf16 a[144:147], a[132:135], a[72:75], a[144:147]\n"
"ds_read_b128 a[4:7], v6 offset:34816\n"
"v_mfma_f32_16x16x32_bf16 a[176:179], a[136:139], a[72:75], a[176:179]\n"
"v_mfma_f32_16x16x32_bf16 a[208:211], a[140:143], a[72:75], a[208:211]\n"
"ds_read_b64_tr_b16 a[36:37], v9 offset:40960\n"
"v_mfma_f32_16x16x32_bf16 a[212:215], a[140:143], a[76:79], a[212:215]\n"
"v_mfma_f32_16x16x32_bf16 a[180:183], a[136:139], a[76:79], a[180:183]\n"
"ds_read_b64_tr_b16 a[38:39], v9 offset:41472\n"
"v_mfma_f32_16x16x32_bf16 a[148:151], a[132:135], a[76:79], a[148:151]\n"
"buffer_load_dwordx4 v20, s[44:47], s36 offen offset:2048 lds\n"
"s_add_u32 s36, s36, 15360\n"
"v_mfma_f32_16x16x32_bf16 v[228:231], a[128:131], a[76:79], v[228:231]\n"
"v_mfma_f32_16x16x32_bf16 v[196:199], a[124:127], a[76:79], v[196:199]\n"
"ds_read_b64_tr_b16 a[40:41], v10 offset:40960\n"
"v_mfma_f32_16x16x32_bf16 v[164:167], a[120:123], a[76:79], v[164:167]\n"
"v_mfma_f32_16x16x32_bf16 v[132:135], a[116:119], a[76:79], v[132:135]\n"
"ds_read_b128 a[8:11], v6 offset:36864\n"
"v_mfma_f32_16x16x32_bf16 v[100:103], a[112:115], a[76:79], v[100:103]\n"
"v_mfma_f32_16x16x32_bf16 v[68:71], a[108:111], a[76:79], v[68:71]\n"
"ds_read_b64_tr_b16 a[42:43], v10 offset:41984\n"
"buffer_load_dwordx4 v21, s[44:47], s36 offen offset:3072 lds\n"
"s_add_u32 s36, s36, 19456\n"
"v_mfma_f32_16x16x32_bf16 v[36:39], a[104:107], a[76:79], v[36:39]\n"
"v_mfma_f32_16x16x32_bf16 v[40:43], a[104:107], a[80:83], v[40:43]\n"
"v_mfma_f32_16x16x32_bf16 v[72:75], a[108:111], a[80:83], v[72:75]\n"
"ds_read_b64_tr_b16 a[44:45], v11 offset:40960\n"
"v_mfma_f32_16x16x32_bf16 v[104:107], a[112:115], a[80:83], v[104:107]\n"
"v_mfma_f32_16x16x32_bf16 v[136:139], a[116:119], a[80:83], v[136:139]\n"
"ds_read_b128 a[12:15], v6 offset:38912\n"
"s_add_u32 m0, s39, 4096\n"
"v_mfma_f32_16x16x32_bf16 v[168:171], a[120:123], a[80:83], v[168:171]\n"
"buffer_load_dwordx4 v20, s[44:47], s36 offen offset:0 lds\n"
"s_add_u32 s36, s36, 15360\n"
"v_mfma_f32_16x16x32_bf16 v[200:203], a[124:127], a[80:83], v[200:203]\n"
"ds_read_b64_tr_b16 a[46:47], v11 offset:41984\n"
"v_mfma_f32_16x16x32_bf16 v[232:235], a[128:131], a[80:83], v[232:235]\n"
"v_mfma_f32_16x16x32_bf16 a[152:155], a[132:135], a[80:83], a[152:155]\n"
"v_mfma_f32_16x16x32_bf16 a[184:187], a[136:139], a[80:83], a[184:187]\n"
"ds_read_b64_tr_b16 a[48:49], v12 offset:40960\n"
"v_mfma_f32_16x16x32_bf16 a[216:219], a[140:143], a[80:83], a[216:219]\n"
"v_mfma_f32_16x16x32_bf16 a[220:223], a[140:143], a[84:87], a[220:223]\n"
"ds_read_b64_tr_b16 a[50:51], v12 offset:41984\n"
"buffer_load_dwordx4 v21, s[44:47], s36 offen offset:1024 lds\n"
"s_add_u32 s36, s36, 15360\n"
"v_mfma_f32_16x16x32_bf16 a[188:191], a[136:139], a[84:87], a[188:191]\n"
"v_mfma_f32_16x16x32_bf16 a[156:159], a[132:135], a[84:87], a[156:159]\n"
"v_mfma_f32_16x16x32_bf16 v[236:239], a[128:131], a[84:87], v[236:239]\n"
"ds_read_b128 a[16:19], v6 offset:40960\n"
"v_mfma_f32_16x16x32_bf16 v[204:207], a[124:127], a[84:87], v[204:207]\n"
"v_mfma_f32_16x16x32_bf16 v[172:175], a[120:123], a[84:87], v[172:175]\n"
"ds_read_b64_tr_b16 a[52:53], v13 offset:40960\n"
"v_mfma_f32_16x16x32_bf16 v[140:143], a[116:119], a[84:87], v[140:143]\n"
"buffer_load_dwordx4 v20, s[44:47], s36 offen offset:2048 lds\n"
"s_add_u32 s36, s36, 15360\n"
"v_mfma_f32_16x16x32_bf16 v[108:111], a[112:115], a[84:87], v[108:111]\n"
"ds_read_b64_tr_b16 a[54:55], v13 offset:41984\n"
"v_mfma_f32_16x16x32_bf16 v[76:79], a[108:111], a[84:87], v[76:79]\n"
"v_mfma_f32_16x16x32_bf16 v[44:47], a[104:107], a[84:87], v[44:47]\n"
"v_mfma_f32_16x16x32_bf16 v[48:51], a[104:107], a[88:91], v[48:51]\n"
"ds_read_b128 a[20:23], v6 offset:43008\n"
"v_mfma_f32_16x16x32_bf16 v[80:83], a[108:111], a[88:91], v[80:83]\n"
"v_mfma_f32_16x16x32_bf16 v[112:115], a[112:115], a[88:91], v[112:115]\n"
"ds_read_b64_tr_b16 a[56:57], v10 offset:43008\n"
"buffer_load_dwordx4 v21, s[44:47], s36 offen offset:3072 lds\n"
"s_sub_u32 s36, s36, 111488\n"
"v_mfma_f32_16x16x32_bf16 v[144:147], a[116:119], a[88:91], v[144:147]\n"
"v_mfma_f32_16x16x32_bf16 v[176:179], a[120:123], a[88:91], v[176:179]\n"
"ds_read_b64_tr_b16 a[58:59], v10 offset:44032\n"
"v_mfma_f32_16x16x32_bf16 v[208:211], a[124:127], a[88:91], v[208:211]\n"
"v_mfma_f32_16x16x32_bf16 v[240:243], a[128:131], a[88:91], v[240:243]\n"
"v_mfma_f32_16x16x32_bf16 a[160:163], a[132:135], a[88:91], a[160:163]\n"
"ds_read_b64_tr_b16 a[60:61], v11 offset:43008\n"
"s_add_u32 m0, s40, 20480\n"
"v_mfma_f32_16x16x32_bf16 a[192:195], a[136:139], a[88:91], a[192:195]\n"
"buffer_load_dwordx4 v1, s[24:27], s37 offen offset:0 lds\n"
"s_add_u32 s37, s37, 196608\n"
"v_mfma_f32_16x16x32_bf16 a[224:227], a[140:143], a[88:91], a[224:227]\n"
"ds_read_b128 a[24:27], v6 offset:45056\n"
"v_mfma_f32_16x16x32_bf16 a[228:231], a[140:143], a[92:95], a[228:231]\n"
"v_mfma_f32_16x16x32_bf16 a[196:199], a[136:139], a[92:95], a[196:199]\n"
"ds_read_b64_tr_b16 a[62:63], v11 offset:44032\n"
"v_mfma_f32_16x16x32_bf16 a[164:167], a[132:135], a[92:95], a[164:167]\n"
"v_mfma_f32_16x16x32_bf16 v[244:247], a[128:131], a[92:95], v[244:247]\n"
"v_mfma_f32_16x16x32_bf16 v[212:215], a[124:127], a[92:95], v[212:215]\n"
"ds_read_b64_tr_b16 a[64:65], v12 offset:43008\n"
"buffer_load_dwordx4 v2, s[28:31], s38 offen offset:1024 lds\n"
"v_mfma_f32_16x16x32_bf16 v[180:183], a[120:123], a[92:95], v[180:183]\n"
"v_mfma_f32_16x16x32_bf16 v[148:151], a[116:119], a[92:95], v[148:151]\n"
"ds_read_b128 a[28:31], v6 offset:47104\n"
"v_mfma_f32_16x16x32_bf16 v[116:119], a[112:115], a[92:95], v[116:119]\n"
"v_mfma_f32_16x16x32_bf16 v[84:87], a[108:111], a[92:95], v[84:87]\n"
"ds_read_b64_tr_b16 a[66:67], v12 offset:44032\n"
"v_mfma_f32_16x16x32_bf16 v[52:55], a[104:107], a[92:95], v[52:55]\n"
"v_mfma_f32_16x16x32_bf16 v[56:59], a[104:107], a[96:99], v[56:59]\n"
"buffer_load_dwordx4 v3, s[28:31], s38 offen offset:2048 lds\n"
"v_mfma_f32_16x16x32_bf16 v[88:91], a[108:111], a[96:99], v[88:91]\n"
"ds_read_b64_tr_b16 a[68:69], v13 offset:43008\n"
"v_mfma_f32_16x16x32_bf16 v[120:123], a[112:115], a[96:99], v[120:123]\n"
"v_mfma_f32_16x16x32_bf16 v[152:155], a[116:119], a[96:99], v[152:155]\n"
"ds_read_b64_tr_b16 a[70:71], v13 offset:44032\n"
"v_mfma_f32_16x16x32_bf16 v[184:187], a[120:123], a[96:99], v[184:187]\n"
"v_mfma_f32_16x16x32_bf16 v[216:219], a[124:127], a[96:99], v[216:219]\n"
"v_mfma_f32_16x16x32_bf16 v[248:251], a[128:131], a[96:99], v[248:251]\n"
"buffer_load_dwordx4 v4, s[32:35], s38 offen offset:3072 lds\n"
"v_mfma_f32_16x16x32_bf16 a[168:171], a[132:135], a[96:99], a[168:171]\n"
"v_mfma_f32_16x16x32_bf16 a[200:203], a[136:139], a[96:99], a[200:203]\n"
"v_mfma_f32_16x16x32_bf16 a[232:235], a[140:143], a[96:99], a[232:235]\n"
"v_mfma_f32_16x16x32_bf16 a[236:239], a[140:143], a[100:103], a[236:239]\n"
"v_mfma_f32_16x16x32_bf16 a[204:207], a[136:139], a[100:103], a[204:207]\n"
"s_add_u32 m0, s40, 24576\n"
"v_mfma_f32_16x16x32_bf16 a[172:175], a[132:135], a[100:103], a[172:175]\n"
"buffer_load_dwordx4 v5, s[32:35], s38 offen offset:0 lds\n"
"s_add_u32 s38, s38, 229376\n"
"v_mfma_f32_16x16x32_bf16 v[252:255], a[128:131], a[100:103], v[252:255]\n"
"v_mfma_f32_16x16x32_bf16 v[220:223], a[124:127], a[100:103], v[220:223]\n"
"v_mfma_f32_16x16x32_bf16 v[188:191], a[120:123], a[100:103], v[188:191]\n"
"v_mfma_f32_16x16x32_bf16 v[156:159], a[116:119], a[100:103], v[156:159]\n"
"v_mfma_f32_16x16x32_bf16 v[124:127], a[112:115], a[100:103], v[124:127]\n"
"v_mfma_f32_16x16x32_bf16 v[92:95], a[108:111], a[100:103], v[92:95]\n"
"v_mfma_f32_16x16x32_bf16 v[60:63], a[104:107], a[100:103], v[60:63]\n"
"s_waitcnt vmcnt(20) lgkmcnt(0)\n"
"s_barrier\n"
"v_mfma_f32_16x16x32_bf16 v[32:35], a[32:35], a[0:3], v[32:35]\n"
"ds_read_b128 a[72:75], v7 offset:32768\n"
"s_add_u32 m0, s40, 40960\n"
"v_mfma_f32_16x16x32_bf16 v[64:67], a[36:39], a[0:3], v[64:67]\n"
"buffer_load_dwordx4 v1, s[24:27], s37 offen offset:0 lds\n"
"s_add_u32 s37, s37, 196608\n"
"v_mfma_f32_16x16x32_bf16 v[96:99], a[40:43], a[0:3], v[96:99]\n"
"ds_read_b64_tr_b16 a[104:105], v8 offset:61440\n"
"v_mfma_f32_16x16x32_bf16 v[128:131], a[44:47], a[0:3], v[128:131]\n"
"v_mfma_f32_16x16x32_bf16 v[160:163], a[48:51], a[0:3], v[160:163]\n"
"ds_read_b64_tr_b16 a[106:107], v8 offset:61952\n"
"v_mfma_f32_16x16x32_bf16 v[192:195], a[52:55], a[0:3], v[192:195]\n"
"v_mfma_f32_16x16x32_bf16 v[224:227], a[56:59], a[0:3], v[224:227]\n"
"v_mfma_f32_16x16x32_bf16 a[144:147], a[60:63], a[0:3], a[144:147]\n"
"ds_read_b128 a[76:79], v7 offset:34816\n"
"v_mfma_f32_16x16x32_bf16 a[176:179], a[64:67], a[0:3], a[176:179]\n"
"v_mfma_f32_16x16x32_bf16 a[208:211], a[68:71], a[0:3], a[208:211]\n"
"ds_read_b64_tr_b16 a[108:109], v9 offset:61440\n"
"v_mfma_f32_16x16x32_bf16 a[212:215], a[68:71], a[4:7], a[212:215]\n"
"v_mfma_f32_16x16x32_bf16 a[180:183], a[64:67], a[4:7], a[180:183]\n"
"ds_read_b64_tr_b16 a[110:111], v9 offset:61952\n"
"v_mfma_f32_16x16x32_bf16 a[148:151], a[60:63], a[4:7], a[148:151]\n"
"v_mfma_f32_16x16x32_bf16 v[228:231], a[56:59], a[4:7], v[228:231]\n"
"v_mfma_f32_16x16x32_bf16 v[196:199], a[52:55], a[4:7], v[196:199]\n"
"ds_read_b64_tr_b16 a[112:113], v10 offset:61440\n"
"v_mfma_f32_16x16x32_bf16 v[164:167], a[48:51], a[4:7], v[164:167]\n"
"v_mfma_f32_16x16x32_bf16 v[132:135], a[44:47], a[4:7], v[132:135]\n"
"ds_read_b128 a[80:83], v7 offset:36864\n"
"buffer_load_dwordx4 v2, s[28:31], s38 offen offset:1024 lds\n"
"v_mfma_f32_16x16x32_bf16 v[100:103], a[40:43], a[4:7], v[100:103]\n"
"v_mfma_f32_16x16x32_bf16 v[68:71], a[36:39], a[4:7], v[68:71]\n"
"ds_read_b64_tr_b16 a[114:115], v10 offset:62464\n"
"v_mfma_f32_16x16x32_bf16 v[36:39], a[32:35], a[4:7], v[36:39]\n"
"v_mfma_f32_16x16x32_bf16 v[40:43], a[32:35], a[8:11], v[40:43]\n"
"v_mfma_f32_16x16x32_bf16 v[72:75], a[36:39], a[8:11], v[72:75]\n"
"ds_read_b64_tr_b16 a[116:117], v11 offset:61440\n"
"v_mfma_f32_16x16x32_bf16 v[104:107], a[40:43], a[8:11], v[104:107]\n"
"v_mfma_f32_16x16x32_bf16 v[136:139], a[44:47], a[8:11], v[136:139]\n"
"ds_read_b128 a[84:87], v7 offset:38912\n"
"v_mfma_f32_16x16x32_bf16 v[168:171], a[48:51], a[8:11], v[168:171]\n"
"v_mfma_f32_16x16x32_bf16 v[200:203], a[52:55], a[8:11], v[200:203]\n"
"ds_read_b64_tr_b16 a[118:119], v11 offset:62464\n"
"v_mfma_f32_16x16x32_bf16 v[232:235], a[56:59], a[8:11], v[232:235]\n"
"v_mfma_f32_16x16x32_bf16 a[152:155], a[60:63], a[8:11], a[152:155]\n"
"v_mfma_f32_16x16x32_bf16 a[184:187], a[64:67], a[8:11], a[184:187]\n"
"ds_read_b64_tr_b16 a[120:121], v12 offset:61440\n"
"v_mfma_f32_16x16x32_bf16 a[216:219], a[68:71], a[8:11], a[216:219]\n"
"v_mfma_f32_16x16x32_bf16 a[220:223], a[68:71], a[12:15], a[220:223]\n"
"ds_read_b64_tr_b16 a[122:123], v12 offset:62464\n"
"v_mfma_f32_16x16x32_bf16 a[188:191], a[64:67], a[12:15], a[188:191]\n"
"buffer_load_dwordx4 v3, s[28:31], s38 offen offset:2048 lds\n"
"v_mfma_f32_16x16x32_bf16 a[156:159], a[60:63], a[12:15], a[156:159]\n"
"v_mfma_f32_16x16x32_bf16 v[236:239], a[56:59], a[12:15], v[236:239]\n"
"ds_read_b128 a[88:91], v7 offset:40960\n"
"v_mfma_f32_16x16x32_bf16 v[204:207], a[52:55], a[12:15], v[204:207]\n"
"v_mfma_f32_16x16x32_bf16 v[172:175], a[48:51], a[12:15], v[172:175]\n"
"ds_read_b64_tr_b16 a[124:125], v13 offset:61440\n"
"v_mfma_f32_16x16x32_bf16 v[140:143], a[44:47], a[12:15], v[140:143]\n"
"v_mfma_f32_16x16x32_bf16 v[108:111], a[40:43], a[12:15], v[108:111]\n"
"ds_read_b64_tr_b16 a[126:127], v13 offset:62464\n"
"v_mfma_f32_16x16x32_bf16 v[76:79], a[36:39], a[12:15], v[76:79]\n"
"v_mfma_f32_16x16x32_bf16 v[44:47], a[32:35], a[12:15], v[44:47]\n"
"v_mfma_f32_16x16x32_bf16 v[48:51], a[32:35], a[16:19], v[48:51]\n"
"ds_read_b128 a[92:95], v7 offset:43008\n"
"v_mfma_f32_16x16x32_bf16 v[80:83], a[36:39], a[16:19], v[80:83]\n"
"v_mfma_f32_16x16x32_bf16 v[112:115], a[40:43], a[16:19], v[112:115]\n"
"ds_read_b64_tr_b16 a[128:129], v10 offset:63488\n"
"v_mfma_f32_16x16x32_bf16 v[144:147], a[44:47], a[16:19], v[144:147]\n"
"v_mfma_f32_16x16x32_bf16 v[176:179], a[48:51], a[16:19], v[176:179]\n"
"ds_read_b64_tr_b16 a[130:131], v10 offset:64512\n"
"v_mfma_f32_16x16x32_bf16 v[208:211], a[52:55], a[16:19], v[208:211]\n"
"v_mfma_f32_16x16x32_bf16 v[240:243], a[56:59], a[16:19], v[240:243]\n"
"v_mfma_f32_16x16x32_bf16 a[160:163], a[60:63], a[16:19], a[160:163]\n"
"ds_read_b64_tr_b16 a[132:133], v11 offset:63488\n"
"buffer_load_dwordx4 v4, s[32:35], s38 offen offset:3072 lds\n"
"v_mfma_f32_16x16x32_bf16 a[192:195], a[64:67], a[16:19], a[192:195]\n"
"v_mfma_f32_16x16x32_bf16 a[224:227], a[68:71], a[16:19], a[224:227]\n"
"ds_read_b128 a[96:99], v7 offset:45056\n"
"v_mfma_f32_16x16x32_bf16 a[228:231], a[68:71], a[20:23], a[228:231]\n"
"v_mfma_f32_16x16x32_bf16 a[196:199], a[64:67], a[20:23], a[196:199]\n"
"ds_read_b64_tr_b16 a[134:135], v11 offset:64512\n"
"v_mfma_f32_16x16x32_bf16 a[164:167], a[60:63], a[20:23], a[164:167]\n"
"v_mfma_f32_16x16x32_bf16 v[244:247], a[56:59], a[20:23], v[244:247]\n"
"v_mfma_f32_16x16x32_bf16 v[212:215], a[52:55], a[20:23], v[212:215]\n"
"ds_read_b64_tr_b16 a[136:137], v12 offset:63488\n"
"v_mfma_f32_16x16x32_bf16 v[180:183], a[48:51], a[20:23], v[180:183]\n"
"v_mfma_f32_16x16x32_bf16 v[148:151], a[44:47], a[20:23], v[148:151]\n"
"ds_read_b128 a[100:103], v7 offset:47104\n"
"v_mfma_f32_16x16x32_bf16 v[116:119], a[40:43], a[20:23], v[116:119]\n"
"v_mfma_f32_16x16x32_bf16 v[84:87], a[36:39], a[20:23], v[84:87]\n"
"ds_read_b64_tr_b16 a[138:139], v12 offset:64512\n"
"v_mfma_f32_16x16x32_bf16 v[52:55], a[32:35], a[20:23], v[52:55]\n"
"v_mfma_f32_16x16x32_bf16 v[56:59], a[32:35], a[24:27], v[56:59]\n"
"v_mfma_f32_16x16x32_bf16 v[88:91], a[36:39], a[24:27], v[88:91]\n"
"ds_read_b64_tr_b16 a[140:141], v13 offset:63488\n"
"s_add_u32 m0, s40, 45056\n"
"v_mfma_f32_16x16x32_bf16 v[120:123], a[40:43], a[24:27], v[120:123]\n"
"buffer_load_dwordx4 v5, s[32:35], s38 offen offset:0 lds\n"
"s_add_u32 s38, s38, 229376\n"
"v_mfma_f32_16x16x32_bf16 v[152:155], a[44:47], a[24:27], v[152:155]\n"
"ds_read_b64_tr_b16 a[142:143], v13 offset:64512\n"
"v_mfma_f32_16x16x32_bf16 v[184:187], a[48:51], a[24:27], v[184:187]\n"
"v_mfma_f32_16x16x32_bf16 v[216:219], a[52:55], a[24:27], v[216:219]\n"
"v_mfma_f32_16x16x32_bf16 v[248:251], a[56:59], a[24:27], v[248:251]\n"
"v_mfma_f32_16x16x32_bf16 a[168:171], a[60:63], a[24:27], a[168:171]\n"
"v_mfma_f32_16x16x32_bf16 a[200:203], a[64:67], a[24:27], a[200:203]\n"
"v_mfma_f32_16x16x32_bf16 a[232:235], a[68:71], a[24:27], a[232:235]\n"
"v_mfma_f32_16x16x32_bf16 a[236:239], a[68:71], a[28:31], a[236:239]\n"
"v_mfma_f32_16x16x32_bf16 a[204:207], a[64:67], a[28:31], a[204:207]\n"
"v_mfma_f32_16x16x32_bf16 a[172:175], a[60:63], a[28:31], a[172:175]\n"
"v_mfma_f32_16x16x32_bf16 v[252:255], a[56:59], a[28:31], v[252:255]\n"
"v_mfma_f32_16x16x32_bf16 v[220:223], a[52:55], a[28:31], v[220:223]\n"
"v_mfma_f32_16x16x32_bf16 v[188:191], a[48:51], a[28:31], v[188:191]\n"
"v_mfma_f32_16x16x32_bf16 v[156:159], a[44:47], a[28:31], v[156:159]\n"
"v_mfma_f32_16x16x32_bf16 v[124:127], a[40:43], a[28:31], v[124:127]\n"
"v_mfma_f32_16x16x32_bf16 v[92:95], a[36:39], a[28:31], v[92:95]\n"
"v_mfma_f32_16x16x32_bf16 v[60:63], a[32:35], a[28:31], v[60:63]\n"
"s_waitcnt vmcnt(10) lgkmcnt(0)\n"
"s_barrier\n"
"v_mfma_f32_16x16x32_bf16 v[32:35], a[104:107], a[72:75], v[32:35]\n"
"ds_read_b128 a[0:3], v6 offset:0\n"
"s_add_u32 m0, s39, 32768\n"
"v_mfma_f32_16x16x32_bf16 v[64:67], a[108:111], a[72:75], v[64:67]\n"
"buffer_load_dwordx4 v20, s[44:47], s36 offen offset:0 lds\n"
"s_add_u32 s36, s36, 15360\n"
"v_mfma_f32_16x16x32_bf16 v[96:99], a[112:115], a[72:75], v[96:99]\n"
"ds_read_b64_tr_b16 a[32:33], v8 offset:0\n"
"v_mfma_f32_16x16x32_bf16 v[128:131], a[116:119], a[72:75], v[128:131]\n"
"v_mfma_f32_16x16x32_bf16 v[160:163], a[120:123], a[72:75], v[160:163]\n"
"ds_read_b64_tr_b16 a[34:35], v8 offset:512\n"
"v_mfma_f32_16x16x32_bf16 v[192:195], a[124:127], a[72:75], v[192:195]\n"
"v_mfma_f32_16x16x32_bf16 v[224:227], a[128:131], a[72:75], v[224:227]\n"
"buffer_load_dwordx4 v21, s[44:47], s36 offen offset:1024 lds\n"
"s_add_u32 s36, s36, 15360\n"
"v_mfma_f32_16x16x32_bf16 a[144:147], a[132:135], a[72:75], a[144:147]\n"
"ds_read_b128 a[4:7], v6 offset:2048\n"
"v_mfma_f32_16x16x32_bf16 a[176:179], a[136:139], a[72:75], a[176:179]\n"
"v_mfma_f32_16x16x32_bf16 a[208:211], a[140:143], a[72:75], a[208:211]\n"
"ds_read_b64_tr_b16 a[36:37], v9 offset:0\n"
"v_mfma_f32_16x16x32_bf16 a[212:215], a[140:143], a[76:79], a[212:215]\n"
"v_mfma_f32_16x16x32_bf16 a[180:183], a[136:139], a[76:79], a[180:183]\n"
"ds_read_b64_tr_b16 a[38:39], v9 offset:512\n"
"v_mfma_f32_16x16x32_bf16 a[148:151], a[132:135], a[76:79], a[148:151]\n"
"buffer_load_dwordx4 v20, s[44:47], s36 offen offset:2048 lds\n"
"s_add_u32 s36, s36, 15360\n"
"v_mfma_f32_16x16x32_bf16 v[228:231], a[128:131], a[76:79], v[228:231]\n"
"v_mfma_f32_16x16x32_bf16 v[196:199], a[124:127], a[76:79], v[196:199]\n"
"ds_read_b64_tr_b16 a[40:41], v10 offset:0\n"
"v_mfma_f32_16x16x32_bf16 v[164:167], a[120:123], a[76:79], v[164:167]\n"
"v_mfma_f32_16x16x32_bf16 v[132:135], a[116:119], a[76:79], v[132:135]\n"
"ds_read_b128 a[8:11], v6 offset:4096\n"
"v_mfma_f32_16x16x32_bf16 v[100:103], a[112:115], a[76:79], v[100:103]\n"
"v_mfma_f32_16x16x32_bf16 v[68:71], a[108:111], a[76:79], v[68:71]\n"
"ds_read_b64_tr_b16 a[42:43], v10 offset:1024\n"
"buffer_load_dwordx4 v21, s[44:47], s36 offen offset:3072 lds\n"
"s_add_u32 s36, s36, 19456\n"
"v_mfma_f32_16x16x32_bf16 v[36:39], a[104:107], a[76:79], v[36:39]\n"
"v_mfma_f32_16x16x32_bf16 v[40:43], a[104:107], a[80:83], v[40:43]\n"
"v_mfma_f32_16x16x32_bf16 v[72:75], a[108:111], a[80:83], v[72:75]\n"
"ds_read_b64_tr_b16 a[44:45], v11 offset:0\n"
"v_mfma_f32_16x16x32_bf16 v[104:107], a[112:115], a[80:83], v[104:107]\n"
"v_mfma_f32_16x16x32_bf16 v[136:139], a[116:119], a[80:83], v[136:139]\n"
"ds_read_b128 a[12:15], v6 offset:6144\n"
"s_add_u32 m0, s39, 36864\n"
"v_mfma_f32_16x16x32_bf16 v[168:171], a[120:123], a[80:83], v[168:171]\n"
"buffer_load_dwordx4 v20, s[44:47], s36 offen offset:0 lds\n"
"s_add_u32 s36, s36, 15360\n"
"v_mfma_f32_16x16x32_bf16 v[200:203], a[124:127], a[80:83], v[200:203]\n"
"ds_read_b64_tr_b16 a[46:47], v11 offset:1024\n"
"v_mfma_f32_16x16x32_bf16 v[232:235], a[128:131], a[80:83], v[232:235]\n"
"v_mfma_f32_16x16x32_bf16 a[152:155], a[132:135], a[80:83], a[152:155]\n"
"v_mfma_f32_16x16x32_bf16 a[184:187], a[136:139], a[80:83], a[184:187]\n"
"ds_read_b64_tr_b16 a[48:49], v12 offset:0\n"
"v_mfma_f32_16x16x32_bf16 a[216:219], a[140:143], a[80:83], a[216:219]\n"
"v_mfma_f32_16x16x32_bf16 a[220:223], a[140:143], a[84:87], a[220:223]\n"
"ds_read_b64_tr_b16 a[50:51], v12 offset:1024\n"
"buffer_load_dwordx4 v21, s[44:47], s36 offen offset:1024 lds\n"
"s_add_u32 s36, s36, 15360\n"
"v_mfma_f32_16x16x32_bf16 a[188:191], a[136:139], a[84:87], a[188:191]\n"
"v_mfma_f32_16x16x32_bf16 a[156:159], a[132:135], a[84:87], a[156:159]\n"
"v_mfma_f32_16x16x32_bf16 v[236:239], a[128:131], a[84:87], v[236:239]\n"
"ds_read_b128 a[16:19], v6 offset:8192\n"
"v_mfma_f32_16x16x32_bf16 v[204:207], a[124:127], a[84:87], v[204:207]\n"
"v_mfma_f32_16x16x32_bf16 v[172:175], a[120:123], a[84:87], v[172:175]\n"
"ds_read_b64_tr_b16 a[52:53], v13 offset:0\n"
"v_mfma_f32_16x16x32_bf16 v[140:143], a[116:119], a[84:87], v[140:143]\n"
"buffer_load_dwordx4 v20, s[44:47], s36 offen offset:2048 lds\n"
"s_add_u32 s36, s36, 15360\n"
"v_mfma_f32_16x16x32_bf16 v[108:111], a[112:115], a[84:87], v[108:111]\n"
"ds_read_b64_tr_b16 a[54:55], v13 offset:1024\n"
"v_mfma_f32_16x16x32_bf16 v[76:79], a[108:111], a[84:87], v[76:79]\n"
"v_mfma_f32_16x16x32_bf16 v[44:47], a[104:107], a[84:87], v[44:47]\n"
"v_mfma_f32_16x16x32_bf16 v[48:51], a[104:107], a[88:91], v[48:51]\n"
"ds_read_b128 a[20:23], v6 offset:10240\n"
"v_mfma_f32_16x16x32_bf16 v[80:83], a[108:111], a[88:91], v[80:83]\n"
"v_mfma_f32_16x16x32_bf16 v[112:115], a[112:115], a[88:91], v[112:115]\n"
"ds_read_b64_tr_b16 a[56:57], v10 offset:2048\n"
"buffer_load_dwordx4 v21, s[44:47], s36 offen offset:3072 lds\n"
"s_sub_u32 s36, s36, 111488\n"
"v_mfma_f32_16x16x32_bf16 v[144:147], a[116:119], a[88:91], v[144:147]\n"
"v_mfma_f32_16x16x32_bf16 v[176:179], a[120:123], a[88:91], v[176:179]\n"
"ds_read_b64_tr_b16 a[58:59], v10 offset:3072\n"
"v_mfma_f32_16x16x32_bf16 v[208:211], a[124:127], a[88:91], v[208:211]\n"
"v_mfma_f32_16x16x32_bf16 v[240:243], a[128:131], a[88:91], v[240:243]\n"
"v_mfma_f32_16x16x32_bf16 a[160:163], a[132:135], a[88:91], a[160:163]\n"
"ds_read_b64_tr_b16 a[60:61], v11 offset:2048\n"
"s_add_u32 m0, s40, 61440\n"
"v_mfma_f32_16x16x32_bf16 a[192:195], a[136:139], a[88:91], a[192:195]\n"
"buffer_load_dwordx4 v1, s[24:27], s37 offen offset:0 lds\n"
"s_add_u32 s37, s37, 196608\n"
"v_mfma_f32_16x16x32_bf16 a[224:227], a[140:143], a[88:91], a[224:227]\n"
"ds_read_b128 a[24:27], v6 offset:12288\n"
"v_mfma_f32_16x16x32_bf16 a[228:231], a[140:143], a[92:95], a[228:231]\n"
"v_mfma_f32_16x16x32_bf16 a[196:199], a[136:139], a[92:95], a[196:199]\n"
"ds_read_b64_tr_b16 a[62:63], v11 offset:3072\n"
"v_mfma_f32_16x16x32_bf16 a[164:167], a[132:135], a[92:95], a[164:167]\n"
"v_mfma_f32_16x16x32_bf16 v[244:247], a[128:131], a[92:95], v[244:247]\n"
"v_mfma_f32_16x16x32_bf16 v[212:215], a[124:127], a[92:95], v[212:215]\n"
"ds_read_b64_tr_b16 a[64:65], v12 offset:2048\n"
"buffer_load_dwordx4 v2, s[28:31], s38 offen offset:1024 lds\n"
"v_mfma_f32_16x16x32_bf16 v[180:183], a[120:123], a[92:95], v[180:183]\n"
"v_mfma_f32_16x16x32_bf16 v[148:151], a[116:119], a[92:95], v[148:151]\n"
"ds_read_b128 a[28:31], v6 offset:14336\n"
"v_mfma_f32_16x16x32_bf16 v[116:119], a[112:115], a[92:95], v[116:119]\n"
"v_mfma_f32_16x16x32_bf16 v[84:87], a[108:111], a[92:95], v[84:87]\n"
"ds_read_b64_tr_b16 a[66:67], v12 offset:3072\n"
"v_mfma_f32_16x16x32_bf16 v[52:55], a[104:107], a[92:95], v[52:55]\n"
"v_mfma_f32_16x16x32_bf16 v[56:59], a[104:107], a[96:99], v[56:59]\n"
"buffer_load_dwordx4 v3, s[28:31], s38 offen offset:2048 lds\n"
"v_mfma_f32_16x16x32_bf16 v[88:91], a[108:111], a[96:99], v[88:91]\n"
"ds_read_b64_tr_b16 a[68:69], v13 offset:2048\n"
"v_mfma_f32_16x16x32_bf16 v[120:123], a[112:115], a[96:99], v[120:123]\n"
"v_mfma_f32_16x16x32_bf16 v[152:155], a[116:119], a[96:99], v[152:155]\n"
"ds_read_b64_tr_b16 a[70:71], v13 offset:3072\n"
"v_mfma_f32_16x16x32_bf16 v[184:187], a[120:123], a[96:99], v[184:187]\n"
"v_mfma_f32_16x16x32_bf16 v[216:219], a[124:127], a[96:99], v[216:219]\n"
"v_mfma_f32_16x16x32_bf16 v[248:251], a[128:131], a[96:99], v[248:251]\n"
"buffer_load_dwordx4 v4, s[32:35], s38 offen offset:3072 lds\n"
"v_mfma_f32_16x16x32_bf16 a[168:171], a[132:135], a[96:99], a[168:171]\n"
"v_mfma_f32_16x16x32_bf16 a[200:203], a[136:139], a[96:99], a[200:203]\n"
"v_mfma_f32_16x16x32_bf16 a[232:235], a[140:143], a[96:99], a[232:235]\n"
"v_mfma_f32_16x16x32_bf16 a[236:239], a[140:143], a[100:103], a[236:239]\n"
"v_mfma_f32_16x16x32_bf16 a[204:207], a[136:139], a[100:103], a[204:207]\n"
"s_sub_u32 s41, s41, 1\n"
"s_add_u32 m0, s40, 65536\n"
"v_mfma_f32_16x16x32_bf16 a[172:175], a[132:135], a[100:103], a[172:175]\n"
"buffer_load_dwordx4 v5, s[32:35], s38 offen offset:0 lds\n"
"s_add_u32 s38, s38, 229376\n"
"v_mfma_f32_16x16x32_bf16 v[252:255], a[128:131], a[100:103], v[252:255]\n"
"s_cmp_eq_u32 s2, s41\n"
"v_mfma_f32_16x16x32_bf16 v[220:223], a[124:127], a[100:103], v[220:223]\n"
"s_cselect_b32 s96, 6291456, 0\n"
"v_mfma_f32_16x16x32_bf16 v[188:191], a[120:123], a[100:103], v[188:191]\n"
"s_cselect_b32 s97, 7340032, 0\n"
"v_mfma_f32_16x16x32_bf16 v[156:159], a[116:119], a[100:103], v[156:159]\n"
"s_cselect_b32 s16, 2048, 0\n"
"v_mfma_f32_16x16x32_bf16 v[124:127], a[112:115], a[100:103], v[124:127]\n"
"s_sub_u32 s37, s37, s96\n"
"v_mfma_f32_16x16x32_bf16 v[92:95], a[108:111], a[100:103], v[92:95]\n"
"s_sub_u32 s38, s38, s97\n"
"v_mfma_f32_16x16x32_bf16 v[60:63], a[104:107], a[100:103], v[60:63]\n"
"s_sub_u32 s36, s36, s16\n"
"s_bitcmp0_b32 s0, s41\n"
"s_cbranch_scc1 G1Q1_%=\n"
"s_cmp_lg_u32 s41, 0\n"
"s_cbranch_scc1 SDQ2_%=\n"
"s_waitcnt vmcnt(18) lgkmcnt(0)\n"
"s_barrier\n"
"v_mfma_f32_16x16x32_bf16 v[32:35], a[32:35], a[0:3], v[32:35]\n"
"ds_read_b128 a[72:75], v7 offset:0\n"
"buffer_load_dwordx4 v[24:27], v0, s[20:23], s65 offen sc1\n"
"v_mfma_f32_16x16x32_bf16 v[64:67], a[36:39], a[0:3], v[64:67]\n"
"buffer_load_dwordx4 v[20:23], v0, s[20:23], s5 offen sc1\n"
"v_mfma_f32_16x16x32_bf16 v[96:99], a[40:43], a[0:3], v[96:99]\n"
"ds_read_b64_tr_b16 a[104:105], v8 offset:20480\n"
"v_mfma_f32_16x16x32_bf16 v[128:131], a[44:47], a[0:3], v[128:131]\n"
"v_mfma_f32_16x16x32_bf16 v[160:163], a[48:51], a[0:3], v[160:163]\n"
"ds_read_b64_tr_b16 a[106:107], v8 offset:20992\n"
"v_mfma_f32_16x16x32_bf16 v[192:195], a[52:55], a[0:3], v[192:195]\n"
"v_mfma_f32_16x16x32_bf16 v[224:227], a[56:59], a[0:3], v[224:227]\n"
"v_mfma_f32_16x16x32_bf16 a[144:147], a[60:63], a[0:3], a[144:147]\n"
"ds_read_b128 a[76:79], v7 offset:2048\n"
"v_mfma_f32_16x16x32_bf16 a[176:179], a[64:67], a[0:3], a[176:179]\n"
"v_mfma_f32_16x16x32_bf16 a[208:211], a[68:71], a[0:3], a[208:211]\n"
"ds_read_b64_tr_b16 a[108:109], v9 offset:20480\n"
"v_mfma_f32_16x16x32_bf16 a[212:215], a[68:71], a[4:7], a[212:215]\n"
"v_mfma_f32_16x16x32_bf16 a[180:183], a[64:67], a[4:7], a[180:183]\n"
"ds_read_b64_tr_b16 a[110:111], v9 offset:20992\n"
"v_mfma_f32_16x16x32_bf16 a[148:151], a[60:63], a[4:7], a[148:151]\n"
"v_mfma_f32_16x16x32_bf16 v[228:231], a[56:59], a[4:7], v[228:231]\n"
"v_mfma_f32_16x16x32_bf16 v[196:199], a[52:55], a[4:7], v[196:199]\n"
"ds_read_b64_tr_b16 a[112:113], v10 offset:20480\n"
"v_mfma_f32_16x16x32_bf16 v[164:167], a[48:51], a[4:7], v[164:167]\n"
"v_mfma_f32_16x16x32_bf16 v[132:135], a[44:47], a[4:7], v[132:135]\n"
"ds_read_b128 a[80:83], v7 offset:4096\n"
"v_mfma_f32_16x16x32_bf16 v[100:103], a[40:43], a[4:7], v[100:103]\n"
"v_mfma_f32_16x16x32_bf16 v[68:71], a[36:39], a[4:7], v[68:71]\n"
"ds_read_b64_tr_b16 a[114:115], v10 offset:21504\n"
"v_mfma_f32_16x16x32_bf16 v[36:39], a[32:35], a[4:7], v[36:39]\n"
"v_mfma_f32_16x16x32_bf16 v[40:43], a[32:35], a[8:11], v[40:43]\n"
"v_mfma_f32_16x16x32_bf16 v[72:75], a[36:39], a[8:11], v[72:75]\n"
"ds_read_b64_tr_b16 a[116:117], v11 offset:20480\n"
"v_mfma_f32_16x16x32_bf16 v[104:107], a[40:43], a[8:11], v[104:107]\n"
"v_mfma_f32_16x16x32_bf16 v[136:139], a[44:47], a[8:11], v[136:139]\n"
"ds_read_b128 a[84:87], v7 offset:6144\n"
"v_mfma_f32_16x16x32_bf16 v[168:171], a[48:51], a[8:11], v[168:171]\n"
"v_mfma_f32_16x16x32_bf16 v[200:203], a[52:55], a[8:11], v[200:203]\n"
"ds_read_b64_tr_b16 a[118:119], v11 offset:21504\n"
"v_mfma_f32_16x16x32_bf16 v[232:235], a[56:59], a[8:11], v[232:235]\n"
"v_mfma_f32_16x16x32_bf16 a[152:155], a[60:63], a[8:11], a[152:155]\n"
"v_mfma_f32_16x16x32_bf16 a[184:187], a[64:67], a[8:11], a[184:187]\n"
"ds_read_b64_tr_b16 a[120:121], v12 offset:20480\n"
"v_mfma_f32_16x16x32_bf16 a[216:219], a[68:71], a[8:11], a[216:219]\n"
"v_mfma_f32_16x16x32_bf16 a[220:223], a[68:71], a[12:15], a[220:223]\n"
"ds_read_b64_tr_b16 a[122:123], v12 offset:21504\n"
"v_lshrrev_b32 v30, 2, v0\n"
"v_mfma_f32_16x16x32_bf16 a[188:191], a[64:67], a[12:15], a[188:191]\n"
"v_mfma_f32_16x16x32_bf16 a[156:159], a[60:63], a[12:15], a[156:159]\n"
"v_mfma_f32_16x16x32_bf16 v[236:239], a[56:59], a[12:15], v[236:239]\n"
"ds_read_b128 a[88:91], v7 offset:8192\n"
"v_mfma_f32_16x16x32_bf16 v[204:207], a[52:55], a[12:15], v[204:207]\n"
"v_mfma_f32_16x16x32_bf16 v[172:175], a[48:51], a[12:15], v[172:175]\n"
"ds_read_b64_tr_b16 a[124:125], v13 offset:20480\n"
"v_mfma_f32_16x16x32_bf16 v[140:143], a[44:47], a[12:15], v[140:143]\n"
"v_mfma_f32_16x16x32_bf16 v[108:111], a[40:43], a[12:15], v[108:111]\n"
"ds_read_b64_tr_b16 a[126:127], v13 offset:21504\n"
"v_mfma_f32_16x16x32_bf16 v[76:79], a[36:39], a[12:15], v[76:79]\n"
"v_mfma_f32_16x16x32_bf16 v[44:47], a[32:35], a[12:15], v[44:47]\n"
"v_mfma_f32_16x16x32_bf16 v[48:51], a[32:35], a[16:19], v[48:51]\n"
"ds_read_b128 a[92:95], v7 offset:10240\n"
"v_mfma_f32_16x16x32_bf16 v[80:83], a[36:39], a[16:19], v[80:83]\n"
"v_mfma_f32_16x16x32_bf16 v[112:115], a[40:43], a[16:19], v[112:115]\n"
"ds_read_b64_tr_b16 a[128:129], v10 offset:22528\n"
"v_mfma_f32_16x16x32_bf16 v[144:147], a[44:47], a[16:19], v[144:147]\n"
"v_mfma_f32_16x16x32_bf16 v[176:179], a[48:51], a[16:19], v[176:179]\n"
"ds_read_b64_tr_b16 a[130:131], v10 offset:23552\n"
"v_mfma_f32_16x16x32_bf16 v[208:211], a[52:55], a[16:19], v[208:211]\n"
"v_mfma_f32_16x16x32_bf16 v[240:243], a[56:59], a[16:19], v[240:243]\n"
"v_mfma_f32_16x16x32_bf16 a[160:163], a[60:63], a[16:19], a[160:163]\n"
"ds_read_b64_tr_b16 a[132:133], v11 offset:22528\n"
"v_mfma_f32_16x16x32_bf16 a[192:195], a[64:67], a[16:19], a[192:195]\n"
"v_mfma_f32_16x16x32_bf16 a[224:227], a[68:71], a[16:19], a[224:227]\n"
"ds_read_b128 a[96:99], v7 offset:12288\n"
"v_mfma_f32_16x16x32_bf16 a[228:231], a[68:71], a[20:23], a[228:231]\n"
"v_mfma_f32_16x16x32_bf16 a[196:199], a[64:67], a[20:23], a[196:199]\n"
"ds_read_b64_tr_b16 a[134:135], v11 offset:23552\n"
"v_mfma_f32_16x16x32_bf16 a[164:167], a[60:63], a[20:23], a[164:167]\n"
"v_mfma_f32_16x16x32_bf16 v[244:247], a[56:59], a[20:23], v[244:247]\n"
"v_mfma_f32_16x16x32_bf16 v[212:215], a[52:55], a[20:23], v[212:215]\n"
"ds_read_b64_tr_b16 a[136:137], v12 offset:22528\n"
"v_mfma_f32_16x16x32_bf16 v[180:183], a[48:51], a[20:23], v[180:183]\n"
"v_mfma_f32_16x16x32_bf16 v[148:151], a[44:47], a[20:23], v[148:151]\n"
"ds_read_b128 a[100:103], v7 offset:14336\n"
"v_mfma_f32_16x16x32_bf16 v[116:119], a[40:43], a[20:23], v[116:119]\n"
"v_mfma_f32_16x16x32_bf16 v[84:87], a[36:39], a[20:23], v[84:87]\n"
"ds_read_b64_tr_b16 a[138:139], v12 offset:23552\n"
"v_mfma_f32_16x16x32_bf16 v[52:55], a[32:35], a[20:23], v[52:55]\n"
"v_mfma_f32_16x16x32_bf16 v[56:59], a[32:35], a[24:27], v[56:59]\n"
"v_mfma_f32_16x16x32_bf16 v[88:91], a[36:39], a[24:27], v[88:91]\n"
"ds_read_b64_tr_b16 a[140:141], v13 offset:22528\n"
"v_mfma_f32_16x16x32_bf16 v[120:123], a[40:43], a[24:27], v[120:123]\n"
"v_mfma_f32_16x16x32_bf16 v[152:155], a[44:47], a[24:27], v[152:155]\n"
"ds_read_b64_tr_b16 a[142:143], v13 offset:23552\n"
"v_mfma_f32_16x16x32_bf16 v[184:187], a[48:51], a[24:27], v[184:187]\n"
"v_mfma_f32_16x16x32_bf16 v[216:219], a[52:55], a[24:27], v[216:219]\n"
"v_mfma_f32_16x16x32_bf16 v[248:251], a[56:59], a[24:27], v[248:251]\n"
"v_mfma_f32_16x16x32_bf16 a[168:171], a[60:63], a[24:27], a[168:171]\n"
"v_mfma_f32_16x16x32_bf16 a[200:203], a[64:67], a[24:27], a[200:203]\n"
"v_mfma_f32_16x16x32_bf16 a[232:235], a[68:71], a[24:27], a[232:235]\n"
"v_mfma_f32_16x16x32_bf16 a[236:239], a[68:71], a[28:31], a[236:239]\n"
"v_mfma_f32_16x16x32_bf16 a[204:207], a[64:67], a[28:31], a[204:207]\n"
"v_mfma_f32_16x16x32_bf16 a[172:175], a[60:63], a[28:31], a[172:175]\n"
"v_mfma_f32_16x16x32_bf16 v[252:255], a[56:59], a[28:31], v[252:255]\n"
"v_mfma_f32_16x16x32_bf16 v[220:223], a[52:55], a[28:31], v[220:223]\n"
"v_mfma_f32_16x16x32_bf16 v[188:191], a[48:51], a[28:31], v[188:191]\n"
"v_mfma_f32_16x16x32_bf16 v[156:159], a[44:47], a[28:31], v[156:159]\n"
"v_mfma_f32_16x16x32_bf16 v[124:127], a[40:43], a[28:31], v[124:127]\n"
"v_mfma_f32_16x16x32_bf16 v[92:95], a[36:39], a[28:31], v[92:95]\n"
"v_mfma_f32_16x16x32_bf16 v[60:63], a[32:35], a[28:31], v[60:63]\n"
"s_waitcnt vmcnt(7) lgkmcnt(0)\n"
"s_barrier\n"
"v_mfma_f32_16x16x32_bf16 v[32:35], a[104:107], a[72:75], v[32:35]\n"
"ds_read_b128 a[0:3], v6 offset:32768\n"
"v_mfma_f32_16x16x32_bf16 v[64:67], a[108:111], a[72:75], v[64:67]\n"
"v_mfma_f32_16x16x32_bf16 v[96:99], a[112:115], a[72:75], v[96:99]\n"
"ds_read_b64_tr_b16 a[32:33], v8 offset:40960\n"
"v_mfma_f32_16x16x32_bf16 v[128:131], a[116:119], a[72:75], v[128:131]\n"
"v_mfma_f32_16x16x32_bf16 v[160:163], a[120:123], a[72:75], v[160:163]\n"
"ds_read_b64_tr_b16 a[34:35], v8 offset:41472\n"
"s_waitcnt vmcnt(1)\n"
"v_cmp_ne_u64 vcc, s[90:91], v[24:25]\n"
"v_cmp_ne_u64_e64 s[96:97], s[90:91], v[26:27]\n"
"s_nop 0\n"
"s_or_b64 vcc, vcc, s[96:97]\n"
"s_cbranch_vccnz SPQ5_%=\n"
"BKQ6_%=:\n"
"v_mfma_f32_16x16x32_bf16 v[192:195], a[124:127], a[72:75], v[192:195]\n"
"v_mfma_f32_16x16x32_bf16 v[224:227], a[128:131], a[72:75], v[224:227]\n"
"v_mfma_f32_16x16x32_bf16 a[144:147], a[132:135], a[72:75], a[144:147]\n"
"ds_read_b128 a[4:7], v6 offset:34816\n"
"v_mfma_f32_16x16x32_bf16 a[176:179], a[136:139], a[72:75], a[176:179]\n"
"s_waitcnt vmcnt(0)\n"
"v_cmp_ne_u64 vcc, s[90:91], v[20:21]\n"
"v_cmp_ne_u64_e64 s[96:97], s[90:91], v[22:23]\n"
"s_nop 0\n"
"s_or_b64 vcc, vcc, s[96:97]\n"
"s_cbranch_vccnz SPQ8_%=\n"
"BKQ9_%=:\n"
"v_mfma_f32_16x16x32_bf16 a[208:211], a[140:143], a[72:75], a[208:211]\n"
"ds_read_b64_tr_b16 a[36:37], v9 offset:40960\n"
"v_mfma_f32_16x16x32_bf16 a[212:215], a[140:143], a[76:79], a[212:215]\n"
"v_mfma_f32_16x16x32_bf16 a[180:183], a[136:139], a[76:79], a[180:183]\n"
"ds_read_b64_tr_b16 a[38:39], v9 offset:41472\n"
"s_add_u32 m0, s11, 0\n"
"v_mfma_f32_16x16x32_bf16 a[148:151], a[132:135], a[76:79], a[148:151]\n"
"buffer_load_dwordx4 v0, s[20:23], s6 offen offset:0 sc1 lds\n"
"v_mfma_f32_16x16x32_bf16 v[228:231], a[128:131], a[76:79], v[228:231]\n"
"v_mfma_f32_16x16x32_bf16 v[196:199], a[124:127], a[76:79], v[196:199]\n"
"ds_read_b64_tr_b16 a[40:41], v10 offset:40960\n"
"v_mfma_f32_16x16x32_bf16 v[164:167], a[120:123], a[76:79], v[164:167]\n"
"v_mfma_f32_16x16x32_bf16 v[132:135], a[116:119], a[76:79], v[132:135]\n"
"ds_read_b128 a[8:11], v6 offset:36864\n"
"v_mfma_f32_16x16x32_bf16 v[100:103], a[112:115], a[76:79], v[100:103]\n"
"buffer_load_dwordx4 v0, s[20:23], s6 offen offset:1024 sc1 lds\n"
"v_mfma_f32_16x16x32_bf16 v[68:71], a[108:111], a[76:79], v[68:71]\n"
"ds_read_b64_tr_b16 a[42:43], v10 offset:41984\n"
"v_mfma_f32_16x16x32_bf16 v[36:39], a[104:107], a[76:79], v[36:39]\n"
"v_mfma_f32_16x16x32_bf16 v[40:43], a[104:107], a[80:83], v[40:43]\n"
"v_mfma_f32_16x16x32_bf16 v[72:75], a[108:111], a[80:83], v[72:75]\n"
"ds_read_b64_tr_b16 a[44:45], v11 offset:40960\n"
"v_mfma_f32_16x16x32_bf16 v[104:107], a[112:115], a[80:83], v[104:107]\n"
"buffer_load_dwordx4 v0, s[20:23], s6 offen offset:2048 sc1 lds\n"
"v_mfma_f32_16x16x32_bf16 v[136:139], a[116:119], a[80:83], v[136:139]\n"
"ds_read_b128 a[12:15], v6 offset:38912\n"
"v_mfma_f32_16x16x32_bf16 v[168:171], a[120:123], a[80:83], v[168:171]\n"
"v_mfma_f32_16x16x32_bf16 v[200:203], a[124:127], a[80:83], v[200:203]\n"
"ds_read_b64_tr_b16 a[46:47], v11 offset:41984\n"
"v_mfma_f32_16x16x32_bf16 v[232:235], a[128:131], a[80:83], v[232:235]\n"
"v_mfma_f32_16x16x32_bf16 a[152:155], a[132:135], a[80:83], a[152:155]\n"
"v_mfma_f32_16x16x32_bf16 a[184:187], a[136:139], a[80:83], a[184:187]\n"
"ds_read_b64_tr_b16 a[48:49], v12 offset:40960\n"
"buffer_load_dwordx4 v0, s[20:23], s6 offen offset:3072 sc1 lds\n"
"s_add_u32 s6, s6, 4096\n"
"v_mfma_f32_16x16x32_bf16 a[216:219], a[140:143], a[80:83], a[216:219]\n"
"v_mfma_f32_16x16x32_bf16 a[220:223], a[140:143], a[84:87], a[220:223]\n"
"ds_read_b64_tr_b16 a[50:51], v12 offset:41984\n"
"v_mfma_f32_16x16x32_bf16 a[188:191], a[136:139], a[84:87], a[188:191]\n"
"v_mfma_f32_16x16x32_bf16 a[156:159], a[132:135], a[84:87], a[156:159]\n"
"s_add_u32 m0, s11, 4096\n"
"v_mfma_f32_16x16x32_bf16 v[236:239], a[128:131], a[84:87], v[236:239]\n"
"ds_read_b128 a[16:19], v6 offset:40960\n"
"buffer_load_dwordx4 v0, s[20:23], s6 offen offset:0 sc1 lds\n"
"v_mfma_f32_16x16x32_bf16 v[204:207], a[124:127], a[84:87], v[204:207]\n"
"v_mfma_f32_16x16x32_bf16 v[172:175], a[120:123], a[84:87], v[172:175]\n"
"ds_read_b64_tr_b16 a[52:53], v13 offset:40960\n"
"v_mfma_f32_16x16x32_bf16 v[140:143], a[116:119], a[84:87], v[140:143]\n"
"v_mfma_f32_16x16x32_bf16 v[108:111], a[112:115], a[84:87], v[108:111]\n"
"ds_read_b64_tr_b16 a[54:55], v13 offset:41984\n"
"v_mfma_f32_16x16x32_bf16 v[76:79], a[108:111], a[84:87], v[76:79]\n"
"s_add_u32 m0, s12, 0\n"
"v_mfma_f32_16x16x32_bf16 v[44:47], a[104:107], a[84:87], v[44:47]\n"
"buffer_load_dwordx4 v0, s[20:23], s7 offen offset:0 sc1 lds\n"
"v_mfma_f32_16x16x32_bf16 v[48:51], a[104:107], a[88:91], v[48:51]\n"
"ds_read_b128 a[20:23], v6 offset:43008\n"
"v_mfma_f32_16x16x32_bf16 v[80:83], a[108:111], a[88:91], v[80:83]\n"
"v_mfma_f32_16x16x32_bf16 v[112:115], a[112:115], a[88:91], v[112:115]\n"
"ds_read_b64_tr_b16 a[56:57], v10 offset:43008\n"
"v_mfma_f32_16x16x32_bf16 v[144:147], a[116:119], a[88:91], v[144:147]\n"
"v_mfma_f32_16x16x32_bf16 v[176:179], a[120:123], a[88:91], v[176:179]\n"
"ds_read_b64_tr_b16 a[58:59], v10 offset:44032\n"
"buffer_load_dwordx4 v0, s[20:23], s7 offen offset:1024 sc1 lds\n"
"v_mfma_f32_16x16x32_bf16 v[208:211], a[124:127], a[88:91], v[208:211]\n"
"v_mfma_f32_16x16x32_bf16 v[240:243], a[128:131], a[88:91], v[240:243]\n"
"v_mfma_f32_16x16x32_bf16 a[160:163], a[132:135], a[88:91], a[160:163]\n"
"ds_read_b64_tr_b16 a[60:61], v11 offset:43008\n"
"v_mfma_f32_16x16x32_bf16 a[192:195], a[136:139], a[88:91], a[192:195]\n"
"v_mfma_f32_16x16x32_bf16 a[224:227], a[140:143], a[88:91], a[224:227]\n"
"ds_read_b128 a[24:27], v6 offset:45056\n"
"v_mfma_f32_16x16x32_bf16 a[228:231], a[140:143], a[92:95], a[228:231]\n"
"v_mfma_f32_16x16x32_bf16 a[196:199], a[136:139], a[92:95], a[196:199]\n"
"ds_read_b64_tr_b16 a[62:63], v11 offset:44032\n"
"v_mfma_f32_16x16x32_bf16 a[164:167], a[132:135], a[92:95], a[164:167]\n"
"v_mfma_f32_16x16x32_bf16 v[244:247], a[128:131], a[92:95], v[244:247]\n"
"v_mfma_f32_16x16x32_bf16 v[212:215], a[124:127], a[92:95], v[212:215]\n"
"ds_read_b64_tr_b16 a[64:65], v12 offset:43008\n"
"v_mfma_f32_16x16x32_bf16 v[180:183], a[120:123], a[92:95], v[180:183]\n"
"v_mfma_f32_16x16x32_bf16 v[148:151], a[116:119], a[92:95], v[148:151]\n"
"ds_read_b128 a[28:31], v6 offset:47104\n"
"v_mfma_f32_16x16x32_bf16 v[116:119], a[112:115], a[92:95], v[116:119]\n"
"v_mfma_f32_16x16x32_bf16 v[84:87], a[108:111], a[92:95], v[84:87]\n"
"ds_read_b64_tr_b16 a[66:67], v12 offset:44032\n"
"v_mfma_f32_16x16x32_bf16 v[52:55], a[104:107], a[92:95], v[52:55]\n"
"v_mfma_f32_16x16x32_bf16 v[56:59], a[104:107], a[96:99], v[56:59]\n"
"v_mfma_f32_16x16x32_bf16 v[88:91], a[108:111], a[96:99], v[88:91]\n"
"ds_read_b64_tr_b16 a[68:69], v13 offset:43008\n"
"v_mfma_f32_16x16x32_bf16 v[120:123], a[112:115], a[96:99], v[120:123]\n"
"v_mfma_f32_16x16x32_bf16 v[152:155], a[116:119], a[96:99], v[152:155]\n"
"ds_read_b64_tr_b16 a[70:71], v13 offset:44032\n"
"v_mfma_f32_16x16x32_bf16 v[184:187], a[120:123], a[96:99], v[184:187]\n"
"v_mfma_f32_16x16x32_bf16 v[216:219], a[124:127], a[96:99], v[216:219]\n"
"v_mfma_f32_16x16x32_bf16 v[248:251], a[128:131], a[96:99], v[248:251]\n"
"v_mfma_f32_16x16x32_bf16 a[168:171], a[132:135], a[96:99], a[168:171]\n"
"v_mfma_f32_16x16x32_bf16 a[200:203], a[136:139], a[96:99], a[200:203]\n"
"v_mfma_f32_16x16x32_bf16 a[232:235], a[140:143], a[96:99], a[232:235]\n"
"v_mfma_f32_16x16x32_bf16 a[236:239], a[140:143], a[100:103], a[236:239]\n"
"v_mfma_f32_16x16x32_bf16 a[204:207], a[136:139], a[100:103], a[204:207]\n"
"v_mfma_f32_16x16x32_bf16 a[172:175], a[132:135], a[100:103], a[172:175]\n"
"v_mfma_f32_16x16x32_bf16 v[252:255], a[128:131], a[100:103], v[252:255]\n"
"v_mfma_f32_16x16x32_bf16 v[220:223], a[124:127], a[100:103], v[220:223]\n"
"v_mfma_f32_16x16x32_bf16 v[188:191], a[120:123], a[100:103], v[188:191]\n"
"v_mfma_f32_16x16x32_bf16 v[156:159], a[116:119], a[100:103], v[156:159]\n"
"v_mfma_f32_16x16x32_bf16 v[124:127], a[112:115], a[100:103], v[124:127]\n"
"v_mfma_f32_16x16x32_bf16 v[92:95], a[108:111], a[100:103], v[92:95]\n"
"v_mfma_f32_16x16x32_bf16 v[60:63], a[104:107], a[100:103], v[60:63]\n"
"s_waitcnt vmcnt(9) lgkmcnt(0)\n"
"s_barrier\n"
"v_mfma_f32_16x16x32_bf16 v[32:35], a[32:35], a[0:3], v[32:35]\n"
"ds_read_b128 a[72:75], v7 offset:32768\n"
"v_mfma_f32_16x16x32_bf16 v[64:67], a[36:39], a[0:3], v[64:67]\n"
"v_mfma_f32_16x16x32_bf16 v[96:99], a[40:43], a[0:3], v[96:99]\n"
"ds_read_b64_tr_b16 a[104:105], v8 offset:61440\n"
"v_mfma_f32_16x16x32_bf16 v[128:131], a[44:47], a[0:3], v[128:131]\n"
"v_mfma_f32_16x16x32_bf16 v[160:163], a[48:51], a[0:3], v[160:163]\n"
"ds_read_b64_tr_b16 a[106:107], v8 offset:61952\n"
"v_mfma_f32_16x16x32_bf16 v[192:195], a[52:55], a[0:3], v[192:195]\n"
"v_mfma_f32_16x16x32_bf16 v[224:227], a[56:59], a[0:3], v[224:227]\n"
"v_mfma_f32_16x16x32_bf16 a[144:147], a[60:63], a[0:3], a[144:147]\n"
"ds_read_b128 a[76:79], v7 offset:34816\n"
"v_mfma_f32_16x16x32_bf16 a[176:179], a[64:67], a[0:3], a[176:179]\n"
"v_mfma_f32_16x16x32_bf16 a[208:211], a[68:71], a[0:3], a[208:211]\n"
"ds_read_b64_tr_b16 a[108:109], v9 offset:61440\n"
"v_mfma_f32_16x16x32_bf16 a[212:215], a[68:71], a[4:7], a[212:215]\n"
"v_mfma_f32_16x16x32_bf16 a[180:183], a[64:67], a[4:7], a[180:183]\n"
"ds_read_b64_tr_b16 a[110:111], v9 offset:61952\n"
"v_mfma_f32_16x16x32_bf16 a[148:151], a[60:63], a[4:7], a[148:151]\n"
"v_mfma_f32_16x16x32_bf16 v[228:231], a[56:59], a[4:7], v[228:231]\n"
"v_mfma_f32_16x16x32_bf16 v[196:199], a[52:55], a[4:7], v[196:199]\n"
"ds_read_b64_tr_b16 a[112:113], v10 offset:61440\n"
"v_mfma_f32_16x16x32_bf16 v[164:167], a[48:51], a[4:7], v[164:167]\n"
"v_mfma_f32_16x16x32_bf16 v[132:135], a[44:47], a[4:7], v[132:135]\n"
"ds_read_b128 a[80:83], v7 offset:36864\n"
"v_mfma_f32_16x16x32_bf16 v[100:103], a[40:43], a[4:7], v[100:103]\n"
"v_mfma_f32_16x16x32_bf16 v[68:71], a[36:39], a[4:7], v[68:71]\n"
"ds_read_b64_tr_b16 a[114:115], v10 offset:62464\n"
"v_mfma_f32_16x16x32_bf16 v[36:39], a[32:35], a[4:7], v[36:39]\n"
"v_mfma_f32_16x16x32_bf16 v[40:43], a[32:35], a[8:11], v[40:43]\n"
"v_mfma_f32_16x16x32_bf16 v[72:75], a[36:39], a[8:11], v[72:75]\n"
"ds_read_b64_tr_b16 a[116:117], v11 offset:61440\n"
"v_mfma_f32_16x16x32_bf16 v[104:107], a[40:43], a[8:11], v[104:107]\n"
"v_mfma_f32_16x16x32_bf16 v[136:139], a[44:47], a[8:11], v[136:139]\n"
"ds_read_b128 a[84:87], v7 offset:38912\n"
"v_mfma_f32_16x16x32_bf16 v[168:171], a[48:51], a[8:11], v[168:171]\n"
"v_mfma_f32_16x16x32_bf16 v[200:203], a[52:55], a[8:11], v[200:203]\n"
"ds_read_b64_tr_b16 a[118:119], v11 offset:62464\n"
"v_mfma_f32_16x16x32_bf16 v[232:235], a[56:59], a[8:11], v[232:235]\n"
"v_mfma_f32_16x16x32_bf16 a[152:155], a[60:63], a[8:11], a[152:155]\n"
"v_mfma_f32_16x16x32_bf16 a[184:187], a[64:67], a[8:11], a[184:187]\n"
"ds_read_b64_tr_b16 a[120:121], v12 offset:61440\n"
"v_mfma_f32_16x16x32_bf16 a[216:219], a[68:71], a[8:11], a[216:219]\n"
"v_mfma_f32_16x16x32_bf16 a[220:223], a[68:71], a[12:15], a[220:223]\n"
"ds_read_b64_tr_b16 a[122:123], v12 offset:62464\n"
"v_mfma_f32_16x16x32_bf16 a[188:191], a[64:67], a[12:15], a[188:191]\n"
"v_mfma_f32_16x16x32_bf16 a[156:159], a[60:63], a[12:15], a[156:159]\n"
"v_mfma_f32_16x16x32_bf16 v[236:239], a[56:59], a[12:15], v[236:239]\n"
"ds_read_b128 a[88:91], v7 offset:40960\n"
"v_mfma_f32_16x16x32_bf16 v[204:207], a[52:55], a[12:15], v[204:207]\n"
"v_mfma_f32_16x16x32_bf16 v[172:175], a[48:51], a[12:15], v[172:175]\n"
"ds_read_b64_tr_b16 a[124:125], v13 offset:61440\n"
"v_mfma_f32_16x16x32_bf16 v[140:143], a[44:47], a[12:15], v[140:143]\n"
"v_mfma_f32_16x16x32_bf16 v[108:111], a[40:43], a[12:15], v[108:111]\n"
"ds_read_b64_tr_b16 a[126:127], v13 offset:62464\n"
"v_mfma_f32_16x16x32_bf16 v[76:79], a[36:39], a[12:15], v[76:79]\n"
"v_mfma_f32_16x16x32_bf16 v[44:47], a[32:35], a[12:15], v[44:47]\n"
"v_mfma_f32_16x16x32_bf16 v[48:51], a[32:35], a[16:19], v[48:51]\n"
"ds_read_b128 a[92:95], v7 offset:43008\n"
"v_accvgpr_read_b32 v20, a241\n"
"v_mfma_f32_16x16x32_bf16 v[80:83], a[36:39], a[16:19], v[80:83]\n"
"v_mfma_f32_16x16x32_bf16 v[112:115], a[40:43], a[16:19], v[112:115]\n"
"ds_read_b64_tr_b16 a[128:129], v10 offset:63488\n"
"v_accvgpr_read_b32 v21, a242\n"
"v_mfma_f32_16x16x32_bf16 v[144:147], a[44:47], a[16:19], v[144:147]\n"
"v_mfma_f32_16x16x32_bf16 v[176:179], a[48:51], a[16:19], v[176:179]\n"
"ds_read_b64_tr_b16 a[130:131], v10 offset:64512\n"
"v_accvgpr_read_b32 v22, a243\n"
"v_mfma_f32_16x16x32_bf16 v[208:211], a[52:55], a[16:19], v[208:211]\n"
"v_mfma_f32_16x16x32_bf16 v[240:243], a[56:59], a[16:19], v[240:243]\n"
"v_accvgpr_read_b32 v23, a244\n"
"v_mfma_f32_16x16x32_bf16 a[160:163], a[60:63], a[16:19], a[160:163]\n"
"ds_read_b64_tr_b16 a[132:133], v11 offset:63488\n"
"v_mfma_f32_16x16x32_bf16 a[192:195], a[64:67], a[16:19], a[192:195]\n"
"v_accvgpr_read_b32 v24, a245\n"
"v_mfma_f32_16x16x32_bf16 a[224:227], a[68:71], a[16:19], a[224:227]\n"
"ds_read_b128 a[96:99], v7 offset:45056\n"
"v_mfma_f32_16x16x32_bf16 a[228:231], a[68:71], a[20:23], a[228:231]\n"
"v_accvgpr_read_b32 v25, a250\n"
"v_mfma_f32_16x16x32_bf16 a[196:199], a[64:67], a[20:23], a[196:199]\n"
"ds_read_b64_tr_b16 a[134:135], v11 offset:64512\n"
"v_mfma_f32_16x16x32_bf16 a[164:167], a[60:63], a[20:23], a[164:167]\n"
"v_accvgpr_read_b32 v27, a251\n"
"v_mfma_f32_16x16x32_bf16 v[244:247], a[56:59], a[20:23], v[244:247]\n"
"v_mfma_f32_16x16x32_bf16 v[212:215], a[52:55], a[20:23], v[212:215]\n"
"ds_read_b64_tr_b16 a[136:137], v12 offset:63488\n"
"v_mfma_f32_16x16x32_bf16 v[180:183], a[48:51], a[20:23], v[180:183]\n"
"v_mfma_f32_16x16x32_bf16 v[148:151], a[44:47], a[20:23], v[148:151]\n"
"ds_read_b128 a[100:103], v7 offset:47104\n"
"v_add_u32 v26, 7168, v25\n"
"v_mfma_f32_16x16x32_bf16 v[116:119], a[40:43], a[20:23], v[116:119]\n"
"v_mfma_f32_16x16x32_bf16 v[84:87], a[36:39], a[20:23], v[84:87]\n"
"ds_read_b64_tr_b16 a[138:139], v12 offset:64512\n"
"v_mfma_f32_16x16x32_bf16 v[52:55], a[32:35], a[20:23], v[52:55]\n"
"v_mfma_f32_16x16x32_bf16 v[56:59], a[32:35], a[24:27], v[56:59]\n"
"v_mfma_f32_16x16x32_bf16 v[88:91], a[36:39], a[24:27], v[88:91]\n"
"ds_read_b64_tr_b16 a[140:141], v13 offset:63488\n"
"v_mfma_f32_16x16x32_bf16 v[120:123], a[40:43], a[24:27], v[120:123]\n"
"v_mfma_f32_16x16x32_bf16 v[152:155], a[44:47], a[24:27], v[152:155]\n"
"ds_read_b64_tr_b16 a[142:143], v13 offset:64512\n"
"v_mfma_f32_16x16x32_bf16 v[184:187], a[48:51], a[24:27], v[184:187]\n"
"v_mfma_f32_16x16x32_bf16 v[216:219], a[52:55], a[24:27], v[216:219]\n"
"v_mfma_f32_16x16x32_bf16 v[248:251], a[56:59], a[24:27], v[248:251]\n"
"v_add_u32 v29, s15, v0\n"
"s_lshl_b32 s94, s14, 12\n"
"s_lshl_b32 s95, s14, 9\n"
"v_mfma_f32_16x16x32_bf16 a[168:171], a[60:63], a[24:27], a[168:171]\n"
"v_and_b32 v30, 0xf0, v0\n"
"s_add_u32 s94, s94, 20480\n"
"s_add_u32 s95, s95, 28672\n"
"v_mfma_f32_16x16x32_bf16 a[200:203], a[64:67], a[24:27], a[200:203]\n"
"v_lshl_add_u32 v30, v30, 1, s94\n"
"v_mfma_f32_16x16x32_bf16 a[232:235], a[68:71], a[24:27], a[232:235]\n"
"v_lshrrev_b32 v1, 8, v0\n"
"v_mfma_f32_16x16x32_bf16 a[236:239], a[68:71], a[28:31], a[236:239]\n"
"v_lshl_add_u32 v1, v1, 4, s95\n"
"v_mfma_f32_16x16x32_bf16 a[204:207], a[64:67], a[28:31], a[204:207]\n"
"v_mfma_f32_16x16x32_bf16 a[172:175], a[60:63], a[28:31], a[172:175]\n"
"v_mfma_f32_16x16x32_bf16 v[252:255], a[56:59], a[28:31], v[252:255]\n"
"v_mfma_f32_16x16x32_bf16 v[220:223], a[52:55], a[28:31], v[220:223]\n"
"v_mfma_f32_16x16x32_bf16 v[188:191], a[48:51], a[28:31], v[188:191]\n"
"v_mfma_f32_16x16x32_bf16 v[156:159], a[44:47], a[28:31], v[156:159]\n"
"v_mfma_f32_16x16x32_bf16 v[124:127], a[40:43], a[28:31], v[124:127]\n"
"v_mfma_f32_16x16x32_bf16 v[92:95], a[36:39], a[28:31], v[92:95]\n"
"v_mfma_f32_16x16x32_bf16 v[60:63], a[32:35], a[28:31], v[60:63]\n"
"s_waitcnt vmcnt(0) lgkmcnt(0)\n"
"s_barrier\n"
"v_mfma_f32_16x16x32_bf16 v[32:35], a[104:107], a[72:75], v[32:35]\n"
"ds_read_b64_tr_b16 a[0:1], v16 offset:0\n"
"s_add_u32 m0, s77, 0\n"
"v_mfma_f32_16x16x32_bf16 v[64:67], a[108:111], a[72:75], v[64:67]\n"
"ds_read_b128 a[56:59], v29 offset:0\n"
"buffer_load_dwordx4 v25, s[52:55], s74 offen offset:0 lds\n"
"v_mfma_f32_16x16x32_bf16 v[96:99], a[112:115], a[72:75], v[96:99]\n"
"v_mfma_f32_16x16x32_bf16 v[128:131], a[116:119], a[72:75], v[128:131]\n"
"v_mfma_f32_16x16x32_bf16 v[160:163], a[120:123], a[72:75], v[160:163]\n"
"ds_read_b64_tr_b16 a[2:3], v16 offset:256\n"
"ds_read_b128 a[60:63], v29 offset:1024\n"
"v_mfma_f32_16x16x32_bf16 v[192:195], a[124:127], a[72:75], v[192:195]\n"
"buffer_load_dwordx4 v26, s[52:55], s74 offen offset:1024 lds\n"
"v_mfma_f32_16x16x32_bf16 v[224:227], a[128:131], a[72:75], v[224:227]\n"
"v_mfma_f32_16x16x32_bf16 a[144:147], a[132:135], a[72:75], a[144:147]\n"
"ds_read_b128 a[8:11], v29 offset:2048\n"
"v_mfma_f32_16x16x32_bf16 a[176:179], a[136:139], a[72:75], a[176:179]\n"
"ds_read_b64_tr_b16 a[4:5], v16 offset:32\n"
"v_mfma_f32_16x16x32_bf16 a[208:211], a[140:143], a[72:75], a[208:211]\n"
"v_mfma_f32_16x16x32_bf16 a[212:215], a[140:143], a[76:79], a[212:215]\n"
"ds_read_b128 a[12:15], v29 offset:3072\n"
"buffer_load_dwordx4 v27, s[52:55], s74 offen offset:2048 lds\n"
"v_mfma_f32_16x16x32_bf16 a[180:183], a[136:139], a[76:79], a[180:183]\n"
"v_mfma_f32_16x16x32_bf16 a[148:151], a[132:135], a[76:79], a[148:151]\n"
"ds_read_b64_tr_b16 a[6:7], v16 offset:288\n"
"v_mfma_f32_16x16x32_bf16 v[228:231], a[128:131], a[76:79], v[228:231]\n"
"ds_read_b128 a[16:19], v29 offset:4096\n"
"v_mfma_f32_16x16x32_bf16 v[196:199], a[124:127], a[76:79], v[196:199]\n"
"v_mfma_f32_16x16x32_bf16 v[164:167], a[120:123], a[76:79], v[164:167]\n"
"buffer_load_dwordx4 v28, s[52:55], s74 offen offset:3072 lds\n"
"s_add_u32 s74, s74, 131072\n"
"v_mfma_f32_16x16x32_bf16 v[132:135], a[116:119], a[76:79], v[132:135]\n"
"ds_read_b128 a[20:23], v29 offset:5120\n"
"v_mfma_f32_16x16x32_bf16 v[100:103], a[112:115], a[76:79], v[100:103]\n"
"v_mfma_f32_16x16x32_bf16 v[68:71], a[108:111], a[76:79], v[68:71]\n"
"v_mfma_f32_16x16x32_bf16 v[36:39], a[104:107], a[76:79], v[36:39]\n"
"ds_read_b128 a[24:27], v29 offset:6144\n"
"s_add_u32 m0, s77, 16384\n"
"v_mfma_f32_16x16x32_bf16 v[40:43], a[104:107], a[80:83], v[40:43]\n"
"buffer_load_dwordx4 v25, s[52:55], s74 offen offset:0 lds\n"
"v_mfma_f32_16x16x32_bf16 v[72:75], a[108:111], a[80:83], v[72:75]\n"
"v_mfma_f32_16x16x32_bf16 v[104:107], a[112:115], a[80:83], v[104:107]\n"
"ds_read_b128 a[28:31], v29 offset:7168\n"
"v_mfma_f32_16x16x32_bf16 v[136:139], a[116:119], a[80:83], v[136:139]\n"
"v_mfma_f32_16x16x32_bf16 v[168:171], a[120:123], a[80:83], v[168:171]\n"
"v_mfma_f32_16x16x32_bf16 v[200:203], a[124:127], a[80:83], v[200:203]\n"
"buffer_load_dwordx4 v26, s[52:55], s74 offen offset:1024 lds\n"
"v_mfma_f32_16x16x32_bf16 v[232:235], a[128:131], a[80:83], v[232:235]\n"
"ds_read_b128 a[32:35], v29 offset:8192\n"
"v_mfma_f32_16x16x32_bf16 a[152:155], a[132:135], a[80:83], a[152:155]\n"
"v_mfma_f32_16x16x32_bf16 a[184:187], a[136:139], a[80:83], a[184:187]\n"
"v_mfma_f32_16x16x32_bf16 a[216:219], a[140:143], a[80:83], a[216:219]\n"
"ds_read_b128 a[36:39], v29 offset:9216\n"
"buffer_load_dwordx4 v27, s[52:55], s74 offen offset:2048 lds\n"
"v_mfma_f32_16x16x32_bf16 a[220:223], a[140:143], a[84:87], a[220:223]\n"
"v_mfma_f32_16x16x32_bf16 a[188:191], a[136:139], a[84:87], a[188:191]\n"
"v_mfma_f32_16x16x32_bf16 a[156:159], a[132:135], a[84:87], a[156:159]\n"
"ds_read_b128 a[40:43], v30 offset:0\n"
"v_mfma_f32_16x16x32_bf16 v[236:239], a[128:131], a[84:87], v[236:239]\n"
"v_mfma_f32_16x16x32_bf16 v[204:207], a[124:127], a[84:87], v[204:207]\n"
"buffer_load_dwordx4 v28, s[52:55], s74 offen offset:3072 lds\n"
"s_add_u32 s74, s74, 524288\n"
"v_mfma_f32_16x16x32_bf16 v[172:175], a[120:123], a[84:87], v[172:175]\n"
"ds_read_b128 a[44:47], v30 offset:512\n"
"v_mfma_f32_16x16x32_bf16 v[140:143], a[116:119], a[84:87], v[140:143]\n"
"v_mfma_f32_16x16x32_bf16 v[108:111], a[112:115], a[84:87], v[108:111]\n"
"v_mfma_f32_16x16x32_bf16 v[76:79], a[108:111], a[84:87], v[76:79]\n"
"ds_read_b128 a[48:51], v30 offset:1024\n"
"s_add_u32 m0, s77, 32768\n"
"v_mfma_f32_16x16x32_bf16 v[44:47], a[104:107], a[84:87], v[44:47]\n"
"buffer_load_dwordx4 v25, s[52:55], s74 offen offset:0 lds\n"
"v_mfma_f32_16x16x32_bf16 v[48:51], a[104:107], a[88:91], v[48:51]\n"
"v_mfma_f32_16x16x32_bf16 v[80:83], a[108:111], a[88:91], v[80:83]\n"
"ds_read_b128 a[52:55], v30 offset:1536\n"
"v_mfma_f32_16x16x32_bf16 v[112:115], a[112:115], a[88:91], v[112:115]\n"
"v_mfma_f32_16x16x32_bf16 v[144:147], a[116:119], a[88:91], v[144:147]\n"
"v_mfma_f32_16x16x32_bf16 v[176:179], a[120:123], a[88:91], v[176:179]\n"
"ds_read_b64 a[240:241], v30 offset:16\n"
"buffer_load_dwordx4 v26, s[52:55], s74 offen offset:1024 lds\n"
"v_mfma_f32_16x16x32_bf16 v[208:211], a[124:127], a[88:91], v[208:211]\n"
"v_mfma_f32_16x16x32_bf16 v[240:243], a[128:131], a[88:91], v[240:243]\n"
"v_mfma_f32_16x16x32_bf16 a[160:163], a[132:135], a[88:91], a[160:163]\n"
"v_mfma_f32_16x16x32_bf16 a[192:195], a[136:139], a[88:91], a[192:195]\n"
"ds_read_b64 a[242:243], v30 offset:528\n"
"v_mfma_f32_16x16x32_bf16 a[224:227], a[140:143], a[88:91], a[224:227]\n"
"buffer_load_dwordx4 v27, s[52:55], s74 offen offset:2048 lds\n"
"v_mfma_f32_16x16x32_bf16 a[228:231], a[140:143], a[92:95], a[228:231]\n"
"v_mfma_f32_16x16x32_bf16 a[196:199], a[136:139], a[92:95], a[196:199]\n"
"ds_read_b64 a[244:245], v30 offset:1040\n"
"v_mfma_f32_16x16x32_bf16 a[164:167], a[132:135], a[92:95], a[164:167]\n"
"v_mfma_f32_16x16x32_bf16 v[244:247], a[128:131], a[92:95], v[244:247]\n"
"buffer_load_dwordx4 v28, s[52:55], s74 offen offset:3072 lds\n"
"s_add_u32 s74, s74, 131072\n"
"v_mfma_f32_16x16x32_bf16 v[212:215], a[124:127], a[92:95], v[212:215]\n"
"ds_read_b64 a[246:247], v30 offset:1552\n"
"v_mfma_f32_16x16x32_bf16 v[180:183], a[120:123], a[92:95], v[180:183]\n"
"v_mfma_f32_16x16x32_bf16 v[148:151], a[116:119], a[92:95], v[148:151]\n"
"v_mfma_f32_16x16x32_bf16 v[116:119], a[112:115], a[92:95], v[116:119]\n"
"ds_read_b64 a[248:249], v30 offset:2064\n"
"s_add_u32 m0, s77, 49152\n"
"v_mfma_f32_16x16x32_bf16 v[84:87], a[108:111], a[92:95], v[84:87]\n"
"buffer_load_dwordx4 v25, s[52:55], s74 offen offset:0 lds\n"
"v_mfma_f32_16x16x32_bf16 v[52:55], a[104:107], a[92:95], v[52:55]\n"
"v_mfma_f32_16x16x32_bf16 v[56:59], a[104:107], a[96:99], v[56:59]\n"
"ds_read_b64 a[250:251], v30 offset:2576\n"
"v_mfma_f32_16x16x32_bf16 v[88:91], a[108:111], a[96:99], v[88:91]\n"
"v_mfma_f32_16x16x32_bf16 v[120:123], a[112:115], a[96:99], v[120:123]\n"
"v_mfma_f32_16x16x32_bf16 v[152:155], a[116:119], a[96:99], v[152:155]\n"
"ds_read_b64 a[252:253], v30 offset:3088\n"
"buffer_load_dwordx4 v26, s[52:55], s74 offen offset:1024 lds\n"
"v_mfma_f32_16x16x32_bf16 v[184:187], a[120:123], a[96:99], v[184:187]\n"
"v_mfma_f32_16x16x32_bf16 v[216:219], a[124:127], a[96:99], v[216:219]\n"
"v_mfma_f32_16x16x32_bf16 v[248:251], a[128:131], a[96:99], v[248:251]\n"
"ds_read_b64 a[254:255], v30 offset:3600\n"
"v_mfma_f32_16x16x32_bf16 a[168:171], a[132:135], a[96:99], a[168:171]\n"
"v_mfma_f32_16x16x32_bf16 a[200:203], a[136:139], a[96:99], a[200:203]\n"
"buffer_load_dwordx4 v27, s[52:55], s74 offen offset:2048 lds\n"
"v_mfma_f32_16x16x32_bf16 a[232:235], a[140:143], a[96:99], a[232:235]\n"
"v_mfma_f32_16x16x32_bf16 a[236:239], a[140:143], a[100:103], a[236:239]\n"
"v_mfma_f32_16x16x32_bf16 a[204:207], a[136:139], a[100:103], a[204:207]\n"
"v_mfma_f32_16x16x32_bf16 a[172:175], a[132:135], a[100:103], a[172:175]\n"
"v_mfma_f32_16x16x32_bf16 v[252:255], a[128:131], a[100:103], v[252:255]\n"
"buffer_load_dwordx4 v28, s[52:55], s74 offen offset:3072 lds\n"
"s_add_u32 s74, s74, 131072\n"
"v_mfma_f32_16x16x32_bf16 v[220:223], a[124:127], a[100:103], v[220:223]\n"
"v_mfma_f32_16x16x32_bf16 v[188:191], a[120:123], a[100:103], v[188:191]\n"
"v_mfma_f32_16x16x32_bf16 v[156:159], a[116:119], a[100:103], v[156:159]\n"
"v_mfma_f32_16x16x32_bf16 v[124:127], a[112:115], a[100:103], v[124:127]\n"
"v_mfma_f32_16x16x32_bf16 v[92:95], a[108:111], a[100:103], v[92:95]\n"
"v_mfma_f32_16x16x32_bf16 v[60:63], a[104:107], a[100:103], v[60:63]\n"
"s_waitcnt lgkmcnt(0)\n"
"ds_read_b128 a[72:75], v30 offset:2048\n"
"ds_read_b128 a[76:79], v30 offset:2560\n"
"ds_read_b128 a[80:83], v30 offset:3072\n"
"ds_read_b128 a[84:87], v30 offset:3584\n"
"v_mfma_f32_16x16x32_bf16 v[96:99], a[8:11], a[40:43], v[96:99]\n"
"v_mfma_f32_16x16x32_bf16 v[128:131], a[12:15], a[40:43], v[128:131]\n"
"v_mfma_f32_16x16x32_bf16 v[224:227], a[24:27], a[40:43], v[224:227]\n"
"v_mfma_f32_16x16x32_bf16 a[144:147], a[28:31], a[40:43], a[144:147]\n"
"v_mfma_f32_16x16x32_bf16 v[160:163], a[16:19], a[40:43], v[160:163]\n"
"v_mfma_f32_16x16x32_bf16 v[192:195], a[20:23], a[40:43], v[192:195]\n"
"v_mfma_f32_16x16x32_bf16 a[176:179], a[32:35], a[40:43], a[176:179]\n"
"v_mfma_f32_16x16x32_bf16 a[208:211], a[36:39], a[40:43], a[208:211]\n"
"v_accvgpr_read_b32 v14, a240\n"
"v_accvgpr_read_b32 v15, a241\n"
"v_pk_mul_f32 v[96:97], v[96:97], v[14:15] op_sel_hi:[1,0] neg_lo:[0,1] neg_hi:[0,1]\n"
"v_pk_mul_f32 v[98:99], v[98:99], v[14:15] op_sel_hi:[1,0] neg_lo:[0,1] neg_hi:[0,1]\n"
"v_pk_mul_f32 v[128:129], v[128:129], v[14:15] op_sel_hi:[1,0] neg_lo:[0,1] neg_hi:[0,1]\n"
"v_pk_mul_f32 v[130:131], v[130:131], v[14:15] op_sel_hi:[1,0] neg_lo:[0,1] neg_hi:[0,1]\n"
"v_exp_f32 v96, v96\n"
"v_exp_f32 v97, v97\n"
"v_mfma_f32_16x16x32_bf16 v[100:103], a[8:11], a[44:47], v[100:103]\n"
"v_exp_f32 v98, v98\n"
"v_exp_f32 v99, v99\n"
"v_mfma_f32_16x16x32_bf16 v[132:135], a[12:15], a[44:47], v[132:135]\n"
"v_exp_f32 v128, v128\n"
"v_exp_f32 v129, v129\n"
"v_exp_f32 v130, v130\n"
"v_mfma_f32_16x16x32_bf16 v[228:231], a[24:27], a[44:47], v[228:231]\n"
"v_exp_f32 v131, v131\n"
"v_accvgpr_read_b32 v2, a144\n"
"v_accvgpr_read_b32 v3, a145\n"
"v_accvgpr_read_b32 v4, a146\n"
"v_accvgpr_read_b32 v5, a147\n"
"v_pk_fma_f32 v[96:97], v[96:97], v[14:15], v[14:15] op_sel:[0,1,1] op_sel_hi:[1,1,1]\n"
"v_pk_fma_f32 v[98:99], v[98:99], v[14:15], v[14:15] op_sel:[0,1,1] op_sel_hi:[1,1,1]\n"
"v_pk_fma_f32 v[128:129], v[128:129], v[14:15], v[14:15] op_sel:[0,1,1] op_sel_hi:[1,1,1]\n"
"v_pk_fma_f32 v[130:131], v[130:131], v[14:15], v[14:15] op_sel:[0,1,1] op_sel_hi:[1,1,1]\n"
"v_rcp_f32 v96, v96\n"
"v_rcp_f32 v97, v97\n"
"v_rcp_f32 v98, v98\n"
"v_mfma_f32_16x16x32_bf16 a[148:151], a[28:31], a[44:47], a[148:151]\n"
"v_rcp_f32 v99, v99\n"
"v_rcp_f32 v128, v128\n"
"v_mfma_f32_16x16x32_bf16 v[164:167], a[16:19], a[44:47], v[164:167]\n"
"v_rcp_f32 v129, v129\n"
"v_rcp_f32 v130, v130\n"
"v_rcp_f32 v131, v131\n"
"v_mfma_f32_16x16x32_bf16 v[196:199], a[20:23], a[44:47], v[196:199]\n"
"v_pk_mul_f32 v[96:97], v[96:97], v[224:225]\n"
"v_pk_mul_f32 v[98:99], v[98:99], v[226:227]\n"
"v_pk_mul_f32 v[128:129], v[128:129], v[2:3]\n"
"v_pk_mul_f32 v[130:131], v[130:131], v[4:5]\n"
"v_cvt_pk_bf16_f32 v96, v96, v97\n"
"v_cvt_pk_bf16_f32 v97, v98, v99\n"
"v_cvt_pk_bf16_f32 v98, v128, v129\n"
"v_cvt_pk_bf16_f32 v99, v130, v131\n"
"s_mov_b32 s94, 0\n"
"buffer_store_dwordx4 v[96:99], v19, s[20:23], s94 offen offset:0 sc1\n"
"v_pk_mul_f32 v[160:161], v[160:161], v[14:15] op_sel_hi:[1,0] neg_lo:[0,1] neg_hi:[0,1]\n"
"v_pk_mul_f32 v[162:163], v[162:163], v[14:15] op_sel_hi:[1,0] neg_lo:[0,1] neg_hi:[0,1]\n"
"v_pk_mul_f32 v[192:193], v[192:193], v[14:15] op_sel_hi:[1,0] neg_lo:[0,1] neg_hi:[0,1]\n"
"v_pk_mul_f32 v[194:195], v[194:195], v[14:15] op_sel_hi:[1,0] neg_lo:[0,1] neg_hi:[0,1]\n"
"v_exp_f32 v160, v160\n"
"v_exp_f32 v161, v161\n"
"v_exp_f32 v162, v162\n"
"v_exp_f32 v163, v163\n"
"v_mfma_f32_16x16x32_bf16 a[180:183], a[32:35], a[44:47], a[180:183]\n"
"v_exp_f32 v192, v192\n"
"v_exp_f32 v193, v193\n"
"v_mfma_f32_16x16x32_bf16 a[212:215], a[36:39], a[44:47], a[212:215]\n"
"v_exp_f32 v194, v194\n"
"v_exp_f32 v195, v195\n"
"v_accvgpr_read_b32 v6, a176\n"
"v_accvgpr_read_b32 v7, a177\n"
"v_accvgpr_read_b32 v8, a178\n"
"v_accvgpr_read_b32 v9, a179\n"
"v_accvgpr_read_b32 v10, a208\n"
"v_accvgpr_read_b32 v11, a209\n"
"v_accvgpr_read_b32 v12, a210\n"
"v_accvgpr_read_b32 v13, a211\n"
"v_pk_fma_f32 v[160:161], v[160:161], v[14:15], v[14:15] op_sel:[0,1,1] op_sel_hi:[1,1,1]\n"
"v_pk_fma_f32 v[162:163], v[162:163], v[14:15], v[14:15] op_sel:[0,1,1] op_sel_hi:[1,1,1]\n"
"v_pk_fma_f32 v[192:193], v[192:193], v[14:15], v[14:15] op_sel:[0,1,1] op_sel_hi:[1,1,1]\n"
"v_pk_fma_f32 v[194:195], v[194:195], v[14:15], v[14:15] op_sel:[0,1,1] op_sel_hi:[1,1,1]\n"
"v_rcp_f32 v160, v160\n"
"v_rcp_f32 v161, v161\n"
"v_mfma_f32_16x16x32_bf16 v[32:35], a[56:59], a[40:43], v[32:35]\n"
"v_rcp_f32 v162, v162\n"
"v_rcp_f32 v163, v163\n"
"v_rcp_f32 v192, v192\n"
"v_mfma_f32_16x16x32_bf16 v[64:67], a[60:63], a[40:43], v[64:67]\n"
"v_rcp_f32 v193, v193\n"
"v_rcp_f32 v194, v194\n"
"v_rcp_f32 v195, v195\n"
"v_pk_mul_f32 v[160:161], v[160:161], v[6:7]\n"
"v_pk_mul_f32 v[162:163], v[162:163], v[8:9]\n"
"v_pk_mul_f32 v[192:193], v[192:193], v[10:11]\n"
"v_pk_mul_f32 v[194:195], v[194:195], v[12:13]\n"
"v_cvt_pk_bf16_f32 v160, v160, v161\n"
"v_cvt_pk_bf16_f32 v161, v162, v163\n"
"v_cvt_pk_bf16_f32 v162, v192, v193\n"
"v_cvt_pk_bf16_f32 v163, v194, v195\n"
"buffer_store_dwordx4 v[160:163], v20, s[20:23], s94 offen offset:0 sc1\n"
"v_accvgpr_read_b32 v14, a242\n"
"v_accvgpr_read_b32 v15, a243\n"
"v_pk_mul_f32 v[100:101], v[100:101], v[14:15] op_sel_hi:[1,0] neg_lo:[0,1] neg_hi:[0,1]\n"
"v_pk_mul_f32 v[102:103], v[102:103], v[14:15] op_sel_hi:[1,0] neg_lo:[0,1] neg_hi:[0,1]\n"
"v_pk_mul_f32 v[132:133], v[132:133], v[14:15] op_sel_hi:[1,0] neg_lo:[0,1] neg_hi:[0,1]\n"
"v_pk_mul_f32 v[134:135], v[134:135], v[14:15] op_sel_hi:[1,0] neg_lo:[0,1] neg_hi:[0,1]\n"
"v_exp_f32 v100, v100\n"
"v_exp_f32 v101, v101\n"
"v_mfma_f32_16x16x32_bf16 v[104:107], a[8:11], a[48:51], v[104:107]\n"
"v_exp_f32 v102, v102\n"
"v_exp_f32 v103, v103\n"
"v_mfma_f32_16x16x32_bf16 v[136:139], a[12:15], a[48:51], v[136:139]\n"
"v_exp_f32 v132, v132\n"
"v_exp_f32 v133, v133\n"
"v_exp_f32 v134, v134\n"
"v_mfma_f32_16x16x32_bf16 v[232:235], a[24:27], a[48:51], v[232:235]\n"
"v_exp_f32 v135, v135\n"
"v_accvgpr_read_b32 v2, a148\n"
"v_accvgpr_read_b32 v3, a149\n"
"v_accvgpr_read_b32 v4, a150\n"
"v_accvgpr_read_b32 v5, a151\n"
"v_pk_fma_f32 v[100:101], v[100:101], v[14:15], v[14:15] op_sel:[0,1,1] op_sel_hi:[1,1,1]\n"
"v_pk_fma_f32 v[102:103], v[102:103], v[14:15], v[14:15] op_sel:[0,1,1] op_sel_hi:[1,1,1]\n"
"v_pk_fma_f32 v[132:133], v[132:133], v[14:15], v[14:15] op_sel:[0,1,1] op_sel_hi:[1,1,1]\n"
"v_pk_fma_f32 v[134:135], v[134:135], v[14:15], v[14:15] op_sel:[0,1,1] op_sel_hi:[1,1,1]\n"
"v_rcp_f32 v100, v100\n"
"v_rcp_f32 v101, v101\n"
"v_rcp_f32 v102, v102\n"
"v_mfma_f32_16x16x32_bf16 a[152:155], a[28:31], a[48:51], a[152:155]\n"
"v_rcp_f32 v103, v103\n"
"v_rcp_f32 v132, v132\n"
"v_mfma_f32_16x16x32_bf16 v[168:171], a[16:19], a[48:51], v[168:171]\n"
"v_rcp_f32 v133, v133\n"
"v_rcp_f32 v134, v134\n"
"v_rcp_f32 v135, v135\n"
"v_mfma_f32_16x16x32_bf16 v[200:203], a[20:23], a[48:51], v[200:203]\n"
"v_pk_mul_f32 v[100:101], v[100:101], v[228:229]\n"
"v_pk_mul_f32 v[102:103], v[102:103], v[230:231]\n"
"v_pk_mul_f32 v[132:133], v[132:133], v[2:3]\n"
"v_pk_mul_f32 v[134:135], v[134:135], v[4:5]\n"
"v_cvt_pk_bf16_f32 v100, v100, v101\n"
"v_cvt_pk_bf16_f32 v101, v102, v103\n"
"v_cvt_pk_bf16_f32 v102, v132, v133\n"
"v_cvt_pk_bf16_f32 v103, v134, v135\n"
"s_mov_b32 s94, 0\n"
"buffer_store_dwordx4 v[100:103], v19, s[20:23], s94 offen offset:2048 sc1\n"
"v_pk_mul_f32 v[164:165], v[164:165], v[14:15] op_sel_hi:[1,0] neg_lo:[0,1] neg_hi:[0,1]\n"
"v_pk_mul_f32 v[166:167], v[166:167], v[14:15] op_sel_hi:[1,0] neg_lo:[0,1] neg_hi:[0,1]\n"
"v_pk_mul_f32 v[196:197], v[196:197], v[14:15] op_sel_hi:[1,0] neg_lo:[0,1] neg_hi:[0,1]\n"
"v_pk_mul_f32 v[198:199], v[198:199], v[14:15] op_sel_hi:[1,0] neg_lo:[0,1] neg_hi:[0,1]\n"
"v_exp_f32 v164, v164\n"
"v_exp_f32 v165, v165\n"
"v_exp_f32 v166, v166\n"
"v_exp_f32 v167, v167\n"
"v_mfma_f32_16x16x32_bf16 a[184:187], a[32:35], a[48:51], a[184:187]\n"
"v_exp_f32 v196, v196\n"
"v_exp_f32 v197, v197\n"
"v_mfma_f32_16x16x32_bf16 a[216:219], a[36:39], a[48:51], a[216:219]\n"
"v_exp_f32 v198, v198\n"
"v_exp_f32 v199, v199\n"
"v_accvgpr_read_b32 v6, a180\n"
"v_accvgpr_read_b32 v7, a181\n"
"v_accvgpr_read_b32 v8, a182\n"
"v_accvgpr_read_b32 v9, a183\n"
"v_accvgpr_read_b32 v10, a212\n"
"v_accvgpr_read_b32 v11, a213\n"
"v_accvgpr_read_b32 v12, a214\n"
"v_accvgpr_read_b32 v13, a215\n"
"v_pk_fma_f32 v[164:165], v[164:165], v[14:15], v[14:15] op_sel:[0,1,1] op_sel_hi:[1,1,1]\n"
"v_pk_fma_f32 v[166:167], v[166:167], v[14:15], v[14:15] op_sel:[0,1,1] op_sel_hi:[1,1,1]\n"
"v_pk_fma_f32 v[196:197], v[196:197], v[14:15], v[14:15] op_sel:[0,1,1] op_sel_hi:[1,1,1]\n"
"v_pk_fma_f32 v[198:199], v[198:199], v[14:15], v[14:15] op_sel:[0,1,1] op_sel_hi:[1,1,1]\n"
"v_rcp_f32 v164, v164\n"
"v_rcp_f32 v165, v165\n"
"v_mfma_f32_16x16x32_bf16 v[36:39], a[56:59], a[44:47], v[36:39]\n"
"v_rcp_f32 v166, v166\n"
"v_rcp_f32 v167, v167\n"
"v_rcp_f32 v196, v196\n"
"v_mfma_f32_16x16x32_bf16 v[68:71], a[60:63], a[44:47], v[68:71]\n"
"v_rcp_f32 v197, v197\n"
"v_rcp_f32 v198, v198\n"
"v_rcp_f32 v199, v199\n"
"v_pk_mul_f32 v[164:165], v[164:165], v[6:7]\n"
"v_pk_mul_f32 v[166:167], v[166:167], v[8:9]\n"
"v_pk_mul_f32 v[196:197], v[196:197], v[10:11]\n"
"v_pk_mul_f32 v[198:199], v[198:199], v[12:13]\n"
"v_cvt_pk_bf16_f32 v164, v164, v165\n"
"v_cvt_pk_bf16_f32 v165, v166, v167\n"
"v_cvt_pk_bf16_f32 v166, v196, v197\n"
"v_cvt_pk_bf16_f32 v167, v198, v199\n"
"buffer_store_dwordx4 v[164:167], v20, s[20:23], s94 offen offset:2048 sc1\n"
"s_waitcnt lgkmcnt(0)\n"
"s_barrier\n"
"v_accvgpr_read_b32 v14, a244\n"
"v_accvgpr_read_b32 v15, a245\n"
"v_pk_mul_f32 v[104:105], v[104:105], v[14:15] op_sel_hi:[1,0] neg_lo:[0,1] neg_hi:[0,1]\n"
"v_pk_mul_f32 v[106:107], v[106:107], v[14:15] op_sel_hi:[1,0] neg_lo:[0,1] neg_hi:[0,1]\n"
"v_pk_mul_f32 v[136:137], v[136:137], v[14:15] op_sel_hi:[1,0] neg_lo:[0,1] neg_hi:[0,1]\n"
"v_pk_mul_f32 v[138:139], v[138:139], v[14:15] op_sel_hi:[1,0] neg_lo:[0,1] neg_hi:[0,1]\n"
"v_exp_f32 v104, v104\n"
"v_exp_f32 v105, v105\n"
"v_mfma_f32_16x16x32_bf16 v[108:111], a[8:11], a[52:55], v[108:111]\n"
"v_exp_f32 v106, v106\n"
"v_exp_f32 v107, v107\n"
"v_mfma_f32_16x16x32_bf16 v[140:143], a[12:15], a[52:55], v[140:143]\n"
"v_exp_f32 v136, v136\n"
"v_exp_f32 v137, v137\n"
"v_exp_f32 v138, v138\n"
"v_mfma_f32_16x16x32_bf16 v[236:239], a[24:27], a[52:55], v[236:239]\n"
"v_exp_f32 v139, v139\n"
"v_accvgpr_read_b32 v2, a152\n"
"v_accvgpr_read_b32 v3, a153\n"
"v_accvgpr_read_b32 v4, a154\n"
"v_accvgpr_read_b32 v5, a155\n"
"v_pk_fma_f32 v[104:105], v[104:105], v[14:15], v[14:15] op_sel:[0,1,1] op_sel_hi:[1,1,1]\n"
"v_pk_fma_f32 v[106:107], v[106:107], v[14:15], v[14:15] op_sel:[0,1,1] op_sel_hi:[1,1,1]\n"
"v_pk_fma_f32 v[136:137], v[136:137], v[14:15], v[14:15] op_sel:[0,1,1] op_sel_hi:[1,1,1]\n"
"v_pk_fma_f32 v[138:139], v[138:139], v[14:15], v[14:15] op_sel:[0,1,1] op_sel_hi:[1,1,1]\n"
"v_rcp_f32 v104, v104\n"
"v_rcp_f32 v105, v105\n"
"v_rcp_f32 v106, v106\n"
"v_mfma_f32_16x16x32_bf16 a[156:159], a[28:31], a[52:55], a[156:159]\n"
"v_rcp_f32 v107, v107\n"
"v_rcp_f32 v136, v136\n"
"v_mfma_f32_16x16x32_bf16 v[172:175], a[16:19], a[52:55], v[172:175]\n"
"v_rcp_f32 v137, v137\n"
"v_rcp_f32 v138, v138\n"
"v_rcp_f32 v139, v139\n"
"v_mfma_f32_16x16x32_bf16 v[204:207], a[20:23], a[52:55], v[204:207]\n"
"v_pk_mul_f32 v[104:105], v[104:105], v[232:233]\n"
"v_pk_mul_f32 v[106:107], v[106:107], v[234:235]\n"
"v_pk_mul_f32 v[136:137], v[136:137], v[2:3]\n"
"v_pk_mul_f32 v[138:139], v[138:139], v[4:5]\n"
"v_cvt_pk_bf16_f32 v104, v104, v105\n"
"v_cvt_pk_bf16_f32 v105, v106, v107\n"
"v_cvt_pk_bf16_f32 v106, v136, v137\n"
"v_cvt_pk_bf16_f32 v107, v138, v139\n"
"s_mov_b32 s94, 4096\n"
"buffer_store_dwordx4 v[104:107], v19, s[20:23], s94 offen offset:0 sc1\n"
"v_pk_mul_f32 v[168:169], v[168:169], v[14:15] op_sel_hi:[1,0] neg_lo:[0,1] neg_hi:[0,1]\n"
"v_pk_mul_f32 v[170:171], v[170:171], v[14:15] op_sel_hi:[1,0] neg_lo:[0,1] neg_hi:[0,1]\n"
"v_pk_mul_f32 v[200:201], v[200:201], v[14:15] op_sel_hi:[1,0] neg_lo:[0,1] neg_hi:[0,1]\n"
"v_pk_mul_f32 v[202:203], v[202:203], v[14:15] op_sel_hi:[1,0] neg_lo:[0,1] neg_hi:[0,1]\n"
"v_exp_f32 v168, v168\n"
"v_exp_f32 v169, v169\n"
"v_exp_f32 v170, v170\n"
"v_exp_f32 v171, v171\n"
"v_mfma_f32_16x16x32_bf16 a[188:191], a[32:35], a[52:55], a[188:191]\n"
"v_exp_f32 v200, v200\n"
"v_exp_f32 v201, v201\n"
"v_mfma_f32_16x16x32_bf16 a[220:223], a[36:39], a[52:55], a[220:223]\n"
"v_exp_f32 v202, v202\n"
"v_exp_f32 v203, v203\n"
"v_accvgpr_read_b32 v6, a184\n"
"v_accvgpr_read_b32 v7, a185\n"
"v_accvgpr_read_b32 v8, a186\n"
"v_accvgpr_read_b32 v9, a187\n"
"v_accvgpr_read_b32 v10, a216\n"
"v_accvgpr_read_b32 v11, a217\n"
"v_accvgpr_read_b32 v12, a218\n"
"v_accvgpr_read_b32 v13, a219\n"
"v_pk_fma_f32 v[168:169], v[168:169], v[14:15], v[14:15] op_sel:[0,1,1] op_sel_hi:[1,1,1]\n"
"v_pk_fma_f32 v[170:171], v[170:171], v[14:15], v[14:15] op_sel:[0,1,1] op_sel_hi:[1,1,1]\n"
"v_pk_fma_f32 v[200:201], v[200:201], v[14:15], v[14:15] op_sel:[0,1,1] op_sel_hi:[1,1,1]\n"
"v_pk_fma_f32 v[202:203], v[202:203], v[14:15], v[14:15] op_sel:[0,1,1] op_sel_hi:[1,1,1]\n"
"v_rcp_f32 v168, v168\n"
"v_rcp_f32 v169, v169\n"
"v_mfma_f32_16x16x32_bf16 v[40:43], a[56:59], a[48:51], v[40:43]\n"
"v_rcp_f32 v170, v170\n"
"v_rcp_f32 v171, v171\n"
"v_rcp_f32 v200, v200\n"
"v_mfma_f32_16x16x32_bf16 v[72:75], a[60:63], a[48:51], v[72:75]\n"
"v_rcp_f32 v201, v201\n"
"v_rcp_f32 v202, v202\n"
"v_rcp_f32 v203, v203\n"
"v_pk_mul_f32 v[168:169], v[168:169], v[6:7]\n"
"v_pk_mul_f32 v[170:171], v[170:171], v[8:9]\n"
"v_pk_mul_f32 v[200:201], v[200:201], v[10:11]\n"
"v_pk_mul_f32 v[202:203], v[202:203], v[12:13]\n"
"v_cvt_pk_bf16_f32 v168, v168, v169\n"
"v_cvt_pk_bf16_f32 v169, v170, v171\n"
"v_cvt_pk_bf16_f32 v170, v200, v201\n"
"v_cvt_pk_bf16_f32 v171, v202, v203\n"
"buffer_store_dwordx4 v[168:171], v20, s[20:23], s94 offen offset:0 sc1\n"
"v_accvgpr_read_b32 v14, a246\n"
"v_accvgpr_read_b32 v15, a247\n"
"v_pk_mul_f32 v[108:109], v[108:109], v[14:15] op_sel_hi:[1,0] neg_lo:[0,1] neg_hi:[0,1]\n"
"v_pk_mul_f32 v[110:111], v[110:111], v[14:15] op_sel_hi:[1,0] neg_lo:[0,1] neg_hi:[0,1]\n"
"v_pk_mul_f32 v[140:141], v[140:141], v[14:15] op_sel_hi:[1,0] neg_lo:[0,1] neg_hi:[0,1]\n"
"v_pk_mul_f32 v[142:143], v[142:143], v[14:15] op_sel_hi:[1,0] neg_lo:[0,1] neg_hi:[0,1]\n"
"v_exp_f32 v108, v108\n"
"v_exp_f32 v109, v109\n"
"v_mfma_f32_16x16x32_bf16 v[112:115], a[8:11], a[72:75], v[112:115]\n"
"v_exp_f32 v110, v110\n"
"v_exp_f32 v111, v111\n"
"v_mfma_f32_16x16x32_bf16 v[144:147], a[12:15], a[72:75], v[144:147]\n"
"v_exp_f32 v140, v140\n"
"v_exp_f32 v141, v141\n"
"v_exp_f32 v142, v142\n"
"v_mfma_f32_16x16x32_bf16 v[240:243], a[24:27], a[72:75], v[240:243]\n"
"v_exp_f32 v143, v143\n"
"v_accvgpr_read_b32 v2, a156\n"
"v_accvgpr_read_b32 v3, a157\n"
"v_accvgpr_read_b32 v4, a158\n"
"v_accvgpr_read_b32 v5, a159\n"
"v_pk_fma_f32 v[108:109], v[108:109], v[14:15], v[14:15] op_sel:[0,1,1] op_sel_hi:[1,1,1]\n"
"v_pk_fma_f32 v[110:111], v[110:111], v[14:15], v[14:15] op_sel:[0,1,1] op_sel_hi:[1,1,1]\n"
"v_pk_fma_f32 v[140:141], v[140:141], v[14:15], v[14:15] op_sel:[0,1,1] op_sel_hi:[1,1,1]\n"
"v_pk_fma_f32 v[142:143], v[142:143], v[14:15], v[14:15] op_sel:[0,1,1] op_sel_hi:[1,1,1]\n"
"v_rcp_f32 v108, v108\n"
"v_rcp_f32 v109, v109\n"
"v_rcp_f32 v110, v110\n"
"v_mfma_f32_16x16x32_bf16 a[160:163], a[28:31], a[72:75], a[160:163]\n"
"v_rcp_f32 v111, v111\n"
"v_rcp_f32 v140, v140\n"
"v_mfma_f32_16x16x32_bf16 v[176:179], a[16:19], a[72:75], v[176:179]\n"
"v_rcp_f32 v141, v141\n"
"v_rcp_f32 v142, v142\n"
"v_rcp_f32 v143, v143\n"
"v_mfma_f32_16x16x32_bf16 v[208:211], a[20:23], a[72:75], v[208:211]\n"
"v_pk_mul_f32 v[108:109], v[108:109], v[236:237]\n"
"v_pk_mul_f32 v[110:111], v[110:111], v[238:239]\n"
"v_pk_mul_f32 v[140:141], v[140:141], v[2:3]\n"
"v_pk_mul_f32 v[142:143], v[142:143], v[4:5]\n"
"v_cvt_pk_bf16_f32 v108, v108, v109\n"
"v_cvt_pk_bf16_f32 v109, v110, v111\n"
"v_cvt_pk_bf16_f32 v110, v140, v141\n"
"v_cvt_pk_bf16_f32 v111, v142, v143\n"
"s_mov_b32 s94, 4096\n"
"buffer_store_dwordx4 v[108:111], v19, s[20:23], s94 offen offset:2048 sc1\n"
"v_pk_mul_f32 v[172:173], v[172:173], v[14:15] op_sel_hi:[1,0] neg_lo:[0,1] neg_hi:[0,1]\n"
"v_pk_mul_f32 v[174:175], v[174:175], v[14:15] op_sel_hi:[1,0] neg_lo:[0,1] neg_hi:[0,1]\n"
"v_pk_mul_f32 v[204:205], v[204:205], v[14:15] op_sel_hi:[1,0] neg_lo:[0,1] neg_hi:[0,1]\n"
"v_pk_mul_f32 v[206:207], v[206:207], v[14:15] op_sel_hi:[1,0] neg_lo:[0,1] neg_hi:[0,1]\n"
"v_exp_f32 v172, v172\n"
"v_exp_f32 v173, v173\n"
"v_exp_f32 v174, v174\n"
"v_exp_f32 v175, v175\n"
"v_mfma_f32_16x16x32_bf16 a[192:195], a[32:35], a[72:75], a[192:195]\n"
"v_exp_f32 v204, v204\n"
"v_exp_f32 v205, v205\n"
"v_mfma_f32_16x16x32_bf16 a[224:227], a[36:39], a[72:75], a[224:227]\n"
"v_exp_f32 v206, v206\n"
"v_exp_f32 v207, v207\n"
"v_accvgpr_read_b32 v6, a188\n"
"v_accvgpr_read_b32 v7, a189\n"
"v_accvgpr_read_b32 v8, a190\n"
"v_accvgpr_read_b32 v9, a191\n"
"v_accvgpr_read_b32 v10, a220\n"
"v_accvgpr_read_b32 v11, a221\n"
"v_accvgpr_read_b32 v12, a222\n"
"v_accvgpr_read_b32 v13, a223\n"
"v_pk_fma_f32 v[172:173], v[172:173], v[14:15], v[14:15] op_sel:[0,1,1] op_sel_hi:[1,1,1]\n"
"v_pk_fma_f32 v[174:175], v[174:175], v[14:15], v[14:15] op_sel:[0,1,1] op_sel_hi:[1,1,1]\n"
"v_pk_fma_f32 v[204:205], v[204:205], v[14:15], v[14:15] op_sel:[0,1,1] op_sel_hi:[1,1,1]\n"
"v_pk_fma_f32 v[206:207], v[206:207], v[14:15], v[14:15] op_sel:[0,1,1] op_sel_hi:[1,1,1]\n"
"v_rcp_f32 v172, v172\n"
"v_rcp_f32 v173, v173\n"
"v_mfma_f32_16x16x32_bf16 v[44:47], a[56:59], a[52:55], v[44:47]\n"
"v_rcp_f32 v174, v174\n"
"v_rcp_f32 v175, v175\n"
"v_rcp_f32 v204, v204\n"
"v_mfma_f32_16x16x32_bf16 v[76:79], a[60:63], a[52:55], v[76:79]\n"
"v_rcp_f32 v205, v205\n"
"v_rcp_f32 v206, v206\n"
"v_rcp_f32 v207, v207\n"
"v_pk_mul_f32 v[172:173], v[172:173], v[6:7]\n"
"v_pk_mul_f32 v[174:175], v[174:175], v[8:9]\n"
"v_pk_mul_f32 v[204:205], v[204:205], v[10:11]\n"
"v_pk_mul_f32 v[206:207], v[206:207], v[12:13]\n"
"v_cvt_pk_bf16_f32 v172, v172, v173\n"
"v_cvt_pk_bf16_f32 v173, v174, v175\n"
"v_cvt_pk_bf16_f32 v174, v204, v205\n"
"v_cvt_pk_bf16_f32 v175, v206, v207\n"
"buffer_store_dwordx4 v[172:175], v20, s[20:23], s94 offen offset:2048 sc1\n"
"v_accvgpr_read_b32 v14, a248\n"
"v_accvgpr_read_b32 v15, a249\n"
"v_pk_mul_f32 v[112:113], v[112:113], v[14:15] op_sel_hi:[1,0] neg_lo:[0,1] neg_hi:[0,1]\n"
"v_pk_mul_f32 v[114:115], v[114:115], v[14:15] op_sel_hi:[1,0] neg_lo:[0,1] neg_hi:[0,1]\n"
"v_pk_mul_f32 v[144:145], v[144:145], v[14:15] op_sel_hi:[1,0] neg_lo:[0,1] neg_hi:[0,1]\n"
"v_pk_mul_f32 v[146:147], v[146:147], v[14:15] op_sel_hi:[1,0] neg_lo:[0,1] neg_hi:[0,1]\n"
"v_exp_f32 v112, v112\n"
"v_exp_f32 v113, v113\n"
"v_mfma_f32_16x16x32_bf16 v[116:119], a[8:11], a[76:79], v[116:119]\n"
"v_exp_f32 v114, v114\n"
"v_exp_f32 v115, v115\n"
"v_mfma_f32_16x16x32_bf16 v[148:151], a[12:15], a[76:79], v[148:151]\n"
"v_exp_f32 v144, v144\n"
"v_exp_f32 v145, v145\n"
"v_exp_f32 v146, v146\n"
"v_mfma_f32_16x16x32_bf16 v[244:247], a[24:27], a[76:79], v[244:247]\n"
"v_exp_f32 v147, v147\n"
"v_accvgpr_read_b32 v2, a160\n"
"v_accvgpr_read_b32 v3, a161\n"
"v_accvgpr_read_b32 v4, a162\n"
"v_accvgpr_read_b32 v5, a163\n"
"v_pk_fma_f32 v[112:113], v[112:113], v[14:15], v[14:15] op_sel:[0,1,1] op_sel_hi:[1,1,1]\n"
"v_pk_fma_f32 v[114:115], v[114:115], v[14:15], v[14:15] op_sel:[0,1,1] op_sel_hi:[1,1,1]\n"
"v_pk_fma_f32 v[144:145], v[144:145], v[14:15], v[14:15] op_sel:[0,1,1] op_sel_hi:[1,1,1]\n"
"v_pk_fma_f32 v[146:147], v[146:147], v[14:15], v[14:15] op_sel:[0,1,1] op_sel_hi:[1,1,1]\n"
"v_rcp_f32 v112, v112\n"
"v_rcp_f32 v113, v113\n"
"v_rcp_f32 v114, v114\n"
"v_mfma_f32_16x16x32_bf16 a[164:167], a[28:31], a[76:79], a[164:167]\n"
"v_rcp_f32 v115, v115\n"
"v_rcp_f32 v144, v144\n"
"v_mfma_f32_16x16x32_bf16 v[180:183], a[16:19], a[76:79], v[180:183]\n"
"v_rcp_f32 v145, v145\n"
"v_rcp_f32 v146, v146\n"
"v_rcp_f32 v147, v147\n"
"v_mfma_f32_16x16x32_bf16 v[212:215], a[20:23], a[76:79], v[212:215]\n"
"v_pk_mul_f32 v[112:113], v[112:113], v[240:241]\n"
"v_pk_mul_f32 v[114:115], v[114:115], v[242:243]\n"
"v_pk_mul_f32 v[144:145], v[144:145], v[2:3]\n"
"v_pk_mul_f32 v[146:147], v[146:147], v[4:5]\n"
"v_cvt_pk_bf16_f32 v112, v112, v113\n"
"v_cvt_pk_bf16_f32 v113, v114, v115\n"
"v_cvt_pk_bf16_f32 v114, v144, v145\n"
"v_cvt_pk_bf16_f32 v115, v146, v147\n"
"s_mov_b32 s94, 8192\n"
"buffer_store_dwordx4 v[112:115], v19, s[20:23], s94 offen offset:0 sc1\n"
"v_pk_mul_f32 v[176:177], v[176:177], v[14:15] op_sel_hi:[1,0] neg_lo:[0,1] neg_hi:[0,1]\n"
"v_pk_mul_f32 v[178:179], v[178:179], v[14:15] op_sel_hi:[1,0] neg_lo:[0,1] neg_hi:[0,1]\n"
"v_pk_mul_f32 v[208:209], v[208:209], v[14:15] op_sel_hi:[1,0] neg_lo:[0,1] neg_hi:[0,1]\n"
"v_pk_mul_f32 v[210:211], v[210:211], v[14:15] op_sel_hi:[1,0] neg_lo:[0,1] neg_hi:[0,1]\n"
"v_exp_f32 v176, v176\n"
"v_exp_f32 v177, v177\n"
"v_exp_f32 v178, v178\n"
"v_exp_f32 v179, v179\n"
"v_mfma_f32_16x16x32_bf16 a[196:199], a[32:35], a[76:79], a[196:199]\n"
"v_exp_f32 v208, v208\n"
"v_exp_f32 v209, v209\n"
"v_mfma_f32_16x16x32_bf16 a[228:231], a[36:39], a[76:79], a[228:231]\n"
"v_exp_f32 v210, v210\n"
"v_exp_f32 v211, v211\n"
"v_accvgpr_read_b32 v6, a192\n"
"v_accvgpr_read_b32 v7, a193\n"
"v_accvgpr_read_b32 v8, a194\n"
"v_accvgpr_read_b32 v9, a195\n"
"v_accvgpr_read_b32 v10, a224\n"
"v_accvgpr_read_b32 v11, a225\n"
"v_accvgpr_read_b32 v12, a226\n"
"v_accvgpr_read_b32 v13, a227\n"
"v_pk_fma_f32 v[176:177], v[176:177], v[14:15], v[14:15] op_sel:[0,1,1] op_sel_hi:[1,1,1]\n"
"v_pk_fma_f32 v[178:179], v[178:179], v[14:15], v[14:15] op_sel:[0,1,1] op_sel_hi:[1,1,1]\n"
"v_pk_fma_f32 v[208:209], v[208:209], v[14:15], v[14:15] op_sel:[0,1,1] op_sel_hi:[1,1,1]\n"
"v_pk_fma_f32 v[210:211], v[210:211], v[14:15], v[14:15] op_sel:[0,1,1] op_sel_hi:[1,1,1]\n"
"v_rcp_f32 v176, v176\n"
"v_rcp_f32 v177, v177\n"
"v_mfma_f32_16x16x32_bf16 v[48:51], a[56:59], a[72:75], v[48:51]\n"
"v_rcp_f32 v178, v178\n"
"v_rcp_f32 v179, v179\n"
"v_rcp_f32 v208, v208\n"
"v_mfma_f32_16x16x32_bf16 v[80:83], a[60:63], a[72:75], v[80:83]\n"
"v_rcp_f32 v209, v209\n"
"v_rcp_f32 v210, v210\n"
"v_rcp_f32 v211, v211\n"
"v_pk_mul_f32 v[176:177], v[176:177], v[6:7]\n"
"v_pk_mul_f32 v[178:179], v[178:179], v[8:9]\n"
"v_pk_mul_f32 v[208:209], v[208:209], v[10:11]\n"
"v_pk_mul_f32 v[210:211], v[210:211], v[12:13]\n"
"v_cvt_pk_bf16_f32 v176, v176, v177\n"
"v_cvt_pk_bf16_f32 v177, v178, v179\n"
"v_cvt_pk_bf16_f32 v178, v208, v209\n"
"v_cvt_pk_bf16_f32 v179, v210, v211\n"
"buffer_store_dwordx4 v[176:179], v20, s[20:23], s94 offen offset:0 sc1\n"
"v_accvgpr_read_b32 v14, a250\n"
"v_accvgpr_read_b32 v15, a251\n"
"v_pk_mul_f32 v[116:117], v[116:117], v[14:15] op_sel_hi:[1,0] neg_lo:[0,1] neg_hi:[0,1]\n"
"v_pk_mul_f32 v[118:119], v[118:119], v[14:15] op_sel_hi:[1,0] neg_lo:[0,1] neg_hi:[0,1]\n"
"v_pk_mul_f32 v[148:149], v[148:149], v[14:15] op_sel_hi:[1,0] neg_lo:[0,1] neg_hi:[0,1]\n"
"v_pk_mul_f32 v[150:151], v[150:151], v[14:15] op_sel_hi:[1,0] neg_lo:[0,1] neg_hi:[0,1]\n"
"v_exp_f32 v116, v116\n"
"v_exp_f32 v117, v117\n"
"v_mfma_f32_16x16x32_bf16 v[120:123], a[8:11], a[80:83], v[120:123]\n"
"v_exp_f32 v118, v118\n"
"v_exp_f32 v119, v119\n"
"v_mfma_f32_16x16x32_bf16 v[152:155], a[12:15], a[80:83], v[152:155]\n"
"v_exp_f32 v148, v148\n"
"v_exp_f32 v149, v149\n"
"v_exp_f32 v150, v150\n"
"v_mfma_f32_16x16x32_bf16 v[248:251], a[24:27], a[80:83], v[248:251]\n"
"v_exp_f32 v151, v151\n"
"v_accvgpr_read_b32 v2, a164\n"
"v_accvgpr_read_b32 v3, a165\n"
"v_accvgpr_read_b32 v4, a166\n"
"v_accvgpr_read_b32 v5, a167\n"
"v_pk_fma_f32 v[116:117], v[116:117], v[14:15], v[14:15] op_sel:[0,1,1] op_sel_hi:[1,1,1]\n"
"v_pk_fma_f32 v[118:119], v[118:119], v[14:15], v[14:15] op_sel:[0,1,1] op_sel_hi:[1,1,1]\n"
"v_pk_fma_f32 v[148:149], v[148:149], v[14:15], v[14:15] op_sel:[0,1,1] op_sel_hi:[1,1,1]\n"
"v_pk_fma_f32 v[150:151], v[150:151], v[14:15], v[14:15] op_sel:[0,1,1] op_sel_hi:[1,1,1]\n"
"v_rcp_f32 v116, v116\n"
"v_rcp_f32 v117, v117\n"
"v_rcp_f32 v118, v118\n"
"v_mfma_f32_16x16x32_bf16 a[168:171], a[28:31], a[80:83], a[168:171]\n"
"v_rcp_f32 v119, v119\n"
"v_rcp_f32 v148, v148\n"
"v_mfma_f32_16x16x32_bf16 v[184:187], a[16:19], a[80:83], v[184:187]\n"
"v_rcp_f32 v149, v149\n"
"v_rcp_f32 v150, v150\n"
"v_rcp_f32 v151, v151\n"
"v_mfma_f32_16x16x32_bf16 v[216:219], a[20:23], a[80:83], v[216:219]\n"
"v_pk_mul_f32 v[116:117], v[116:117], v[244:245]\n"
"v_pk_mul_f32 v[118:119], v[118:119], v[246:247]\n"
"v_pk_mul_f32 v[148:149], v[148:149], v[2:3]\n"
"v_pk_mul_f32 v[150:151], v[150:151], v[4:5]\n"
"v_cvt_pk_bf16_f32 v116, v116, v117\n"
"v_cvt_pk_bf16_f32 v117, v118, v119\n"
"v_cvt_pk_bf16_f32 v118, v148, v149\n"
"v_cvt_pk_bf16_f32 v119, v150, v151\n"
"s_mov_b32 s94, 8192\n"
"buffer_store_dwordx4 v[116:119], v19, s[20:23], s94 offen offset:2048 sc1\n"
"v_pk_mul_f32 v[180:181], v[180:181], v[14:15] op_sel_hi:[1,0] neg_lo:[0,1] neg_hi:[0,1]\n"
"v_pk_mul_f32 v[182:183], v[182:183], v[14:15] op_sel_hi:[1,0] neg_lo:[0,1] neg_hi:[0,1]\n"
"v_pk_mul_f32 v[212:213], v[212:213], v[14:15] op_sel_hi:[1,0] neg_lo:[0,1] neg_hi:[0,1]\n"
"v_pk_mul_f32 v[214:215], v[214:215], v[14:15] op_sel_hi:[1,0] neg_lo:[0,1] neg_hi:[0,1]\n"
"v_exp_f32 v180, v180\n"
"v_exp_f32 v181, v181\n"
"v_exp_f32 v182, v182\n"
"v_exp_f32 v183, v183\n"
"v_mfma_f32_16x16x32_bf16 a[200:203], a[32:35], a[80:83], a[200:203]\n"
"v_exp_f32 v212, v212\n"
"v_exp_f32 v213, v213\n"
"v_mfma_f32_16x16x32_bf16 a[232:235], a[36:39], a[80:83], a[232:235]\n"
"v_exp_f32 v214, v214\n"
"v_exp_f32 v215, v215\n"
"v_accvgpr_read_b32 v6, a196\n"
"v_accvgpr_read_b32 v7, a197\n"
"v_accvgpr_read_b32 v8, a198\n"
"v_accvgpr_read_b32 v9, a199\n"
"v_accvgpr_read_b32 v10, a228\n"
"v_accvgpr_read_b32 v11, a229\n"
"v_accvgpr_read_b32 v12, a230\n"
"v_accvgpr_read_b32 v13, a231\n"
"v_pk_fma_f32 v[180:181], v[180:181], v[14:15], v[14:15] op_sel:[0,1,1] op_sel_hi:[1,1,1]\n"
"v_pk_fma_f32 v[182:183], v[182:183], v[14:15], v[14:15] op_sel:[0,1,1] op_sel_hi:[1,1,1]\n"
"v_pk_fma_f32 v[212:213], v[212:213], v[14:15], v[14:15] op_sel:[0,1,1] op_sel_hi:[1,1,1]\n"
"v_pk_fma_f32 v[214:215], v[214:215], v[14:15], v[14:15] op_sel:[0,1,1] op_sel_hi:[1,1,1]\n"
"v_rcp_f32 v180, v180\n"
"v_rcp_f32 v181, v181\n"
"v_mfma_f32_16x16x32_bf16 v[52:55], a[56:59], a[76:79], v[52:55]\n"
"v_rcp_f32 v182, v182\n"
"v_rcp_f32 v183, v183\n"
"v_rcp_f32 v212, v212\n"
"v_mfma_f32_16x16x32_bf16 v[84:87], a[60:63], a[76:79], v[84:87]\n"
"v_rcp_f32 v213, v213\n"
"v_rcp_f32 v214, v214\n"
"v_rcp_f32 v215, v215\n"
"v_pk_mul_f32 v[180:181], v[180:181], v[6:7]\n"
"v_pk_mul_f32 v[182:183], v[182:183], v[8:9]\n"
"v_pk_mul_f32 v[212:213], v[212:213], v[10:11]\n"
"v_pk_mul_f32 v[214:215], v[214:215], v[12:13]\n"
"v_cvt_pk_bf16_f32 v180, v180, v181\n"
"v_cvt_pk_bf16_f32 v181, v182, v183\n"
"v_cvt_pk_bf16_f32 v182, v212, v213\n"
"v_cvt_pk_bf16_f32 v183, v214, v215\n"
"buffer_store_dwordx4 v[180:183], v20, s[20:23], s94 offen offset:2048 sc1\n"
"v_accvgpr_read_b32 v14, a252\n"
"v_accvgpr_read_b32 v15, a253\n"
"v_pk_mul_f32 v[120:121], v[120:121], v[14:15] op_sel_hi:[1,0] neg_lo:[0,1] neg_hi:[0,1]\n"
"v_pk_mul_f32 v[122:123], v[122:123], v[14:15] op_sel_hi:[1,0] neg_lo:[0,1] neg_hi:[0,1]\n"
"v_pk_mul_f32 v[152:153], v[152:153], v[14:15] op_sel_hi:[1,0] neg_lo:[0,1] neg_hi:[0,1]\n"
"v_pk_mul_f32 v[154:155], v[154:155], v[14:15] op_sel_hi:[1,0] neg_lo:[0,1] neg_hi:[0,1]\n"
"v_exp_f32 v120, v120\n"
"v_exp_f32 v121, v121\n"
"v_mfma_f32_16x16x32_bf16 v[124:127], a[8:11], a[84:87], v[124:127]\n"
"v_exp_f32 v122, v122\n"
"v_exp_f32 v123, v123\n"
"v_mfma_f32_16x16x32_bf16 v[156:159], a[12:15], a[84:87], v[156:159]\n"
"v_exp_f32 v152, v152\n"
"v_exp_f32 v153, v153\n"
"v_exp_f32 v154, v154\n"
"v_mfma_f32_16x16x32_bf16 v[252:255], a[24:27], a[84:87], v[252:255]\n"
"v_exp_f32 v155, v155\n"
"v_accvgpr_read_b32 v2, a168\n"
"v_accvgpr_read_b32 v3, a169\n"
"v_accvgpr_read_b32 v4, a170\n"
"v_accvgpr_read_b32 v5, a171\n"
"v_pk_fma_f32 v[120:121], v[120:121], v[14:15], v[14:15] op_sel:[0,1,1] op_sel_hi:[1,1,1]\n"
"v_pk_fma_f32 v[122:123], v[122:123], v[14:15], v[14:15] op_sel:[0,1,1] op_sel_hi:[1,1,1]\n"
"v_pk_fma_f32 v[152:153], v[152:153], v[14:15], v[14:15] op_sel:[0,1,1] op_sel_hi:[1,1,1]\n"
"v_pk_fma_f32 v[154:155], v[154:155], v[14:15], v[14:15] op_sel:[0,1,1] op_sel_hi:[1,1,1]\n"
"v_rcp_f32 v120, v120\n"
"v_rcp_f32 v121, v121\n"
"v_rcp_f32 v122, v122\n"
"v_mfma_f32_16x16x32_bf16 a[172:175], a[28:31], a[84:87], a[172:175]\n"
"v_rcp_f32 v123, v123\n"
"v_rcp_f32 v152, v152\n"
"v_mfma_f32_16x16x32_bf16 v[188:191], a[16:19], a[84:87], v[188:191]\n"
"v_rcp_f32 v153, v153\n"
"v_rcp_f32 v154, v154\n"
"v_rcp_f32 v155, v155\n"
"v_mfma_f32_16x16x32_bf16 v[220:223], a[20:23], a[84:87], v[220:223]\n"
"v_pk_mul_f32 v[120:121], v[120:121], v[248:249]\n"
"v_pk_mul_f32 v[122:123], v[122:123], v[250:251]\n"
"v_pk_mul_f32 v[152:153], v[152:153], v[2:3]\n"
"v_pk_mul_f32 v[154:155], v[154:155], v[4:5]\n"
"v_cvt_pk_bf16_f32 v120, v120, v121\n"
"v_cvt_pk_bf16_f32 v121, v122, v123\n"
"v_cvt_pk_bf16_f32 v122, v152, v153\n"
"v_cvt_pk_bf16_f32 v123, v154, v155\n"
"s_mov_b32 s94, 12288\n"
"buffer_store_dwordx4 v[120:123], v19, s[20:23], s94 offen offset:0 sc1\n"
"v_pk_mul_f32 v[184:185], v[184:185], v[14:15] op_sel_hi:[1,0] neg_lo:[0,1] neg_hi:[0,1]\n"
"v_pk_mul_f32 v[186:187], v[186:187], v[14:15] op_sel_hi:[1,0] neg_lo:[0,1] neg_hi:[0,1]\n"
"v_pk_mul_f32 v[216:217], v[216:217], v[14:15] op_sel_hi:[1,0] neg_lo:[0,1] neg_hi:[0,1]\n"
"v_pk_mul_f32 v[218:219], v[218:219], v[14:15] op_sel_hi:[1,0] neg_lo:[0,1] neg_hi:[0,1]\n"
"v_exp_f32 v184, v184\n"
"v_exp_f32 v185, v185\n"
"v_exp_f32 v186, v186\n"
"v_exp_f32 v187, v187\n"
"v_mfma_f32_16x16x32_bf16 a[204:207], a[32:35], a[84:87], a[204:207]\n"
"v_exp_f32 v216, v216\n"
"v_exp_f32 v217, v217\n"
"v_mfma_f32_16x16x32_bf16 a[236:239], a[36:39], a[84:87], a[236:239]\n"
"v_exp_f32 v218, v218\n"
"v_exp_f32 v219, v219\n"
"v_accvgpr_read_b32 v6, a200\n"
"v_accvgpr_read_b32 v7, a201\n"
"v_accvgpr_read_b32 v8, a202\n"
"v_accvgpr_read_b32 v9, a203\n"
"v_accvgpr_read_b32 v10, a232\n"
"v_accvgpr_read_b32 v11, a233\n"
"v_accvgpr_read_b32 v12, a234\n"
"v_accvgpr_read_b32 v13, a235\n"
"v_pk_fma_f32 v[184:185], v[184:185], v[14:15], v[14:15] op_sel:[0,1,1] op_sel_hi:[1,1,1]\n"
"v_pk_fma_f32 v[186:187], v[186:187], v[14:15], v[14:15] op_sel:[0,1,1] op_sel_hi:[1,1,1]\n"
"v_pk_fma_f32 v[216:217], v[216:217], v[14:15], v[14:15] op_sel:[0,1,1] op_sel_hi:[1,1,1]\n"
"v_pk_fma_f32 v[218:219], v[218:219], v[14:15], v[14:15] op_sel:[0,1,1] op_sel_hi:[1,1,1]\n"
"v_rcp_f32 v184, v184\n"
"v_rcp_f32 v185, v185\n"
"v_mfma_f32_16x16x32_bf16 v[56:59], a[56:59], a[80:83], v[56:59]\n"
"v_rcp_f32 v186, v186\n"
"v_rcp_f32 v187, v187\n"
"v_rcp_f32 v216, v216\n"
"v_mfma_f32_16x16x32_bf16 v[88:91], a[60:63], a[80:83], v[88:91]\n"
"v_rcp_f32 v217, v217\n"
"v_rcp_f32 v218, v218\n"
"v_rcp_f32 v219, v219\n"
"v_pk_mul_f32 v[184:185], v[184:185], v[6:7]\n"
"v_pk_mul_f32 v[186:187], v[186:187], v[8:9]\n"
"v_pk_mul_f32 v[216:217], v[216:217], v[10:11]\n"
"v_pk_mul_f32 v[218:219], v[218:219], v[12:13]\n"
"v_cvt_pk_bf16_f32 v184, v184, v185\n"
"v_cvt_pk_bf16_f32 v185, v186, v187\n"
"v_cvt_pk_bf16_f32 v186, v216, v217\n"
"v_cvt_pk_bf16_f32 v187, v218, v219\n"
"buffer_store_dwordx4 v[184:187], v20, s[20:23], s94 offen offset:0 sc1\n"
"v_accvgpr_read_b32 v14, a254\n"
"v_accvgpr_read_b32 v15, a255\n"
"v_pk_mul_f32 v[124:125], v[124:125], v[14:15] op_sel_hi:[1,0] neg_lo:[0,1] neg_hi:[0,1]\n"
"v_pk_mul_f32 v[126:127], v[126:127], v[14:15] op_sel_hi:[1,0] neg_lo:[0,1] neg_hi:[0,1]\n"
"v_pk_mul_f32 v[156:157], v[156:157], v[14:15] op_sel_hi:[1,0] neg_lo:[0,1] neg_hi:[0,1]\n"
"v_pk_mul_f32 v[158:159], v[158:159], v[14:15] op_sel_hi:[1,0] neg_lo:[0,1] neg_hi:[0,1]\n"
"v_exp_f32 v124, v124\n"
"v_exp_f32 v125, v125\n"
"v_mfma_f32_16x16x32_bf16 v[60:63], a[56:59], a[84:87], v[60:63]\n"
"v_exp_f32 v126, v126\n"
"v_exp_f32 v127, v127\n"
"v_exp_f32 v156, v156\n"
"v_exp_f32 v157, v157\n"
"v_exp_f32 v158, v158\n"
"v_exp_f32 v159, v159\n"
"v_accvgpr_read_b32 v2, a172\n"
"v_accvgpr_read_b32 v3, a173\n"
"v_accvgpr_read_b32 v4, a174\n"
"v_accvgpr_read_b32 v5, a175\n"
"v_pk_fma_f32 v[124:125], v[124:125], v[14:15], v[14:15] op_sel:[0,1,1] op_sel_hi:[1,1,1]\n"
"v_pk_fma_f32 v[126:127], v[126:127], v[14:15], v[14:15] op_sel:[0,1,1] op_sel_hi:[1,1,1]\n"
"v_pk_fma_f32 v[156:157], v[156:157], v[14:15], v[14:15] op_sel:[0,1,1] op_sel_hi:[1,1,1]\n"
"v_pk_fma_f32 v[158:159], v[158:159], v[14:15], v[14:15] op_sel:[0,1,1] op_sel_hi:[1,1,1]\n"
"v_rcp_f32 v124, v124\n"
"v_rcp_f32 v125, v125\n"
"v_rcp_f32 v126, v126\n"
"v_rcp_f32 v127, v127\n"
"v_rcp_f32 v156, v156\n"
"v_rcp_f32 v157, v157\n"
"v_rcp_f32 v158, v158\n"
"v_rcp_f32 v159, v159\n"
"v_mfma_f32_16x16x32_bf16 v[92:95], a[60:63], a[84:87], v[92:95]\n"
"v_pk_mul_f32 v[124:125], v[124:125], v[252:253]\n"
"v_pk_mul_f32 v[126:127], v[126:127], v[254:255]\n"
"v_pk_mul_f32 v[156:157], v[156:157], v[2:3]\n"
"v_pk_mul_f32 v[158:159], v[158:159], v[4:5]\n"
"v_cvt_pk_bf16_f32 v124, v124, v125\n"
"v_cvt_pk_bf16_f32 v125, v126, v127\n"
"v_cvt_pk_bf16_f32 v126, v156, v157\n"
"v_cvt_pk_bf16_f32 v127, v158, v159\n"
"s_mov_b32 s94, 12288\n"
"buffer_store_dwordx4 v[124:127], v19, s[20:23], s94 offen offset:2048 sc1\n"
"v_pk_mul_f32 v[188:189], v[188:189], v[14:15] op_sel_hi:[1,0] neg_lo:[0,1] neg_hi:[0,1]\n"
"v_pk_mul_f32 v[190:191], v[190:191], v[14:15] op_sel_hi:[1,0] neg_lo:[0,1] neg_hi:[0,1]\n"
"v_pk_mul_f32 v[220:221], v[220:221], v[14:15] op_sel_hi:[1,0] neg_lo:[0,1] neg_hi:[0,1]\n"
"v_pk_mul_f32 v[222:223], v[222:223], v[14:15] op_sel_hi:[1,0] neg_lo:[0,1] neg_hi:[0,1]\n"
"v_exp_f32 v188, v188\n"
"v_exp_f32 v189, v189\n"
"v_exp_f32 v190, v190\n"
"v_exp_f32 v191, v191\n"
"v_exp_f32 v220, v220\n"
"v_exp_f32 v221, v221\n"
"v_exp_f32 v222, v222\n"
"v_exp_f32 v223, v223\n"
"v_accvgpr_read_b32 v6, a204\n"
"v_accvgpr_read_b32 v7, a205\n"
"v_accvgpr_read_b32 v8, a206\n"
"v_accvgpr_read_b32 v9, a207\n"
"v_accvgpr_read_b32 v10, a236\n"
"v_accvgpr_read_b32 v11, a237\n"
"v_accvgpr_read_b32 v12, a238\n"
"v_accvgpr_read_b32 v13, a239\n"
"v_pk_fma_f32 v[188:189], v[188:189], v[14:15], v[14:15] op_sel:[0,1,1] op_sel_hi:[1,1,1]\n"
"v_pk_fma_f32 v[190:191], v[190:191], v[14:15], v[14:15] op_sel:[0,1,1] op_sel_hi:[1,1,1]\n"
"v_pk_fma_f32 v[220:221], v[220:221], v[14:15], v[14:15] op_sel:[0,1,1] op_sel_hi:[1,1,1]\n"
"v_pk_fma_f32 v[222:223], v[222:223], v[14:15], v[14:15] op_sel:[0,1,1] op_sel_hi:[1,1,1]\n"
"v_rcp_f32 v188, v188\n"
"v_rcp_f32 v189, v189\n"
"v_rcp_f32 v190, v190\n"
"v_rcp_f32 v191, v191\n"
"v_rcp_f32 v220, v220\n"
"v_rcp_f32 v221, v221\n"
"v_rcp_f32 v222, v222\n"
"v_rcp_f32 v223, v223\n"
"v_pk_mul_f32 v[188:189], v[188:189], v[6:7]\n"
"v_pk_mul_f32 v[190:191], v[190:191], v[8:9]\n"
"v_pk_mul_f32 v[220:221], v[220:221], v[10:11]\n"
"v_pk_mul_f32 v[222:223], v[222:223], v[12:13]\n"
"v_cvt_pk_bf16_f32 v188, v188, v189\n"
"v_cvt_pk_bf16_f32 v189, v190, v191\n"
"v_cvt_pk_bf16_f32 v190, v220, v221\n"
"v_cvt_pk_bf16_f32 v191, v222, v223\n"
"buffer_store_dwordx4 v[188:191], v20, s[20:23], s94 offen offset:2048 sc1\n"
"v_cvt_pk_bf16_f32 v32, v32, v33\n"
"v_cvt_pk_bf16_f32 v33, v34, v35\n"
"v_cvt_pk_bf16_f32 v34, v64, v65\n"
"v_cvt_pk_bf16_f32 v35, v66, v67\n"
"v_cvt_pk_bf16_f32 v36, v36, v37\n"
"v_cvt_pk_bf16_f32 v37, v38, v39\n"
"v_cvt_pk_bf16_f32 v38, v68, v69\n"
"v_cvt_pk_bf16_f32 v39, v70, v71\n"
"v_cvt_pk_bf16_f32 v40, v40, v41\n"
"v_cvt_pk_bf16_f32 v41, v42, v43\n"
"v_cvt_pk_bf16_f32 v42, v72, v73\n"
"v_cvt_pk_bf16_f32 v43, v74, v75\n"
"v_cvt_pk_bf16_f32 v44, v44, v45\n"
"v_cvt_pk_bf16_f32 v45, v46, v47\n"
"v_cvt_pk_bf16_f32 v46, v76, v77\n"
"v_cvt_pk_bf16_f32 v47, v78, v79\n"
"v_cvt_pk_bf16_f32 v48, v48, v49\n"
"v_cvt_pk_bf16_f32 v49, v50, v51\n"
"v_cvt_pk_bf16_f32 v50, v80, v81\n"
"v_cvt_pk_bf16_f32 v51, v82, v83\n"
"v_cvt_pk_bf16_f32 v52, v52, v53\n"
"v_cvt_pk_bf16_f32 v53, v54, v55\n"
"v_cvt_pk_bf16_f32 v54, v84, v85\n"
"v_cvt_pk_bf16_f32 v55, v86, v87\n"
"v_cvt_pk_bf16_f32 v56, v56, v57\n"
"v_cvt_pk_bf16_f32 v57, v58, v59\n"
"v_cvt_pk_bf16_f32 v58, v88, v89\n"
"v_cvt_pk_bf16_f32 v59, v90, v91\n"
"v_cvt_pk_bf16_f32 v60, v60, v61\n"
"v_cvt_pk_bf16_f32 v61, v62, v63\n"
"v_cvt_pk_bf16_f32 v62, v92, v93\n"
"v_cvt_pk_bf16_f32 v63, v94, v95\n"
"v_mfma_f32_16x16x32_bf16 v[128:131], a[0:3], v[32:35], 0\n"
"v_mfma_f32_16x16x32_bf16 v[132:135], a[4:7], v[32:35], 0\n"
"v_mfma_f32_16x16x32_bf16 v[136:139], a[0:3], v[36:39], 0\n"
"v_mfma_f32_16x16x32_bf16 v[140:143], a[4:7], v[36:39], 0\n"
"v_mfma_f32_16x16x32_bf16 v[144:147], a[0:3], v[40:43], 0\n"
"v_mfma_f32_16x16x32_bf16 v[148:151], a[4:7], v[40:43], 0\n"
"v_mfma_f32_16x16x32_bf16 v[152:155], a[0:3], v[44:47], 0\n"
"v_mfma_f32_16x16x32_bf16 v[156:159], a[4:7], v[44:47], 0\n"
"v_mfma_f32_16x16x32_bf16 v[160:163], a[0:3], v[48:51], 0\n"
"v_mfma_f32_16x16x32_bf16 v[164:167], a[4:7], v[48:51], 0\n"
"v_mfma_f32_16x16x32_bf16 v[168:171], a[0:3], v[52:55], 0\n"
"v_mfma_f32_16x16x32_bf16 v[172:175], a[4:7], v[52:55], 0\n"
"v_mfma_f32_16x16x32_bf16 v[176:179], a[0:3], v[56:59], 0\n"
"v_mfma_f32_16x16x32_bf16 v[180:183], a[4:7], v[56:59], 0\n"
"v_mfma_f32_16x16x32_bf16 v[184:187], a[0:3], v[60:63], 0\n"
"v_mfma_f32_16x16x32_bf16 v[188:191], a[4:7], v[60:63], 0\n"
"v_xor_b32 v4, 64, v23\n"
"v_xor_b32 v5, 8, v24\n"
"v_xor_b32 v6, 64, v24\n"
"v_xor_b32 v7, 0x48, v24\n"
"v_lshrrev_b32 v8, 1, v0\n"
"v_and_b32 v9, 0x78, v8\n"
"v_and_b32 v10, 0xf8, v8\n"
"v_and_b32 v8, 0x200, v0\n"
"v_lshrrev_b32 v11, 4, v8\n"
"v_xor_b32 v11, v11, v23\n"
"v_lshl_add_u32 v11, v8, 2, v11\n"
"v_xor_b32 v12, 32, v11\n"
"v_xor_b32 v13, 64, v11\n"
"v_xor_b32 v1, 0x60, v11\n"
"v_mov_b32 v2, s90\n"
"v_mov_b32 v3, s91\n"
"s_mov_b32 s95, 32768\n"
"v_accvgpr_read_b32 v48, a240\n"
"v_accvgpr_read_b32 v49, a242\n"
"v_max3_f32 v32, v128, v129, v130\n"
"v_max3_f32 v34, -v128, -v129, -v130\n"
"v_max3_f32 v36, v136, v137, v138\n"
"v_max3_f32 v38, -v136, -v137, -v138\n"
"v_max3_f32 v32, v32, v131, v132\n"
"v_max3_f32 v34, v34, -v131, -v132\n"
"v_max3_f32 v36, v36, v139, v140\n"
"v_max3_f32 v38, v38, -v139, -v140\n"
"v_max3_f32 v32, v32, v133, v134\n"
"v_max3_f32 v34, v34, -v133, -v134\n"
"v_max3_f32 v36, v36, v141, v142\n"
"v_max3_f32 v38, v38, -v141, -v142\n"
"v_max_f32 v32, v32, v135\n"
"v_max_f32_e64 v34, v34, -v135\n"
"v_max_f32 v36, v36, v143\n"
"v_max_f32_e64 v38, v38, -v143\n"
"v_permlane32_swap_b32 v32, v34\n"
"v_accvgpr_read_b32 v50, a244\n"
"buffer_load_dwordx4 a[144:147], v22, s[44:47], 0 offen offset:0\n"
"v_permlane32_swap_b32 v36, v38\n"
"v_accvgpr_read_b32 v51, a246\n"
"v_max_f32 v32, v32, v34\n"
"v_max3_f32 v40, v144, v145, v146\n"
"v_max_f32 v36, v36, v38\n"
"v_max3_f32 v42, -v144, -v145, -v146\n"
"s_nop 0\n"
"v_permlane16_swap_b32 v32, v36\n"
"v_max3_f32 v44, v152, v153, v154\n"
"v_max_f32 v32, v32, v36\n"
"v_max3_f32 v46, -v152, -v153, -v154\n"
"v_mov_b32 v36, v32\n"
"v_max3_f32 v40, v40, v147, v148\n"
"s_nop 0\n"
"v_permlane16_swap_b32 v32, v36\n"
"v_max3_f32 v42, v42, -v147, -v148\n"
"v_mov_b32 v34, v32\n"
"v_max3_f32 v44, v44, v155, v156\n"
"v_mov_b32 v38, v36\n"
"v_max3_f32 v46, v46, -v155, -v156\n"
"v_permlane32_swap_b32 v32, v34\n"
"v_max3_f32 v40, v40, v149, v150\n"
"buffer_load_dwordx4 a[148:151], v22, s[44:47], 0 offen offset:64\n"
"v_permlane32_swap_b32 v36, v38\n"
"v_max3_f32 v42, v42, -v149, -v150\n"
"v_mul_f32_e64 v32, -v48, v32\n"
"v_max3_f32 v44, v44, v157, v158\n"
"v_mul_f32_e64 v34, -v48, v34\n"
"v_max3_f32 v46, v46, -v157, -v158\n"
"v_mul_f32_e64 v36, -v49, v36\n"
"v_max_f32 v40, v40, v151\n"
"v_mul_f32_e64 v38, -v49, v38\n"
"v_max_f32_e64 v42, v42, -v151\n"
"v_max_f32 v44, v44, v159\n"
"v_max_f32_e64 v46, v46, -v159\n"
"v_pk_fma_f32 v[192:193], v[128:129], v[48:49], v[32:33] op_sel:[0,0,0] op_sel_hi:[1,0,0]\n"
"v_permlane32_swap_b32 v40, v42\n"
"v_pk_fma_f32 v[194:195], v[130:131], v[48:49], v[32:33] op_sel:[0,0,0] op_sel_hi:[1,0,0]\n"
"v_permlane32_swap_b32 v44, v46\n"
"v_pk_fma_f32 v[196:197], v[132:133], v[48:49], v[32:33] op_sel:[0,0,0] op_sel_hi:[1,0,0]\n"
"v_max_f32 v40, v40, v42\n"
"v_pk_fma_f32 v[198:199], v[134:135], v[48:49], v[32:33] op_sel:[0,0,0] op_sel_hi:[1,0,0]\n"
"v_max_f32 v44, v44, v46\n"
"buffer_load_dwordx4 a[152:155], v22, s[44:47], s95 offen offset:0\n"
"v_pk_fma_f32 v[200:201], v[128:129], v[48:49], v[34:35] op_sel:[0,0,0] op_sel_hi:[1,0,0] neg_lo:[0,1,0] neg_hi:[0,1,0]\n"
"v_permlane16_swap_b32 v40, v44\n"
"v_pk_fma_f32 v[202:203], v[130:131], v[48:49], v[34:35] op_sel:[0,0,0] op_sel_hi:[1,0,0] neg_lo:[0,1,0] neg_hi:[0,1,0]\n"
"v_max_f32 v40, v40, v44\n"
"v_pk_fma_f32 v[204:205], v[132:133], v[48:49], v[34:35] op_sel:[0,0,0] op_sel_hi:[1,0,0] neg_lo:[0,1,0] neg_hi:[0,1,0]\n"
"v_mov_b32 v44, v40\n"
"v_pk_fma_f32 v[206:207], v[134:135], v[48:49], v[34:35] op_sel:[0,0,0] op_sel_hi:[1,0,0] neg_lo:[0,1,0] neg_hi:[0,1,0]\n"
"s_nop 0\n"
"v_permlane16_swap_b32 v40, v44\n"
"v_pk_fma_f32 v[208:209], v[136:137], v[48:49], v[36:37] op_sel:[0,1,0] op_sel_hi:[1,1,0]\n"
"v_mov_b32 v42, v40\n"
"v_pk_fma_f32 v[210:211], v[138:139], v[48:49], v[36:37] op_sel:[0,1,0] op_sel_hi:[1,1,0]\n"
"v_mov_b32 v46, v44\n"
"v_pk_fma_f32 v[212:213], v[140:141], v[48:49], v[36:37] op_sel:[0,1,0] op_sel_hi:[1,1,0]\n"
"v_permlane32_swap_b32 v40, v42\n"
"v_pk_fma_f32 v[214:215], v[142:143], v[48:49], v[36:37] op_sel:[0,1,0] op_sel_hi:[1,1,0]\n"
"v_permlane32_swap_b32 v44, v46\n"
"v_pk_fma_f32 v[216:217], v[136:137], v[48:49], v[38:39] op_sel:[0,1,0] op_sel_hi:[1,1,0] neg_lo:[0,1,0] neg_hi:[0,1,0]\n"
"v_mul_f32_e64 v40, -v50, v40\n"
"v_pk_fma_f32 v[218:219], v[138:139], v[48:49], v[38:39] op_sel:[0,1,0] op_sel_hi:[1,1,0] neg_lo:[0,1,0] neg_hi:[0,1,0]\n"
"v_mul_f32_e64 v42, -v50, v42\n"
"buffer_load_dwordx4 a[156:159], v22, s[44:47], s95 offen offset:64\n"
"v_pk_fma_f32 v[220:221], v[140:141], v[48:49], v[38:39] op_sel:[0,1,0] op_sel_hi:[1,1,0] neg_lo:[0,1,0] neg_hi:[0,1,0]\n"
"v_mul_f32_e64 v44, -v51, v44\n"
"v_pk_fma_f32 v[222:223], v[142:143], v[48:49], v[38:39] op_sel:[0,1,0] op_sel_hi:[1,1,0] neg_lo:[0,1,0] neg_hi:[0,1,0]\n"
"v_mul_f32_e64 v46, -v51, v46\n"
"v_exp_f32 v192, v192\n"
"v_exp_f32 v193, v193\n"
"v_exp_f32 v194, v194\n"
"v_exp_f32 v195, v195\n"
"v_exp_f32 v196, v196\n"
"v_exp_f32 v197, v197\n"
"v_exp_f32 v198, v198\n"
"v_exp_f32 v199, v199\n"
"v_exp_f32 v200, v200\n"
"v_exp_f32 v201, v201\n"
"v_exp_f32 v202, v202\n"
"v_exp_f32 v203, v203\n"
"v_exp_f32 v204, v204\n"
"v_exp_f32 v205, v205\n"
"v_exp_f32 v206, v206\n"
"v_exp_f32 v207, v207\n"
"v_exp_f32 v208, v208\n"
"v_exp_f32 v209, v209\n"
"v_exp_f32 v210, v210\n"
"v_exp_f32 v211, v211\n"
"v_exp_f32 v212, v212\n"
"v_exp_f32 v213, v213\n"
"v_exp_f32 v214, v214\n"
"v_exp_f32 v215, v215\n"
"v_exp_f32 v216, v216\n"
"v_exp_f32 v217, v217\n"
"v_exp_f32 v218, v218\n"
"v_exp_f32 v219, v219\n"
"v_exp_f32 v220, v220\n"
"v_exp_f32 v221, v221\n"
"v_exp_f32 v222, v222\n"
"v_exp_f32 v223, v223\n"
"v_pk_fma_f32 v[224:225], v[144:145], v[50:51], v[40:41] op_sel:[0,0,0] op_sel_hi:[1,0,0]\n"
"v_pk_fma_f32 v[226:227], v[146:147], v[50:51], v[40:41] op_sel:[0,0,0] op_sel_hi:[1,0,0]\n"
"v_pk_fma_f32 v[228:229], v[148:149], v[50:51], v[40:41] op_sel:[0,0,0] op_sel_hi:[1,0,0]\n"
"v_pk_fma_f32 v[230:231], v[150:151], v[50:51], v[40:41] op_sel:[0,0,0] op_sel_hi:[1,0,0]\n"
"v_pk_fma_f32 v[232:233], v[144:145], v[50:51], v[42:43] op_sel:[0,0,0] op_sel_hi:[1,0,0] neg_lo:[0,1,0] neg_hi:[0,1,0]\n"
"v_pk_fma_f32 v[234:235], v[146:147], v[50:51], v[42:43] op_sel:[0,0,0] op_sel_hi:[1,0,0] neg_lo:[0,1,0] neg_hi:[0,1,0]\n"
"v_pk_fma_f32 v[236:237], v[148:149], v[50:51], v[42:43] op_sel:[0,0,0] op_sel_hi:[1,0,0] neg_lo:[0,1,0] neg_hi:[0,1,0]\n"
"v_pk_fma_f32 v[238:239], v[150:151], v[50:51], v[42:43] op_sel:[0,0,0] op_sel_hi:[1,0,0] neg_lo:[0,1,0] neg_hi:[0,1,0]\n"
"v_pk_fma_f32 v[240:241], v[152:153], v[50:51], v[44:45] op_sel:[0,1,0] op_sel_hi:[1,1,0]\n"
"v_pk_fma_f32 v[242:243], v[154:155], v[50:51], v[44:45] op_sel:[0,1,0] op_sel_hi:[1,1,0]\n"
"v_pk_fma_f32 v[244:245], v[156:157], v[50:51], v[44:45] op_sel:[0,1,0] op_sel_hi:[1,1,0]\n"
"v_pk_fma_f32 v[246:247], v[158:159], v[50:51], v[44:45] op_sel:[0,1,0] op_sel_hi:[1,1,0]\n"
"v_pk_fma_f32 v[248:249], v[152:153], v[50:51], v[46:47] op_sel:[0,1,0] op_sel_hi:[1,1,0] neg_lo:[0,1,0] neg_hi:[0,1,0]\n"
"v_pk_fma_f32 v[250:251], v[154:155], v[50:51], v[46:47] op_sel:[0,1,0] op_sel_hi:[1,1,0] neg_lo:[0,1,0] neg_hi:[0,1,0]\n"
"v_pk_fma_f32 v[252:253], v[156:157], v[50:51], v[46:47] op_sel:[0,1,0] op_sel_hi:[1,1,0] neg_lo:[0,1,0] neg_hi:[0,1,0]\n"
"v_pk_fma_f32 v[254:255], v[158:159], v[50:51], v[46:47] op_sel:[0,1,0] op_sel_hi:[1,1,0] neg_lo:[0,1,0] neg_hi:[0,1,0]\n"
"v_pk_add_f32 v[128:129], v[192:193], v[194:195]\n"
"v_pk_add_f32 v[132:133], v[200:201], v[202:203]\n"
"v_pk_add_f32 v[136:137], v[208:209], v[210:211]\n"
"v_pk_add_f32 v[140:141], v[216:217], v[218:219]\n"
"v_pk_add_f32 v[130:131], v[196:197], v[198:199]\n"
"v_pk_add_f32 v[134:135], v[204:205], v[206:207]\n"
"v_pk_add_f32 v[138:139], v[212:213], v[214:215]\n"
"v_pk_add_f32 v[142:143], v[220:221], v[222:223]\n"
"v_pk_add_f32 v[128:129], v[128:129], v[130:131]\n"
"v_pk_add_f32 v[132:133], v[132:133], v[134:135]\n"
"v_pk_add_f32 v[136:137], v[136:137], v[138:139]\n"
"v_pk_add_f32 v[140:141], v[140:141], v[142:143]\n"
"v_add_f32 v32, v128, v129\n"
"v_add_f32 v34, v132, v133\n"
"v_add_f32 v36, v136, v137\n"
"v_add_f32 v38, v140, v141\n"
"v_permlane32_swap_b32 v32, v34\n"
"v_exp_f32 v224, v224\n"
"v_permlane32_swap_b32 v36, v38\n"
"v_exp_f32 v225, v225\n"
"v_add_f32 v32, v32, v34\n"
"v_exp_f32 v226, v226\n"
"v_add_f32 v36, v36, v38\n"
"v_exp_f32 v227, v227\n"
"s_nop 0\n"
"v_permlane16_swap_b32 v32, v36\n"
"v_exp_f32 v228, v228\n"
"v_add_f32 v32, v32, v36\n"
"v_exp_f32 v229, v229\n"
"v_rcp_f32 v32, v32\n"
"v_exp_f32 v230, v230\n"
"v_mov_b32 v36, v32\n"
"v_exp_f32 v231, v231\n"
"s_nop 0\n"
"v_permlane16_swap_b32 v32, v36\n"
"v_exp_f32 v232, v232\n"
"v_mov_b32 v34, v32\n"
"v_exp_f32 v233, v233\n"
"v_mov_b32 v38, v36\n"
"v_exp_f32 v234, v234\n"
"v_permlane32_swap_b32 v32, v34\n"
"v_exp_f32 v235, v235\n"
"v_permlane32_swap_b32 v36, v38\n"
"v_exp_f32 v236, v236\n"
"v_exp_f32 v237, v237\n"
"v_exp_f32 v238, v238\n"
"v_exp_f32 v239, v239\n"
"v_exp_f32 v240, v240\n"
"v_exp_f32 v241, v241\n"
"v_exp_f32 v242, v242\n"
"v_exp_f32 v243, v243\n"
"v_exp_f32 v244, v244\n"
"v_exp_f32 v245, v245\n"
"v_exp_f32 v246, v246\n"
"v_exp_f32 v247, v247\n"
"v_exp_f32 v248, v248\n"
"v_exp_f32 v249, v249\n"
"v_exp_f32 v250, v250\n"
"v_exp_f32 v251, v251\n"
"v_exp_f32 v252, v252\n"
"v_exp_f32 v253, v253\n"
"v_exp_f32 v254, v254\n"
"v_exp_f32 v255, v255\n"
"v_pk_add_f32 v[144:145], v[224:225], v[226:227]\n"
"v_pk_add_f32 v[148:149], v[232:233], v[234:235]\n"
"v_pk_add_f32 v[152:153], v[240:241], v[242:243]\n"
"v_pk_add_f32 v[156:157], v[248:249], v[250:251]\n"
"v_pk_add_f32 v[146:147], v[228:229], v[230:231]\n"
"v_pk_add_f32 v[150:151], v[236:237], v[238:239]\n"
"v_pk_add_f32 v[154:155], v[244:245], v[246:247]\n"
"v_pk_add_f32 v[158:159], v[252:253], v[254:255]\n"
"v_pk_add_f32 v[144:145], v[144:145], v[146:147]\n"
"v_pk_add_f32 v[148:149], v[148:149], v[150:151]\n"
"v_pk_add_f32 v[152:153], v[152:153], v[154:155]\n"
"v_pk_add_f32 v[156:157], v[156:157], v[158:159]\n"
"v_add_f32 v40, v144, v145\n"
"v_add_f32 v42, v148, v149\n"
"v_add_f32 v44, v152, v153\n"
"v_add_f32 v46, v156, v157\n"
"v_pk_mul_f32 v[192:193], v[192:193], v[32:33] op_sel_hi:[1,0]\n"
"v_permlane32_swap_b32 v40, v42\n"
"v_pk_mul_f32 v[194:195], v[194:195], v[32:33] op_sel_hi:[1,0]\n"
"v_permlane32_swap_b32 v44, v46\n"
"v_pk_mul_f32 v[196:197], v[196:197], v[32:33] op_sel_hi:[1,0]\n"
"v_add_f32 v40, v40, v42\n"
"v_pk_mul_f32 v[198:199], v[198:199], v[32:33] op_sel_hi:[1,0]\n"
"v_add_f32 v44, v44, v46\n"
"v_cvt_pk_bf16_f32 v64, v192, v193\n"
"s_nop 0\n"
"v_permlane16_swap_b32 v40, v44\n"
"v_cvt_pk_bf16_f32 v65, v194, v195\n"
"v_add_f32 v40, v40, v44\n"
"v_cvt_pk_bf16_f32 v66, v196, v197\n"
"v_rcp_f32 v40, v40\n"
"v_cvt_pk_bf16_f32 v67, v198, v199\n"
"v_mov_b32 v44, v40\n"
"s_waitcnt vmcnt(4)\n"
"s_mov_b64 exec, 1\n"
"buffer_store_dwordx2 v[2:3], off, s[20:23], s68 sc1\n"
"s_mov_b64 exec, -1\n"
"v_pk_mul_f32 v[200:201], v[200:201], v[34:35] op_sel_hi:[1,0]\n"
"v_permlane16_swap_b32 v40, v44\n"
"v_pk_mul_f32 v[202:203], v[202:203], v[34:35] op_sel_hi:[1,0]\n"
"v_mov_b32 v42, v40\n"
"v_pk_mul_f32 v[204:205], v[204:205], v[34:35] op_sel_hi:[1,0]\n"
"v_mov_b32 v46, v44\n"
"v_pk_mul_f32 v[206:207], v[206:207], v[34:35] op_sel_hi:[1,0]\n"
"v_permlane32_swap_b32 v40, v42\n"
"v_cvt_pk_bf16_f32 v68, v200, v201\n"
"v_permlane32_swap_b32 v44, v46\n"
"v_cvt_pk_bf16_f32 v69, v202, v203\n"
"v_cvt_pk_bf16_f32 v70, v204, v205\n"
"v_cvt_pk_bf16_f32 v71, v206, v207\n"
"v_pk_mul_f32 v[208:209], v[208:209], v[36:37] op_sel_hi:[1,0]\n"
"v_pk_mul_f32 v[210:211], v[210:211], v[36:37] op_sel_hi:[1,0]\n"
"v_pk_mul_f32 v[212:213], v[212:213], v[36:37] op_sel_hi:[1,0]\n"
"v_pk_mul_f32 v[214:215], v[214:215], v[36:37] op_sel_hi:[1,0]\n"
"v_cvt_pk_bf16_f32 v72, v208, v209\n"
"v_cvt_pk_bf16_f32 v73, v210, v211\n"
"v_cvt_pk_bf16_f32 v74, v212, v213\n"
"v_cvt_pk_bf16_f32 v75, v214, v215\n"
"v_pk_mul_f32 v[216:217], v[216:217], v[38:39] op_sel_hi:[1,0]\n"
"v_pk_mul_f32 v[218:219], v[218:219], v[38:39] op_sel_hi:[1,0]\n"
"v_pk_mul_f32 v[220:221], v[220:221], v[38:39] op_sel_hi:[1,0]\n"
"v_pk_mul_f32 v[222:223], v[222:223], v[38:39] op_sel_hi:[1,0]\n"
"v_cvt_pk_bf16_f32 v76, v216, v217\n"
"v_cvt_pk_bf16_f32 v77, v218, v219\n"
"v_cvt_pk_bf16_f32 v78, v220, v221\n"
"v_cvt_pk_bf16_f32 v79, v222, v223\n"
"v_pk_mul_f32 v[224:225], v[224:225], v[40:41] op_sel_hi:[1,0]\n"
"v_pk_mul_f32 v[226:227], v[226:227], v[40:41] op_sel_hi:[1,0]\n"
"v_pk_mul_f32 v[228:229], v[228:229], v[40:41] op_sel_hi:[1,0]\n"
"v_pk_mul_f32 v[230:231], v[230:231], v[40:41] op_sel_hi:[1,0]\n"
"v_cvt_pk_bf16_f32 v80, v224, v225\n"
"v_cvt_pk_bf16_f32 v81, v226, v227\n"
"v_cvt_pk_bf16_f32 v82, v228, v229\n"
"v_cvt_pk_bf16_f32 v83, v230, v231\n"
"v_pk_mul_f32 v[232:233], v[232:233], v[42:43] op_sel_hi:[1,0]\n"
"v_pk_mul_f32 v[234:235], v[234:235], v[42:43] op_sel_hi:[1,0]\n"
"v_pk_mul_f32 v[236:237], v[236:237], v[42:43] op_sel_hi:[1,0]\n"
"v_pk_mul_f32 v[238:239], v[238:239], v[42:43] op_sel_hi:[1,0]\n"
"v_cvt_pk_bf16_f32 v84, v232, v233\n"
"v_cvt_pk_bf16_f32 v85, v234, v235\n"
"v_cvt_pk_bf16_f32 v86, v236, v237\n"
"v_cvt_pk_bf16_f32 v87, v238, v239\n"
"v_pk_mul_f32 v[240:241], v[240:241], v[44:45] op_sel_hi:[1,0]\n"
"v_pk_mul_f32 v[242:243], v[242:243], v[44:45] op_sel_hi:[1,0]\n"
"v_pk_mul_f32 v[244:245], v[244:245], v[44:45] op_sel_hi:[1,0]\n"
"v_pk_mul_f32 v[246:247], v[246:247], v[44:45] op_sel_hi:[1,0]\n"
"v_cvt_pk_bf16_f32 v88, v240, v241\n"
"v_cvt_pk_bf16_f32 v89, v242, v243\n"
"v_cvt_pk_bf16_f32 v90, v244, v245\n"
"v_cvt_pk_bf16_f32 v91, v246, v247\n"
"v_pk_mul_f32 v[248:249], v[248:249], v[46:47] op_sel_hi:[1,0]\n"
"v_pk_mul_f32 v[250:251], v[250:251], v[46:47] op_sel_hi:[1,0]\n"
"v_pk_mul_f32 v[252:253], v[252:253], v[46:47] op_sel_hi:[1,0]\n"
"v_pk_mul_f32 v[254:255], v[254:255], v[46:47] op_sel_hi:[1,0]\n"
"v_cvt_pk_bf16_f32 v92, v248, v249\n"
"v_cvt_pk_bf16_f32 v93, v250, v251\n"
"v_cvt_pk_bf16_f32 v94, v252, v253\n"
"v_cvt_pk_bf16_f32 v95, v254, v255\n"
"v_accvgpr_read_b32 v48, a248\n"
"v_accvgpr_read_b32 v49, a250\n"
"v_max3_f32 v32, v160, v161, v162\n"
"v_max3_f32 v34, -v160, -v161, -v162\n"
"v_max3_f32 v36, v168, v169, v170\n"
"v_max3_f32 v38, -v168, -v169, -v170\n"
"v_max3_f32 v32, v32, v163, v164\n"
"v_max3_f32 v34, v34, -v163, -v164\n"
"v_max3_f32 v36, v36, v171, v172\n"
"v_max3_f32 v38, v38, -v171, -v172\n"
"v_max3_f32 v32, v32, v165, v166\n"
"v_max3_f32 v34, v34, -v165, -v166\n"
"v_max3_f32 v36, v36, v173, v174\n"
"v_max3_f32 v38, v38, -v173, -v174\n"
"v_max_f32 v32, v32, v167\n"
"v_max_f32_e64 v34, v34, -v167\n"
"v_max_f32 v36, v36, v175\n"
"v_max_f32_e64 v38, v38, -v175\n"
"v_permlane32_swap_b32 v32, v34\n"
"v_accvgpr_read_b32 v50, a252\n"
"v_permlane32_swap_b32 v36, v38\n"
"v_accvgpr_read_b32 v51, a254\n"
"v_max_f32 v32, v32, v34\n"
"v_max3_f32 v40, v176, v177, v178\n"
"v_max_f32 v36, v36, v38\n"
"v_max3_f32 v42, -v176, -v177, -v178\n"
"s_nop 0\n"
"v_permlane16_swap_b32 v32, v36\n"
"v_max3_f32 v44, v184, v185, v186\n"
"v_max_f32 v32, v32, v36\n"
"v_max3_f32 v46, -v184, -v185, -v186\n"
"v_mov_b32 v36, v32\n"
"v_max3_f32 v40, v40, v179, v180\n"
"s_nop 0\n"
"v_permlane16_swap_b32 v32, v36\n"
"v_max3_f32 v42, v42, -v179, -v180\n"
"v_mov_b32 v34, v32\n"
"v_max3_f32 v44, v44, v187, v188\n"
"v_mov_b32 v38, v36\n"
"v_max3_f32 v46, v46, -v187, -v188\n"
"v_permlane32_swap_b32 v32, v34\n"
"v_max3_f32 v40, v40, v181, v182\n"
"v_permlane32_swap_b32 v36, v38\n"
"v_max3_f32 v42, v42, -v181, -v182\n"
"v_mul_f32_e64 v32, -v48, v32\n"
"v_max3_f32 v44, v44, v189, v190\n"
"v_mul_f32_e64 v34, -v48, v34\n"
"v_max3_f32 v46, v46, -v189, -v190\n"
"v_mul_f32_e64 v36, -v49, v36\n"
"v_max_f32 v40, v40, v183\n"
"v_mul_f32_e64 v38, -v49, v38\n"
"v_max_f32_e64 v42, v42, -v183\n"
"v_max_f32 v44, v44, v191\n"
"v_max_f32_e64 v46, v46, -v191\n"
"v_pk_fma_f32 v[192:193], v[160:161], v[48:49], v[32:33] op_sel:[0,0,0] op_sel_hi:[1,0,0]\n"
"v_permlane32_swap_b32 v40, v42\n"
"v_pk_fma_f32 v[194:195], v[162:163], v[48:49], v[32:33] op_sel:[0,0,0] op_sel_hi:[1,0,0]\n"
"buffer_load_dwordx2 v[14:15], v9, s[20:23], s69 offen sc1\n"
"v_permlane32_swap_b32 v44, v46\n"
"v_pk_fma_f32 v[196:197], v[164:165], v[48:49], v[32:33] op_sel:[0,0,0] op_sel_hi:[1,0,0]\n"
"v_max_f32 v40, v40, v42\n"
"v_pk_fma_f32 v[198:199], v[166:167], v[48:49], v[32:33] op_sel:[0,0,0] op_sel_hi:[1,0,0]\n"
"v_max_f32 v44, v44, v46\n"
"v_pk_fma_f32 v[200:201], v[160:161], v[48:49], v[34:35] op_sel:[0,0,0] op_sel_hi:[1,0,0] neg_lo:[0,1,0] neg_hi:[0,1,0]\n"
"s_nop 0\n"
"v_permlane16_swap_b32 v40, v44\n"
"v_pk_fma_f32 v[202:203], v[162:163], v[48:49], v[34:35] op_sel:[0,0,0] op_sel_hi:[1,0,0] neg_lo:[0,1,0] neg_hi:[0,1,0]\n"
"v_max_f32 v40, v40, v44\n"
"v_pk_fma_f32 v[204:205], v[164:165], v[48:49], v[34:35] op_sel:[0,0,0] op_sel_hi:[1,0,0] neg_lo:[0,1,0] neg_hi:[0,1,0]\n"
"v_mov_b32 v44, v40\n"
"v_pk_fma_f32 v[206:207], v[166:167], v[48:49], v[34:35] op_sel:[0,0,0] op_sel_hi:[1,0,0] neg_lo:[0,1,0] neg_hi:[0,1,0]\n"
"s_nop 0\n"
"v_permlane16_swap_b32 v40, v44\n"
"v_pk_fma_f32 v[208:209], v[168:169], v[48:49], v[36:37] op_sel:[0,1,0] op_sel_hi:[1,1,0]\n"
"v_mov_b32 v42, v40\n"
"v_pk_fma_f32 v[210:211], v[170:171], v[48:49], v[36:37] op_sel:[0,1,0] op_sel_hi:[1,1,0]\n"
"v_mov_b32 v46, v44\n"
"v_pk_fma_f32 v[212:213], v[172:173], v[48:49], v[36:37] op_sel:[0,1,0] op_sel_hi:[1,1,0]\n"
"v_permlane32_swap_b32 v40, v42\n"
"v_pk_fma_f32 v[214:215], v[174:175], v[48:49], v[36:37] op_sel:[0,1,0] op_sel_hi:[1,1,0]\n"
"v_permlane32_swap_b32 v44, v46\n"
"v_pk_fma_f32 v[216:217], v[168:169], v[48:49], v[38:39] op_sel:[0,1,0] op_sel_hi:[1,1,0] neg_lo:[0,1,0] neg_hi:[0,1,0]\n"
"v_mul_f32_e64 v40, -v50, v40\n"
"v_pk_fma_f32 v[218:219], v[170:171], v[48:49], v[38:39] op_sel:[0,1,0] op_sel_hi:[1,1,0] neg_lo:[0,1,0] neg_hi:[0,1,0]\n"
"v_mul_f32_e64 v42, -v50, v42\n"
"v_pk_fma_f32 v[220:221], v[172:173], v[48:49], v[38:39] op_sel:[0,1,0] op_sel_hi:[1,1,0] neg_lo:[0,1,0] neg_hi:[0,1,0]\n"
"v_mul_f32_e64 v44, -v51, v44\n"
"v_pk_fma_f32 v[222:223], v[174:175], v[48:49], v[38:39] op_sel:[0,1,0] op_sel_hi:[1,1,0] neg_lo:[0,1,0] neg_hi:[0,1,0]\n"
"v_mul_f32_e64 v46, -v51, v46\n"
"v_exp_f32 v192, v192\n"
"v_exp_f32 v193, v193\n"
"v_exp_f32 v194, v194\n"
"v_exp_f32 v195, v195\n"
"v_exp_f32 v196, v196\n"
"v_exp_f32 v197, v197\n"
"v_exp_f32 v198, v198\n"
"v_exp_f32 v199, v199\n"
"v_exp_f32 v200, v200\n"
"v_exp_f32 v201, v201\n"
"v_exp_f32 v202, v202\n"
"v_exp_f32 v203, v203\n"
"v_exp_f32 v204, v204\n"
"v_exp_f32 v205, v205\n"
"v_exp_f32 v206, v206\n"
"v_exp_f32 v207, v207\n"
"v_exp_f32 v208, v208\n"
"v_exp_f32 v209, v209\n"
"v_exp_f32 v210, v210\n"
"v_exp_f32 v211, v211\n"
"v_exp_f32 v212, v212\n"
"v_exp_f32 v213, v213\n"
"v_exp_f32 v214, v214\n"
"v_exp_f32 v215, v215\n"
"v_exp_f32 v216, v216\n"
"v_exp_f32 v217, v217\n"
"v_exp_f32 v218, v218\n"
"v_exp_f32 v219, v219\n"
"v_exp_f32 v220, v220\n"
"v_exp_f32 v221, v221\n"
"v_exp_f32 v222, v222\n"
"v_exp_f32 v223, v223\n"
"v_pk_fma_f32 v[224:225], v[176:177], v[50:51], v[40:41] op_sel:[0,0,0] op_sel_hi:[1,0,0]\n"
"v_pk_fma_f32 v[226:227], v[178:179], v[50:51], v[40:41] op_sel:[0,0,0] op_sel_hi:[1,0,0]\n"
"v_pk_fma_f32 v[228:229], v[180:181], v[50:51], v[40:41] op_sel:[0,0,0] op_sel_hi:[1,0,0]\n"
"v_pk_fma_f32 v[230:231], v[182:183], v[50:51], v[40:41] op_sel:[0,0,0] op_sel_hi:[1,0,0]\n"
"v_pk_fma_f32 v[232:233], v[176:177], v[50:51], v[42:43] op_sel:[0,0,0] op_sel_hi:[1,0,0] neg_lo:[0,1,0] neg_hi:[0,1,0]\n"
"v_pk_fma_f32 v[234:235], v[178:179], v[50:51], v[42:43] op_sel:[0,0,0] op_sel_hi:[1,0,0] neg_lo:[0,1,0] neg_hi:[0,1,0]\n"
"v_pk_fma_f32 v[236:237], v[180:181], v[50:51], v[42:43] op_sel:[0,0,0] op_sel_hi:[1,0,0] neg_lo:[0,1,0] neg_hi:[0,1,0]\n"
"v_pk_fma_f32 v[238:239], v[182:183], v[50:51], v[42:43] op_sel:[0,0,0] op_sel_hi:[1,0,0] neg_lo:[0,1,0] neg_hi:[0,1,0]\n"
"v_pk_fma_f32 v[240:241], v[184:185], v[50:51], v[44:45] op_sel:[0,1,0] op_sel_hi:[1,1,0]\n"
"v_pk_fma_f32 v[242:243], v[186:187], v[50:51], v[44:45] op_sel:[0,1,0] op_sel_hi:[1,1,0]\n"
"v_pk_fma_f32 v[244:245], v[188:189], v[50:51], v[44:45] op_sel:[0,1,0] op_sel_hi:[1,1,0]\n"
"v_pk_fma_f32 v[246:247], v[190:191], v[50:51], v[44:45] op_sel:[0,1,0] op_sel_hi:[1,1,0]\n"
"v_pk_fma_f32 v[248:249], v[184:185], v[50:51], v[46:47] op_sel:[0,1,0] op_sel_hi:[1,1,0] neg_lo:[0,1,0] neg_hi:[0,1,0]\n"
"v_pk_fma_f32 v[250:251], v[186:187], v[50:51], v[46:47] op_sel:[0,1,0] op_sel_hi:[1,1,0] neg_lo:[0,1,0] neg_hi:[0,1,0]\n"
"v_pk_fma_f32 v[252:253], v[188:189], v[50:51], v[46:47] op_sel:[0,1,0] op_sel_hi:[1,1,0] neg_lo:[0,1,0] neg_hi:[0,1,0]\n"
"v_pk_fma_f32 v[254:255], v[190:191], v[50:51], v[46:47] op_sel:[0,1,0] op_sel_hi:[1,1,0] neg_lo:[0,1,0] neg_hi:[0,1,0]\n"
"v_pk_add_f32 v[160:161], v[192:193], v[194:195]\n"
"v_pk_add_f32 v[164:165], v[200:201], v[202:203]\n"
"v_pk_add_f32 v[168:169], v[208:209], v[210:211]\n"
"v_pk_add_f32 v[172:173], v[216:217], v[218:219]\n"
"v_pk_add_f32 v[162:163], v[196:197], v[198:199]\n"
"v_pk_add_f32 v[166:167], v[204:205], v[206:207]\n"
"v_pk_add_f32 v[170:171], v[212:213], v[214:215]\n"
"v_pk_add_f32 v[174:175], v[220:221], v[222:223]\n"
"v_pk_add_f32 v[160:161], v[160:161], v[162:163]\n"
"v_pk_add_f32 v[164:165], v[164:165], v[166:167]\n"
"v_pk_add_f32 v[168:169], v[168:169], v[170:171]\n"
"v_pk_add_f32 v[172:173], v[172:173], v[174:175]\n"
"v_add_f32 v32, v160, v161\n"
"v_add_f32 v34, v164, v165\n"
"v_add_f32 v36, v168, v169\n"
"v_add_f32 v38, v172, v173\n"
"v_permlane32_swap_b32 v32, v34\n"
"v_exp_f32 v224, v224\n"
"v_permlane32_swap_b32 v36, v38\n"
"v_exp_f32 v225, v225\n"
"v_add_f32 v32, v32, v34\n"
"v_exp_f32 v226, v226\n"
"v_add_f32 v36, v36, v38\n"
"v_exp_f32 v227, v227\n"
"s_nop 0\n"
"v_permlane16_swap_b32 v32, v36\n"
"v_exp_f32 v228, v228\n"
"v_add_f32 v32, v32, v36\n"
"v_exp_f32 v229, v229\n"
"v_rcp_f32 v32, v32\n"
"v_exp_f32 v230, v230\n"
"v_mov_b32 v36, v32\n"
"v_exp_f32 v231, v231\n"
"s_nop 0\n"
"v_permlane16_swap_b32 v32, v36\n"
"v_exp_f32 v232, v232\n"
"v_mov_b32 v34, v32\n"
"v_exp_f32 v233, v233\n"
"v_mov_b32 v38, v36\n"
"v_exp_f32 v234, v234\n"
"v_permlane32_swap_b32 v32, v34\n"
"v_exp_f32 v235, v235\n"
"v_permlane32_swap_b32 v36, v38\n"
"v_exp_f32 v236, v236\n"
"v_exp_f32 v237, v237\n"
"v_exp_f32 v238, v238\n"
"v_exp_f32 v239, v239\n"
"v_exp_f32 v240, v240\n"
"v_exp_f32 v241, v241\n"
"v_exp_f32 v242, v242\n"
"v_exp_f32 v243, v243\n"
"v_exp_f32 v244, v244\n"
"v_exp_f32 v245, v245\n"
"v_exp_f32 v246, v246\n"
"v_exp_f32 v247, v247\n"
"v_exp_f32 v248, v248\n"
"v_exp_f32 v249, v249\n"
"v_exp_f32 v250, v250\n"
"v_exp_f32 v251, v251\n"
"v_exp_f32 v252, v252\n"
"v_exp_f32 v253, v253\n"
"v_exp_f32 v254, v254\n"
"v_exp_f32 v255, v255\n"
"v_pk_add_f32 v[176:177], v[224:225], v[226:227]\n"
"v_pk_add_f32 v[180:181], v[232:233], v[234:235]\n"
"v_pk_add_f32 v[184:185], v[240:241], v[242:243]\n"
"v_pk_add_f32 v[188:189], v[248:249], v[250:251]\n"
"v_pk_add_f32 v[178:179], v[228:229], v[230:231]\n"
"v_pk_add_f32 v[182:183], v[236:237], v[238:239]\n"
"v_pk_add_f32 v[186:187], v[244:245], v[246:247]\n"
"v_pk_add_f32 v[190:191], v[252:253], v[254:255]\n"
"v_pk_add_f32 v[176:177], v[176:177], v[178:179]\n"
"v_pk_add_f32 v[180:181], v[180:181], v[182:183]\n"
"v_pk_add_f32 v[184:185], v[184:185], v[186:187]\n"
"v_pk_add_f32 v[188:189], v[188:189], v[190:191]\n"
"v_add_f32 v40, v176, v177\n"
"v_add_f32 v42, v180, v181\n"
"v_add_f32 v44, v184, v185\n"
"v_add_f32 v46, v188, v189\n"
"v_pk_mul_f32 v[192:193], v[192:193], v[32:33] op_sel_hi:[1,0]\n"
"v_permlane32_swap_b32 v40, v42\n"
"v_pk_mul_f32 v[194:195], v[194:195], v[32:33] op_sel_hi:[1,0]\n"
"v_permlane32_swap_b32 v44, v46\n"
"v_pk_mul_f32 v[196:197], v[196:197], v[32:33] op_sel_hi:[1,0]\n"
"v_add_f32 v40, v40, v42\n"
"v_pk_mul_f32 v[198:199], v[198:199], v[32:33] op_sel_hi:[1,0]\n"
"v_add_f32 v44, v44, v46\n"
"v_cvt_pk_bf16_f32 v96, v192, v193\n"
"s_nop 0\n"
"v_permlane16_swap_b32 v40, v44\n"
"v_cvt_pk_bf16_f32 v97, v194, v195\n"
"v_add_f32 v40, v40, v44\n"
"v_cvt_pk_bf16_f32 v98, v196, v197\n"
"v_rcp_f32 v40, v40\n"
"v_cvt_pk_bf16_f32 v99, v198, v199\n"
"v_mov_b32 v44, v40\n"
"v_pk_mul_f32 v[200:201], v[200:201], v[34:35] op_sel_hi:[1,0]\n"
"s_nop 0\n"
"v_permlane16_swap_b32 v40, v44\n"
"v_pk_mul_f32 v[202:203], v[202:203], v[34:35] op_sel_hi:[1,0]\n"
"s_waitcnt vmcnt(0)\n"
"v_cmp_ne_u64 vcc, s[90:91], v[14:15]\n"
"s_nop 0\n"
"s_mov_b64 s[18:19], vcc\n"
"s_cbranch_vccnz EXQ11_%=\n"
"s_add_u32 m0, s76, 0\n"
"s_nop 0\n"
"buffer_load_dwordx4 v0, s[20:23], s72 offen offset:0 sc1 lds\n"
"buffer_load_dwordx4 v0, s[20:23], s72 offen offset:1024 sc1 lds\n"
"buffer_load_dwordx4 v0, s[20:23], s72 offen offset:2048 sc1 lds\n"
"buffer_load_dwordx4 v0, s[20:23], s72 offen offset:3072 sc1 lds\n"
"s_add_u32 s72, s72, 16384\n"
"s_add_u32 m0, s76, 16384\n"
"s_nop 0\n"
"buffer_load_dwordx4 v0, s[20:23], s72 offen offset:0 sc1 lds\n"
"buffer_load_dwordx4 v0, s[20:23], s72 offen offset:1024 sc1 lds\n"
"buffer_load_dwordx4 v0, s[20:23], s72 offen offset:2048 sc1 lds\n"
"buffer_load_dwordx4 v0, s[20:23], s72 offen offset:3072 sc1 lds\n"
"s_add_u32 s72, s72, 65536\n"
"s_add_u32 m0, s76, 32768\n"
"s_nop 0\n"
"buffer_load_dwordx4 v0, s[20:23], s72 offen offset:0 sc1 lds\n"
"buffer_load_dwordx4 v0, s[20:23], s72 offen offset:1024 sc1 lds\n"
"buffer_load_dwordx4 v0, s[20:23], s72 offen offset:2048 sc1 lds\n"
"buffer_load_dwordx4 v0, s[20:23], s72 offen offset:3072 sc1 lds\n"
"s_add_u32 s72, s72, 16384\n"
"s_add_u32 m0, s76, 49152\n"
"s_nop 0\n"
"buffer_load_dwordx4 v0, s[20:23], s72 offen offset:0 sc1 lds\n"
"buffer_load_dwordx4 v0, s[20:23], s72 offen offset:1024 sc1 lds\n"
"buffer_load_dwordx4 v0, s[20:23], s72 offen offset:2048 sc1 lds\n"
"buffer_load_dwordx4 v0, s[20:23], s72 offen offset:3072 sc1 lds\n"
"s_add_u32 s72, s72, 16384\n"
"EXQ11_%=:\n"
"v_mov_b32 v42, v40\n"
"v_pk_mul_f32 v[204:205], v[204:205], v[34:35] op_sel_hi:[1,0]\n"
"v_mov_b32 v46, v44\n"
"v_pk_mul_f32 v[206:207], v[206:207], v[34:35] op_sel_hi:[1,0]\n"
"v_permlane32_swap_b32 v40, v42\n"
"v_cvt_pk_bf16_f32 v100, v200, v201\n"
"v_permlane32_swap_b32 v44, v46\n"
"v_cvt_pk_bf16_f32 v101, v202, v203\n"
"v_cvt_pk_bf16_f32 v102, v204, v205\n"
"v_cvt_pk_bf16_f32 v103, v206, v207\n"
"v_pk_mul_f32 v[208:209], v[208:209], v[36:37] op_sel_hi:[1,0]\n"
"v_pk_mul_f32 v[210:211], v[210:211], v[36:37] op_sel_hi:[1,0]\n"
"v_pk_mul_f32 v[212:213], v[212:213], v[36:37] op_sel_hi:[1,0]\n"
"v_pk_mul_f32 v[214:215], v[214:215], v[36:37] op_sel_hi:[1,0]\n"
"v_cvt_pk_bf16_f32 v104, v208, v209\n"
"v_cvt_pk_bf16_f32 v105, v210, v211\n"
"v_cvt_pk_bf16_f32 v106, v212, v213\n"
"v_cvt_pk_bf16_f32 v107, v214, v215\n"
"v_pk_mul_f32 v[216:217], v[216:217], v[38:39] op_sel_hi:[1,0]\n"
"v_pk_mul_f32 v[218:219], v[218:219], v[38:39] op_sel_hi:[1,0]\n"
"v_pk_mul_f32 v[220:221], v[220:221], v[38:39] op_sel_hi:[1,0]\n"
"v_pk_mul_f32 v[222:223], v[222:223], v[38:39] op_sel_hi:[1,0]\n"
"v_cvt_pk_bf16_f32 v108, v216, v217\n"
"v_cvt_pk_bf16_f32 v109, v218, v219\n"
"v_cvt_pk_bf16_f32 v110, v220, v221\n"
"v_cvt_pk_bf16_f32 v111, v222, v223\n"
"v_pk_mul_f32 v[224:225], v[224:225], v[40:41] op_sel_hi:[1,0]\n"
"v_pk_mul_f32 v[226:227], v[226:227], v[40:41] op_sel_hi:[1,0]\n"
"v_pk_mul_f32 v[228:229], v[228:229], v[40:41] op_sel_hi:[1,0]\n"
"v_pk_mul_f32 v[230:231], v[230:231], v[40:41] op_sel_hi:[1,0]\n"
"v_cvt_pk_bf16_f32 v112, v224, v225\n"
"v_cvt_pk_bf16_f32 v113, v226, v227\n"
"v_cvt_pk_bf16_f32 v114, v228, v229\n"
"v_cvt_pk_bf16_f32 v115, v230, v231\n"
"v_pk_mul_f32 v[232:233], v[232:233], v[42:43] op_sel_hi:[1,0]\n"
"v_pk_mul_f32 v[234:235], v[234:235], v[42:43] op_sel_hi:[1,0]\n"
"v_pk_mul_f32 v[236:237], v[236:237], v[42:43] op_sel_hi:[1,0]\n"
"v_pk_mul_f32 v[238:239], v[238:239], v[42:43] op_sel_hi:[1,0]\n"
"v_cvt_pk_bf16_f32 v116, v232, v233\n"
"v_cvt_pk_bf16_f32 v117, v234, v235\n"
"v_cvt_pk_bf16_f32 v118, v236, v237\n"
"v_cvt_pk_bf16_f32 v119, v238, v239\n"
"v_pk_mul_f32 v[240:241], v[240:241], v[44:45] op_sel_hi:[1,0]\n"
"v_pk_mul_f32 v[242:243], v[242:243], v[44:45] op_sel_hi:[1,0]\n"
"v_pk_mul_f32 v[244:245], v[244:245], v[44:45] op_sel_hi:[1,0]\n"
"v_pk_mul_f32 v[246:247], v[246:247], v[44:45] op_sel_hi:[1,0]\n"
"v_cvt_pk_bf16_f32 v120, v240, v241\n"
"v_cvt_pk_bf16_f32 v121, v242, v243\n"
"v_cvt_pk_bf16_f32 v122, v244, v245\n"
"v_cvt_pk_bf16_f32 v123, v246, v247\n"
"v_pk_mul_f32 v[248:249], v[248:249], v[46:47] op_sel_hi:[1,0]\n"
"v_pk_mul_f32 v[250:251], v[250:251], v[46:47] op_sel_hi:[1,0]\n"
"v_pk_mul_f32 v[252:253], v[252:253], v[46:47] op_sel_hi:[1,0]\n"
"v_pk_mul_f32 v[254:255], v[254:255], v[46:47] op_sel_hi:[1,0]\n"
"v_cvt_pk_bf16_f32 v124, v248, v249\n"
"v_cvt_pk_bf16_f32 v125, v250, v251\n"
"v_cvt_pk_bf16_f32 v126, v252, v253\n"
"v_cvt_pk_bf16_f32 v127, v254, v255\n"
"s_cmp_lg_u64 s[18:19], 0\n"
"s_cbranch_scc1 LXQ24_%=\n"
"LBQ25_%=:\n"
"s_waitcnt vmcnt(12)\n"
"s_barrier\n"
"ds_read_b128 a[0:3], v23 offset:0\n"
"ds_read_b64_tr_b16 a[32:33], v24 offset:0\n"
"ds_read_b64_tr_b16 a[34:35], v24 offset:1024\n"
"ds_read_b128 a[8:11], v23 offset:2048\n"
"ds_read_b64_tr_b16 a[40:41], v5 offset:0\n"
"ds_read_b64_tr_b16 a[42:43], v5 offset:1024\n"
"ds_read_b128 a[16:19], v23 offset:4096\n"
"ds_read_b64_tr_b16 a[48:49], v6 offset:0\n"
"ds_read_b64_tr_b16 a[50:51], v6 offset:1024\n"
"ds_read_b128 a[24:27], v23 offset:6144\n"
"ds_read_b64_tr_b16 a[56:57], v7 offset:0\n"
"ds_read_b64_tr_b16 a[58:59], v7 offset:1024\n"
"ds_read_b128 a[4:7], v4 offset:0\n"
"ds_read_b64_tr_b16 a[36:37], v24 offset:8192\n"
"ds_read_b64_tr_b16 a[38:39], v24 offset:9216\n"
"ds_read_b128 a[12:15], v4 offset:2048\n"
"ds_read_b64_tr_b16 a[44:45], v5 offset:8192\n"
"ds_read_b64_tr_b16 a[46:47], v5 offset:9216\n"
"ds_read_b128 a[20:23], v4 offset:4096\n"
"ds_read_b64_tr_b16 a[52:53], v6 offset:8192\n"
"ds_read_b64_tr_b16 a[54:55], v6 offset:9216\n"
"ds_read_b128 a[28:31], v4 offset:6144\n"
"ds_read_b64_tr_b16 a[60:61], v7 offset:8192\n"
"ds_read_b64_tr_b16 a[62:63], v7 offset:9216\n"
"s_waitcnt vmcnt(8) lgkmcnt(0)\n"
"s_barrier\n"
"v_mfma_f32_16x16x32_bf16 v[128:131], a[32:35], a[0:3], 0\n"
"ds_read_b128 a[64:67], v23 offset:16384\n"
"s_add_u32 m0, s76, 0\n"
"v_mfma_f32_16x16x32_bf16 v[132:135], a[40:43], a[0:3], 0\n"
"ds_read_b64_tr_b16 a[96:97], v24 offset:16384\n"
"buffer_load_dwordx4 v0, s[20:23], s72 offen offset:0 sc1 lds\n"
"v_mfma_f32_16x16x32_bf16 v[136:139], a[48:51], a[0:3], 0\n"
"ds_read_b64_tr_b16 a[98:99], v24 offset:17408\n"
"v_mfma_f32_16x16x32_bf16 v[140:143], a[56:59], a[0:3], 0\n"
"ds_read_b128 a[72:75], v23 offset:18432\n"
"v_mfma_f32_16x16x32_bf16 v[156:159], a[56:59], a[8:11], 0\n"
"ds_read_b64_tr_b16 a[104:105], v5 offset:16384\n"
"buffer_load_dwordx4 v0, s[20:23], s72 offen offset:1024 sc1 lds\n"
"v_mfma_f32_16x16x32_bf16 v[152:155], a[48:51], a[8:11], 0\n"
"ds_read_b64_tr_b16 a[106:107], v5 offset:17408\n"
"v_mfma_f32_16x16x32_bf16 v[148:151], a[40:43], a[8:11], 0\n"
"ds_read_b128 a[80:83], v23 offset:20480\n"
"v_mfma_f32_16x16x32_bf16 v[144:147], a[32:35], a[8:11], 0\n"
"ds_read_b64_tr_b16 a[112:113], v6 offset:16384\n"
"v_mfma_f32_16x16x32_bf16 v[160:163], a[32:35], a[16:19], 0\n"
"ds_read_b64_tr_b16 a[114:115], v6 offset:17408\n"
"buffer_load_dwordx4 v0, s[20:23], s72 offen offset:2048 sc1 lds\n"
"v_mfma_f32_16x16x32_bf16 v[164:167], a[40:43], a[16:19], 0\n"
"ds_read_b128 a[88:91], v23 offset:22528\n"
"v_mfma_f32_16x16x32_bf16 v[168:171], a[48:51], a[16:19], 0\n"
"ds_read_b64_tr_b16 a[120:121], v7 offset:16384\n"
"v_mfma_f32_16x16x32_bf16 v[172:175], a[56:59], a[16:19], 0\n"
"ds_read_b64_tr_b16 a[122:123], v7 offset:17408\n"
"v_mfma_f32_16x16x32_bf16 v[188:191], a[56:59], a[24:27], 0\n"
"buffer_load_dwordx4 v0, s[20:23], s72 offen offset:3072 sc1 lds\n"
"s_add_u32 s72, s72, 16384\n"
"v_mfma_f32_16x16x32_bf16 v[184:187], a[48:51], a[24:27], 0\n"
"ds_read_b128 a[68:71], v4 offset:16384\n"
"v_mfma_f32_16x16x32_bf16 v[180:183], a[40:43], a[24:27], 0\n"
"ds_read_b64_tr_b16 a[100:101], v24 offset:24576\n"
"v_mfma_f32_16x16x32_bf16 v[176:179], a[32:35], a[24:27], 0\n"
"ds_read_b64_tr_b16 a[102:103], v24 offset:25600\n"
"s_add_u32 m0, s77, 0\n"
"v_mfma_f32_16x16x32_bf16 v[128:131], a[36:39], a[4:7], v[128:131]\n"
"ds_read_b128 a[76:79], v4 offset:18432\n"
"buffer_load_dwordx4 v25, s[52:55], s74 offen offset:0 lds\n"
"v_mfma_f32_16x16x32_bf16 v[132:135], a[44:47], a[4:7], v[132:135]\n"
"ds_read_b64_tr_b16 a[108:109], v5 offset:24576\n"
"v_mfma_f32_16x16x32_bf16 v[136:139], a[52:55], a[4:7], v[136:139]\n"
"ds_read_b64_tr_b16 a[110:111], v5 offset:25600\n"
"v_mfma_f32_16x16x32_bf16 v[140:143], a[60:63], a[4:7], v[140:143]\n"
"ds_read_b128 a[84:87], v4 offset:20480\n"
"buffer_load_dwordx4 v26, s[52:55], s74 offen offset:1024 lds\n"
"v_mfma_f32_16x16x32_bf16 v[156:159], a[60:63], a[12:15], v[156:159]\n"
"ds_read_b64_tr_b16 a[116:117], v6 offset:24576\n"
"v_mfma_f32_16x16x32_bf16 v[152:155], a[52:55], a[12:15], v[152:155]\n"
"ds_read_b64_tr_b16 a[118:119], v6 offset:25600\n"
"v_mfma_f32_16x16x32_bf16 v[148:151], a[44:47], a[12:15], v[148:151]\n"
"ds_read_b128 a[92:95], v4 offset:22528\n"
"v_mfma_f32_16x16x32_bf16 v[144:147], a[36:39], a[12:15], v[144:147]\n"
"ds_read_b64_tr_b16 a[124:125], v7 offset:24576\n"
"buffer_load_dwordx4 v27, s[52:55], s74 offen offset:2048 lds\n"
"v_mfma_f32_16x16x32_bf16 v[160:163], a[36:39], a[20:23], v[160:163]\n"
"ds_read_b64_tr_b16 a[126:127], v7 offset:25600\n"
"v_mfma_f32_16x16x32_bf16 v[164:167], a[44:47], a[20:23], v[164:167]\n"
"v_mfma_f32_16x16x32_bf16 v[168:171], a[52:55], a[20:23], v[168:171]\n"
"v_mfma_f32_16x16x32_bf16 v[172:175], a[60:63], a[20:23], v[172:175]\n"
"buffer_load_dwordx4 v28, s[52:55], s74 offen offset:3072 lds\n"
"s_add_u32 s74, s74, 131072\n"
"v_mfma_f32_16x16x32_bf16 v[188:191], a[60:63], a[28:31], v[188:191]\n"
"v_mfma_f32_16x16x32_bf16 v[184:187], a[52:55], a[28:31], v[184:187]\n"
"v_mfma_f32_16x16x32_bf16 v[180:183], a[44:47], a[28:31], v[180:183]\n"
"v_mfma_f32_16x16x32_bf16 v[176:179], a[36:39], a[28:31], v[176:179]\n"
"s_waitcnt vmcnt(12) lgkmcnt(0)\n"
"s_barrier\n"
"v_mfma_f32_16x16x32_bf16 v[128:131], a[96:99], a[64:67], v[128:131]\n"
"ds_read_b128 a[0:3], v23 offset:32768\n"
"s_add_u32 m0, s76, 16384\n"
"v_mfma_f32_16x16x32_bf16 v[132:135], a[104:107], a[64:67], v[132:135]\n"
"ds_read_b64_tr_b16 a[32:33], v24 offset:32768\n"
"buffer_load_dwordx4 v0, s[20:23], s72 offen offset:0 sc1 lds\n"
"v_mfma_f32_16x16x32_bf16 v[136:139], a[112:115], a[64:67], v[136:139]\n"
"ds_read_b64_tr_b16 a[34:35], v24 offset:33792\n"
"v_mfma_f32_16x16x32_bf16 v[140:143], a[120:123], a[64:67], v[140:143]\n"
"ds_read_b128 a[8:11], v23 offset:34816\n"
"v_mfma_f32_16x16x32_bf16 v[156:159], a[120:123], a[72:75], v[156:159]\n"
"ds_read_b64_tr_b16 a[40:41], v5 offset:32768\n"
"buffer_load_dwordx4 v0, s[20:23], s72 offen offset:1024 sc1 lds\n"
"v_mfma_f32_16x16x32_bf16 v[152:155], a[112:115], a[72:75], v[152:155]\n"
"ds_read_b64_tr_b16 a[42:43], v5 offset:33792\n"
"v_mfma_f32_16x16x32_bf16 v[148:151], a[104:107], a[72:75], v[148:151]\n"
"ds_read_b128 a[16:19], v23 offset:36864\n"
"v_mfma_f32_16x16x32_bf16 v[144:147], a[96:99], a[72:75], v[144:147]\n"
"ds_read_b64_tr_b16 a[48:49], v6 offset:32768\n"
"v_mfma_f32_16x16x32_bf16 v[160:163], a[96:99], a[80:83], v[160:163]\n"
"ds_read_b64_tr_b16 a[50:51], v6 offset:33792\n"
"buffer_load_dwordx4 v0, s[20:23], s72 offen offset:2048 sc1 lds\n"
"v_mfma_f32_16x16x32_bf16 v[164:167], a[104:107], a[80:83], v[164:167]\n"
"ds_read_b128 a[24:27], v23 offset:38912\n"
"v_mfma_f32_16x16x32_bf16 v[168:171], a[112:115], a[80:83], v[168:171]\n"
"ds_read_b64_tr_b16 a[56:57], v7 offset:32768\n"
"v_mfma_f32_16x16x32_bf16 v[172:175], a[120:123], a[80:83], v[172:175]\n"
"ds_read_b64_tr_b16 a[58:59], v7 offset:33792\n"
"v_mfma_f32_16x16x32_bf16 v[188:191], a[120:123], a[88:91], v[188:191]\n"
"buffer_load_dwordx4 v0, s[20:23], s72 offen offset:3072 sc1 lds\n"
"s_add_u32 s72, s72, 65536\n"
"v_mfma_f32_16x16x32_bf16 v[184:187], a[112:115], a[88:91], v[184:187]\n"
"ds_read_b128 a[4:7], v4 offset:32768\n"
"v_mfma_f32_16x16x32_bf16 v[180:183], a[104:107], a[88:91], v[180:183]\n"
"ds_read_b64_tr_b16 a[36:37], v24 offset:40960\n"
"v_mfma_f32_16x16x32_bf16 v[176:179], a[96:99], a[88:91], v[176:179]\n"
"ds_read_b64_tr_b16 a[38:39], v24 offset:41984\n"
"s_add_u32 m0, s77, 16384\n"
"v_mfma_f32_16x16x32_bf16 v[128:131], a[100:103], a[68:71], v[128:131]\n"
"ds_read_b128 a[12:15], v4 offset:34816\n"
"buffer_load_dwordx4 v25, s[52:55], s74 offen offset:0 lds\n"
"v_mfma_f32_16x16x32_bf16 v[132:135], a[108:111], a[68:71], v[132:135]\n"
"ds_read_b64_tr_b16 a[44:45], v5 offset:40960\n"
"v_mfma_f32_16x16x32_bf16 v[136:139], a[116:119], a[68:71], v[136:139]\n"
"ds_read_b64_tr_b16 a[46:47], v5 offset:41984\n"
"v_mfma_f32_16x16x32_bf16 v[140:143], a[124:127], a[68:71], v[140:143]\n"
"ds_read_b128 a[20:23], v4 offset:36864\n"
"buffer_load_dwordx4 v26, s[52:55], s74 offen offset:1024 lds\n"
"v_mfma_f32_16x16x32_bf16 v[156:159], a[124:127], a[76:79], v[156:159]\n"
"ds_read_b64_tr_b16 a[52:53], v6 offset:40960\n"
"v_mfma_f32_16x16x32_bf16 v[152:155], a[116:119], a[76:79], v[152:155]\n"
"ds_read_b64_tr_b16 a[54:55], v6 offset:41984\n"
"v_mfma_f32_16x16x32_bf16 v[148:151], a[108:111], a[76:79], v[148:151]\n"
"ds_read_b128 a[28:31], v4 offset:38912\n"
"v_mfma_f32_16x16x32_bf16 v[144:147], a[100:103], a[76:79], v[144:147]\n"
"ds_read_b64_tr_b16 a[60:61], v7 offset:40960\n"
"buffer_load_dwordx4 v27, s[52:55], s74 offen offset:2048 lds\n"
"v_mfma_f32_16x16x32_bf16 v[160:163], a[100:103], a[84:87], v[160:163]\n"
"ds_read_b64_tr_b16 a[62:63], v7 offset:41984\n"
"v_mfma_f32_16x16x32_bf16 v[164:167], a[108:111], a[84:87], v[164:167]\n"
"v_mfma_f32_16x16x32_bf16 v[168:171], a[116:119], a[84:87], v[168:171]\n"
"v_mfma_f32_16x16x32_bf16 v[172:175], a[124:127], a[84:87], v[172:175]\n"
"buffer_load_dwordx4 v28, s[52:55], s74 offen offset:3072 lds\n"
"s_add_u32 s74, s74, 524288\n"
"v_mfma_f32_16x16x32_bf16 v[188:191], a[124:127], a[92:95], v[188:191]\n"
"v_mfma_f32_16x16x32_bf16 v[184:187], a[116:119], a[92:95], v[184:187]\n"
"v_mfma_f32_16x16x32_bf16 v[180:183], a[108:111], a[92:95], v[180:183]\n"
"v_mfma_f32_16x16x32_bf16 v[176:179], a[100:103], a[92:95], v[176:179]\n"
"s_waitcnt vmcnt(16) lgkmcnt(0)\n"
"s_barrier\n"
"v_mfma_f32_16x16x32_bf16 v[128:131], a[32:35], a[0:3], v[128:131]\n"
"ds_read_b128 a[64:67], v23 offset:49152\n"
"s_add_u32 m0, s76, 32768\n"
"v_mfma_f32_16x16x32_bf16 v[132:135], a[40:43], a[0:3], v[132:135]\n"
"ds_read_b64_tr_b16 a[96:97], v24 offset:49152\n"
"buffer_load_dwordx4 v0, s[20:23], s72 offen offset:0 sc1 lds\n"
"v_mfma_f32_16x16x32_bf16 v[136:139], a[48:51], a[0:3], v[136:139]\n"
"ds_read_b64_tr_b16 a[98:99], v24 offset:50176\n"
"v_mfma_f32_16x16x32_bf16 v[140:143], a[56:59], a[0:3], v[140:143]\n"
"ds_read_b128 a[72:75], v23 offset:51200\n"
"v_mfma_f32_16x16x32_bf16 v[156:159], a[56:59], a[8:11], v[156:159]\n"
"ds_read_b64_tr_b16 a[104:105], v5 offset:49152\n"
"buffer_load_dwordx4 v0, s[20:23], s72 offen offset:1024 sc1 lds\n"
"v_mfma_f32_16x16x32_bf16 v[152:155], a[48:51], a[8:11], v[152:155]\n"
"ds_read_b64_tr_b16 a[106:107], v5 offset:50176\n"
"v_mfma_f32_16x16x32_bf16 v[148:151], a[40:43], a[8:11], v[148:151]\n"
"ds_read_b128 a[80:83], v23 offset:53248\n"
"v_mfma_f32_16x16x32_bf16 v[144:147], a[32:35], a[8:11], v[144:147]\n"
"ds_read_b64_tr_b16 a[112:113], v6 offset:49152\n"
"v_mfma_f32_16x16x32_bf16 v[160:163], a[32:35], a[16:19], v[160:163]\n"
"ds_read_b64_tr_b16 a[114:115], v6 offset:50176\n"
"buffer_load_dwordx4 v0, s[20:23], s72 offen offset:2048 sc1 lds\n"
"v_mfma_f32_16x16x32_bf16 v[164:167], a[40:43], a[16:19], v[164:167]\n"
"ds_read_b128 a[88:91], v23 offset:55296\n"
"v_mfma_f32_16x16x32_bf16 v[168:171], a[48:51], a[16:19], v[168:171]\n"
"ds_read_b64_tr_b16 a[120:121], v7 offset:49152\n"
"v_mfma_f32_16x16x32_bf16 v[172:175], a[56:59], a[16:19], v[172:175]\n"
"ds_read_b64_tr_b16 a[122:123], v7 offset:50176\n"
"v_mfma_f32_16x16x32_bf16 v[188:191], a[56:59], a[24:27], v[188:191]\n"
"buffer_load_dwordx4 v0, s[20:23], s72 offen offset:3072 sc1 lds\n"
"s_add_u32 s72, s72, 16384\n"
"v_mfma_f32_16x16x32_bf16 v[184:187], a[48:51], a[24:27], v[184:187]\n"
"ds_read_b128 a[68:71], v4 offset:49152\n"
"v_mfma_f32_16x16x32_bf16 v[180:183], a[40:43], a[24:27], v[180:183]\n"
"ds_read_b64_tr_b16 a[100:101], v24 offset:57344\n"
"v_mfma_f32_16x16x32_bf16 v[176:179], a[32:35], a[24:27], v[176:179]\n"
"ds_read_b64_tr_b16 a[102:103], v24 offset:58368\n"
"s_add_u32 m0, s77, 32768\n"
"v_mfma_f32_16x16x32_bf16 v[128:131], a[36:39], a[4:7], v[128:131]\n"
"ds_read_b128 a[76:79], v4 offset:51200\n"
"buffer_load_dwordx4 v25, s[52:55], s74 offen offset:0 lds\n"
"v_mfma_f32_16x16x32_bf16 v[132:135], a[44:47], a[4:7], v[132:135]\n"
"ds_read_b64_tr_b16 a[108:109], v5 offset:57344\n"
"v_mfma_f32_16x16x32_bf16 v[136:139], a[52:55], a[4:7], v[136:139]\n"
"ds_read_b64_tr_b16 a[110:111], v5 offset:58368\n"
"v_mfma_f32_16x16x32_bf16 v[140:143], a[60:63], a[4:7], v[140:143]\n"
"ds_read_b128 a[84:87], v4 offset:53248\n"
"buffer_load_dwordx4 v26, s[52:55], s74 offen offset:1024 lds\n"
"v_mfma_f32_16x16x32_bf16 v[156:159], a[60:63], a[12:15], v[156:159]\n"
"ds_read_b64_tr_b16 a[116:117], v6 offset:57344\n"
"v_mfma_f32_16x16x32_bf16 v[152:155], a[52:55], a[12:15], v[152:155]\n"
"ds_read_b64_tr_b16 a[118:119], v6 offset:58368\n"
"v_mfma_f32_16x16x32_bf16 v[148:151], a[44:47], a[12:15], v[148:151]\n"
"ds_read_b128 a[92:95], v4 offset:55296\n"
"v_mfma_f32_16x16x32_bf16 v[144:147], a[36:39], a[12:15], v[144:147]\n"
"ds_read_b64_tr_b16 a[124:125], v7 offset:57344\n"
"buffer_load_dwordx4 v27, s[52:55], s74 offen offset:2048 lds\n"
"v_mfma_f32_16x16x32_bf16 v[160:163], a[36:39], a[20:23], v[160:163]\n"
"ds_read_b64_tr_b16 a[126:127], v7 offset:58368\n"
"v_mfma_f32_16x16x32_bf16 v[164:167], a[44:47], a[20:23], v[164:167]\n"
"v_mfma_f32_16x16x32_bf16 v[168:171], a[52:55], a[20:23], v[168:171]\n"
"v_mfma_f32_16x16x32_bf16 v[172:175], a[60:63], a[20:23], v[172:175]\n"
"buffer_load_dwordx4 v28, s[52:55], s74 offen offset:3072 lds\n"
"s_add_u32 s74, s74, 131072\n"
"v_mfma_f32_16x16x32_bf16 v[188:191], a[60:63], a[28:31], v[188:191]\n"
"v_mfma_f32_16x16x32_bf16 v[184:187], a[52:55], a[28:31], v[184:187]\n"
"v_mfma_f32_16x16x32_bf16 v[180:183], a[44:47], a[28:31], v[180:183]\n"
"v_mfma_f32_16x16x32_bf16 v[176:179], a[36:39], a[28:31], v[176:179]\n"
"s_waitcnt vmcnt(16) lgkmcnt(0)\n"
"s_barrier\n"
"v_mfma_f32_16x16x32_bf16 v[128:131], a[96:99], a[64:67], v[128:131]\n"
"ds_read_b128 a[0:3], v23 offset:0\n"
"s_add_u32 m0, s76, 49152\n"
"v_mfma_f32_16x16x32_bf16 v[132:135], a[104:107], a[64:67], v[132:135]\n"
"ds_read_b64_tr_b16 a[32:33], v24 offset:0\n"
"buffer_load_dwordx4 v0, s[20:23], s72 offen offset:0 sc1 lds\n"
"v_mfma_f32_16x16x32_bf16 v[136:139], a[112:115], a[64:67], v[136:139]\n"
"ds_read_b64_tr_b16 a[34:35], v24 offset:1024\n"
"v_mfma_f32_16x16x32_bf16 v[140:143], a[120:123], a[64:67], v[140:143]\n"
"ds_read_b128 a[8:11], v23 offset:2048\n"
"v_mfma_f32_16x16x32_bf16 v[156:159], a[120:123], a[72:75], v[156:159]\n"
"ds_read_b64_tr_b16 a[40:41], v5 offset:0\n"
"buffer_load_dwordx4 v0, s[20:23], s72 offen offset:1024 sc1 lds\n"
"v_mfma_f32_16x16x32_bf16 v[152:155], a[112:115], a[72:75], v[152:155]\n"
"ds_read_b64_tr_b16 a[42:43], v5 offset:1024\n"
"v_mfma_f32_16x16x32_bf16 v[148:151], a[104:107], a[72:75], v[148:151]\n"
"ds_read_b128 a[16:19], v23 offset:4096\n"
"v_mfma_f32_16x16x32_bf16 v[144:147], a[96:99], a[72:75], v[144:147]\n"
"ds_read_b64_tr_b16 a[48:49], v6 offset:0\n"
"v_mfma_f32_16x16x32_bf16 v[160:163], a[96:99], a[80:83], v[160:163]\n"
"ds_read_b64_tr_b16 a[50:51], v6 offset:1024\n"
"buffer_load_dwordx4 v0, s[20:23], s72 offen offset:2048 sc1 lds\n"
"v_mfma_f32_16x16x32_bf16 v[164:167], a[104:107], a[80:83], v[164:167]\n"
"ds_read_b128 a[24:27], v23 offset:6144\n"
"v_mfma_f32_16x16x32_bf16 v[168:171], a[112:115], a[80:83], v[168:171]\n"
"ds_read_b64_tr_b16 a[56:57], v7 offset:0\n"
"v_mfma_f32_16x16x32_bf16 v[172:175], a[120:123], a[80:83], v[172:175]\n"
"ds_read_b64_tr_b16 a[58:59], v7 offset:1024\n"
"v_mfma_f32_16x16x32_bf16 v[188:191], a[120:123], a[88:91], v[188:191]\n"
"buffer_load_dwordx4 v0, s[20:23], s72 offen offset:3072 sc1 lds\n"
"s_add_u32 s72, s72, 16384\n"
"v_mfma_f32_16x16x32_bf16 v[184:187], a[112:115], a[88:91], v[184:187]\n"
"ds_read_b128 a[4:7], v4 offset:0\n"
"v_mfma_f32_16x16x32_bf16 v[180:183], a[104:107], a[88:91], v[180:183]\n"
"ds_read_b64_tr_b16 a[36:37], v24 offset:8192\n"
"v_mfma_f32_16x16x32_bf16 v[176:179], a[96:99], a[88:91], v[176:179]\n"
"ds_read_b64_tr_b16 a[38:39], v24 offset:9216\n"
"s_add_u32 m0, s77, 49152\n"
"v_mfma_f32_16x16x32_bf16 v[128:131], a[100:103], a[68:71], v[128:131]\n"
"ds_read_b128 a[12:15], v4 offset:2048\n"
"buffer_load_dwordx4 v25, s[52:55], s74 offen offset:0 lds\n"
"v_mfma_f32_16x16x32_bf16 v[132:135], a[108:111], a[68:71], v[132:135]\n"
"ds_read_b64_tr_b16 a[44:45], v5 offset:8192\n"
"v_mfma_f32_16x16x32_bf16 v[136:139], a[116:119], a[68:71], v[136:139]\n"
"ds_read_b64_tr_b16 a[46:47], v5 offset:9216\n"
"v_mfma_f32_16x16x32_bf16 v[140:143], a[124:127], a[68:71], v[140:143]\n"
"ds_read_b128 a[20:23], v4 offset:4096\n"
"buffer_load_dwordx4 v26, s[52:55], s74 offen offset:1024 lds\n"
"v_mfma_f32_16x16x32_bf16 v[156:159], a[124:127], a[76:79], v[156:159]\n"
"ds_read_b64_tr_b16 a[52:53], v6 offset:8192\n"
"v_mfma_f32_16x16x32_bf16 v[152:155], a[116:119], a[76:79], v[152:155]\n"
"ds_read_b64_tr_b16 a[54:55], v6 offset:9216\n"
"v_mfma_f32_16x16x32_bf16 v[148:151], a[108:111], a[76:79], v[148:151]\n"
"ds_read_b128 a[28:31], v4 offset:6144\n"
"v_mfma_f32_16x16x32_bf16 v[144:147], a[100:103], a[76:79], v[144:147]\n"
"ds_read_b64_tr_b16 a[60:61], v7 offset:8192\n"
"buffer_load_dwordx4 v27, s[52:55], s74 offen offset:2048 lds\n"
"v_mfma_f32_16x16x32_bf16 v[160:163], a[100:103], a[84:87], v[160:163]\n"
"ds_read_b64_tr_b16 a[62:63], v7 offset:9216\n"
"v_mfma_f32_16x16x32_bf16 v[164:167], a[108:111], a[84:87], v[164:167]\n"
"v_mfma_f32_16x16x32_bf16 v[168:171], a[116:119], a[84:87], v[168:171]\n"
"v_mfma_f32_16x16x32_bf16 v[172:175], a[124:127], a[84:87], v[172:175]\n"
"buffer_load_dwordx4 v28, s[52:55], s74 offen offset:3072 lds\n"
"s_add_u32 s74, s74, 131072\n"
"v_mfma_f32_16x16x32_bf16 v[188:191], a[124:127], a[92:95], v[188:191]\n"
"v_mfma_f32_16x16x32_bf16 v[184:187], a[116:119], a[92:95], v[184:187]\n"
"v_mfma_f32_16x16x32_bf16 v[180:183], a[108:111], a[92:95], v[180:183]\n"
"v_mfma_f32_16x16x32_bf16 v[176:179], a[100:103], a[92:95], v[176:179]\n"
"s_waitcnt vmcnt(16) lgkmcnt(0)\n"
"s_barrier\n"
"v_mfma_f32_16x16x32_bf16 v[128:131], a[32:35], a[0:3], v[128:131]\n"
"ds_read_b128 a[64:67], v23 offset:16384\n"
"s_add_u32 m0, s76, 0\n"
"v_mfma_f32_16x16x32_bf16 v[132:135], a[40:43], a[0:3], v[132:135]\n"
"ds_read_b64_tr_b16 a[96:97], v24 offset:16384\n"
"buffer_load_dwordx4 v0, s[20:23], s72 offen offset:0 sc1 lds\n"
"v_mfma_f32_16x16x32_bf16 v[136:139], a[48:51], a[0:3], v[136:139]\n"
"ds_read_b64_tr_b16 a[98:99], v24 offset:17408\n"
"v_mfma_f32_16x16x32_bf16 v[140:143], a[56:59], a[0:3], v[140:143]\n"
"ds_read_b128 a[72:75], v23 offset:18432\n"
"v_mfma_f32_16x16x32_bf16 v[156:159], a[56:59], a[8:11], v[156:159]\n"
"ds_read_b64_tr_b16 a[104:105], v5 offset:16384\n"
"buffer_load_dwordx4 v0, s[20:23], s72 offen offset:1024 sc1 lds\n"
"v_mfma_f32_16x16x32_bf16 v[152:155], a[48:51], a[8:11], v[152:155]\n"
"ds_read_b64_tr_b16 a[106:107], v5 offset:17408\n"
"v_mfma_f32_16x16x32_bf16 v[148:151], a[40:43], a[8:11], v[148:151]\n"
"ds_read_b128 a[80:83], v23 offset:20480\n"
"v_mfma_f32_16x16x32_bf16 v[144:147], a[32:35], a[8:11], v[144:147]\n"
"ds_read_b64_tr_b16 a[112:113], v6 offset:16384\n"
"v_mfma_f32_16x16x32_bf16 v[160:163], a[32:35], a[16:19], v[160:163]\n"
"ds_read_b64_tr_b16 a[114:115], v6 offset:17408\n"
"buffer_load_dwordx4 v0, s[20:23], s72 offen offset:2048 sc1 lds\n"
"v_mfma_f32_16x16x32_bf16 v[164:167], a[40:43], a[16:19], v[164:167]\n"
"ds_read_b128 a[88:91], v23 offset:22528\n"
"v_mfma_f32_16x16x32_bf16 v[168:171], a[48:51], a[16:19], v[168:171]\n"
"ds_read_b64_tr_b16 a[120:121], v7 offset:16384\n"
"v_mfma_f32_16x16x32_bf16 v[172:175], a[56:59], a[16:19], v[172:175]\n"
"ds_read_b64_tr_b16 a[122:123], v7 offset:17408\n"
"v_mfma_f32_16x16x32_bf16 v[188:191], a[56:59], a[24:27], v[188:191]\n"
"buffer_load_dwordx4 v0, s[20:23], s72 offen offset:3072 sc1 lds\n"
"s_add_u32 s72, s72, 16384\n"
"v_mfma_f32_16x16x32_bf16 v[184:187], a[48:51], a[24:27], v[184:187]\n"
"ds_read_b128 a[68:71], v4 offset:16384\n"
"v_mfma_f32_16x16x32_bf16 v[180:183], a[40:43], a[24:27], v[180:183]\n"
"ds_read_b64_tr_b16 a[100:101], v24 offset:24576\n"
"v_mfma_f32_16x16x32_bf16 v[176:179], a[32:35], a[24:27], v[176:179]\n"
"ds_read_b64_tr_b16 a[102:103], v24 offset:25600\n"
"s_add_u32 m0, s77, 0\n"
"v_mfma_f32_16x16x32_bf16 v[128:131], a[36:39], a[4:7], v[128:131]\n"
"ds_read_b128 a[76:79], v4 offset:18432\n"
"buffer_load_dwordx4 v25, s[52:55], s74 offen offset:0 lds\n"
"v_mfma_f32_16x16x32_bf16 v[132:135], a[44:47], a[4:7], v[132:135]\n"
"ds_read_b64_tr_b16 a[108:109], v5 offset:24576\n"
"v_mfma_f32_16x16x32_bf16 v[136:139], a[52:55], a[4:7], v[136:139]\n"
"ds_read_b64_tr_b16 a[110:111], v5 offset:25600\n"
"v_mfma_f32_16x16x32_bf16 v[140:143], a[60:63], a[4:7], v[140:143]\n"
"ds_read_b128 a[84:87], v4 offset:20480\n"
"buffer_load_dwordx4 v26, s[52:55], s74 offen offset:1024 lds\n"
"v_mfma_f32_16x16x32_bf16 v[156:159], a[60:63], a[12:15], v[156:159]\n"
"ds_read_b64_tr_b16 a[116:117], v6 offset:24576\n"
"v_mfma_f32_16x16x32_bf16 v[152:155], a[52:55], a[12:15], v[152:155]\n"
"ds_read_b64_tr_b16 a[118:119], v6 offset:25600\n"
"v_mfma_f32_16x16x32_bf16 v[148:151], a[44:47], a[12:15], v[148:151]\n"
"ds_read_b128 a[92:95], v4 offset:22528\n"
"v_mfma_f32_16x16x32_bf16 v[144:147], a[36:39], a[12:15], v[144:147]\n"
"ds_read_b64_tr_b16 a[124:125], v7 offset:24576\n"
"buffer_load_dwordx4 v27, s[52:55], s74 offen offset:2048 lds\n"
"v_mfma_f32_16x16x32_bf16 v[160:163], a[36:39], a[20:23], v[160:163]\n"
"ds_read_b64_tr_b16 a[126:127], v7 offset:25600\n"
"v_mfma_f32_16x16x32_bf16 v[164:167], a[44:47], a[20:23], v[164:167]\n"
"v_mfma_f32_16x16x32_bf16 v[168:171], a[52:55], a[20:23], v[168:171]\n"
"v_mfma_f32_16x16x32_bf16 v[172:175], a[60:63], a[20:23], v[172:175]\n"
"buffer_load_dwordx4 v28, s[52:55], s74 offen offset:3072 lds\n"
"s_add_u32 s74, s74, 131072\n"
"v_mfma_f32_16x16x32_bf16 v[188:191], a[60:63], a[28:31], v[188:191]\n"
"v_mfma_f32_16x16x32_bf16 v[184:187], a[52:55], a[28:31], v[184:187]\n"
"v_mfma_f32_16x16x32_bf16 v[180:183], a[44:47], a[28:31], v[180:183]\n"
"v_mfma_f32_16x16x32_bf16 v[176:179], a[36:39], a[28:31], v[176:179]\n"
"s_waitcnt vmcnt(16) lgkmcnt(0)\n"
"s_barrier\n"
"v_mfma_f32_16x16x32_bf16 v[128:131], a[96:99], a[64:67], v[128:131]\n"
"ds_read_b128 a[0:3], v23 offset:32768\n"
"s_add_u32 m0, s76, 16384\n"
"buffer_load_dwordx2 v[14:15], v9, s[20:23], s78 offen sc1\n"
"v_mfma_f32_16x16x32_bf16 v[132:135], a[104:107], a[64:67], v[132:135]\n"
"ds_read_b64_tr_b16 a[32:33], v24 offset:32768\n"
"buffer_load_dwordx4 v0, s[20:23], s72 offen offset:0 sc1 lds\n"
"v_mfma_f32_16x16x32_bf16 v[136:139], a[112:115], a[64:67], v[136:139]\n"
"ds_read_b64_tr_b16 a[34:35], v24 offset:33792\n"
"v_lshrrev_b32 v32, 9, v0\n"
"v_mfma_f32_16x16x32_bf16 v[140:143], a[120:123], a[64:67], v[140:143]\n"
"ds_read_b128 a[8:11], v23 offset:34816\n"
"v_mfma_f32_16x16x32_bf16 v[156:159], a[120:123], a[72:75], v[156:159]\n"
"ds_read_b64_tr_b16 a[40:41], v5 offset:32768\n"
"buffer_load_dwordx4 v0, s[20:23], s72 offen offset:1024 sc1 lds\n"
"v_and_b32 v33, 0x100, v0\n"
"v_mfma_f32_16x16x32_bf16 v[152:155], a[112:115], a[72:75], v[152:155]\n"
"ds_read_b64_tr_b16 a[42:43], v5 offset:33792\n"
"v_mfma_f32_16x16x32_bf16 v[148:151], a[104:107], a[72:75], v[148:151]\n"
"ds_read_b128 a[16:19], v23 offset:36864\n"
"v_mfma_f32_16x16x32_bf16 v[144:147], a[96:99], a[72:75], v[144:147]\n"
"ds_read_b64_tr_b16 a[48:49], v6 offset:32768\n"
"v_and_b32 v34, 0xf0, v0\n"
"v_mfma_f32_16x16x32_bf16 v[160:163], a[96:99], a[80:83], v[160:163]\n"
"ds_read_b64_tr_b16 a[50:51], v6 offset:33792\n"
"buffer_load_dwordx4 v0, s[20:23], s72 offen offset:2048 sc1 lds\n"
"v_mfma_f32_16x16x32_bf16 v[164:167], a[104:107], a[80:83], v[164:167]\n"
"ds_read_b128 a[24:27], v23 offset:38912\n"
"v_mul_u32_u24 v32, 0x2100, v32\n"
"v_mfma_f32_16x16x32_bf16 v[168:171], a[112:115], a[80:83], v[168:171]\n"
"ds_read_b64_tr_b16 a[56:57], v7 offset:32768\n"
"v_mfma_f32_16x16x32_bf16 v[172:175], a[120:123], a[80:83], v[172:175]\n"
"ds_read_b64_tr_b16 a[58:59], v7 offset:33792\n"
"v_mfma_f32_16x16x32_bf16 v[188:191], a[120:123], a[88:91], v[188:191]\n"
"buffer_load_dwordx4 v0, s[20:23], s72 offen offset:3072 sc1 lds\n"
"s_add_u32 s72, s72, 65536\n"
"v_lshl_add_u32 v33, v33, 3, v34\n"
"v_mfma_f32_16x16x32_bf16 v[184:187], a[112:115], a[88:91], v[184:187]\n"
"ds_read_b128 a[4:7], v4 offset:32768\n"
"v_mfma_f32_16x16x32_bf16 v[180:183], a[104:107], a[88:91], v[180:183]\n"
"ds_read_b64_tr_b16 a[36:37], v24 offset:40960\n"
"v_add_u32 v29, v32, v33\n"
"v_mfma_f32_16x16x32_bf16 v[176:179], a[96:99], a[88:91], v[176:179]\n"
"ds_read_b64_tr_b16 a[38:39], v24 offset:41984\n"
"s_add_u32 m0, s77, 16384\n"
"v_mfma_f32_16x16x32_bf16 v[128:131], a[100:103], a[68:71], v[128:131]\n"
"ds_read_b128 a[12:15], v4 offset:34816\n"
"buffer_load_dwordx4 v25, s[52:55], s74 offen offset:0 lds\n"
"v_mfma_f32_16x16x32_bf16 v[132:135], a[108:111], a[68:71], v[132:135]\n"
"ds_read_b64_tr_b16 a[44:45], v5 offset:40960\n"
"v_bfe_u32 v35, v0, 6, 2\n"
"v_mfma_f32_16x16x32_bf16 v[136:139], a[116:119], a[68:71], v[136:139]\n"
"ds_read_b64_tr_b16 a[46:47], v5 offset:41984\n"
"v_mfma_f32_16x16x32_bf16 v[140:143], a[124:127], a[68:71], v[140:143]\n"
"ds_read_b128 a[20:23], v4 offset:36864\n"
"buffer_load_dwordx4 v26, s[52:55], s74 offen offset:1024 lds\n"
"v_and_b32 v34, 0x30, v0\n"
"v_mfma_f32_16x16x32_bf16 v[156:159], a[124:127], a[76:79], v[156:159]\n"
"ds_read_b64_tr_b16 a[52:53], v6 offset:40960\n"
"v_mfma_f32_16x16x32_bf16 v[152:155], a[116:119], a[76:79], v[152:155]\n"
"ds_read_b64_tr_b16 a[54:55], v6 offset:41984\n"
"v_mfma_f32_16x16x32_bf16 v[148:151], a[108:111], a[76:79], v[148:151]\n"
"ds_read_b128 a[28:31], v4 offset:38912\n"
"v_mul_u32_u24 v35, 0x8400, v35\n"
"v_mfma_f32_16x16x32_bf16 v[144:147], a[100:103], a[76:79], v[144:147]\n"
"ds_read_b64_tr_b16 a[60:61], v7 offset:40960\n"
"buffer_load_dwordx4 v27, s[52:55], s74 offen offset:2048 lds\n"
"v_mfma_f32_16x16x32_bf16 v[160:163], a[100:103], a[84:87], v[160:163]\n"
"ds_read_b64_tr_b16 a[62:63], v7 offset:41984\n"
"v_lshrrev_b32 v33, 8, v0\n"
"v_mfma_f32_16x16x32_bf16 v[164:167], a[108:111], a[84:87], v[164:167]\n"
"v_mfma_f32_16x16x32_bf16 v[168:171], a[116:119], a[84:87], v[168:171]\n"
"v_mfma_f32_16x16x32_bf16 v[172:175], a[124:127], a[84:87], v[172:175]\n"
"buffer_load_dwordx4 v28, s[52:55], s74 offen offset:3072 lds\n"
"s_add_u32 s74, s74, 524288\n"
"v_lshl_add_u32 v35, v34, 2, v35\n"
"v_mfma_f32_16x16x32_bf16 v[188:191], a[124:127], a[92:95], v[188:191]\n"
"v_mfma_f32_16x16x32_bf16 v[184:187], a[116:119], a[92:95], v[184:187]\n"
"v_lshl_add_u32 v30, v33, 4, v35\n"
"v_mfma_f32_16x16x32_bf16 v[180:183], a[108:111], a[92:95], v[180:183]\n"
"v_mfma_f32_16x16x32_bf16 v[176:179], a[100:103], a[92:95], v[176:179]\n"
"s_waitcnt vmcnt(17) lgkmcnt(0)\n"
"s_barrier\n"
"s_waitcnt vmcnt(8)\n"
"v_cmp_ne_u64 vcc, s[90:91], v[14:15]\n"
"s_cbranch_vccnz SPQ12_%=\n"
"BKQ13_%=:\n"
"v_mfma_f32_16x16x32_bf16 v[128:131], a[32:35], a[0:3], v[128:131]\n"
"ds_read_b128 a[64:67], v23 offset:49152\n"
"s_add_u32 m0, s76, 32768\n"
"buffer_load_dwordx4 v[192:195], v29, s[20:23], s79 offen sc1\n"
"s_add_u32 s79, s79, 16896\n"
"v_mfma_f32_16x16x32_bf16 v[132:135], a[40:43], a[0:3], v[132:135]\n"
"ds_read_b64_tr_b16 a[96:97], v24 offset:49152\n"
"buffer_load_dwordx4 v0, s[20:23], s72 offen offset:0 sc1 lds\n"
"v_mfma_f32_16x16x32_bf16 v[136:139], a[48:51], a[0:3], v[136:139]\n"
"ds_read_b64_tr_b16 a[98:99], v24 offset:50176\n"
"buffer_load_dwordx4 v[196:199], v29, s[20:23], s79 offen sc1\n"
"s_add_u32 s79, s79, 16896\n"
"v_mfma_f32_16x16x32_bf16 v[140:143], a[56:59], a[0:3], v[140:143]\n"
"ds_read_b128 a[72:75], v23 offset:51200\n"
"v_mfma_f32_16x16x32_bf16 v[156:159], a[56:59], a[8:11], v[156:159]\n"
"ds_read_b64_tr_b16 a[104:105], v5 offset:49152\n"
"buffer_load_dwordx4 v0, s[20:23], s72 offen offset:1024 sc1 lds\n"
"v_mfma_f32_16x16x32_bf16 v[152:155], a[48:51], a[8:11], v[152:155]\n"
"ds_read_b64_tr_b16 a[106:107], v5 offset:50176\n"
"buffer_load_dwordx4 v[200:203], v29, s[20:23], s79 offen sc1\n"
"s_add_u32 s79, s79, 16896\n"
"v_mfma_f32_16x16x32_bf16 v[148:151], a[40:43], a[8:11], v[148:151]\n"
"ds_read_b128 a[80:83], v23 offset:53248\n"
"v_mfma_f32_16x16x32_bf16 v[144:147], a[32:35], a[8:11], v[144:147]\n"
"ds_read_b64_tr_b16 a[112:113], v6 offset:49152\n"
"v_mfma_f32_16x16x32_bf16 v[160:163], a[32:35], a[16:19], v[160:163]\n"
"ds_read_b64_tr_b16 a[114:115], v6 offset:50176\n"
"buffer_load_dwordx4 v0, s[20:23], s72 offen offset:2048 sc1 lds\n"
"buffer_load_dwordx4 v[204:207], v29, s[20:23], s79 offen sc1\n"
"s_add_u32 s79, s79, 16896\n"
"v_mfma_f32_16x16x32_bf16 v[164:167], a[40:43], a[16:19], v[164:167]\n"
"ds_read_b128 a[88:91], v23 offset:55296\n"
"v_mfma_f32_16x16x32_bf16 v[168:171], a[48:51], a[16:19], v[168:171]\n"
"ds_read_b64_tr_b16 a[120:121], v7 offset:49152\n"
"buffer_load_dwordx4 v[208:211], v29, s[20:23], s79 offen sc1\n"
"s_add_u32 s79, s79, 16896\n"
"v_mfma_f32_16x16x32_bf16 v[172:175], a[56:59], a[16:19], v[172:175]\n"
"ds_read_b64_tr_b16 a[122:123], v7 offset:50176\n"
"v_mfma_f32_16x16x32_bf16 v[188:191], a[56:59], a[24:27], v[188:191]\n"
"buffer_load_dwordx4 v0, s[20:23], s72 offen offset:3072 sc1 lds\n"
"s_add_u32 s72, s72, 16384\n"
"v_mfma_f32_16x16x32_bf16 v[184:187], a[48:51], a[24:27], v[184:187]\n"
"ds_read_b128 a[68:71], v4 offset:49152\n"
"buffer_load_dwordx4 v[212:215], v29, s[20:23], s79 offen sc1\n"
"s_add_u32 s79, s79, 16896\n"
"v_mfma_f32_16x16x32_bf16 v[180:183], a[40:43], a[24:27], v[180:183]\n"
"ds_read_b64_tr_b16 a[100:101], v24 offset:57344\n"
"v_mfma_f32_16x16x32_bf16 v[176:179], a[32:35], a[24:27], v[176:179]\n"
"ds_read_b64_tr_b16 a[102:103], v24 offset:58368\n"
"s_add_u32 m0, s77, 32768\n"
"v_mfma_f32_16x16x32_bf16 v[128:131], a[36:39], a[4:7], v[128:131]\n"
"ds_read_b128 a[76:79], v4 offset:51200\n"
"buffer_load_dwordx4 v25, s[52:55], s74 offen offset:0 lds\n"
"buffer_load_dwordx4 v[216:219], v29, s[20:23], s79 offen sc1\n"
"s_add_u32 s79, s79, 16896\n"
"v_mfma_f32_16x16x32_bf16 v[132:135], a[44:47], a[4:7], v[132:135]\n"
"ds_read_b64_tr_b16 a[108:109], v5 offset:57344\n"
"v_mfma_f32_16x16x32_bf16 v[136:139], a[52:55], a[4:7], v[136:139]\n"
"ds_read_b64_tr_b16 a[110:111], v5 offset:58368\n"
"buffer_load_dwordx4 v[220:223], v29, s[20:23], s79 offen sc1\n"
"s_add_u32 s79, s79, 16896\n"
"v_mfma_f32_16x16x32_bf16 v[140:143], a[60:63], a[4:7], v[140:143]\n"
"ds_read_b128 a[84:87], v4 offset:53248\n"
"buffer_load_dwordx4 v26, s[52:55], s74 offen offset:1024 lds\n"
"v_mfma_f32_16x16x32_bf16 v[156:159], a[60:63], a[12:15], v[156:159]\n"
"ds_read_b64_tr_b16 a[116:117], v6 offset:57344\n"
"v_mfma_f32_16x16x32_bf16 v[152:155], a[52:55], a[12:15], v[152:155]\n"
"ds_read_b64_tr_b16 a[118:119], v6 offset:58368\n"
"buffer_load_dwordx4 v[224:227], v30, s[20:23], s81 offen sc1\n"
"s_add_u32 s81, s81, 8448\n"
"v_mfma_f32_16x16x32_bf16 v[148:151], a[44:47], a[12:15], v[148:151]\n"
"ds_read_b128 a[92:95], v4 offset:55296\n"
"v_mfma_f32_16x16x32_bf16 v[144:147], a[36:39], a[12:15], v[144:147]\n"
"ds_read_b64_tr_b16 a[124:125], v7 offset:57344\n"
"buffer_load_dwordx4 v27, s[52:55], s74 offen offset:2048 lds\n"
"v_mfma_f32_16x16x32_bf16 v[160:163], a[36:39], a[20:23], v[160:163]\n"
"ds_read_b64_tr_b16 a[126:127], v7 offset:58368\n"
"buffer_load_dwordx4 v[228:231], v30, s[20:23], s81 offen sc1\n"
"s_add_u32 s81, s81, 8448\n"
"v_mfma_f32_16x16x32_bf16 v[164:167], a[44:47], a[20:23], v[164:167]\n"
"v_mfma_f32_16x16x32_bf16 v[168:171], a[52:55], a[20:23], v[168:171]\n"
"buffer_load_dwordx4 v[232:235], v30, s[20:23], s81 offen sc1\n"
"s_add_u32 s81, s81, 8448\n"
"v_mfma_f32_16x16x32_bf16 v[172:175], a[60:63], a[20:23], v[172:175]\n"
"buffer_load_dwordx4 v28, s[52:55], s74 offen offset:3072 lds\n"
"s_add_u32 s74, s74, 131072\n"
"v_mfma_f32_16x16x32_bf16 v[188:191], a[60:63], a[28:31], v[188:191]\n"
"v_mfma_f32_16x16x32_bf16 v[184:187], a[52:55], a[28:31], v[184:187]\n"
"buffer_load_dwordx4 v[236:239], v30, s[20:23], s81 offen sc1\n"
"s_add_u32 s81, s81, 8448\n"
"v_mfma_f32_16x16x32_bf16 v[180:183], a[44:47], a[28:31], v[180:183]\n"
"v_mfma_f32_16x16x32_bf16 v[176:179], a[36:39], a[28:31], v[176:179]\n"
"s_waitcnt vmcnt(29) lgkmcnt(0)\n"
"s_barrier\n"
"v_mfma_f32_16x16x32_bf16 v[128:131], a[96:99], a[64:67], v[128:131]\n"
"ds_read_b128 a[0:3], v23 offset:0\n"
"s_add_u32 m0, s76, 49152\n"
"v_mfma_f32_16x16x32_bf16 v[132:135], a[104:107], a[64:67], v[132:135]\n"
"ds_read_b64_tr_b16 a[32:33], v24 offset:0\n"
"buffer_load_dwordx4 v0, s[20:23], s72 offen offset:0 sc1 lds\n"
"v_mfma_f32_16x16x32_bf16 v[136:139], a[112:115], a[64:67], v[136:139]\n"
"ds_read_b64_tr_b16 a[34:35], v24 offset:1024\n"
"v_mfma_f32_16x16x32_bf16 v[140:143], a[120:123], a[64:67], v[140:143]\n"
"ds_read_b128 a[8:11], v23 offset:2048\n"
"v_mfma_f32_16x16x32_bf16 v[156:159], a[120:123], a[72:75], v[156:159]\n"
"ds_read_b64_tr_b16 a[40:41], v5 offset:0\n"
"buffer_load_dwordx4 v0, s[20:23], s72 offen offset:1024 sc1 lds\n"
"v_mfma_f32_16x16x32_bf16 v[152:155], a[112:115], a[72:75], v[152:155]\n"
"ds_read_b64_tr_b16 a[42:43], v5 offset:1024\n"
"v_mfma_f32_16x16x32_bf16 v[148:151], a[104:107], a[72:75], v[148:151]\n"
"ds_read_b128 a[16:19], v23 offset:4096\n"
"v_mfma_f32_16x16x32_bf16 v[144:147], a[96:99], a[72:75], v[144:147]\n"
"ds_read_b64_tr_b16 a[48:49], v6 offset:0\n"
"v_mfma_f32_16x16x32_bf16 v[160:163], a[96:99], a[80:83], v[160:163]\n"
"ds_read_b64_tr_b16 a[50:51], v6 offset:1024\n"
"buffer_load_dwordx4 v0, s[20:23], s72 offen offset:2048 sc1 lds\n"
"v_mfma_f32_16x16x32_bf16 v[164:167], a[104:107], a[80:83], v[164:167]\n"
"ds_read_b128 a[24:27], v23 offset:6144\n"
"v_mfma_f32_16x16x32_bf16 v[168:171], a[112:115], a[80:83], v[168:171]\n"
"ds_read_b64_tr_b16 a[56:57], v7 offset:0\n"
"v_mfma_f32_16x16x32_bf16 v[172:175], a[120:123], a[80:83], v[172:175]\n"
"ds_read_b64_tr_b16 a[58:59], v7 offset:1024\n"
"v_mfma_f32_16x16x32_bf16 v[188:191], a[120:123], a[88:91], v[188:191]\n"
"buffer_load_dwordx4 v0, s[20:23], s72 offen offset:3072 sc1 lds\n"
"s_add_u32 s72, s72, 16384\n"
"v_mfma_f32_16x16x32_bf16 v[184:187], a[112:115], a[88:91], v[184:187]\n"
"ds_read_b128 a[4:7], v4 offset:0\n"
"v_mfma_f32_16x16x32_bf16 v[180:183], a[104:107], a[88:91], v[180:183]\n"
"ds_read_b64_tr_b16 a[36:37], v24 offset:8192\n"
"v_mfma_f32_16x16x32_bf16 v[176:179], a[96:99], a[88:91], v[176:179]\n"
"ds_read_b64_tr_b16 a[38:39], v24 offset:9216\n"
"s_add_u32 m0, s77, 49152\n"
"v_mfma_f32_16x16x32_bf16 v[128:131], a[100:103], a[68:71], v[128:131]\n"
"ds_read_b128 a[12:15], v4 offset:2048\n"
"buffer_load_dwordx4 v25, s[52:55], s74 offen offset:0 lds\n"
"v_mfma_f32_16x16x32_bf16 v[132:135], a[108:111], a[68:71], v[132:135]\n"
"ds_read_b64_tr_b16 a[44:45], v5 offset:8192\n"
"v_mfma_f32_16x16x32_bf16 v[136:139], a[116:119], a[68:71], v[136:139]\n"
"ds_read_b64_tr_b16 a[46:47], v5 offset:9216\n"
"v_mfma_f32_16x16x32_bf16 v[140:143], a[124:127], a[68:71], v[140:143]\n"
"ds_read_b128 a[20:23], v4 offset:4096\n"
"buffer_load_dwordx4 v26, s[52:55], s74 offen offset:1024 lds\n"
"v_mfma_f32_16x16x32_bf16 v[156:159], a[124:127], a[76:79], v[156:159]\n"
"ds_read_b64_tr_b16 a[52:53], v6 offset:8192\n"
"v_mfma_f32_16x16x32_bf16 v[152:155], a[116:119], a[76:79], v[152:155]\n"
"ds_read_b64_tr_b16 a[54:55], v6 offset:9216\n"
"v_mfma_f32_16x16x32_bf16 v[148:151], a[108:111], a[76:79], v[148:151]\n"
"ds_read_b128 a[28:31], v4 offset:6144\n"
"v_mfma_f32_16x16x32_bf16 v[144:147], a[100:103], a[76:79], v[144:147]\n"
"ds_read_b64_tr_b16 a[60:61], v7 offset:8192\n"
"buffer_load_dwordx4 v27, s[52:55], s74 offen offset:2048 lds\n"
"v_mfma_f32_16x16x32_bf16 v[160:163], a[100:103], a[84:87], v[160:163]\n"
"ds_read_b64_tr_b16 a[62:63], v7 offset:9216\n"
"v_mfma_f32_16x16x32_bf16 v[164:167], a[108:111], a[84:87], v[164:167]\n"
"v_mfma_f32_16x16x32_bf16 v[168:171], a[116:119], a[84:87], v[168:171]\n"
"v_mfma_f32_16x16x32_bf16 v[172:175], a[124:127], a[84:87], v[172:175]\n"
"buffer_load_dwordx4 v28, s[52:55], s74 offen offset:3072 lds\n"
"s_add_u32 s74, s74, 131072\n"
"v_mfma_f32_16x16x32_bf16 v[188:191], a[124:127], a[92:95], v[188:191]\n"
"v_mfma_f32_16x16x32_bf16 v[184:187], a[116:119], a[92:95], v[184:187]\n"
"v_mfma_f32_16x16x32_bf16 v[180:183], a[108:111], a[92:95], v[180:183]\n"
"v_mfma_f32_16x16x32_bf16 v[176:179], a[100:103], a[92:95], v[176:179]\n"
"s_waitcnt vmcnt(28) lgkmcnt(0)\n"
"s_barrier\n"
"s_waitcnt vmcnt(8)\n"
"v_mfma_f32_16x16x32_bf16 v[128:131], a[32:35], a[0:3], v[128:131]\n"
"ds_read_b128 a[64:67], v23 offset:16384\n"
"s_add_u32 m0, s76, 0\n"
"v_pk_add_f32 v[192:193], v[192:193], v[208:209]\n"
"v_mfma_f32_16x16x32_bf16 v[132:135], a[40:43], a[0:3], v[132:135]\n"
"ds_read_b64_tr_b16 a[96:97], v24 offset:16384\n"
"buffer_load_dwordx4 v0, s[20:23], s72 offen offset:0 sc1 lds\n"
"v_pk_add_f32 v[194:195], v[194:195], v[210:211]\n"
"v_mfma_f32_16x16x32_bf16 v[136:139], a[48:51], a[0:3], v[136:139]\n"
"ds_read_b64_tr_b16 a[98:99], v24 offset:17408\n"
"v_pk_add_f32 v[196:197], v[196:197], v[212:213]\n"
"v_mfma_f32_16x16x32_bf16 v[140:143], a[56:59], a[0:3], v[140:143]\n"
"ds_read_b128 a[72:75], v23 offset:18432\n"
"v_pk_add_f32 v[198:199], v[198:199], v[214:215]\n"
"v_mfma_f32_16x16x32_bf16 v[156:159], a[56:59], a[8:11], v[156:159]\n"
"ds_read_b64_tr_b16 a[104:105], v5 offset:16384\n"
"buffer_load_dwordx4 v0, s[20:23], s72 offen offset:1024 sc1 lds\n"
"v_pk_add_f32 v[200:201], v[200:201], v[216:217]\n"
"v_mfma_f32_16x16x32_bf16 v[152:155], a[48:51], a[8:11], v[152:155]\n"
"ds_read_b64_tr_b16 a[106:107], v5 offset:17408\n"
"v_mfma_f32_16x16x32_bf16 v[148:151], a[40:43], a[8:11], v[148:151]\n"
"ds_read_b128 a[80:83], v23 offset:20480\n"
"v_pk_add_f32 v[202:203], v[202:203], v[218:219]\n"
"v_mfma_f32_16x16x32_bf16 v[144:147], a[32:35], a[8:11], v[144:147]\n"
"ds_read_b64_tr_b16 a[112:113], v6 offset:16384\n"
"v_pk_add_f32 v[204:205], v[204:205], v[220:221]\n"
"v_mfma_f32_16x16x32_bf16 v[160:163], a[32:35], a[16:19], v[160:163]\n"
"ds_read_b64_tr_b16 a[114:115], v6 offset:17408\n"
"buffer_load_dwordx4 v0, s[20:23], s72 offen offset:2048 sc1 lds\n"
"v_pk_add_f32 v[206:207], v[206:207], v[222:223]\n"
"v_mfma_f32_16x16x32_bf16 v[164:167], a[40:43], a[16:19], v[164:167]\n"
"ds_read_b128 a[88:91], v23 offset:22528\n"
"v_pk_add_f32 v[192:193], v[192:193], v[200:201]\n"
"v_mfma_f32_16x16x32_bf16 v[168:171], a[48:51], a[16:19], v[168:171]\n"
"ds_read_b64_tr_b16 a[120:121], v7 offset:16384\n"
"v_mfma_f32_16x16x32_bf16 v[172:175], a[56:59], a[16:19], v[172:175]\n"
"ds_read_b64_tr_b16 a[122:123], v7 offset:17408\n"
"v_pk_add_f32 v[194:195], v[194:195], v[202:203]\n"
"v_mfma_f32_16x16x32_bf16 v[188:191], a[56:59], a[24:27], v[188:191]\n"
"buffer_load_dwordx4 v0, s[20:23], s72 offen offset:3072 sc1 lds\n"
"s_add_u32 s72, s72, 16384\n"
"v_pk_add_f32 v[196:197], v[196:197], v[204:205]\n"
"v_mfma_f32_16x16x32_bf16 v[184:187], a[48:51], a[24:27], v[184:187]\n"
"ds_read_b128 a[68:71], v4 offset:16384\n"
"v_pk_add_f32 v[198:199], v[198:199], v[206:207]\n"
"v_mfma_f32_16x16x32_bf16 v[180:183], a[40:43], a[24:27], v[180:183]\n"
"ds_read_b64_tr_b16 a[100:101], v24 offset:24576\n"
"v_pk_add_f32 v[192:193], v[192:193], v[196:197]\n"
"v_mfma_f32_16x16x32_bf16 v[176:179], a[32:35], a[24:27], v[176:179]\n"
"ds_read_b64_tr_b16 a[102:103], v24 offset:25600\n"
"s_add_u32 m0, s77, 0\n"
"v_mfma_f32_16x16x32_bf16 v[128:131], a[36:39], a[4:7], v[128:131]\n"
"ds_read_b128 a[76:79], v4 offset:18432\n"
"buffer_load_dwordx4 v25, s[52:55], s74 offen offset:0 lds\n"
"v_pk_add_f32 v[194:195], v[194:195], v[198:199]\n"
"v_mfma_f32_16x16x32_bf16 v[132:135], a[44:47], a[4:7], v[132:135]\n"
"ds_read_b64_tr_b16 a[108:109], v5 offset:24576\n"
"v_permlane32_swap_b32 v192, v194\n"
"v_mfma_f32_16x16x32_bf16 v[136:139], a[52:55], a[4:7], v[136:139]\n"
"ds_read_b64_tr_b16 a[110:111], v5 offset:25600\n"
"v_permlane32_swap_b32 v193, v195\n"
"v_mfma_f32_16x16x32_bf16 v[140:143], a[60:63], a[4:7], v[140:143]\n"
"ds_read_b128 a[84:87], v4 offset:20480\n"
"buffer_load_dwordx4 v26, s[52:55], s74 offen offset:1024 lds\n"
"v_add_f32 v192, v192, v194\n"
"v_mfma_f32_16x16x32_bf16 v[156:159], a[60:63], a[12:15], v[156:159]\n"
"ds_read_b64_tr_b16 a[116:117], v6 offset:24576\n"
"v_add_f32 v193, v193, v195\n"
"v_mfma_f32_16x16x32_bf16 v[152:155], a[52:55], a[12:15], v[152:155]\n"
"ds_read_b64_tr_b16 a[118:119], v6 offset:25600\n"
"v_mfma_f32_16x16x32_bf16 v[148:151], a[44:47], a[12:15], v[148:151]\n"
"ds_read_b128 a[92:95], v4 offset:22528\n"
"v_cvt_pk_bf16_f32 v200, v192, v193\n"
"v_mfma_f32_16x16x32_bf16 v[144:147], a[36:39], a[12:15], v[144:147]\n"
"ds_read_b64_tr_b16 a[124:125], v7 offset:24576\n"
"buffer_load_dwordx4 v27, s[52:55], s74 offen offset:2048 lds\n"
"v_mov_b32 v201, v200\n"
"v_mfma_f32_16x16x32_bf16 v[160:163], a[36:39], a[20:23], v[160:163]\n"
"ds_read_b64_tr_b16 a[126:127], v7 offset:25600\n"
"v_permlane32_swap_b32 v200, v201\n"
"v_mfma_f32_16x16x32_bf16 v[164:167], a[44:47], a[20:23], v[164:167]\n"
"v_mov_b32 v202, v200\n"
"v_mfma_f32_16x16x32_bf16 v[168:171], a[52:55], a[20:23], v[168:171]\n"
"v_mfma_f32_16x16x32_bf16 v[172:175], a[60:63], a[20:23], v[172:175]\n"
"buffer_load_dwordx4 v28, s[52:55], s74 offen offset:3072 lds\n"
"s_add_u32 s74, s74, 131072\n"
"v_mov_b32 v203, v201\n"
"v_mfma_f32_16x16x32_bf16 v[188:191], a[60:63], a[28:31], v[188:191]\n"
"v_permlane16_swap_b32 v200, v202\n"
"v_mfma_f32_16x16x32_bf16 v[184:187], a[52:55], a[28:31], v[184:187]\n"
"v_permlane16_swap_b32 v201, v203\n"
"v_mfma_f32_16x16x32_bf16 v[180:183], a[44:47], a[28:31], v[180:183]\n"
"s_mov_b64 exec, 0xffff\n"
"buffer_store_dwordx4 v[200:203], v0, s[20:23], s80 offen sc1\n"
"s_mov_b64 exec, -1\n"
"v_mfma_f32_16x16x32_bf16 v[176:179], a[36:39], a[28:31], v[176:179]\n"
"s_waitcnt vmcnt(18) lgkmcnt(0)\n"
"s_barrier\n"
"v_mfma_f32_16x16x32_bf16 v[128:131], a[96:99], a[64:67], v[128:131]\n"
"ds_read_b128 a[0:3], v23 offset:32768\n"
"s_add_u32 m0, s76, 16384\n"
"v_pk_add_f32 v[224:225], v[224:225], v[228:229]\n"
"v_mfma_f32_16x16x32_bf16 v[132:135], a[104:107], a[64:67], v[132:135]\n"
"ds_read_b64_tr_b16 a[32:33], v24 offset:32768\n"
"buffer_load_dwordx4 v0, s[20:23], s72 offen offset:0 sc1 lds\n"
"v_pk_add_f32 v[226:227], v[226:227], v[230:231]\n"
"v_mfma_f32_16x16x32_bf16 v[136:139], a[112:115], a[64:67], v[136:139]\n"
"ds_read_b64_tr_b16 a[34:35], v24 offset:33792\n"
"v_pk_add_f32 v[232:233], v[232:233], v[236:237]\n"
"v_mfma_f32_16x16x32_bf16 v[140:143], a[120:123], a[64:67], v[140:143]\n"
"ds_read_b128 a[8:11], v23 offset:34816\n"
"v_pk_add_f32 v[234:235], v[234:235], v[238:239]\n"
"v_mfma_f32_16x16x32_bf16 v[156:159], a[120:123], a[72:75], v[156:159]\n"
"ds_read_b64_tr_b16 a[40:41], v5 offset:32768\n"
"buffer_load_dwordx4 v0, s[20:23], s72 offen offset:1024 sc1 lds\n"
"v_mfma_f32_16x16x32_bf16 v[152:155], a[112:115], a[72:75], v[152:155]\n"
"ds_read_b64_tr_b16 a[42:43], v5 offset:33792\n"
"v_pk_add_f32 v[224:225], v[224:225], v[232:233]\n"
"v_mfma_f32_16x16x32_bf16 v[148:151], a[104:107], a[72:75], v[148:151]\n"
"ds_read_b128 a[16:19], v23 offset:36864\n"
"v_pk_add_f32 v[226:227], v[226:227], v[234:235]\n"
"v_mfma_f32_16x16x32_bf16 v[144:147], a[96:99], a[72:75], v[144:147]\n"
"ds_read_b64_tr_b16 a[48:49], v6 offset:32768\n"
"v_add_f32_dpp v224, v224, v224 row_ror:4 row_mask:0xf bank_mask:0xf\n"
"v_mfma_f32_16x16x32_bf16 v[160:163], a[96:99], a[80:83], v[160:163]\n"
"ds_read_b64_tr_b16 a[50:51], v6 offset:33792\n"
"buffer_load_dwordx4 v0, s[20:23], s72 offen offset:2048 sc1 lds\n"
"v_add_f32_dpp v225, v225, v225 row_ror:4 row_mask:0xf bank_mask:0xf\n"
"v_mfma_f32_16x16x32_bf16 v[164:167], a[104:107], a[80:83], v[164:167]\n"
"ds_read_b128 a[24:27], v23 offset:38912\n"
"v_mfma_f32_16x16x32_bf16 v[168:171], a[112:115], a[80:83], v[168:171]\n"
"ds_read_b64_tr_b16 a[56:57], v7 offset:32768\n"
"v_add_f32_dpp v226, v226, v226 row_ror:4 row_mask:0xf bank_mask:0xf\n"
"v_mfma_f32_16x16x32_bf16 v[172:175], a[120:123], a[80:83], v[172:175]\n"
"ds_read_b64_tr_b16 a[58:59], v7 offset:33792\n"
"v_add_f32_dpp v227, v227, v227 row_ror:4 row_mask:0xf bank_mask:0xf\n"
"v_mfma_f32_16x16x32_bf16 v[188:191], a[120:123], a[88:91], v[188:191]\n"
"buffer_load_dwordx4 v0, s[20:23], s72 offen offset:3072 sc1 lds\n"
"s_add_u32 s72, s72, 65536\n"
"v_add_f32_dpp v224, v224, v224 row_ror:8 row_mask:0xf bank_mask:0xf\n"
"v_mfma_f32_16x16x32_bf16 v[184:187], a[112:115], a[88:91], v[184:187]\n"
"ds_read_b128 a[4:7], v4 offset:32768\n"
"v_mfma_f32_16x16x32_bf16 v[180:183], a[104:107], a[88:91], v[180:183]\n"
"ds_read_b64_tr_b16 a[36:37], v24 offset:40960\n"
"v_add_f32_dpp v225, v225, v225 row_ror:8 row_mask:0xf bank_mask:0xf\n"
"v_mfma_f32_16x16x32_bf16 v[176:179], a[96:99], a[88:91], v[176:179]\n"
"ds_read_b64_tr_b16 a[38:39], v24 offset:41984\n"
"s_add_u32 m0, s77, 16384\n"
"v_add_f32_dpp v226, v226, v226 row_ror:8 row_mask:0xf bank_mask:0xf\n"
"v_mfma_f32_16x16x32_bf16 v[128:131], a[100:103], a[68:71], v[128:131]\n"
"ds_read_b128 a[12:15], v4 offset:34816\n"
"buffer_load_dwordx4 v25, s[52:55], s74 offen offset:0 lds\n"
"v_add_f32_dpp v227, v227, v227 row_ror:8 row_mask:0xf bank_mask:0xf\n"
"v_mfma_f32_16x16x32_bf16 v[132:135], a[108:111], a[68:71], v[132:135]\n"
"ds_read_b64_tr_b16 a[44:45], v5 offset:40960\n"
"v_cvt_pk_bf16_f32 v228, v224, v225\n"
"v_mfma_f32_16x16x32_bf16 v[136:139], a[116:119], a[68:71], v[136:139]\n"
"ds_read_b64_tr_b16 a[46:47], v5 offset:41984\n"
"v_mfma_f32_16x16x32_bf16 v[140:143], a[124:127], a[68:71], v[140:143]\n"
"ds_read_b128 a[20:23], v4 offset:36864\n"
"buffer_load_dwordx4 v26, s[52:55], s74 offen offset:1024 lds\n"
"v_cvt_pk_bf16_f32 v229, v226, v227\n"
"v_mfma_f32_16x16x32_bf16 v[156:159], a[124:127], a[76:79], v[156:159]\n"
"ds_read_b64_tr_b16 a[52:53], v6 offset:40960\n"
"v_mov_b32_dpp v240, v228 quad_perm:[0,0,0,0] row_mask:0xf bank_mask:0xf\n"
"v_mfma_f32_16x16x32_bf16 v[152:155], a[116:119], a[76:79], v[152:155]\n"
"ds_read_b64_tr_b16 a[54:55], v6 offset:41984\n"
"v_mov_b32_dpp v241, v229 quad_perm:[0,0,0,0] row_mask:0xf bank_mask:0xf\n"
"v_mfma_f32_16x16x32_bf16 v[148:151], a[108:111], a[76:79], v[148:151]\n"
"ds_read_b128 a[28:31], v4 offset:38912\n"
"v_mfma_f32_16x16x32_bf16 v[144:147], a[100:103], a[76:79], v[144:147]\n"
"ds_read_b64_tr_b16 a[60:61], v7 offset:40960\n"
"buffer_load_dwordx4 v27, s[52:55], s74 offen offset:2048 lds\n"
"v_mov_b32_dpp v242, v228 quad_perm:[1,1,1,1] row_mask:0xf bank_mask:0xf\n"
"v_mfma_f32_16x16x32_bf16 v[160:163], a[100:103], a[84:87], v[160:163]\n"
"ds_read_b64_tr_b16 a[62:63], v7 offset:41984\n"
"v_mov_b32_dpp v243, v229 quad_perm:[1,1,1,1] row_mask:0xf bank_mask:0xf\n"
"v_mfma_f32_16x16x32_bf16 v[164:167], a[108:111], a[84:87], v[164:167]\n"
"v_mov_b32_dpp v244, v228 quad_perm:[2,2,2,2] row_mask:0xf bank_mask:0xf\n"
"v_mfma_f32_16x16x32_bf16 v[168:171], a[116:119], a[84:87], v[168:171]\n"
"v_mov_b32_dpp v245, v229 quad_perm:[2,2,2,2] row_mask:0xf bank_mask:0xf\n"
"v_mfma_f32_16x16x32_bf16 v[172:175], a[124:127], a[84:87], v[172:175]\n"
"buffer_load_dwordx4 v28, s[52:55], s74 offen offset:3072 lds\n"
"s_add_u32 s74, s74, 524288\n"
"v_mfma_f32_16x16x32_bf16 v[188:191], a[124:127], a[92:95], v[188:191]\n"
"v_mov_b32_dpp v246, v228 quad_perm:[3,3,3,3] row_mask:0xf bank_mask:0xf\n"
"v_mfma_f32_16x16x32_bf16 v[184:187], a[116:119], a[92:95], v[184:187]\n"
"v_mov_b32_dpp v247, v229 quad_perm:[3,3,3,3] row_mask:0xf bank_mask:0xf\n"
"v_mfma_f32_16x16x32_bf16 v[180:183], a[108:111], a[92:95], v[180:183]\n"
"buffer_load_dwordx2 v[250:251], v9, s[20:23], s75 offen sc1\n"
"v_mfma_f32_16x16x32_bf16 v[176:179], a[100:103], a[92:95], v[176:179]\n"
"s_waitcnt vmcnt(18) lgkmcnt(0)\n"
"s_barrier\n"
"s_waitcnt vmcnt(9)\n"
"v_mfma_f32_16x16x32_bf16 v[128:131], a[32:35], a[0:3], v[128:131]\n"
"ds_read_b128 a[64:67], v23 offset:49152\n"
"s_add_u32 m0, s76, 32768\n"
"s_mov_b64 exec, 1\n"
"buffer_store_dwordx2 v[2:3], off, s[20:23], s82 sc1\n"
"s_mov_b64 exec, -1\n"
"v_mfma_f32_16x16x32_bf16 v[132:135], a[40:43], a[0:3], v[132:135]\n"
"ds_read_b64_tr_b16 a[96:97], v24 offset:49152\n"
"buffer_load_dwordx4 v0, s[20:23], s72 offen offset:0 sc1 lds\n"
"v_mfma_f32_16x16x32_bf16 v[136:139], a[48:51], a[0:3], v[136:139]\n"
"ds_read_b64_tr_b16 a[98:99], v24 offset:50176\n"
"v_mfma_f32_16x16x32_bf16 v[140:143], a[56:59], a[0:3], v[140:143]\n"
"ds_read_b128 a[72:75], v23 offset:51200\n"
"v_mfma_f32_16x16x32_bf16 v[156:159], a[56:59], a[8:11], v[156:159]\n"
"ds_read_b64_tr_b16 a[104:105], v5 offset:49152\n"
"buffer_load_dwordx4 v0, s[20:23], s72 offen offset:1024 sc1 lds\n"
"v_mfma_f32_16x16x32_bf16 v[152:155], a[48:51], a[8:11], v[152:155]\n"
"ds_read_b64_tr_b16 a[106:107], v5 offset:50176\n"
"v_mfma_f32_16x16x32_bf16 v[148:151], a[40:43], a[8:11], v[148:151]\n"
"ds_read_b128 a[80:83], v23 offset:53248\n"
"v_mfma_f32_16x16x32_bf16 v[144:147], a[32:35], a[8:11], v[144:147]\n"
"ds_read_b64_tr_b16 a[112:113], v6 offset:49152\n"
"v_mfma_f32_16x16x32_bf16 v[160:163], a[32:35], a[16:19], v[160:163]\n"
"ds_read_b64_tr_b16 a[114:115], v6 offset:50176\n"
"buffer_load_dwordx4 v0, s[20:23], s72 offen offset:2048 sc1 lds\n"
"v_mfma_f32_16x16x32_bf16 v[164:167], a[40:43], a[16:19], v[164:167]\n"
"ds_read_b128 a[88:91], v23 offset:55296\n"
"v_mfma_f32_16x16x32_bf16 v[168:171], a[48:51], a[16:19], v[168:171]\n"
"ds_read_b64_tr_b16 a[120:121], v7 offset:49152\n"
"v_mfma_f32_16x16x32_bf16 v[172:175], a[56:59], a[16:19], v[172:175]\n"
"ds_read_b64_tr_b16 a[122:123], v7 offset:50176\n"
"v_mfma_f32_16x16x32_bf16 v[188:191], a[56:59], a[24:27], v[188:191]\n"
"buffer_load_dwordx4 v0, s[20:23], s72 offen offset:3072 sc1 lds\n"
"s_add_u32 s72, s72, 16384\n"
"v_mfma_f32_16x16x32_bf16 v[184:187], a[48:51], a[24:27], v[184:187]\n"
"ds_read_b128 a[68:71], v4 offset:49152\n"
"v_mfma_f32_16x16x32_bf16 v[180:183], a[40:43], a[24:27], v[180:183]\n"
"ds_read_b64_tr_b16 a[100:101], v24 offset:57344\n"
"v_mfma_f32_16x16x32_bf16 v[176:179], a[32:35], a[24:27], v[176:179]\n"
"ds_read_b64_tr_b16 a[102:103], v24 offset:58368\n"
"s_add_u32 m0, s77, 32768\n"
"v_mfma_f32_16x16x32_bf16 v[128:131], a[36:39], a[4:7], v[128:131]\n"
"ds_read_b128 a[76:79], v4 offset:51200\n"
"buffer_load_dwordx4 v25, s[52:55], s74 offen offset:0 lds\n"
"v_mfma_f32_16x16x32_bf16 v[132:135], a[44:47], a[4:7], v[132:135]\n"
"ds_read_b64_tr_b16 a[108:109], v5 offset:57344\n"
"v_mfma_f32_16x16x32_bf16 v[136:139], a[52:55], a[4:7], v[136:139]\n"
"ds_read_b64_tr_b16 a[110:111], v5 offset:58368\n"
"v_mfma_f32_16x16x32_bf16 v[140:143], a[60:63], a[4:7], v[140:143]\n"
"ds_read_b128 a[84:87], v4 offset:53248\n"
"buffer_load_dwordx4 v26, s[52:55], s74 offen offset:1024 lds\n"
"v_mfma_f32_16x16x32_bf16 v[156:159], a[60:63], a[12:15], v[156:159]\n"
"ds_read_b64_tr_b16 a[116:117], v6 offset:57344\n"
"v_mfma_f32_16x16x32_bf16 v[152:155], a[52:55], a[12:15], v[152:155]\n"
"ds_read_b64_tr_b16 a[118:119], v6 offset:58368\n"
"v_mfma_f32_16x16x32_bf16 v[148:151], a[44:47], a[12:15], v[148:151]\n"
"ds_read_b128 a[92:95], v4 offset:55296\n"
"v_mfma_f32_16x16x32_bf16 v[144:147], a[36:39], a[12:15], v[144:147]\n"
"ds_read_b64_tr_b16 a[124:125], v7 offset:57344\n"
"buffer_load_dwordx4 v27, s[52:55], s74 offen offset:2048 lds\n"
"v_mfma_f32_16x16x32_bf16 v[160:163], a[36:39], a[20:23], v[160:163]\n"
"ds_read_b64_tr_b16 a[126:127], v7 offset:58368\n"
"v_mfma_f32_16x16x32_bf16 v[164:167], a[44:47], a[20:23], v[164:167]\n"
"v_mfma_f32_16x16x32_bf16 v[168:171], a[52:55], a[20:23], v[168:171]\n"
"v_mfma_f32_16x16x32_bf16 v[172:175], a[60:63], a[20:23], v[172:175]\n"
"buffer_load_dwordx4 v28, s[52:55], s74 offen offset:3072 lds\n"
"s_add_u32 s74, s74, 131072\n"
"v_mfma_f32_16x16x32_bf16 v[188:191], a[60:63], a[28:31], v[188:191]\n"
"v_mfma_f32_16x16x32_bf16 v[184:187], a[52:55], a[28:31], v[184:187]\n"
"v_mfma_f32_16x16x32_bf16 v[180:183], a[44:47], a[28:31], v[180:183]\n"
"v_mfma_f32_16x16x32_bf16 v[176:179], a[36:39], a[28:31], v[176:179]\n"
"s_waitcnt vmcnt(19) lgkmcnt(0)\n"
"s_barrier\n"
"v_mfma_f32_16x16x32_bf16 v[128:131], a[96:99], a[64:67], v[128:131]\n"
"ds_read_b128 a[0:3], v23 offset:0\n"
"s_add_u32 m0, s76, 49152\n"
"v_mfma_f32_16x16x32_bf16 v[132:135], a[104:107], a[64:67], v[132:135]\n"
"ds_read_b64_tr_b16 a[32:33], v24 offset:0\n"
"buffer_load_dwordx4 v0, s[20:23], s72 offen offset:0 sc1 lds\n"
"v_mfma_f32_16x16x32_bf16 v[136:139], a[112:115], a[64:67], v[136:139]\n"
"ds_read_b64_tr_b16 a[34:35], v24 offset:1024\n"
"v_mfma_f32_16x16x32_bf16 v[140:143], a[120:123], a[64:67], v[140:143]\n"
"ds_read_b128 a[8:11], v23 offset:2048\n"
"v_mfma_f32_16x16x32_bf16 v[156:159], a[120:123], a[72:75], v[156:159]\n"
"ds_read_b64_tr_b16 a[40:41], v5 offset:0\n"
"buffer_load_dwordx4 v0, s[20:23], s72 offen offset:1024 sc1 lds\n"
"v_mfma_f32_16x16x32_bf16 v[152:155], a[112:115], a[72:75], v[152:155]\n"
"ds_read_b64_tr_b16 a[42:43], v5 offset:1024\n"
"v_mfma_f32_16x16x32_bf16 v[148:151], a[104:107], a[72:75], v[148:151]\n"
"ds_read_b128 a[16:19], v23 offset:4096\n"
"v_mfma_f32_16x16x32_bf16 v[144:147], a[96:99], a[72:75], v[144:147]\n"
"ds_read_b64_tr_b16 a[48:49], v6 offset:0\n"
"v_mfma_f32_16x16x32_bf16 v[160:163], a[96:99], a[80:83], v[160:163]\n"
"ds_read_b64_tr_b16 a[50:51], v6 offset:1024\n"
"buffer_load_dwordx4 v0, s[20:23], s72 offen offset:2048 sc1 lds\n"
"v_mfma_f32_16x16x32_bf16 v[164:167], a[104:107], a[80:83], v[164:167]\n"
"ds_read_b128 a[24:27], v23 offset:6144\n"
"v_mfma_f32_16x16x32_bf16 v[168:171], a[112:115], a[80:83], v[168:171]\n"
"ds_read_b64_tr_b16 a[56:57], v7 offset:0\n"
"v_mfma_f32_16x16x32_bf16 v[172:175], a[120:123], a[80:83], v[172:175]\n"
"ds_read_b64_tr_b16 a[58:59], v7 offset:1024\n"
"v_mfma_f32_16x16x32_bf16 v[188:191], a[120:123], a[88:91], v[188:191]\n"
"buffer_load_dwordx4 v0, s[20:23], s72 offen offset:3072 sc1 lds\n"
"s_sub_u32 s72, s72, 409600\n"
"v_mfma_f32_16x16x32_bf16 v[184:187], a[112:115], a[88:91], v[184:187]\n"
"ds_read_b128 a[4:7], v4 offset:0\n"
"v_mfma_f32_16x16x32_bf16 v[180:183], a[104:107], a[88:91], v[180:183]\n"
"ds_read_b64_tr_b16 a[36:37], v24 offset:8192\n"
"v_mfma_f32_16x16x32_bf16 v[176:179], a[96:99], a[88:91], v[176:179]\n"
"ds_read_b64_tr_b16 a[38:39], v24 offset:9216\n"
"s_add_u32 m0, s77, 49152\n"
"v_mfma_f32_16x16x32_bf16 v[128:131], a[100:103], a[68:71], v[128:131]\n"
"ds_read_b128 a[12:15], v4 offset:2048\n"
"buffer_load_dwordx4 v25, s[52:55], s74 offen offset:0 lds\n"
"v_mfma_f32_16x16x32_bf16 v[132:135], a[108:111], a[68:71], v[132:135]\n"
"ds_read_b64_tr_b16 a[44:45], v5 offset:8192\n"
"v_mfma_f32_16x16x32_bf16 v[136:139], a[116:119], a[68:71], v[136:139]\n"
"ds_read_b64_tr_b16 a[46:47], v5 offset:9216\n"
"v_mfma_f32_16x16x32_bf16 v[140:143], a[124:127], a[68:71], v[140:143]\n"
"ds_read_b128 a[20:23], v4 offset:4096\n"
"buffer_load_dwordx4 v26, s[52:55], s74 offen offset:1024 lds\n"
"v_mfma_f32_16x16x32_bf16 v[156:159], a[124:127], a[76:79], v[156:159]\n"
"ds_read_b64_tr_b16 a[52:53], v6 offset:8192\n"
"v_mfma_f32_16x16x32_bf16 v[152:155], a[116:119], a[76:79], v[152:155]\n"
"ds_read_b64_tr_b16 a[54:55], v6 offset:9216\n"
"v_mfma_f32_16x16x32_bf16 v[148:151], a[108:111], a[76:79], v[148:151]\n"
"ds_read_b128 a[28:31], v4 offset:6144\n"
"v_mfma_f32_16x16x32_bf16 v[144:147], a[100:103], a[76:79], v[144:147]\n"
"ds_read_b64_tr_b16 a[60:61], v7 offset:8192\n"
"buffer_load_dwordx4 v27, s[52:55], s74 offen offset:2048 lds\n"
"v_mfma_f32_16x16x32_bf16 v[160:163], a[100:103], a[84:87], v[160:163]\n"
"ds_read_b64_tr_b16 a[62:63], v7 offset:9216\n"
"v_mfma_f32_16x16x32_bf16 v[164:167], a[108:111], a[84:87], v[164:167]\n"
"v_mfma_f32_16x16x32_bf16 v[168:171], a[116:119], a[84:87], v[168:171]\n"
"v_mfma_f32_16x16x32_bf16 v[172:175], a[124:127], a[84:87], v[172:175]\n"
"buffer_load_dwordx4 v28, s[52:55], s74 offen offset:3072 lds\n"
"s_sub_u32 s74, s74, 3276800\n"
"v_mfma_f32_16x16x32_bf16 v[188:191], a[124:127], a[92:95], v[188:191]\n"
"v_mfma_f32_16x16x32_bf16 v[184:187], a[116:119], a[92:95], v[184:187]\n"
"v_mfma_f32_16x16x32_bf16 v[180:183], a[108:111], a[92:95], v[180:183]\n"
"v_mfma_f32_16x16x32_bf16 v[176:179], a[100:103], a[92:95], v[176:179]\n"
"s_waitcnt vmcnt(18) lgkmcnt(0)\n"
"s_barrier\n"
"s_waitcnt vmcnt(17)\n"
"v_cmp_ne_u64 vcc, s[90:91], v[250:251]\n"
"s_cbranch_vccnz SPQ18_%=\n"
"BKQ19_%=:\n"
"v_mfma_f32_16x16x32_bf16 v[128:131], a[32:35], a[0:3], v[128:131]\n"
"ds_read_b128 a[64:67], v23 offset:16384\n"
"s_add_u32 m0, s76, 0\n"
"v_mfma_f32_16x16x32_bf16 v[132:135], a[40:43], a[0:3], v[132:135]\n"
"ds_read_b64_tr_b16 a[96:97], v24 offset:16384\n"
"buffer_load_dwordx4 v0, s[20:23], s72 offen offset:0 sc1 lds\n"
"v_mfma_f32_16x16x32_bf16 v[136:139], a[48:51], a[0:3], v[136:139]\n"
"ds_read_b64_tr_b16 a[98:99], v24 offset:17408\n"
"v_mfma_f32_16x16x32_bf16 v[140:143], a[56:59], a[0:3], v[140:143]\n"
"ds_read_b128 a[72:75], v23 offset:18432\n"
"v_mfma_f32_16x16x32_bf16 v[156:159], a[56:59], a[8:11], v[156:159]\n"
"ds_read_b64_tr_b16 a[104:105], v5 offset:16384\n"
"buffer_load_dwordx4 v0, s[20:23], s72 offen offset:1024 sc1 lds\n"
"v_mfma_f32_16x16x32_bf16 v[152:155], a[48:51], a[8:11], v[152:155]\n"
"ds_read_b64_tr_b16 a[106:107], v5 offset:17408\n"
"v_mfma_f32_16x16x32_bf16 v[148:151], a[40:43], a[8:11], v[148:151]\n"
"ds_read_b128 a[80:83], v23 offset:20480\n"
"v_mfma_f32_16x16x32_bf16 v[144:147], a[32:35], a[8:11], v[144:147]\n"
"ds_read_b64_tr_b16 a[112:113], v6 offset:16384\n"
"v_mfma_f32_16x16x32_bf16 v[160:163], a[32:35], a[16:19], v[160:163]\n"
"ds_read_b64_tr_b16 a[114:115], v6 offset:17408\n"
"buffer_load_dwordx4 v0, s[20:23], s72 offen offset:2048 sc1 lds\n"
"v_mfma_f32_16x16x32_bf16 v[164:167], a[40:43], a[16:19], v[164:167]\n"
"ds_read_b128 a[88:91], v23 offset:22528\n"
"v_mfma_f32_16x16x32_bf16 v[168:171], a[48:51], a[16:19], v[168:171]\n"
"ds_read_b64_tr_b16 a[120:121], v7 offset:16384\n"
"v_mfma_f32_16x16x32_bf16 v[172:175], a[56:59], a[16:19], v[172:175]\n"
"ds_read_b64_tr_b16 a[122:123], v7 offset:17408\n"
"v_mfma_f32_16x16x32_bf16 v[188:191], a[56:59], a[24:27], v[188:191]\n"
"buffer_load_dwordx4 v0, s[20:23], s72 offen offset:3072 sc1 lds\n"
"s_add_u32 s72, s72, 16384\n"
"v_mfma_f32_16x16x32_bf16 v[184:187], a[48:51], a[24:27], v[184:187]\n"
"ds_read_b128 a[68:71], v4 offset:16384\n"
"v_mfma_f32_16x16x32_bf16 v[180:183], a[40:43], a[24:27], v[180:183]\n"
"ds_read_b64_tr_b16 a[100:101], v24 offset:24576\n"
"v_mfma_f32_16x16x32_bf16 v[176:179], a[32:35], a[24:27], v[176:179]\n"
"ds_read_b64_tr_b16 a[102:103], v24 offset:25600\n"
"s_add_u32 m0, s77, 0\n"
"v_mfma_f32_16x16x32_bf16 v[128:131], a[36:39], a[4:7], v[128:131]\n"
"ds_read_b128 a[76:79], v4 offset:18432\n"
"buffer_load_dwordx4 v25, s[52:55], s74 offen offset:0 lds\n"
"v_mfma_f32_16x16x32_bf16 v[132:135], a[44:47], a[4:7], v[132:135]\n"
"ds_read_b64_tr_b16 a[108:109], v5 offset:24576\n"
"v_mfma_f32_16x16x32_bf16 v[136:139], a[52:55], a[4:7], v[136:139]\n"
"ds_read_b64_tr_b16 a[110:111], v5 offset:25600\n"
"v_mfma_f32_16x16x32_bf16 v[140:143], a[60:63], a[4:7], v[140:143]\n"
"ds_read_b128 a[84:87], v4 offset:20480\n"
"buffer_load_dwordx4 v26, s[52:55], s74 offen offset:1024 lds\n"
"v_mfma_f32_16x16x32_bf16 v[156:159], a[60:63], a[12:15], v[156:159]\n"
"ds_read_b64_tr_b16 a[116:117], v6 offset:24576\n"
"v_mfma_f32_16x16x32_bf16 v[152:155], a[52:55], a[12:15], v[152:155]\n"
"ds_read_b64_tr_b16 a[118:119], v6 offset:25600\n"
"buffer_load_dwordx2 v[14:15], v9, s[20:23], s70 offen sc1\n"
"v_mfma_f32_16x16x32_bf16 v[148:151], a[44:47], a[12:15], v[148:151]\n"
"ds_read_b128 a[92:95], v4 offset:22528\n"
"v_mfma_f32_16x16x32_bf16 v[144:147], a[36:39], a[12:15], v[144:147]\n"
"ds_read_b64_tr_b16 a[124:125], v7 offset:24576\n"
"buffer_load_dwordx4 v27, s[52:55], s74 offen offset:2048 lds\n"
"v_mfma_f32_16x16x32_bf16 v[160:163], a[36:39], a[20:23], v[160:163]\n"
"ds_read_b64_tr_b16 a[126:127], v7 offset:25600\n"
"v_mfma_f32_16x16x32_bf16 v[164:167], a[44:47], a[20:23], v[164:167]\n"
"v_mfma_f32_16x16x32_bf16 v[168:171], a[52:55], a[20:23], v[168:171]\n"
"v_mfma_f32_16x16x32_bf16 v[172:175], a[60:63], a[20:23], v[172:175]\n"
"buffer_load_dwordx4 v28, s[52:55], s74 offen offset:3072 lds\n"
"s_add_u32 s74, s74, 131072\n"
"v_mfma_f32_16x16x32_bf16 v[188:191], a[60:63], a[28:31], v[188:191]\n"
"v_mfma_f32_16x16x32_bf16 v[184:187], a[52:55], a[28:31], v[184:187]\n"
"v_mfma_f32_16x16x32_bf16 v[180:183], a[44:47], a[28:31], v[180:183]\n"
"v_mfma_f32_16x16x32_bf16 v[176:179], a[36:39], a[28:31], v[176:179]\n"
"s_waitcnt vmcnt(17) lgkmcnt(0)\n"
"s_barrier\n"
"v_mfma_f32_16x16x32_bf16 v[128:131], a[96:99], a[64:67], v[128:131]\n"
"ds_read_b128 a[0:3], v23 offset:32768\n"
"s_add_u32 m0, s76, 16384\n"
"v_mfma_f32_16x16x32_bf16 v[132:135], a[104:107], a[64:67], v[132:135]\n"
"ds_read_b64_tr_b16 a[32:33], v24 offset:32768\n"
"buffer_load_dwordx4 v0, s[20:23], s72 offen offset:0 sc1 lds\n"
"v_mfma_f32_16x16x32_bf16 v[136:139], a[112:115], a[64:67], v[136:139]\n"
"ds_read_b64_tr_b16 a[34:35], v24 offset:33792\n"
"v_mfma_f32_16x16x32_bf16 v[140:143], a[120:123], a[64:67], v[140:143]\n"
"ds_read_b128 a[8:11], v23 offset:34816\n"
"v_mfma_f32_16x16x32_bf16 v[156:159], a[120:123], a[72:75], v[156:159]\n"
"ds_read_b64_tr_b16 a[40:41], v5 offset:32768\n"
"buffer_load_dwordx4 v0, s[20:23], s72 offen offset:1024 sc1 lds\n"
"v_mfma_f32_16x16x32_bf16 v[152:155], a[112:115], a[72:75], v[152:155]\n"
"ds_read_b64_tr_b16 a[42:43], v5 offset:33792\n"
"v_mfma_f32_16x16x32_bf16 v[148:151], a[104:107], a[72:75], v[148:151]\n"
"ds_read_b128 a[16:19], v23 offset:36864\n"
"v_mfma_f32_16x16x32_bf16 v[144:147], a[96:99], a[72:75], v[144:147]\n"
"ds_read_b64_tr_b16 a[48:49], v6 offset:32768\n"
"v_mfma_f32_16x16x32_bf16 v[160:163], a[96:99], a[80:83], v[160:163]\n"
"ds_read_b64_tr_b16 a[50:51], v6 offset:33792\n"
"buffer_load_dwordx4 v0, s[20:23], s72 offen offset:2048 sc1 lds\n"
"v_mfma_f32_16x16x32_bf16 v[164:167], a[104:107], a[80:83], v[164:167]\n"
"ds_read_b128 a[24:27], v23 offset:38912\n"
"v_mfma_f32_16x16x32_bf16 v[168:171], a[112:115], a[80:83], v[168:171]\n"
"ds_read_b64_tr_b16 a[56:57], v7 offset:32768\n"
"v_mfma_f32_16x16x32_bf16 v[172:175], a[120:123], a[80:83], v[172:175]\n"
"ds_read_b64_tr_b16 a[58:59], v7 offset:33792\n"
"v_mfma_f32_16x16x32_bf16 v[188:191], a[120:123], a[88:91], v[188:191]\n"
"buffer_load_dwordx4 v0, s[20:23], s72 offen offset:3072 sc1 lds\n"
"s_add_u32 s72, s72, 16384\n"
"v_mfma_f32_16x16x32_bf16 v[184:187], a[112:115], a[88:91], v[184:187]\n"
"ds_read_b128 a[4:7], v4 offset:32768\n"
"v_mfma_f32_16x16x32_bf16 v[180:183], a[104:107], a[88:91], v[180:183]\n"
"ds_read_b64_tr_b16 a[36:37], v24 offset:40960\n"
"v_mfma_f32_16x16x32_bf16 v[176:179], a[96:99], a[88:91], v[176:179]\n"
"ds_read_b64_tr_b16 a[38:39], v24 offset:41984\n"
"s_add_u32 m0, s77, 16384\n"
"v_mfma_f32_16x16x32_bf16 v[128:131], a[100:103], a[68:71], v[128:131]\n"
"ds_read_b128 a[12:15], v4 offset:34816\n"
"buffer_load_dwordx4 v25, s[52:55], s74 offen offset:0 lds\n"
"v_mfma_f32_16x16x32_bf16 v[132:135], a[108:111], a[68:71], v[132:135]\n"
"ds_read_b64_tr_b16 a[44:45], v5 offset:40960\n"
"v_mfma_f32_16x16x32_bf16 v[136:139], a[116:119], a[68:71], v[136:139]\n"
"ds_read_b64_tr_b16 a[46:47], v5 offset:41984\n"
"v_mfma_f32_16x16x32_bf16 v[140:143], a[124:127], a[68:71], v[140:143]\n"
"ds_read_b128 a[20:23], v4 offset:36864\n"
"buffer_load_dwordx4 v26, s[52:55], s74 offen offset:1024 lds\n"
"v_mfma_f32_16x16x32_bf16 v[156:159], a[124:127], a[76:79], v[156:159]\n"
"ds_read_b64_tr_b16 a[52:53], v6 offset:40960\n"
"v_mfma_f32_16x16x32_bf16 v[152:155], a[116:119], a[76:79], v[152:155]\n"
"ds_read_b64_tr_b16 a[54:55], v6 offset:41984\n"
"v_mfma_f32_16x16x32_bf16 v[148:151], a[108:111], a[76:79], v[148:151]\n"
"ds_read_b128 a[28:31], v4 offset:38912\n"
"v_mfma_f32_16x16x32_bf16 v[144:147], a[100:103], a[76:79], v[144:147]\n"
"ds_read_b64_tr_b16 a[60:61], v7 offset:40960\n"
"buffer_load_dwordx4 v27, s[52:55], s74 offen offset:2048 lds\n"
"v_mfma_f32_16x16x32_bf16 v[160:163], a[100:103], a[84:87], v[160:163]\n"
"ds_read_b64_tr_b16 a[62:63], v7 offset:41984\n"
"v_mfma_f32_16x16x32_bf16 v[164:167], a[108:111], a[84:87], v[164:167]\n"
"v_mfma_f32_16x16x32_bf16 v[168:171], a[116:119], a[84:87], v[168:171]\n"
"v_mfma_f32_16x16x32_bf16 v[172:175], a[124:127], a[84:87], v[172:175]\n"
"buffer_load_dwordx4 v28, s[52:55], s74 offen offset:3072 lds\n"
"s_add_u32 s74, s74, 131072\n"
"v_mfma_f32_16x16x32_bf16 v[188:191], a[124:127], a[92:95], v[188:191]\n"
"v_mfma_f32_16x16x32_bf16 v[184:187], a[116:119], a[92:95], v[184:187]\n"
"v_mfma_f32_16x16x32_bf16 v[180:183], a[108:111], a[92:95], v[180:183]\n"
"v_mfma_f32_16x16x32_bf16 v[176:179], a[100:103], a[92:95], v[176:179]\n"
"s_waitcnt vmcnt(17) lgkmcnt(0)\n"
"s_barrier\n"
"s_waitcnt vmcnt(10)\n"
"v_cmp_ne_u64 vcc, s[90:91], v[14:15]\n"
"s_cbranch_vccnz SPQ15_%=\n"
"BKQ16_%=:\n"
"v_mfma_f32_16x16x32_bf16 v[128:131], a[32:35], a[0:3], v[128:131]\n"
"ds_read_b128 a[64:67], v23 offset:49152\n"
"s_add_u32 m0, s76, 32768\n"
"buffer_load_dwordx4 a[128:131], v0, s[20:23], s71 offen offset:0 sc1\n"
"v_mfma_f32_16x16x32_bf16 v[132:135], a[40:43], a[0:3], v[132:135]\n"
"ds_read_b64_tr_b16 a[96:97], v24 offset:49152\n"
"buffer_load_dwordx4 v0, s[20:23], s72 offen offset:0 sc1 lds\n"
"v_mfma_f32_16x16x32_bf16 v[136:139], a[48:51], a[0:3], v[136:139]\n"
"ds_read_b64_tr_b16 a[98:99], v24 offset:50176\n"
"v_mfma_f32_16x16x32_bf16 v[140:143], a[56:59], a[0:3], v[140:143]\n"
"ds_read_b128 a[72:75], v23 offset:51200\n"
"v_mfma_f32_16x16x32_bf16 v[156:159], a[56:59], a[8:11], v[156:159]\n"
"ds_read_b64_tr_b16 a[104:105], v5 offset:49152\n"
"buffer_load_dwordx4 v0, s[20:23], s72 offen offset:1024 sc1 lds\n"
"v_mfma_f32_16x16x32_bf16 v[152:155], a[48:51], a[8:11], v[152:155]\n"
"ds_read_b64_tr_b16 a[106:107], v5 offset:50176\n"
"v_mfma_f32_16x16x32_bf16 v[148:151], a[40:43], a[8:11], v[148:151]\n"
"ds_read_b128 a[80:83], v23 offset:53248\n"
"v_mfma_f32_16x16x32_bf16 v[144:147], a[32:35], a[8:11], v[144:147]\n"
"ds_read_b64_tr_b16 a[112:113], v6 offset:49152\n"
"v_mfma_f32_16x16x32_bf16 v[160:163], a[32:35], a[16:19], v[160:163]\n"
"ds_read_b64_tr_b16 a[114:115], v6 offset:50176\n"
"buffer_load_dwordx4 v0, s[20:23], s72 offen offset:2048 sc1 lds\n"
"buffer_load_dwordx4 a[132:135], v0, s[20:23], s71 offen offset:1024 sc1\n"
"v_mfma_f32_16x16x32_bf16 v[164:167], a[40:43], a[16:19], v[164:167]\n"
"ds_read_b128 a[88:91], v23 offset:55296\n"
"v_mfma_f32_16x16x32_bf16 v[168:171], a[48:51], a[16:19], v[168:171]\n"
"ds_read_b64_tr_b16 a[120:121], v7 offset:49152\n"
"v_mfma_f32_16x16x32_bf16 v[172:175], a[56:59], a[16:19], v[172:175]\n"
"ds_read_b64_tr_b16 a[122:123], v7 offset:50176\n"
"v_mfma_f32_16x16x32_bf16 v[188:191], a[56:59], a[24:27], v[188:191]\n"
"buffer_load_dwordx4 v0, s[20:23], s72 offen offset:3072 sc1 lds\n"
"s_add_u32 s72, s72, 81920\n"
"v_mfma_f32_16x16x32_bf16 v[184:187], a[48:51], a[24:27], v[184:187]\n"
"ds_read_b128 a[68:71], v4 offset:49152\n"
"v_mfma_f32_16x16x32_bf16 v[180:183], a[40:43], a[24:27], v[180:183]\n"
"ds_read_b64_tr_b16 a[100:101], v24 offset:57344\n"
"v_mfma_f32_16x16x32_bf16 v[176:179], a[32:35], a[24:27], v[176:179]\n"
"ds_read_b64_tr_b16 a[102:103], v24 offset:58368\n"
"s_add_u32 m0, s77, 32768\n"
"v_mfma_f32_16x16x32_bf16 v[128:131], a[36:39], a[4:7], v[128:131]\n"
"ds_read_b128 a[76:79], v4 offset:51200\n"
"buffer_load_dwordx4 v25, s[52:55], s74 offen offset:0 lds\n"
"buffer_load_dwordx4 a[136:139], v0, s[20:23], s71 offen offset:2048 sc1\n"
"v_mfma_f32_16x16x32_bf16 v[132:135], a[44:47], a[4:7], v[132:135]\n"
"ds_read_b64_tr_b16 a[108:109], v5 offset:57344\n"
"v_mfma_f32_16x16x32_bf16 v[136:139], a[52:55], a[4:7], v[136:139]\n"
"ds_read_b64_tr_b16 a[110:111], v5 offset:58368\n"
"v_mfma_f32_16x16x32_bf16 v[140:143], a[60:63], a[4:7], v[140:143]\n"
"ds_read_b128 a[84:87], v4 offset:53248\n"
"buffer_load_dwordx4 v26, s[52:55], s74 offen offset:1024 lds\n"
"v_mfma_f32_16x16x32_bf16 v[156:159], a[60:63], a[12:15], v[156:159]\n"
"ds_read_b64_tr_b16 a[116:117], v6 offset:57344\n"
"v_mfma_f32_16x16x32_bf16 v[152:155], a[52:55], a[12:15], v[152:155]\n"
"ds_read_b64_tr_b16 a[118:119], v6 offset:58368\n"
"v_mfma_f32_16x16x32_bf16 v[148:151], a[44:47], a[12:15], v[148:151]\n"
"ds_read_b128 a[92:95], v4 offset:55296\n"
"v_mfma_f32_16x16x32_bf16 v[144:147], a[36:39], a[12:15], v[144:147]\n"
"ds_read_b64_tr_b16 a[124:125], v7 offset:57344\n"
"buffer_load_dwordx4 v27, s[52:55], s74 offen offset:2048 lds\n"
"v_mfma_f32_16x16x32_bf16 v[160:163], a[36:39], a[20:23], v[160:163]\n"
"ds_read_b64_tr_b16 a[126:127], v7 offset:58368\n"
"buffer_load_dwordx4 a[140:143], v0, s[20:23], s71 offen offset:3072 sc1\n"
"v_mfma_f32_16x16x32_bf16 v[164:167], a[44:47], a[20:23], v[164:167]\n"
"v_mfma_f32_16x16x32_bf16 v[168:171], a[52:55], a[20:23], v[168:171]\n"
"v_mfma_f32_16x16x32_bf16 v[172:175], a[60:63], a[20:23], v[172:175]\n"
"buffer_load_dwordx4 v28, s[52:55], s74 offen offset:3072 lds\n"
"s_add_u32 s74, s74, 655360\n"
"v_mfma_f32_16x16x32_bf16 v[188:191], a[60:63], a[28:31], v[188:191]\n"
"v_mfma_f32_16x16x32_bf16 v[184:187], a[52:55], a[28:31], v[184:187]\n"
"v_mfma_f32_16x16x32_bf16 v[180:183], a[44:47], a[28:31], v[180:183]\n"
"v_mfma_f32_16x16x32_bf16 v[176:179], a[36:39], a[28:31], v[176:179]\n"
"s_waitcnt vmcnt(20) lgkmcnt(0)\n"
"s_barrier\n"
"v_mfma_f32_16x16x32_bf16 v[128:131], a[96:99], a[64:67], v[128:131]\n"
"ds_read_b128 a[0:3], v23 offset:0\n"
"s_add_u32 m0, s76, 49152\n"
"v_mfma_f32_16x16x32_bf16 v[132:135], a[104:107], a[64:67], v[132:135]\n"
"ds_read_b64_tr_b16 a[32:33], v24 offset:0\n"
"buffer_load_dwordx4 v0, s[20:23], s72 offen offset:0 sc1 lds\n"
"v_mfma_f32_16x16x32_bf16 v[136:139], a[112:115], a[64:67], v[136:139]\n"
"ds_read_b64_tr_b16 a[34:35], v24 offset:1024\n"
"v_mfma_f32_16x16x32_bf16 v[140:143], a[120:123], a[64:67], v[140:143]\n"
"ds_read_b128 a[8:11], v23 offset:2048\n"
"v_mfma_f32_16x16x32_bf16 v[156:159], a[120:123], a[72:75], v[156:159]\n"
"ds_read_b64_tr_b16 a[40:41], v5 offset:0\n"
"buffer_load_dwordx4 v0, s[20:23], s72 offen offset:1024 sc1 lds\n"
"v_mfma_f32_16x16x32_bf16 v[152:155], a[112:115], a[72:75], v[152:155]\n"
"ds_read_b64_tr_b16 a[42:43], v5 offset:1024\n"
"v_mfma_f32_16x16x32_bf16 v[148:151], a[104:107], a[72:75], v[148:151]\n"
"ds_read_b128 a[16:19], v23 offset:4096\n"
"v_mfma_f32_16x16x32_bf16 v[144:147], a[96:99], a[72:75], v[144:147]\n"
"ds_read_b64_tr_b16 a[48:49], v6 offset:0\n"
"v_mfma_f32_16x16x32_bf16 v[160:163], a[96:99], a[80:83], v[160:163]\n"
"ds_read_b64_tr_b16 a[50:51], v6 offset:1024\n"
"buffer_load_dwordx4 v0, s[20:23], s72 offen offset:2048 sc1 lds\n"
"v_mfma_f32_16x16x32_bf16 v[164:167], a[104:107], a[80:83], v[164:167]\n"
"ds_read_b128 a[24:27], v23 offset:6144\n"
"v_mfma_f32_16x16x32_bf16 v[168:171], a[112:115], a[80:83], v[168:171]\n"
"ds_read_b64_tr_b16 a[56:57], v7 offset:0\n"
"v_mfma_f32_16x16x32_bf16 v[172:175], a[120:123], a[80:83], v[172:175]\n"
"ds_read_b64_tr_b16 a[58:59], v7 offset:1024\n"
"v_mfma_f32_16x16x32_bf16 v[188:191], a[120:123], a[88:91], v[188:191]\n"
"buffer_load_dwordx4 v0, s[20:23], s72 offen offset:3072 sc1 lds\n"
"s_add_u32 s72, s72, 16384\n"
"v_mfma_f32_16x16x32_bf16 v[184:187], a[112:115], a[88:91], v[184:187]\n"
"ds_read_b128 a[4:7], v13 offset:0\n"
"v_mfma_f32_16x16x32_bf16 v[180:183], a[104:107], a[88:91], v[180:183]\n"
"ds_read_b64_tr_b16 a[36:37], v24 offset:8192\n"
"v_mfma_f32_16x16x32_bf16 v[176:179], a[96:99], a[88:91], v[176:179]\n"
"ds_read_b64_tr_b16 a[38:39], v24 offset:9216\n"
"s_add_u32 m0, s77, 49152\n"
"v_mfma_f32_16x16x32_bf16 v[128:131], a[100:103], a[68:71], v[128:131]\n"
"ds_read_b128 a[12:15], v1 offset:0\n"
"buffer_load_dwordx4 v25, s[52:55], s74 offen offset:0 lds\n"
"v_mfma_f32_16x16x32_bf16 v[132:135], a[108:111], a[68:71], v[132:135]\n"
"ds_read_b64_tr_b16 a[44:45], v5 offset:8192\n"
"v_mfma_f32_16x16x32_bf16 v[136:139], a[116:119], a[68:71], v[136:139]\n"
"ds_read_b64_tr_b16 a[46:47], v5 offset:9216\n"
"v_mfma_f32_16x16x32_bf16 v[140:143], a[124:127], a[68:71], v[140:143]\n"
"ds_read_b128 a[20:23], v13 offset:4096\n"
"buffer_load_dwordx4 v26, s[52:55], s74 offen offset:1024 lds\n"
"v_mfma_f32_16x16x32_bf16 v[156:159], a[124:127], a[76:79], v[156:159]\n"
"ds_read_b64_tr_b16 a[52:53], v6 offset:8192\n"
"v_mfma_f32_16x16x32_bf16 v[152:155], a[116:119], a[76:79], v[152:155]\n"
"ds_read_b64_tr_b16 a[54:55], v6 offset:9216\n"
"v_mfma_f32_16x16x32_bf16 v[148:151], a[108:111], a[76:79], v[148:151]\n"
"ds_read_b128 a[28:31], v1 offset:4096\n"
"v_mfma_f32_16x16x32_bf16 v[144:147], a[100:103], a[76:79], v[144:147]\n"
"ds_read_b64_tr_b16 a[60:61], v7 offset:8192\n"
"buffer_load_dwordx4 v27, s[52:55], s74 offen offset:2048 lds\n"
"v_mfma_f32_16x16x32_bf16 v[160:163], a[100:103], a[84:87], v[160:163]\n"
"ds_read_b64_tr_b16 a[62:63], v7 offset:9216\n"
"v_mfma_f32_16x16x32_bf16 v[164:167], a[108:111], a[84:87], v[164:167]\n"
"v_mfma_f32_16x16x32_bf16 v[168:171], a[116:119], a[84:87], v[168:171]\n"
"v_mfma_f32_16x16x32_bf16 v[172:175], a[124:127], a[84:87], v[172:175]\n"
"buffer_load_dwordx4 v28, s[52:55], s74 offen offset:3072 lds\n"
"s_add_u32 s74, s74, 131072\n"
"v_mfma_f32_16x16x32_bf16 v[188:191], a[124:127], a[92:95], v[188:191]\n"
"v_mfma_f32_16x16x32_bf16 v[184:187], a[116:119], a[92:95], v[184:187]\n"
"v_mfma_f32_16x16x32_bf16 v[180:183], a[108:111], a[92:95], v[180:183]\n"
"v_mfma_f32_16x16x32_bf16 v[176:179], a[100:103], a[92:95], v[176:179]\n"
"s_waitcnt vmcnt(20) lgkmcnt(0)\n"
"s_barrier\n"
"s_waitcnt vmcnt(9)\n"
"v_mfma_f32_16x16x32_bf16 v[128:131], a[32:35], a[0:3], v[128:131]\n"
"ds_read_b128 a[64:67], v23 offset:16384\n"
"s_add_u32 m0, s76, 0\n"
"v_mfma_f32_16x16x32_bf16 v[32:35], a[128:131], v[64:67], 0\n"
"v_mfma_f32_16x16x32_bf16 v[36:39], a[136:139], v[64:67], 0\n"
"v_mfma_f32_16x16x32_bf16 v[40:43], v[240:243], v[64:67], 0\n"
"v_mfma_f32_16x16x32_bf16 v[132:135], a[40:43], a[0:3], v[132:135]\n"
"ds_read_b64_tr_b16 a[96:97], v24 offset:16384\n"
"buffer_load_dwordx4 v0, s[20:23], s72 offen offset:0 sc1 lds\n"
"v_mfma_f32_16x16x32_bf16 v[32:35], a[132:135], v[68:71], v[32:35]\n"
"v_mfma_f32_16x16x32_bf16 v[36:39], a[140:143], v[68:71], v[36:39]\n"
"v_mfma_f32_16x16x32_bf16 v[136:139], a[48:51], a[0:3], v[136:139]\n"
"ds_read_b64_tr_b16 a[98:99], v24 offset:17408\n"
"v_mfma_f32_16x16x32_bf16 v[40:43], v[244:247], v[68:71], v[40:43]\n"
"v_mfma_f32_16x16x32_bf16 v[48:51], a[128:131], v[72:75], 0\n"
"v_mfma_f32_16x16x32_bf16 v[140:143], a[56:59], a[0:3], v[140:143]\n"
"ds_read_b128 a[72:75], v23 offset:18432\n"
"v_mfma_f32_16x16x32_bf16 v[52:55], a[136:139], v[72:75], 0\n"
"v_mfma_f32_16x16x32_bf16 v[56:59], v[240:243], v[72:75], 0\n"
"v_mfma_f32_16x16x32_bf16 v[156:159], a[56:59], a[8:11], v[156:159]\n"
"ds_read_b64_tr_b16 a[104:105], v5 offset:16384\n"
"buffer_load_dwordx4 v0, s[20:23], s72 offen offset:1024 sc1 lds\n"
"v_mfma_f32_16x16x32_bf16 v[48:51], a[132:135], v[76:79], v[48:51]\n"
"v_mfma_f32_16x16x32_bf16 v[52:55], a[140:143], v[76:79], v[52:55]\n"
"v_mfma_f32_16x16x32_bf16 v[56:59], v[244:247], v[76:79], v[56:59]\n"
"v_mfma_f32_16x16x32_bf16 v[152:155], a[48:51], a[8:11], v[152:155]\n"
"ds_read_b64_tr_b16 a[106:107], v5 offset:17408\n"
"v_add_f32 v40, 0x358637bd, v40\n"
"v_rcp_f32 v40, v40\n"
"s_mov_b32 s95, 0\n"
"v_mfma_f32_16x16x32_bf16 v[148:151], a[40:43], a[8:11], v[148:151]\n"
"ds_read_b128 a[80:83], v23 offset:20480\n"
"v_mov_b32 v41, v40\n"
"v_pk_mul_f32 v[32:33], v[32:33], v[40:41]\n"
"v_mfma_f32_16x16x32_bf16 v[144:147], a[32:35], a[8:11], v[144:147]\n"
"ds_read_b64_tr_b16 a[112:113], v6 offset:16384\n"
"v_pk_mul_f32 v[34:35], v[34:35], v[40:41]\n"
"v_pk_mul_f32 v[36:37], v[36:37], v[40:41]\n"
"v_mfma_f32_16x16x32_bf16 v[160:163], a[32:35], a[16:19], v[160:163]\n"
"ds_read_b64_tr_b16 a[114:115], v6 offset:17408\n"
"buffer_load_dwordx4 v0, s[20:23], s72 offen offset:2048 sc1 lds\n"
"v_pk_mul_f32 v[38:39], v[38:39], v[40:41]\n"
"v_cvt_pk_bf16_f32 v44, v32, v33\n"
"v_cvt_pk_bf16_f32 v45, v34, v35\n"
"v_mfma_f32_16x16x32_bf16 v[164:167], a[40:43], a[16:19], v[164:167]\n"
"ds_read_b128 a[88:91], v23 offset:22528\n"
"v_cvt_pk_bf16_f32 v46, v36, v37\n"
"v_cvt_pk_bf16_f32 v47, v38, v39\n"
"v_mfma_f32_16x16x32_bf16 v[168:171], a[48:51], a[16:19], v[168:171]\n"
"ds_read_b64_tr_b16 a[120:121], v7 offset:16384\n"
"buffer_store_dwordx4 v[44:47], v21, s[20:23], s95 offen offset:0 sc1\n"
"v_mfma_f32_16x16x32_bf16 v[32:35], a[128:131], v[80:83], 0\n"
"v_mfma_f32_16x16x32_bf16 v[172:175], a[56:59], a[16:19], v[172:175]\n"
"ds_read_b64_tr_b16 a[122:123], v7 offset:17408\n"
"v_mfma_f32_16x16x32_bf16 v[36:39], a[136:139], v[80:83], 0\n"
"v_mfma_f32_16x16x32_bf16 v[40:43], v[240:243], v[80:83], 0\n"
"v_mfma_f32_16x16x32_bf16 v[188:191], a[56:59], a[24:27], v[188:191]\n"
"buffer_load_dwordx4 v0, s[20:23], s72 offen offset:3072 sc1 lds\n"
"s_add_u32 s72, s72, 16384\n"
"v_mfma_f32_16x16x32_bf16 v[32:35], a[132:135], v[84:87], v[32:35]\n"
"v_mfma_f32_16x16x32_bf16 v[36:39], a[140:143], v[84:87], v[36:39]\n"
"v_mfma_f32_16x16x32_bf16 v[40:43], v[244:247], v[84:87], v[40:43]\n"
"v_mfma_f32_16x16x32_bf16 v[184:187], a[48:51], a[24:27], v[184:187]\n"
"ds_read_b128 a[68:71], v4 offset:16384\n"
"v_add_f32 v56, 0x358637bd, v56\n"
"v_rcp_f32 v56, v56\n"
"v_mfma_f32_16x16x32_bf16 v[180:183], a[40:43], a[24:27], v[180:183]\n"
"ds_read_b64_tr_b16 a[100:101], v24 offset:24576\n"
"v_mov_b32 v57, v56\n"
"v_pk_mul_f32 v[48:49], v[48:49], v[56:57]\n"
"v_mfma_f32_16x16x32_bf16 v[176:179], a[32:35], a[24:27], v[176:179]\n"
"ds_read_b64_tr_b16 a[102:103], v24 offset:25600\n"
"s_add_u32 m0, s77, 0\n"
"v_pk_mul_f32 v[50:51], v[50:51], v[56:57]\n"
"v_pk_mul_f32 v[52:53], v[52:53], v[56:57]\n"
"v_mfma_f32_16x16x32_bf16 v[128:131], a[36:39], a[4:7], v[128:131]\n"
"ds_read_b128 a[76:79], v4 offset:18432\n"
"buffer_load_dwordx4 v25, s[52:55], s74 offen offset:0 lds\n"
"v_pk_mul_f32 v[54:55], v[54:55], v[56:57]\n"
"v_cvt_pk_bf16_f32 v60, v48, v49\n"
"v_cvt_pk_bf16_f32 v61, v50, v51\n"
"v_mfma_f32_16x16x32_bf16 v[132:135], a[44:47], a[4:7], v[132:135]\n"
"ds_read_b64_tr_b16 a[108:109], v5 offset:24576\n"
"v_cvt_pk_bf16_f32 v62, v52, v53\n"
"v_cvt_pk_bf16_f32 v63, v54, v55\n"
"v_mfma_f32_16x16x32_bf16 v[136:139], a[52:55], a[4:7], v[136:139]\n"
"ds_read_b64_tr_b16 a[110:111], v5 offset:25600\n"
"buffer_store_dwordx4 v[60:63], v21, s[20:23], s95 offen offset:2048 sc1\n"
"v_mfma_f32_16x16x32_bf16 v[48:51], a[128:131], v[88:91], 0\n"
"v_mfma_f32_16x16x32_bf16 v[140:143], a[60:63], a[4:7], v[140:143]\n"
"ds_read_b128 a[84:87], v4 offset:20480\n"
"buffer_load_dwordx4 v26, s[52:55], s74 offen offset:1024 lds\n"
"v_mfma_f32_16x16x32_bf16 v[52:55], a[136:139], v[88:91], 0\n"
"v_mfma_f32_16x16x32_bf16 v[56:59], v[240:243], v[88:91], 0\n"
"v_mfma_f32_16x16x32_bf16 v[156:159], a[60:63], a[12:15], v[156:159]\n"
"ds_read_b64_tr_b16 a[116:117], v6 offset:24576\n"
"v_mfma_f32_16x16x32_bf16 v[48:51], a[132:135], v[92:95], v[48:51]\n"
"v_mfma_f32_16x16x32_bf16 v[52:55], a[140:143], v[92:95], v[52:55]\n"
"v_mfma_f32_16x16x32_bf16 v[56:59], v[244:247], v[92:95], v[56:59]\n"
"v_mfma_f32_16x16x32_bf16 v[152:155], a[52:55], a[12:15], v[152:155]\n"
"ds_read_b64_tr_b16 a[118:119], v6 offset:25600\n"
"v_add_f32 v40, 0x358637bd, v40\n"
"v_rcp_f32 v40, v40\n"
"s_mov_b32 s95, 4096\n"
"v_mfma_f32_16x16x32_bf16 v[148:151], a[44:47], a[12:15], v[148:151]\n"
"ds_read_b128 a[92:95], v4 offset:22528\n"
"v_mov_b32 v41, v40\n"
"v_pk_mul_f32 v[32:33], v[32:33], v[40:41]\n"
"v_mfma_f32_16x16x32_bf16 v[144:147], a[36:39], a[12:15], v[144:147]\n"
"ds_read_b64_tr_b16 a[124:125], v7 offset:24576\n"
"buffer_load_dwordx4 v27, s[52:55], s74 offen offset:2048 lds\n"
"v_pk_mul_f32 v[34:35], v[34:35], v[40:41]\n"
"v_pk_mul_f32 v[36:37], v[36:37], v[40:41]\n"
"v_mfma_f32_16x16x32_bf16 v[160:163], a[36:39], a[20:23], v[160:163]\n"
"ds_read_b64_tr_b16 a[126:127], v7 offset:25600\n"
"v_pk_mul_f32 v[38:39], v[38:39], v[40:41]\n"
"v_cvt_pk_bf16_f32 v44, v32, v33\n"
"v_cvt_pk_bf16_f32 v45, v34, v35\n"
"v_mfma_f32_16x16x32_bf16 v[164:167], a[44:47], a[20:23], v[164:167]\n"
"v_cvt_pk_bf16_f32 v46, v36, v37\n"
"v_cvt_pk_bf16_f32 v47, v38, v39\n"
"v_mfma_f32_16x16x32_bf16 v[168:171], a[52:55], a[20:23], v[168:171]\n"
"buffer_store_dwordx4 v[44:47], v21, s[20:23], s95 offen offset:0 sc1\n"
"v_mfma_f32_16x16x32_bf16 v[32:35], a[128:131], v[96:99], 0\n"
"v_mfma_f32_16x16x32_bf16 v[172:175], a[60:63], a[20:23], v[172:175]\n"
"buffer_load_dwordx4 v28, s[52:55], s74 offen offset:3072 lds\n"
"s_add_u32 s74, s74, 131072\n"
"v_mfma_f32_16x16x32_bf16 v[36:39], a[136:139], v[96:99], 0\n"
"v_mfma_f32_16x16x32_bf16 v[40:43], v[240:243], v[96:99], 0\n"
"v_mfma_f32_16x16x32_bf16 v[188:191], a[60:63], a[28:31], v[188:191]\n"
"v_mfma_f32_16x16x32_bf16 v[32:35], a[132:135], v[100:103], v[32:35]\n"
"v_mfma_f32_16x16x32_bf16 v[36:39], a[140:143], v[100:103], v[36:39]\n"
"v_mfma_f32_16x16x32_bf16 v[40:43], v[244:247], v[100:103], v[40:43]\n"
"v_mfma_f32_16x16x32_bf16 v[184:187], a[52:55], a[28:31], v[184:187]\n"
"v_add_f32 v56, 0x358637bd, v56\n"
"v_rcp_f32 v56, v56\n"
"v_mfma_f32_16x16x32_bf16 v[180:183], a[44:47], a[28:31], v[180:183]\n"
"v_mov_b32 v57, v56\n"
"v_pk_mul_f32 v[48:49], v[48:49], v[56:57]\n"
"v_mfma_f32_16x16x32_bf16 v[176:179], a[36:39], a[28:31], v[176:179]\n"
"v_pk_mul_f32 v[50:51], v[50:51], v[56:57]\n"
"v_pk_mul_f32 v[52:53], v[52:53], v[56:57]\n"
"s_waitcnt vmcnt(19) lgkmcnt(0)\n"
"s_barrier\n"
"v_mfma_f32_16x16x32_bf16 v[128:131], a[96:99], a[64:67], v[128:131]\n"
"ds_read_b128 a[0:3], v11 offset:32768\n"
"s_add_u32 m0, s76, 16384\n"
"v_pk_mul_f32 v[54:55], v[54:55], v[56:57]\n"
"v_cvt_pk_bf16_f32 v60, v48, v49\n"
"v_cvt_pk_bf16_f32 v61, v50, v51\n"
"v_mfma_f32_16x16x32_bf16 v[132:135], a[104:107], a[64:67], v[132:135]\n"
"ds_read_b64_tr_b16 a[32:33], v24 offset:32768\n"
"buffer_load_dwordx4 v0, s[20:23], s72 offen offset:0 sc1 lds\n"
"v_cvt_pk_bf16_f32 v62, v52, v53\n"
"v_cvt_pk_bf16_f32 v63, v54, v55\n"
"v_mfma_f32_16x16x32_bf16 v[136:139], a[112:115], a[64:67], v[136:139]\n"
"ds_read_b64_tr_b16 a[34:35], v24 offset:33792\n"
"buffer_store_dwordx4 v[60:63], v21, s[20:23], s95 offen offset:2048 sc1\n"
"v_mfma_f32_16x16x32_bf16 v[48:51], a[128:131], v[104:107], 0\n"
"v_mfma_f32_16x16x32_bf16 v[140:143], a[120:123], a[64:67], v[140:143]\n"
"ds_read_b128 a[8:11], v12 offset:32768\n"
"v_mfma_f32_16x16x32_bf16 v[52:55], a[136:139], v[104:107], 0\n"
"v_mfma_f32_16x16x32_bf16 v[56:59], v[240:243], v[104:107], 0\n"
"v_mfma_f32_16x16x32_bf16 v[156:159], a[120:123], a[72:75], v[156:159]\n"
"ds_read_b64_tr_b16 a[40:41], v5 offset:32768\n"
"buffer_load_dwordx4 v0, s[20:23], s72 offen offset:1024 sc1 lds\n"
"v_mfma_f32_16x16x32_bf16 v[48:51], a[132:135], v[108:111], v[48:51]\n"
"v_mfma_f32_16x16x32_bf16 v[52:55], a[140:143], v[108:111], v[52:55]\n"
"v_mfma_f32_16x16x32_bf16 v[56:59], v[244:247], v[108:111], v[56:59]\n"
"v_mfma_f32_16x16x32_bf16 v[152:155], a[112:115], a[72:75], v[152:155]\n"
"ds_read_b64_tr_b16 a[42:43], v5 offset:33792\n"
"v_add_f32 v40, 0x358637bd, v40\n"
"v_rcp_f32 v40, v40\n"
"s_mov_b32 s95, 8192\n"
"v_mfma_f32_16x16x32_bf16 v[148:151], a[104:107], a[72:75], v[148:151]\n"
"ds_read_b128 a[16:19], v11 offset:36864\n"
"v_mov_b32 v41, v40\n"
"v_pk_mul_f32 v[32:33], v[32:33], v[40:41]\n"
"v_mfma_f32_16x16x32_bf16 v[144:147], a[96:99], a[72:75], v[144:147]\n"
"ds_read_b64_tr_b16 a[48:49], v6 offset:32768\n"
"v_pk_mul_f32 v[34:35], v[34:35], v[40:41]\n"
"v_pk_mul_f32 v[36:37], v[36:37], v[40:41]\n"
"v_mfma_f32_16x16x32_bf16 v[160:163], a[96:99], a[80:83], v[160:163]\n"
"ds_read_b64_tr_b16 a[50:51], v6 offset:33792\n"
"buffer_load_dwordx4 v0, s[20:23], s72 offen offset:2048 sc1 lds\n"
"v_pk_mul_f32 v[38:39], v[38:39], v[40:41]\n"
"v_cvt_pk_bf16_f32 v44, v32, v33\n"
"v_cvt_pk_bf16_f32 v45, v34, v35\n"
"v_mfma_f32_16x16x32_bf16 v[164:167], a[104:107], a[80:83], v[164:167]\n"
"ds_read_b128 a[24:27], v12 offset:36864\n"
"v_cvt_pk_bf16_f32 v46, v36, v37\n"
"v_cvt_pk_bf16_f32 v47, v38, v39\n"
"v_mfma_f32_16x16x32_bf16 v[168:171], a[112:115], a[80:83], v[168:171]\n"
"ds_read_b64_tr_b16 a[56:57], v7 offset:32768\n"
"buffer_store_dwordx4 v[44:47], v21, s[20:23], s95 offen offset:0 sc1\n"
"v_mfma_f32_16x16x32_bf16 v[32:35], a[128:131], v[112:115], 0\n"
"v_mfma_f32_16x16x32_bf16 v[172:175], a[120:123], a[80:83], v[172:175]\n"
"ds_read_b64_tr_b16 a[58:59], v7 offset:33792\n"
"v_mfma_f32_16x16x32_bf16 v[36:39], a[136:139], v[112:115], 0\n"
"v_mfma_f32_16x16x32_bf16 v[40:43], v[240:243], v[112:115], 0\n"
"v_mfma_f32_16x16x32_bf16 v[188:191], a[120:123], a[88:91], v[188:191]\n"
"buffer_load_dwordx4 v0, s[20:23], s72 offen offset:3072 sc1 lds\n"
"s_add_u32 s72, s72, 81920\n"
"v_mfma_f32_16x16x32_bf16 v[32:35], a[132:135], v[116:119], v[32:35]\n"
"v_mfma_f32_16x16x32_bf16 v[36:39], a[140:143], v[116:119], v[36:39]\n"
"v_mfma_f32_16x16x32_bf16 v[40:43], v[244:247], v[116:119], v[40:43]\n"
"v_mfma_f32_16x16x32_bf16 v[184:187], a[112:115], a[88:91], v[184:187]\n"
"ds_read_b128 a[4:7], v4 offset:32768\n"
"v_add_f32 v56, 0x358637bd, v56\n"
"v_rcp_f32 v56, v56\n"
"v_mfma_f32_16x16x32_bf16 v[180:183], a[104:107], a[88:91], v[180:183]\n"
"ds_read_b64_tr_b16 a[36:37], v24 offset:40960\n"
"v_mov_b32 v57, v56\n"
"v_pk_mul_f32 v[48:49], v[48:49], v[56:57]\n"
"v_mfma_f32_16x16x32_bf16 v[176:179], a[96:99], a[88:91], v[176:179]\n"
"ds_read_b64_tr_b16 a[38:39], v24 offset:41984\n"
"s_add_u32 m0, s77, 16384\n"
"v_pk_mul_f32 v[50:51], v[50:51], v[56:57]\n"
"v_pk_mul_f32 v[52:53], v[52:53], v[56:57]\n"
"v_mfma_f32_16x16x32_bf16 v[128:131], a[100:103], a[68:71], v[128:131]\n"
"ds_read_b128 a[12:15], v4 offset:34816\n"
"buffer_load_dwordx4 v25, s[52:55], s74 offen offset:0 lds\n"
"v_pk_mul_f32 v[54:55], v[54:55], v[56:57]\n"
"v_cvt_pk_bf16_f32 v60, v48, v49\n"
"v_cvt_pk_bf16_f32 v61, v50, v51\n"
"v_mfma_f32_16x16x32_bf16 v[132:135], a[108:111], a[68:71], v[132:135]\n"
"ds_read_b64_tr_b16 a[44:45], v5 offset:40960\n"
"v_cvt_pk_bf16_f32 v62, v52, v53\n"
"v_cvt_pk_bf16_f32 v63, v54, v55\n"
"v_mfma_f32_16x16x32_bf16 v[136:139], a[116:119], a[68:71], v[136:139]\n"
"ds_read_b64_tr_b16 a[46:47], v5 offset:41984\n"
"buffer_store_dwordx4 v[60:63], v21, s[20:23], s95 offen offset:2048 sc1\n"
"v_mfma_f32_16x16x32_bf16 v[48:51], a[128:131], v[120:123], 0\n"
"v_mfma_f32_16x16x32_bf16 v[140:143], a[124:127], a[68:71], v[140:143]\n"
"ds_read_b128 a[20:23], v4 offset:36864\n"
"buffer_load_dwordx4 v26, s[52:55], s74 offen offset:1024 lds\n"
"v_mfma_f32_16x16x32_bf16 v[52:55], a[136:139], v[120:123], 0\n"
"v_mfma_f32_16x16x32_bf16 v[56:59], v[240:243], v[120:123], 0\n"
"v_mfma_f32_16x16x32_bf16 v[156:159], a[124:127], a[76:79], v[156:159]\n"
"ds_read_b64_tr_b16 a[52:53], v6 offset:40960\n"
"v_mfma_f32_16x16x32_bf16 v[48:51], a[132:135], v[124:127], v[48:51]\n"
"v_mfma_f32_16x16x32_bf16 v[52:55], a[140:143], v[124:127], v[52:55]\n"
"v_mfma_f32_16x16x32_bf16 v[56:59], v[244:247], v[124:127], v[56:59]\n"
"v_mfma_f32_16x16x32_bf16 v[152:155], a[116:119], a[76:79], v[152:155]\n"
"ds_read_b64_tr_b16 a[54:55], v6 offset:41984\n"
"v_add_f32 v40, 0x358637bd, v40\n"
"v_rcp_f32 v40, v40\n"
"s_mov_b32 s95, 12288\n"
"v_mfma_f32_16x16x32_bf16 v[148:151], a[108:111], a[76:79], v[148:151]\n"
"ds_read_b128 a[28:31], v4 offset:38912\n"
"v_mov_b32 v41, v40\n"
"v_pk_mul_f32 v[32:33], v[32:33], v[40:41]\n"
"v_mfma_f32_16x16x32_bf16 v[144:147], a[100:103], a[76:79], v[144:147]\n"
"ds_read_b64_tr_b16 a[60:61], v7 offset:40960\n"
"buffer_load_dwordx4 v27, s[52:55], s74 offen offset:2048 lds\n"
"v_pk_mul_f32 v[34:35], v[34:35], v[40:41]\n"
"v_pk_mul_f32 v[36:37], v[36:37], v[40:41]\n"
"v_mfma_f32_16x16x32_bf16 v[160:163], a[100:103], a[84:87], v[160:163]\n"
"ds_read_b64_tr_b16 a[62:63], v7 offset:41984\n"
"v_pk_mul_f32 v[38:39], v[38:39], v[40:41]\n"
"v_cvt_pk_bf16_f32 v44, v32, v33\n"
"v_cvt_pk_bf16_f32 v45, v34, v35\n"
"v_mfma_f32_16x16x32_bf16 v[164:167], a[108:111], a[84:87], v[164:167]\n"
"v_cvt_pk_bf16_f32 v46, v36, v37\n"
"v_cvt_pk_bf16_f32 v47, v38, v39\n"
"v_mfma_f32_16x16x32_bf16 v[168:171], a[116:119], a[84:87], v[168:171]\n"
"buffer_store_dwordx4 v[44:47], v21, s[20:23], s95 offen offset:0 sc1\n"
"v_add_f32 v56, 0x358637bd, v56\n"
"v_mfma_f32_16x16x32_bf16 v[172:175], a[124:127], a[84:87], v[172:175]\n"
"buffer_load_dwordx4 v28, s[52:55], s74 offen offset:3072 lds\n"
"s_add_u32 s74, s74, 655360\n"
"v_rcp_f32 v56, v56\n"
"s_nop 0\n"
"v_mov_b32 v57, v56\n"
"v_mfma_f32_16x16x32_bf16 v[188:191], a[124:127], a[92:95], v[188:191]\n"
"v_pk_mul_f32 v[48:49], v[48:49], v[56:57]\n"
"v_pk_mul_f32 v[50:51], v[50:51], v[56:57]\n"
"v_pk_mul_f32 v[52:53], v[52:53], v[56:57]\n"
"v_mfma_f32_16x16x32_bf16 v[184:187], a[116:119], a[92:95], v[184:187]\n"
"v_pk_mul_f32 v[54:55], v[54:55], v[56:57]\n"
"v_cvt_pk_bf16_f32 v60, v48, v49\n"
"v_mfma_f32_16x16x32_bf16 v[180:183], a[108:111], a[92:95], v[180:183]\n"
"v_cvt_pk_bf16_f32 v61, v50, v51\n"
"v_cvt_pk_bf16_f32 v62, v52, v53\n"
"v_mfma_f32_16x16x32_bf16 v[176:179], a[100:103], a[92:95], v[176:179]\n"
"v_cvt_pk_bf16_f32 v63, v54, v55\n"
"buffer_store_dwordx4 v[60:63], v21, s[20:23], s95 offen offset:2048 sc1\n"
"s_waitcnt vmcnt(24) lgkmcnt(0)\n"
"s_barrier\n"
"v_mfma_f32_16x16x32_bf16 v[128:131], a[32:35], a[0:3], v[128:131]\n"
"ds_read_b128 a[64:67], v23 offset:49152\n"
"s_add_u32 m0, s76, 32768\n"
"v_mfma_f32_16x16x32_bf16 v[132:135], a[40:43], a[0:3], v[132:135]\n"
"ds_read_b64_tr_b16 a[96:97], v24 offset:49152\n"
"buffer_load_dwordx4 v0, s[20:23], s72 offen offset:0 sc1 lds\n"
"v_mfma_f32_16x16x32_bf16 v[136:139], a[48:51], a[0:3], v[136:139]\n"
"ds_read_b64_tr_b16 a[98:99], v24 offset:50176\n"
"v_mfma_f32_16x16x32_bf16 v[140:143], a[56:59], a[0:3], v[140:143]\n"
"ds_read_b128 a[72:75], v23 offset:51200\n"
"v_mfma_f32_16x16x32_bf16 v[156:159], a[56:59], a[8:11], v[156:159]\n"
"ds_read_b64_tr_b16 a[104:105], v5 offset:49152\n"
"buffer_load_dwordx4 v0, s[20:23], s72 offen offset:1024 sc1 lds\n"
"v_mfma_f32_16x16x32_bf16 v[152:155], a[48:51], a[8:11], v[152:155]\n"
"ds_read_b64_tr_b16 a[106:107], v5 offset:50176\n"
"v_mfma_f32_16x16x32_bf16 v[148:151], a[40:43], a[8:11], v[148:151]\n"
"ds_read_b128 a[80:83], v23 offset:53248\n"
"v_mfma_f32_16x16x32_bf16 v[144:147], a[32:35], a[8:11], v[144:147]\n"
"ds_read_b64_tr_b16 a[112:113], v6 offset:49152\n"
"v_mfma_f32_16x16x32_bf16 v[160:163], a[32:35], a[16:19], v[160:163]\n"
"ds_read_b64_tr_b16 a[114:115], v6 offset:50176\n"
"buffer_load_dwordx4 v0, s[20:23], s72 offen offset:2048 sc1 lds\n"
"v_mfma_f32_16x16x32_bf16 v[164:167], a[40:43], a[16:19], v[164:167]\n"
"ds_read_b128 a[88:91], v23 offset:55296\n"
"v_mfma_f32_16x16x32_bf16 v[168:171], a[48:51], a[16:19], v[168:171]\n"
"ds_read_b64_tr_b16 a[120:121], v7 offset:49152\n"
"v_mfma_f32_16x16x32_bf16 v[172:175], a[56:59], a[16:19], v[172:175]\n"
"ds_read_b64_tr_b16 a[122:123], v7 offset:50176\n"
"v_mfma_f32_16x16x32_bf16 v[188:191], a[56:59], a[24:27], v[188:191]\n"
"buffer_load_dwordx4 v0, s[20:23], s72 offen offset:3072 sc1 lds\n"
"s_add_u32 s72, s72, 16384\n"
"v_mfma_f32_16x16x32_bf16 v[184:187], a[48:51], a[24:27], v[184:187]\n"
"ds_read_b128 a[68:71], v13 offset:49152\n"
"v_mfma_f32_16x16x32_bf16 v[180:183], a[40:43], a[24:27], v[180:183]\n"
"ds_read_b64_tr_b16 a[100:101], v24 offset:57344\n"
"v_mfma_f32_16x16x32_bf16 v[176:179], a[32:35], a[24:27], v[176:179]\n"
"ds_read_b64_tr_b16 a[102:103], v24 offset:58368\n"
"s_add_u32 m0, s77, 32768\n"
"v_mfma_f32_16x16x32_bf16 v[128:131], a[36:39], a[4:7], v[128:131]\n"
"ds_read_b128 a[76:79], v1 offset:49152\n"
"buffer_load_dwordx4 v25, s[52:55], s74 offen offset:0 lds\n"
"v_mfma_f32_16x16x32_bf16 v[132:135], a[44:47], a[4:7], v[132:135]\n"
"ds_read_b64_tr_b16 a[108:109], v5 offset:57344\n"
"v_mfma_f32_16x16x32_bf16 v[136:139], a[52:55], a[4:7], v[136:139]\n"
"ds_read_b64_tr_b16 a[110:111], v5 offset:58368\n"
"v_mfma_f32_16x16x32_bf16 v[140:143], a[60:63], a[4:7], v[140:143]\n"
"ds_read_b128 a[84:87], v13 offset:53248\n"
"buffer_load_dwordx4 v26, s[52:55], s74 offen offset:1024 lds\n"
"v_mfma_f32_16x16x32_bf16 v[156:159], a[60:63], a[12:15], v[156:159]\n"
"ds_read_b64_tr_b16 a[116:117], v6 offset:57344\n"
"v_mfma_f32_16x16x32_bf16 v[152:155], a[52:55], a[12:15], v[152:155]\n"
"ds_read_b64_tr_b16 a[118:119], v6 offset:58368\n"
"v_mfma_f32_16x16x32_bf16 v[148:151], a[44:47], a[12:15], v[148:151]\n"
"ds_read_b128 a[92:95], v1 offset:53248\n"
"v_mfma_f32_16x16x32_bf16 v[144:147], a[36:39], a[12:15], v[144:147]\n"
"ds_read_b64_tr_b16 a[124:125], v7 offset:57344\n"
"buffer_load_dwordx4 v27, s[52:55], s74 offen offset:2048 lds\n"
"v_mfma_f32_16x16x32_bf16 v[160:163], a[36:39], a[20:23], v[160:163]\n"
"ds_read_b64_tr_b16 a[126:127], v7 offset:58368\n"
"v_mfma_f32_16x16x32_bf16 v[164:167], a[44:47], a[20:23], v[164:167]\n"
"v_mfma_f32_16x16x32_bf16 v[168:171], a[52:55], a[20:23], v[168:171]\n"
"v_mfma_f32_16x16x32_bf16 v[172:175], a[60:63], a[20:23], v[172:175]\n"
"buffer_load_dwordx4 v28, s[52:55], s74 offen offset:3072 lds\n"
"s_add_u32 s74, s74, 131072\n"
"v_mfma_f32_16x16x32_bf16 v[188:191], a[60:63], a[28:31], v[188:191]\n"
"v_mfma_f32_16x16x32_bf16 v[184:187], a[52:55], a[28:31], v[184:187]\n"
"v_mfma_f32_16x16x32_bf16 v[180:183], a[44:47], a[28:31], v[180:183]\n"
"v_mfma_f32_16x16x32_bf16 v[176:179], a[36:39], a[28:31], v[176:179]\n"
"s_waitcnt vmcnt(21) lgkmcnt(0)\n"
"s_barrier\n"
"v_mfma_f32_16x16x32_bf16 v[128:131], a[96:99], a[64:67], v[128:131]\n"
"ds_read_b128 a[0:3], v23 offset:0\n"
"s_add_u32 m0, s76, 49152\n"
"v_mfma_f32_16x16x32_bf16 v[132:135], a[104:107], a[64:67], v[132:135]\n"
"ds_read_b64_tr_b16 a[32:33], v24 offset:0\n"
"buffer_load_dwordx4 v0, s[20:23], s72 offen offset:0 sc1 lds\n"
"v_mfma_f32_16x16x32_bf16 v[136:139], a[112:115], a[64:67], v[136:139]\n"
"ds_read_b64_tr_b16 a[34:35], v24 offset:1024\n"
"v_mfma_f32_16x16x32_bf16 v[140:143], a[120:123], a[64:67], v[140:143]\n"
"ds_read_b128 a[8:11], v23 offset:2048\n"
"v_mfma_f32_16x16x32_bf16 v[156:159], a[120:123], a[72:75], v[156:159]\n"
"ds_read_b64_tr_b16 a[40:41], v5 offset:0\n"
"buffer_load_dwordx4 v0, s[20:23], s72 offen offset:1024 sc1 lds\n"
"v_mfma_f32_16x16x32_bf16 v[152:155], a[112:115], a[72:75], v[152:155]\n"
"ds_read_b64_tr_b16 a[42:43], v5 offset:1024\n"
"v_mfma_f32_16x16x32_bf16 v[148:151], a[104:107], a[72:75], v[148:151]\n"
"ds_read_b128 a[16:19], v23 offset:4096\n"
"v_mfma_f32_16x16x32_bf16 v[144:147], a[96:99], a[72:75], v[144:147]\n"
"ds_read_b64_tr_b16 a[48:49], v6 offset:0\n"
"v_mfma_f32_16x16x32_bf16 v[160:163], a[96:99], a[80:83], v[160:163]\n"
"ds_read_b64_tr_b16 a[50:51], v6 offset:1024\n"
"buffer_load_dwordx4 v0, s[20:23], s72 offen offset:2048 sc1 lds\n"
"v_mfma_f32_16x16x32_bf16 v[164:167], a[104:107], a[80:83], v[164:167]\n"
"ds_read_b128 a[24:27], v23 offset:6144\n"
"v_mfma_f32_16x16x32_bf16 v[168:171], a[112:115], a[80:83], v[168:171]\n"
"ds_read_b64_tr_b16 a[56:57], v7 offset:0\n"
"v_mfma_f32_16x16x32_bf16 v[172:175], a[120:123], a[80:83], v[172:175]\n"
"ds_read_b64_tr_b16 a[58:59], v7 offset:1024\n"
"v_mfma_f32_16x16x32_bf16 v[188:191], a[120:123], a[88:91], v[188:191]\n"
"buffer_load_dwordx4 v0, s[20:23], s72 offen offset:3072 sc1 lds\n"
"s_add_u32 s72, s72, 16384\n"
"v_mfma_f32_16x16x32_bf16 v[184:187], a[112:115], a[88:91], v[184:187]\n"
"ds_read_b128 a[4:7], v4 offset:0\n"
"v_mfma_f32_16x16x32_bf16 v[180:183], a[104:107], a[88:91], v[180:183]\n"
"ds_read_b64_tr_b16 a[36:37], v24 offset:8192\n"
"v_mfma_f32_16x16x32_bf16 v[176:179], a[96:99], a[88:91], v[176:179]\n"
"ds_read_b64_tr_b16 a[38:39], v24 offset:9216\n"
"s_add_u32 m0, s77, 49152\n"
"v_mfma_f32_16x16x32_bf16 v[128:131], a[100:103], a[68:71], v[128:131]\n"
"ds_read_b128 a[12:15], v4 offset:2048\n"
"buffer_load_dwordx4 v25, s[52:55], s74 offen offset:0 lds\n"
"v_mfma_f32_16x16x32_bf16 v[132:135], a[108:111], a[68:71], v[132:135]\n"
"ds_read_b64_tr_b16 a[44:45], v5 offset:8192\n"
"v_mfma_f32_16x16x32_bf16 v[136:139], a[116:119], a[68:71], v[136:139]\n"
"ds_read_b64_tr_b16 a[46:47], v5 offset:9216\n"
"v_mfma_f32_16x16x32_bf16 v[140:143], a[124:127], a[68:71], v[140:143]\n"
"ds_read_b128 a[20:23], v4 offset:4096\n"
"buffer_load_dwordx4 v26, s[52:55], s74 offen offset:1024 lds\n"
"v_mfma_f32_16x16x32_bf16 v[156:159], a[124:127], a[76:79], v[156:159]\n"
"ds_read_b64_tr_b16 a[52:53], v6 offset:8192\n"
"v_mfma_f32_16x16x32_bf16 v[152:155], a[116:119], a[76:79], v[152:155]\n"
"ds_read_b64_tr_b16 a[54:55], v6 offset:9216\n"
"v_mfma_f32_16x16x32_bf16 v[148:151], a[108:111], a[76:79], v[148:151]\n"
"ds_read_b128 a[28:31], v4 offset:6144\n"
"v_mfma_f32_16x16x32_bf16 v[144:147], a[100:103], a[76:79], v[144:147]\n"
"ds_read_b64_tr_b16 a[60:61], v7 offset:8192\n"
"buffer_load_dwordx4 v27, s[52:55], s74 offen offset:2048 lds\n"
"v_mfma_f32_16x16x32_bf16 v[160:163], a[100:103], a[84:87], v[160:163]\n"
"ds_read_b64_tr_b16 a[62:63], v7 offset:9216\n"
"v_mfma_f32_16x16x32_bf16 v[164:167], a[108:111], a[84:87], v[164:167]\n"
"v_mfma_f32_16x16x32_bf16 v[168:171], a[116:119], a[84:87], v[168:171]\n"
"v_mfma_f32_16x16x32_bf16 v[172:175], a[124:127], a[84:87], v[172:175]\n"
"buffer_load_dwordx4 v28, s[52:55], s74 offen offset:3072 lds\n"
"s_add_u32 s74, s74, 131072\n"
"v_mfma_f32_16x16x32_bf16 v[188:191], a[124:127], a[92:95], v[188:191]\n"
"v_mfma_f32_16x16x32_bf16 v[184:187], a[116:119], a[92:95], v[184:187]\n"
"v_mfma_f32_16x16x32_bf16 v[180:183], a[108:111], a[92:95], v[180:183]\n"
"v_mfma_f32_16x16x32_bf16 v[176:179], a[100:103], a[92:95], v[176:179]\n"
"s_waitcnt vmcnt(17) lgkmcnt(0)\n"
"s_barrier\n"
"s_waitcnt vmcnt(16)\n"
"v_mfma_f32_16x16x32_bf16 v[128:131], a[32:35], a[0:3], v[128:131]\n"
"ds_read_b128 a[64:67], v11 offset:16384\n"
"s_add_u32 m0, s76, 0\n"
"s_mov_b64 exec, 1\n"
"buffer_store_dwordx2 v[2:3], off, s[20:23], s83 sc1\n"
"s_mov_b64 exec, -1\n"
"v_mfma_f32_16x16x32_bf16 v[132:135], a[40:43], a[0:3], v[132:135]\n"
"ds_read_b64_tr_b16 a[96:97], v24 offset:16384\n"
"buffer_load_dwordx4 v0, s[20:23], s72 offen offset:0 sc1 lds\n"
"v_mfma_f32_16x16x32_bf16 v[136:139], a[48:51], a[0:3], v[136:139]\n"
"ds_read_b64_tr_b16 a[98:99], v24 offset:17408\n"
"v_lshrrev_b32 v16, 7, v0\n"
"v_mfma_f32_16x16x32_bf16 v[140:143], a[56:59], a[0:3], v[140:143]\n"
"ds_read_b128 a[72:75], v12 offset:16384\n"
"v_mfma_f32_16x16x32_bf16 v[156:159], a[56:59], a[8:11], v[156:159]\n"
"ds_read_b64_tr_b16 a[104:105], v5 offset:16384\n"
"buffer_load_dwordx4 v0, s[20:23], s72 offen offset:1024 sc1 lds\n"
"v_mfma_f32_16x16x32_bf16 v[152:155], a[48:51], a[8:11], v[152:155]\n"
"ds_read_b64_tr_b16 a[106:107], v5 offset:17408\n"
"v_and_b32 v17, 0x70, v0\n"
"v_mfma_f32_16x16x32_bf16 v[148:151], a[40:43], a[8:11], v[148:151]\n"
"ds_read_b128 a[80:83], v11 offset:20480\n"
"v_mfma_f32_16x16x32_bf16 v[144:147], a[32:35], a[8:11], v[144:147]\n"
"ds_read_b64_tr_b16 a[112:113], v6 offset:16384\n"
"v_mfma_f32_16x16x32_bf16 v[160:163], a[32:35], a[16:19], v[160:163]\n"
"ds_read_b64_tr_b16 a[114:115], v6 offset:17408\n"
"buffer_load_dwordx4 v0, s[20:23], s72 offen offset:2048 sc1 lds\n"
"v_lshlrev_b32 v17, 3, v17\n"
"v_mfma_f32_16x16x32_bf16 v[164:167], a[40:43], a[16:19], v[164:167]\n"
"ds_read_b128 a[88:91], v12 offset:20480\n"
"v_mfma_f32_16x16x32_bf16 v[168:171], a[48:51], a[16:19], v[168:171]\n"
"ds_read_b64_tr_b16 a[120:121], v7 offset:16384\n"
"v_lshl_or_b32 v16, v16, 11, v17\n"
"v_mfma_f32_16x16x32_bf16 v[172:175], a[56:59], a[16:19], v[172:175]\n"
"ds_read_b64_tr_b16 a[122:123], v7 offset:17408\n"
"v_mfma_f32_16x16x32_bf16 v[188:191], a[56:59], a[24:27], v[188:191]\n"
"buffer_load_dwordx4 v0, s[20:23], s72 offen offset:3072 sc1 lds\n"
"s_add_u32 s72, s72, 81920\n"
"v_mfma_f32_16x16x32_bf16 v[184:187], a[48:51], a[24:27], v[184:187]\n"
"ds_read_b128 a[68:71], v4 offset:16384\n"
"s_bfe_u32 s0, s85, 0x50012\n"
"v_mfma_f32_16x16x32_bf16 v[180:183], a[40:43], a[24:27], v[180:183]\n"
"ds_read_b64_tr_b16 a[100:101], v24 offset:24576\n"
"v_mfma_f32_16x16x32_bf16 v[176:179], a[32:35], a[24:27], v[176:179]\n"
"ds_read_b64_tr_b16 a[102:103], v24 offset:25600\n"
"s_add_u32 m0, s77, 0\n"
"v_mfma_f32_16x16x32_bf16 v[128:131], a[36:39], a[4:7], v[128:131]\n"
"ds_read_b128 a[76:79], v4 offset:18432\n"
"buffer_load_dwordx4 v25, s[52:55], s74 offen offset:0 lds\n"
"s_bfe_u32 s1, s85, 0x2000d\n"
"v_mfma_f32_16x16x32_bf16 v[132:135], a[44:47], a[4:7], v[132:135]\n"
"ds_read_b64_tr_b16 a[108:109], v5 offset:24576\n"
"v_mfma_f32_16x16x32_bf16 v[136:139], a[52:55], a[4:7], v[136:139]\n"
"ds_read_b64_tr_b16 a[110:111], v5 offset:25600\n"
"s_lshl2_add_u32 s0, s0, s1\n"
"v_mfma_f32_16x16x32_bf16 v[140:143], a[60:63], a[4:7], v[140:143]\n"
"ds_read_b128 a[84:87], v4 offset:20480\n"
"buffer_load_dwordx4 v26, s[52:55], s74 offen offset:1024 lds\n"
"v_mfma_f32_16x16x32_bf16 v[156:159], a[60:63], a[12:15], v[156:159]\n"
"ds_read_b64_tr_b16 a[116:117], v6 offset:24576\n"
"v_mfma_f32_16x16x32_bf16 v[152:155], a[52:55], a[12:15], v[152:155]\n"
"ds_read_b64_tr_b16 a[118:119], v6 offset:25600\n"
"s_bfe_u32 s1, s85, 65552\n"
"v_mfma_f32_16x16x32_bf16 v[148:151], a[44:47], a[12:15], v[148:151]\n"
"ds_read_b128 a[92:95], v4 offset:22528\n"
"v_mfma_f32_16x16x32_bf16 v[144:147], a[36:39], a[12:15], v[144:147]\n"
"ds_read_b64_tr_b16 a[124:125], v7 offset:24576\n"
"buffer_load_dwordx4 v27, s[52:55], s74 offen offset:2048 lds\n"
"v_mfma_f32_16x16x32_bf16 v[160:163], a[36:39], a[20:23], v[160:163]\n"
"ds_read_b64_tr_b16 a[126:127], v7 offset:25600\n"
"s_lshl4_add_u32 s0, s0, s1\n"
"v_mfma_f32_16x16x32_bf16 v[164:167], a[44:47], a[20:23], v[164:167]\n"
"v_mfma_f32_16x16x32_bf16 v[168:171], a[52:55], a[20:23], v[168:171]\n"
"s_add_u32 s0, s0, 4\n"
"v_mfma_f32_16x16x32_bf16 v[172:175], a[60:63], a[20:23], v[172:175]\n"
"buffer_load_dwordx4 v28, s[52:55], s74 offen offset:3072 lds\n"
"s_add_u32 s74, s74, 655360\n"
"v_mfma_f32_16x16x32_bf16 v[188:191], a[60:63], a[28:31], v[188:191]\n"
"v_mfma_f32_16x16x32_bf16 v[184:187], a[52:55], a[28:31], v[184:187]\n"
"s_lshl_b32 s0, s0, 10\n"
"v_mfma_f32_16x16x32_bf16 v[180:183], a[44:47], a[28:31], v[180:183]\n"
"v_mfma_f32_16x16x32_bf16 v[176:179], a[36:39], a[28:31], v[176:179]\n"
"s_waitcnt vmcnt(17) lgkmcnt(0)\n"
"s_barrier\n"
"v_mfma_f32_16x16x32_bf16 v[128:131], a[96:99], a[64:67], v[128:131]\n"
"ds_read_b128 a[0:3], v23 offset:32768\n"
"s_add_u32 m0, s76, 16384\n"
"v_mfma_f32_16x16x32_bf16 v[132:135], a[104:107], a[64:67], v[132:135]\n"
"ds_read_b64_tr_b16 a[32:33], v24 offset:32768\n"
"buffer_load_dwordx4 v0, s[20:23], s72 offen offset:0 sc1 lds\n"
"v_mfma_f32_16x16x32_bf16 v[136:139], a[112:115], a[64:67], v[136:139]\n"
"ds_read_b64_tr_b16 a[34:35], v24 offset:33792\n"
"v_mfma_f32_16x16x32_bf16 v[140:143], a[120:123], a[64:67], v[140:143]\n"
"ds_read_b128 a[8:11], v23 offset:34816\n"
"v_mfma_f32_16x16x32_bf16 v[156:159], a[120:123], a[72:75], v[156:159]\n"
"ds_read_b64_tr_b16 a[40:41], v5 offset:32768\n"
"buffer_load_dwordx4 v0, s[20:23], s72 offen offset:1024 sc1 lds\n"
"v_mfma_f32_16x16x32_bf16 v[152:155], a[112:115], a[72:75], v[152:155]\n"
"ds_read_b64_tr_b16 a[42:43], v5 offset:33792\n"
"v_mfma_f32_16x16x32_bf16 v[148:151], a[104:107], a[72:75], v[148:151]\n"
"ds_read_b128 a[16:19], v23 offset:36864\n"
"v_mfma_f32_16x16x32_bf16 v[144:147], a[96:99], a[72:75], v[144:147]\n"
"ds_read_b64_tr_b16 a[48:49], v6 offset:32768\n"
"v_mfma_f32_16x16x32_bf16 v[160:163], a[96:99], a[80:83], v[160:163]\n"
"ds_read_b64_tr_b16 a[50:51], v6 offset:33792\n"
"buffer_load_dwordx4 v0, s[20:23], s72 offen offset:2048 sc1 lds\n"
"v_mfma_f32_16x16x32_bf16 v[164:167], a[104:107], a[80:83], v[164:167]\n"
"ds_read_b128 a[24:27], v23 offset:38912\n"
"v_mfma_f32_16x16x32_bf16 v[168:171], a[112:115], a[80:83], v[168:171]\n"
"ds_read_b64_tr_b16 a[56:57], v7 offset:32768\n"
"v_mfma_f32_16x16x32_bf16 v[172:175], a[120:123], a[80:83], v[172:175]\n"
"ds_read_b64_tr_b16 a[58:59], v7 offset:33792\n"
"v_mfma_f32_16x16x32_bf16 v[188:191], a[120:123], a[88:91], v[188:191]\n"
"buffer_load_dwordx4 v0, s[20:23], s72 offen offset:3072 sc1 lds\n"
"s_add_u32 s72, s72, 16384\n"
"v_mfma_f32_16x16x32_bf16 v[184:187], a[112:115], a[88:91], v[184:187]\n"
"ds_read_b128 a[4:7], v13 offset:32768\n"
"v_mfma_f32_16x16x32_bf16 v[180:183], a[104:107], a[88:91], v[180:183]\n"
"ds_read_b64_tr_b16 a[36:37], v24 offset:40960\n"
"v_mfma_f32_16x16x32_bf16 v[176:179], a[96:99], a[88:91], v[176:179]\n"
"ds_read_b64_tr_b16 a[38:39], v24 offset:41984\n"
"s_add_u32 m0, s77, 16384\n"
"v_mfma_f32_16x16x32_bf16 v[128:131], a[100:103], a[68:71], v[128:131]\n"
"ds_read_b128 a[12:15], v1 offset:32768\n"
"buffer_load_dwordx4 v25, s[52:55], s74 offen offset:0 lds\n"
"v_mfma_f32_16x16x32_bf16 v[132:135], a[108:111], a[68:71], v[132:135]\n"
"ds_read_b64_tr_b16 a[44:45], v5 offset:40960\n"
"v_mfma_f32_16x16x32_bf16 v[136:139], a[116:119], a[68:71], v[136:139]\n"
"ds_read_b64_tr_b16 a[46:47], v5 offset:41984\n"
"v_mfma_f32_16x16x32_bf16 v[140:143], a[124:127], a[68:71], v[140:143]\n"
"ds_read_b128 a[20:23], v13 offset:36864\n"
"buffer_load_dwordx4 v26, s[52:55], s74 offen offset:1024 lds\n"
"v_mfma_f32_16x16x32_bf16 v[156:159], a[124:127], a[76:79], v[156:159]\n"
"ds_read_b64_tr_b16 a[52:53], v6 offset:40960\n"
"v_mfma_f32_16x16x32_bf16 v[152:155], a[116:119], a[76:79], v[152:155]\n"
"ds_read_b64_tr_b16 a[54:55], v6 offset:41984\n"
"v_mfma_f32_16x16x32_bf16 v[148:151], a[108:111], a[76:79], v[148:151]\n"
"ds_read_b128 a[28:31], v1 offset:36864\n"
"v_mfma_f32_16x16x32_bf16 v[144:147], a[100:103], a[76:79], v[144:147]\n"
"ds_read_b64_tr_b16 a[60:61], v7 offset:40960\n"
"buffer_load_dwordx4 v27, s[52:55], s74 offen offset:2048 lds\n"
"v_mfma_f32_16x16x32_bf16 v[160:163], a[100:103], a[84:87], v[160:163]\n"
"ds_read_b64_tr_b16 a[62:63], v7 offset:41984\n"
"v_mfma_f32_16x16x32_bf16 v[164:167], a[108:111], a[84:87], v[164:167]\n"
"v_mfma_f32_16x16x32_bf16 v[168:171], a[116:119], a[84:87], v[168:171]\n"
"v_mfma_f32_16x16x32_bf16 v[172:175], a[124:127], a[84:87], v[172:175]\n"
"buffer_load_dwordx4 v28, s[52:55], s74 offen offset:3072 lds\n"
"s_add_u32 s74, s74, 131072\n"
"v_mfma_f32_16x16x32_bf16 v[188:191], a[124:127], a[92:95], v[188:191]\n"
"v_mfma_f32_16x16x32_bf16 v[184:187], a[116:119], a[92:95], v[184:187]\n"
"v_mfma_f32_16x16x32_bf16 v[180:183], a[108:111], a[92:95], v[180:183]\n"
"v_mfma_f32_16x16x32_bf16 v[176:179], a[100:103], a[92:95], v[176:179]\n"
"s_waitcnt vmcnt(17) lgkmcnt(0)\n"
"s_barrier\n"
"v_mfma_f32_16x16x32_bf16 v[128:131], a[32:35], a[0:3], v[128:131]\n"
"ds_read_b128 a[64:67], v23 offset:49152\n"
"s_add_u32 m0, s76, 32768\n"
"v_mfma_f32_16x16x32_bf16 v[132:135], a[40:43], a[0:3], v[132:135]\n"
"ds_read_b64_tr_b16 a[96:97], v24 offset:49152\n"
"buffer_load_dwordx4 v0, s[20:23], s72 offen offset:0 sc1 lds\n"
"v_mfma_f32_16x16x32_bf16 v[136:139], a[48:51], a[0:3], v[136:139]\n"
"ds_read_b64_tr_b16 a[98:99], v24 offset:50176\n"
"v_mfma_f32_16x16x32_bf16 v[140:143], a[56:59], a[0:3], v[140:143]\n"
"ds_read_b128 a[72:75], v23 offset:51200\n"
"v_mfma_f32_16x16x32_bf16 v[156:159], a[56:59], a[8:11], v[156:159]\n"
"ds_read_b64_tr_b16 a[104:105], v5 offset:49152\n"
"buffer_load_dwordx4 v0, s[20:23], s72 offen offset:1024 sc1 lds\n"
"v_mfma_f32_16x16x32_bf16 v[152:155], a[48:51], a[8:11], v[152:155]\n"
"ds_read_b64_tr_b16 a[106:107], v5 offset:50176\n"
"v_mfma_f32_16x16x32_bf16 v[148:151], a[40:43], a[8:11], v[148:151]\n"
"ds_read_b128 a[80:83], v23 offset:53248\n"
"v_mfma_f32_16x16x32_bf16 v[144:147], a[32:35], a[8:11], v[144:147]\n"
"ds_read_b64_tr_b16 a[112:113], v6 offset:49152\n"
"v_mfma_f32_16x16x32_bf16 v[160:163], a[32:35], a[16:19], v[160:163]\n"
"ds_read_b64_tr_b16 a[114:115], v6 offset:50176\n"
"buffer_load_dwordx4 v0, s[20:23], s72 offen offset:2048 sc1 lds\n"
"v_mfma_f32_16x16x32_bf16 v[164:167], a[40:43], a[16:19], v[164:167]\n"
"ds_read_b128 a[88:91], v23 offset:55296\n"
"v_mfma_f32_16x16x32_bf16 v[168:171], a[48:51], a[16:19], v[168:171]\n"
"ds_read_b64_tr_b16 a[120:121], v7 offset:49152\n"
"v_mfma_f32_16x16x32_bf16 v[172:175], a[56:59], a[16:19], v[172:175]\n"
"ds_read_b64_tr_b16 a[122:123], v7 offset:50176\n"
"v_mfma_f32_16x16x32_bf16 v[188:191], a[56:59], a[24:27], v[188:191]\n"
"buffer_load_dwordx4 v0, s[20:23], s72 offen offset:3072 sc1 lds\n"
"s_add_u32 s72, s72, 16384\n"
"v_mfma_f32_16x16x32_bf16 v[184:187], a[48:51], a[24:27], v[184:187]\n"
"ds_read_b128 a[68:71], v4 offset:49152\n"
"v_mfma_f32_16x16x32_bf16 v[180:183], a[40:43], a[24:27], v[180:183]\n"
"ds_read_b64_tr_b16 a[100:101], v24 offset:57344\n"
"v_mfma_f32_16x16x32_bf16 v[176:179], a[32:35], a[24:27], v[176:179]\n"
"ds_read_b64_tr_b16 a[102:103], v24 offset:58368\n"
"s_add_u32 m0, s77, 32768\n"
"v_mfma_f32_16x16x32_bf16 v[128:131], a[36:39], a[4:7], v[128:131]\n"
"ds_read_b128 a[76:79], v4 offset:51200\n"
"buffer_load_dwordx4 v25, s[52:55], s74 offen offset:0 lds\n"
"v_mfma_f32_16x16x32_bf16 v[132:135], a[44:47], a[4:7], v[132:135]\n"
"ds_read_b64_tr_b16 a[108:109], v5 offset:57344\n"
"v_mfma_f32_16x16x32_bf16 v[136:139], a[52:55], a[4:7], v[136:139]\n"
"ds_read_b64_tr_b16 a[110:111], v5 offset:58368\n"
"v_mfma_f32_16x16x32_bf16 v[140:143], a[60:63], a[4:7], v[140:143]\n"
"ds_read_b128 a[84:87], v4 offset:53248\n"
"buffer_load_dwordx4 v26, s[52:55], s74 offen offset:1024 lds\n"
"v_mfma_f32_16x16x32_bf16 v[156:159], a[60:63], a[12:15], v[156:159]\n"
"ds_read_b64_tr_b16 a[116:117], v6 offset:57344\n"
"v_mfma_f32_16x16x32_bf16 v[152:155], a[52:55], a[12:15], v[152:155]\n"
"ds_read_b64_tr_b16 a[118:119], v6 offset:58368\n"
"v_mfma_f32_16x16x32_bf16 v[148:151], a[44:47], a[12:15], v[148:151]\n"
"ds_read_b128 a[92:95], v4 offset:55296\n"
"v_mfma_f32_16x16x32_bf16 v[144:147], a[36:39], a[12:15], v[144:147]\n"
"ds_read_b64_tr_b16 a[124:125], v7 offset:57344\n"
"buffer_load_dwordx4 v27, s[52:55], s74 offen offset:2048 lds\n"
"v_mfma_f32_16x16x32_bf16 v[160:163], a[36:39], a[20:23], v[160:163]\n"
"ds_read_b64_tr_b16 a[126:127], v7 offset:58368\n"
"v_mfma_f32_16x16x32_bf16 v[164:167], a[44:47], a[20:23], v[164:167]\n"
"v_mfma_f32_16x16x32_bf16 v[168:171], a[52:55], a[20:23], v[168:171]\n"
"v_mfma_f32_16x16x32_bf16 v[172:175], a[60:63], a[20:23], v[172:175]\n"
"buffer_load_dwordx4 v28, s[52:55], s74 offen offset:3072 lds\n"
"s_add_u32 s74, s74, 131072\n"
"v_mfma_f32_16x16x32_bf16 v[188:191], a[60:63], a[28:31], v[188:191]\n"
"v_mfma_f32_16x16x32_bf16 v[184:187], a[52:55], a[28:31], v[184:187]\n"
"v_mfma_f32_16x16x32_bf16 v[180:183], a[44:47], a[28:31], v[180:183]\n"
"v_mfma_f32_16x16x32_bf16 v[176:179], a[36:39], a[28:31], v[176:179]\n"
"s_waitcnt vmcnt(16) lgkmcnt(0)\n"
"s_barrier\n"
"v_mfma_f32_16x16x32_bf16 v[128:131], a[96:99], a[64:67], v[128:131]\n"
"ds_read_b128 a[0:3], v11 offset:0\n"
"s_add_u32 m0, s76, 49152\n"
"v_lshlrev_b32 v17, 2, v0\n"
"v_mfma_f32_16x16x32_bf16 v[132:135], a[104:107], a[64:67], v[132:135]\n"
"ds_read_b64_tr_b16 a[32:33], v24 offset:0\n"
"buffer_load_dwordx4 v0, s[20:23], s72 offen offset:0 sc1 lds\n"
"v_mfma_f32_16x16x32_bf16 v[136:139], a[112:115], a[64:67], v[136:139]\n"
"ds_read_b64_tr_b16 a[34:35], v24 offset:1024\n"
"v_mfma_f32_16x16x32_bf16 v[140:143], a[120:123], a[64:67], v[140:143]\n"
"ds_read_b128 a[8:11], v12 offset:0\n"
"v_mfma_f32_16x16x32_bf16 v[156:159], a[120:123], a[72:75], v[156:159]\n"
"ds_read_b64_tr_b16 a[40:41], v5 offset:0\n"
"buffer_load_dwordx4 v0, s[20:23], s72 offen offset:1024 sc1 lds\n"
"v_mfma_f32_16x16x32_bf16 v[152:155], a[112:115], a[72:75], v[152:155]\n"
"ds_read_b64_tr_b16 a[42:43], v5 offset:1024\n"
"v_mfma_f32_16x16x32_bf16 v[148:151], a[104:107], a[72:75], v[148:151]\n"
"ds_read_b128 a[16:19], v11 offset:4096\n"
"v_mfma_f32_16x16x32_bf16 v[144:147], a[96:99], a[72:75], v[144:147]\n"
"ds_read_b64_tr_b16 a[48:49], v6 offset:0\n"
"v_mfma_f32_16x16x32_bf16 v[160:163], a[96:99], a[80:83], v[160:163]\n"
"ds_read_b64_tr_b16 a[50:51], v6 offset:1024\n"
"buffer_load_dwordx4 v0, s[20:23], s72 offen offset:2048 sc1 lds\n"
"v_mfma_f32_16x16x32_bf16 v[164:167], a[104:107], a[80:83], v[164:167]\n"
"ds_read_b128 a[24:27], v12 offset:4096\n"
"v_mfma_f32_16x16x32_bf16 v[168:171], a[112:115], a[80:83], v[168:171]\n"
"ds_read_b64_tr_b16 a[56:57], v7 offset:0\n"
"v_mfma_f32_16x16x32_bf16 v[172:175], a[120:123], a[80:83], v[172:175]\n"
"ds_read_b64_tr_b16 a[58:59], v7 offset:1024\n"
"v_mfma_f32_16x16x32_bf16 v[188:191], a[120:123], a[88:91], v[188:191]\n"
"buffer_load_dwordx4 v0, s[20:23], s72 offen offset:3072 sc1 lds\n"
"s_add_u32 s72, s72, 16384\n"
"v_mfma_f32_16x16x32_bf16 v[184:187], a[112:115], a[88:91], v[184:187]\n"
"ds_read_b128 a[4:7], v4 offset:0\n"
"v_mfma_f32_16x16x32_bf16 v[180:183], a[104:107], a[88:91], v[180:183]\n"
"ds_read_b64_tr_b16 a[36:37], v24 offset:8192\n"
"v_mfma_f32_16x16x32_bf16 v[176:179], a[96:99], a[88:91], v[176:179]\n"
"ds_read_b64_tr_b16 a[38:39], v24 offset:9216\n"
"s_add_u32 m0, s77, 49152\n"
"v_mfma_f32_16x16x32_bf16 v[128:131], a[100:103], a[68:71], v[128:131]\n"
"ds_read_b128 a[12:15], v4 offset:2048\n"
"buffer_load_dwordx4 v25, s[52:55], s74 offen offset:0 lds\n"
"v_mfma_f32_16x16x32_bf16 v[132:135], a[108:111], a[68:71], v[132:135]\n"
"ds_read_b64_tr_b16 a[44:45], v5 offset:8192\n"
"v_mfma_f32_16x16x32_bf16 v[136:139], a[116:119], a[68:71], v[136:139]\n"
"ds_read_b64_tr_b16 a[46:47], v5 offset:9216\n"
"v_mfma_f32_16x16x32_bf16 v[140:143], a[124:127], a[68:71], v[140:143]\n"
"ds_read_b128 a[20:23], v4 offset:4096\n"
"buffer_load_dwordx4 v26, s[52:55], s74 offen offset:1024 lds\n"
"v_mfma_f32_16x16x32_bf16 v[156:159], a[124:127], a[76:79], v[156:159]\n"
"ds_read_b64_tr_b16 a[52:53], v6 offset:8192\n"
"v_mfma_f32_16x16x32_bf16 v[152:155], a[116:119], a[76:79], v[152:155]\n"
"ds_read_b64_tr_b16 a[54:55], v6 offset:9216\n"
"v_mfma_f32_16x16x32_bf16 v[148:151], a[108:111], a[76:79], v[148:151]\n"
"ds_read_b128 a[28:31], v4 offset:6144\n"
"v_mfma_f32_16x16x32_bf16 v[144:147], a[100:103], a[76:79], v[144:147]\n"
"ds_read_b64_tr_b16 a[60:61], v7 offset:8192\n"
"buffer_load_dwordx4 v27, s[52:55], s74 offen offset:2048 lds\n"
"v_mfma_f32_16x16x32_bf16 v[160:163], a[100:103], a[84:87], v[160:163]\n"
"ds_read_b64_tr_b16 a[62:63], v7 offset:9216\n"
"v_mfma_f32_16x16x32_bf16 v[164:167], a[108:111], a[84:87], v[164:167]\n"
"v_mfma_f32_16x16x32_bf16 v[168:171], a[116:119], a[84:87], v[168:171]\n"
"v_mfma_f32_16x16x32_bf16 v[172:175], a[124:127], a[84:87], v[172:175]\n"
"buffer_load_dwordx4 v28, s[52:55], s74 offen offset:3072 lds\n"
"s_add_u32 s74, s74, 131072\n"
"v_mfma_f32_16x16x32_bf16 v[188:191], a[124:127], a[92:95], v[188:191]\n"
"v_mfma_f32_16x16x32_bf16 v[184:187], a[116:119], a[92:95], v[184:187]\n"
"v_mfma_f32_16x16x32_bf16 v[180:183], a[108:111], a[92:95], v[180:183]\n"
"v_mfma_f32_16x16x32_bf16 v[176:179], a[100:103], a[92:95], v[176:179]\n"
"s_waitcnt vmcnt(16) lgkmcnt(0)\n"
"s_barrier\n"
"s_mov_b32 s72, s73\n"
"s_mov_b32 s74, 0\n"
"s_mov_b32 s52, s56\n"
"s_mov_b32 s53, s57\n"
"s_mov_b32 s54, s58\n"
"s_and_b32 s76, s77, 0x2000\n"
"s_and_b32 s95, s77, 0x1000\n"
"s_lshr_b32 s95, s95, 1\n"
"s_or_b32 s76, s76, s95\n"
"v_mfma_f32_16x16x32_bf16 v[128:131], a[32:35], a[0:3], v[128:131]\n"
"ds_read_b128 a[64:67], v23 offset:16384\n"
"s_add_u32 m0, s77, 0\n"
"v_mfma_f32_16x16x32_bf16 v[132:135], a[40:43], a[0:3], v[132:135]\n"
"ds_read_b64_tr_b16 a[96:97], v24 offset:16384\n"
"buffer_load_dwordx4 v25, s[52:55], s74 offen offset:0 lds\n"
"v_mfma_f32_16x16x32_bf16 v[136:139], a[48:51], a[0:3], v[136:139]\n"
"ds_read_b64_tr_b16 a[98:99], v24 offset:17408\n"
"v_mfma_f32_16x16x32_bf16 v[140:143], a[56:59], a[0:3], v[140:143]\n"
"ds_read_b128 a[72:75], v23 offset:18432\n"
"v_mfma_f32_16x16x32_bf16 v[156:159], a[56:59], a[8:11], v[156:159]\n"
"ds_read_b64_tr_b16 a[104:105], v5 offset:16384\n"
"v_mfma_f32_16x16x32_bf16 v[152:155], a[48:51], a[8:11], v[152:155]\n"
"ds_read_b64_tr_b16 a[106:107], v5 offset:17408\n"
"v_mfma_f32_16x16x32_bf16 v[148:151], a[40:43], a[8:11], v[148:151]\n"
"ds_read_b128 a[80:83], v23 offset:20480\n"
"v_mfma_f32_16x16x32_bf16 v[144:147], a[32:35], a[8:11], v[144:147]\n"
"ds_read_b64_tr_b16 a[112:113], v6 offset:16384\n"
"v_mfma_f32_16x16x32_bf16 v[160:163], a[32:35], a[16:19], v[160:163]\n"
"ds_read_b64_tr_b16 a[114:115], v6 offset:17408\n"
"buffer_load_dwordx4 v26, s[52:55], s74 offen offset:1024 lds\n"
"v_mfma_f32_16x16x32_bf16 v[164:167], a[40:43], a[16:19], v[164:167]\n"
"ds_read_b128 a[88:91], v23 offset:22528\n"
"v_mfma_f32_16x16x32_bf16 v[168:171], a[48:51], a[16:19], v[168:171]\n"
"ds_read_b64_tr_b16 a[120:121], v7 offset:16384\n"
"v_mfma_f32_16x16x32_bf16 v[172:175], a[56:59], a[16:19], v[172:175]\n"
"ds_read_b64_tr_b16 a[122:123], v7 offset:17408\n"
"v_mfma_f32_16x16x32_bf16 v[188:191], a[56:59], a[24:27], v[188:191]\n"
"v_mfma_f32_16x16x32_bf16 v[184:187], a[48:51], a[24:27], v[184:187]\n"
"ds_read_b128 a[68:71], v13 offset:16384\n"
"v_mfma_f32_16x16x32_bf16 v[180:183], a[40:43], a[24:27], v[180:183]\n"
"ds_read_b64_tr_b16 a[100:101], v24 offset:24576\n"
"v_mfma_f32_16x16x32_bf16 v[176:179], a[32:35], a[24:27], v[176:179]\n"
"ds_read_b64_tr_b16 a[102:103], v24 offset:25600\n"
"v_mfma_f32_16x16x32_bf16 v[128:131], a[36:39], a[4:7], v[128:131]\n"
"ds_read_b128 a[76:79], v1 offset:16384\n"
"buffer_load_dwordx4 v27, s[52:55], s74 offen offset:2048 lds\n"
"v_mfma_f32_16x16x32_bf16 v[132:135], a[44:47], a[4:7], v[132:135]\n"
"ds_read_b64_tr_b16 a[108:109], v5 offset:24576\n"
"v_mfma_f32_16x16x32_bf16 v[136:139], a[52:55], a[4:7], v[136:139]\n"
"ds_read_b64_tr_b16 a[110:111], v5 offset:25600\n"
"v_mfma_f32_16x16x32_bf16 v[140:143], a[60:63], a[4:7], v[140:143]\n"
"ds_read_b128 a[84:87], v13 offset:20480\n"
"v_mfma_f32_16x16x32_bf16 v[156:159], a[60:63], a[12:15], v[156:159]\n"
"ds_read_b64_tr_b16 a[116:117], v6 offset:24576\n"
"v_mfma_f32_16x16x32_bf16 v[152:155], a[52:55], a[12:15], v[152:155]\n"
"ds_read_b64_tr_b16 a[118:119], v6 offset:25600\n"
"v_mfma_f32_16x16x32_bf16 v[148:151], a[44:47], a[12:15], v[148:151]\n"
"ds_read_b128 a[92:95], v1 offset:20480\n"
"v_mfma_f32_16x16x32_bf16 v[144:147], a[36:39], a[12:15], v[144:147]\n"
"ds_read_b64_tr_b16 a[124:125], v7 offset:24576\n"
"buffer_load_dwordx4 v28, s[52:55], s74 offen offset:3072 lds\n"
"s_add_u32 s74, s74, 131072\n"
"v_mfma_f32_16x16x32_bf16 v[160:163], a[36:39], a[20:23], v[160:163]\n"
"ds_read_b64_tr_b16 a[126:127], v7 offset:25600\n"
"v_mfma_f32_16x16x32_bf16 v[164:167], a[44:47], a[20:23], v[164:167]\n"
"v_mfma_f32_16x16x32_bf16 v[168:171], a[52:55], a[20:23], v[168:171]\n"
"v_mfma_f32_16x16x32_bf16 v[172:175], a[60:63], a[20:23], v[172:175]\n"
"v_mfma_f32_16x16x32_bf16 v[188:191], a[60:63], a[28:31], v[188:191]\n"
"v_mfma_f32_16x16x32_bf16 v[184:187], a[52:55], a[28:31], v[184:187]\n"
"v_mfma_f32_16x16x32_bf16 v[180:183], a[44:47], a[28:31], v[180:183]\n"
"v_mfma_f32_16x16x32_bf16 v[176:179], a[36:39], a[28:31], v[176:179]\n"
"s_waitcnt vmcnt(12) lgkmcnt(0)\n"
"s_barrier\n"
"v_mfma_f32_16x16x32_bf16 v[128:131], a[96:99], a[64:67], v[128:131]\n"
"ds_read_b128 a[0:3], v23 offset:32768\n"
"s_add_u32 m0, s77, 16384\n"
"v_mfma_f32_16x16x32_bf16 v[132:135], a[104:107], a[64:67], v[132:135]\n"
"ds_read_b64_tr_b16 a[32:33], v24 offset:32768\n"
"buffer_load_dwordx4 v25, s[52:55], s74 offen offset:0 lds\n"
"v_mfma_f32_16x16x32_bf16 v[136:139], a[112:115], a[64:67], v[136:139]\n"
"ds_read_b64_tr_b16 a[34:35], v24 offset:33792\n"
"v_mfma_f32_16x16x32_bf16 v[140:143], a[120:123], a[64:67], v[140:143]\n"
"ds_read_b128 a[8:11], v23 offset:34816\n"
"v_mfma_f32_16x16x32_bf16 v[156:159], a[120:123], a[72:75], v[156:159]\n"
"ds_read_b64_tr_b16 a[40:41], v5 offset:32768\n"
"v_mfma_f32_16x16x32_bf16 v[152:155], a[112:115], a[72:75], v[152:155]\n"
"ds_read_b64_tr_b16 a[42:43], v5 offset:33792\n"
"v_mfma_f32_16x16x32_bf16 v[148:151], a[104:107], a[72:75], v[148:151]\n"
"ds_read_b128 a[16:19], v23 offset:36864\n"
"v_mfma_f32_16x16x32_bf16 v[144:147], a[96:99], a[72:75], v[144:147]\n"
"ds_read_b64_tr_b16 a[48:49], v6 offset:32768\n"
"v_mfma_f32_16x16x32_bf16 v[160:163], a[96:99], a[80:83], v[160:163]\n"
"ds_read_b64_tr_b16 a[50:51], v6 offset:33792\n"
"buffer_load_dwordx4 v26, s[52:55], s74 offen offset:1024 lds\n"
"v_mfma_f32_16x16x32_bf16 v[164:167], a[104:107], a[80:83], v[164:167]\n"
"ds_read_b128 a[24:27], v23 offset:38912\n"
"v_mfma_f32_16x16x32_bf16 v[168:171], a[112:115], a[80:83], v[168:171]\n"
"ds_read_b64_tr_b16 a[56:57], v7 offset:32768\n"
"v_mfma_f32_16x16x32_bf16 v[172:175], a[120:123], a[80:83], v[172:175]\n"
"ds_read_b64_tr_b16 a[58:59], v7 offset:33792\n"
"v_mfma_f32_16x16x32_bf16 v[188:191], a[120:123], a[88:91], v[188:191]\n"
"v_mfma_f32_16x16x32_bf16 v[184:187], a[112:115], a[88:91], v[184:187]\n"
"ds_read_b128 a[4:7], v4 offset:32768\n"
"v_mfma_f32_16x16x32_bf16 v[180:183], a[104:107], a[88:91], v[180:183]\n"
"ds_read_b64_tr_b16 a[36:37], v24 offset:40960\n"
"v_mfma_f32_16x16x32_bf16 v[176:179], a[96:99], a[88:91], v[176:179]\n"
"ds_read_b64_tr_b16 a[38:39], v24 offset:41984\n"
"v_mfma_f32_16x16x32_bf16 v[128:131], a[100:103], a[68:71], v[128:131]\n"
"ds_read_b128 a[12:15], v4 offset:34816\n"
"buffer_load_dwordx4 v27, s[52:55], s74 offen offset:2048 lds\n"
"v_mfma_f32_16x16x32_bf16 v[132:135], a[108:111], a[68:71], v[132:135]\n"
"ds_read_b64_tr_b16 a[44:45], v5 offset:40960\n"
"v_mfma_f32_16x16x32_bf16 v[136:139], a[116:119], a[68:71], v[136:139]\n"
"ds_read_b64_tr_b16 a[46:47], v5 offset:41984\n"
"v_mfma_f32_16x16x32_bf16 v[140:143], a[124:127], a[68:71], v[140:143]\n"
"ds_read_b128 a[20:23], v4 offset:36864\n"
"v_mfma_f32_16x16x32_bf16 v[156:159], a[124:127], a[76:79], v[156:159]\n"
"ds_read_b64_tr_b16 a[52:53], v6 offset:40960\n"
"v_mfma_f32_16x16x32_bf16 v[152:155], a[116:119], a[76:79], v[152:155]\n"
"ds_read_b64_tr_b16 a[54:55], v6 offset:41984\n"
"v_mfma_f32_16x16x32_bf16 v[148:151], a[108:111], a[76:79], v[148:151]\n"
"ds_read_b128 a[28:31], v4 offset:38912\n"
"v_mfma_f32_16x16x32_bf16 v[144:147], a[100:103], a[76:79], v[144:147]\n"
"ds_read_b64_tr_b16 a[60:61], v7 offset:40960\n"
"buffer_load_dwordx4 v28, s[52:55], s74 offen offset:3072 lds\n"
"s_add_u32 s74, s74, 131072\n"
"v_mfma_f32_16x16x32_bf16 v[160:163], a[100:103], a[84:87], v[160:163]\n"
"ds_read_b64_tr_b16 a[62:63], v7 offset:41984\n"
"v_mfma_f32_16x16x32_bf16 v[164:167], a[108:111], a[84:87], v[164:167]\n"
"v_mfma_f32_16x16x32_bf16 v[168:171], a[116:119], a[84:87], v[168:171]\n"
"v_mfma_f32_16x16x32_bf16 v[172:175], a[124:127], a[84:87], v[172:175]\n"
"v_mfma_f32_16x16x32_bf16 v[188:191], a[124:127], a[92:95], v[188:191]\n"
"v_mfma_f32_16x16x32_bf16 v[184:187], a[116:119], a[92:95], v[184:187]\n"
"v_mfma_f32_16x16x32_bf16 v[180:183], a[108:111], a[92:95], v[180:183]\n"
"v_mfma_f32_16x16x32_bf16 v[176:179], a[100:103], a[92:95], v[176:179]\n"
"s_waitcnt vmcnt(8) lgkmcnt(0)\n"
"s_barrier\n"
"v_mfma_f32_16x16x32_bf16 v[128:131], a[32:35], a[0:3], v[128:131]\n"
"ds_read_b128 a[64:67], v11 offset:49152\n"
"s_add_u32 m0, s77, 32768\n"
"buffer_load_dwordx2 v[250:251], v10, s[20:23], s84 offen sc1\n"
"v_mfma_f32_16x16x32_bf16 v[132:135], a[40:43], a[0:3], v[132:135]\n"
"ds_read_b64_tr_b16 a[96:97], v24 offset:49152\n"
"buffer_load_dwordx4 v25, s[52:55], s74 offen offset:0 lds\n"
"v_mfma_f32_16x16x32_bf16 v[136:139], a[48:51], a[0:3], v[136:139]\n"
"ds_read_b64_tr_b16 a[98:99], v24 offset:50176\n"
"v_mfma_f32_16x16x32_bf16 v[140:143], a[56:59], a[0:3], v[140:143]\n"
"ds_read_b128 a[72:75], v12 offset:49152\n"
"v_mfma_f32_16x16x32_bf16 v[156:159], a[56:59], a[8:11], v[156:159]\n"
"ds_read_b64_tr_b16 a[104:105], v5 offset:49152\n"
"v_mfma_f32_16x16x32_bf16 v[152:155], a[48:51], a[8:11], v[152:155]\n"
"ds_read_b64_tr_b16 a[106:107], v5 offset:50176\n"
"v_mfma_f32_16x16x32_bf16 v[148:151], a[40:43], a[8:11], v[148:151]\n"
"ds_read_b128 a[80:83], v11 offset:53248\n"
"v_mfma_f32_16x16x32_bf16 v[144:147], a[32:35], a[8:11], v[144:147]\n"
"ds_read_b64_tr_b16 a[112:113], v6 offset:49152\n"
"v_mfma_f32_16x16x32_bf16 v[160:163], a[32:35], a[16:19], v[160:163]\n"
"ds_read_b64_tr_b16 a[114:115], v6 offset:50176\n"
"buffer_load_dwordx4 v26, s[52:55], s74 offen offset:1024 lds\n"
"v_mfma_f32_16x16x32_bf16 v[164:167], a[40:43], a[16:19], v[164:167]\n"
"ds_read_b128 a[88:91], v12 offset:53248\n"
"v_mfma_f32_16x16x32_bf16 v[168:171], a[48:51], a[16:19], v[168:171]\n"
"ds_read_b64_tr_b16 a[120:121], v7 offset:49152\n"
"v_mfma_f32_16x16x32_bf16 v[172:175], a[56:59], a[16:19], v[172:175]\n"
"ds_read_b64_tr_b16 a[122:123], v7 offset:50176\n"
"v_mfma_f32_16x16x32_bf16 v[188:191], a[56:59], a[24:27], v[188:191]\n"
"v_mfma_f32_16x16x32_bf16 v[184:187], a[48:51], a[24:27], v[184:187]\n"
"ds_read_b128 a[68:71], v4 offset:49152\n"
"v_mfma_f32_16x16x32_bf16 v[180:183], a[40:43], a[24:27], v[180:183]\n"
"ds_read_b64_tr_b16 a[100:101], v24 offset:57344\n"
"v_mfma_f32_16x16x32_bf16 v[176:179], a[32:35], a[24:27], v[176:179]\n"
"ds_read_b64_tr_b16 a[102:103], v24 offset:58368\n"
"v_mfma_f32_16x16x32_bf16 v[128:131], a[36:39], a[4:7], v[128:131]\n"
"ds_read_b128 a[76:79], v4 offset:51200\n"
"buffer_load_dwordx4 v27, s[52:55], s74 offen offset:2048 lds\n"
"v_mfma_f32_16x16x32_bf16 v[132:135], a[44:47], a[4:7], v[132:135]\n"
"ds_read_b64_tr_b16 a[108:109], v5 offset:57344\n"
"v_mfma_f32_16x16x32_bf16 v[136:139], a[52:55], a[4:7], v[136:139]\n"
"ds_read_b64_tr_b16 a[110:111], v5 offset:58368\n"
"v_mfma_f32_16x16x32_bf16 v[140:143], a[60:63], a[4:7], v[140:143]\n"
"ds_read_b128 a[84:87], v4 offset:53248\n"
"v_mfma_f32_16x16x32_bf16 v[156:159], a[60:63], a[12:15], v[156:159]\n"
"ds_read_b64_tr_b16 a[116:117], v6 offset:57344\n"
"v_mfma_f32_16x16x32_bf16 v[152:155], a[52:55], a[12:15], v[152:155]\n"
"ds_read_b64_tr_b16 a[118:119], v6 offset:58368\n"
"v_mfma_f32_16x16x32_bf16 v[148:151], a[44:47], a[12:15], v[148:151]\n"
"ds_read_b128 a[92:95], v4 offset:55296\n"
"v_mfma_f32_16x16x32_bf16 v[144:147], a[36:39], a[12:15], v[144:147]\n"
"ds_read_b64_tr_b16 a[124:125], v7 offset:57344\n"
"buffer_load_dwordx4 v28, s[52:55], s74 offen offset:3072 lds\n"
"s_add_u32 s74, s74, 131072\n"
"v_mfma_f32_16x16x32_bf16 v[160:163], a[36:39], a[20:23], v[160:163]\n"
"ds_read_b64_tr_b16 a[126:127], v7 offset:58368\n"
"v_mfma_f32_16x16x32_bf16 v[164:167], a[44:47], a[20:23], v[164:167]\n"
"v_mfma_f32_16x16x32_bf16 v[168:171], a[52:55], a[20:23], v[168:171]\n"
"v_mfma_f32_16x16x32_bf16 v[172:175], a[60:63], a[20:23], v[172:175]\n"
"v_mfma_f32_16x16x32_bf16 v[188:191], a[60:63], a[28:31], v[188:191]\n"
"v_mfma_f32_16x16x32_bf16 v[184:187], a[52:55], a[28:31], v[184:187]\n"
"v_mfma_f32_16x16x32_bf16 v[180:183], a[44:47], a[28:31], v[180:183]\n"
"v_mfma_f32_16x16x32_bf16 v[176:179], a[36:39], a[28:31], v[176:179]\n"
"s_waitcnt vmcnt(9) lgkmcnt(0)\n"
"s_barrier\n"
"v_mfma_f32_16x16x32_bf16 v[128:131], a[96:99], a[64:67], v[128:131]\n"
"ds_read_b64_tr_b16 a[32:33], v24 offset:0\n"
"s_add_u32 m0, s77, 49152\n"
"v_mfma_f32_16x16x32_bf16 v[132:135], a[104:107], a[64:67], v[132:135]\n"
"ds_read_b64_tr_b16 a[34:35], v24 offset:1024\n"
"buffer_load_dwordx4 v25, s[52:55], s74 offen offset:0 lds\n"
"v_mfma_f32_16x16x32_bf16 v[136:139], a[112:115], a[64:67], v[136:139]\n"
"v_mfma_f32_16x16x32_bf16 v[140:143], a[120:123], a[64:67], v[140:143]\n"
"ds_read_b64_tr_b16 a[40:41], v5 offset:0\n"
"v_mfma_f32_16x16x32_bf16 v[156:159], a[120:123], a[72:75], v[156:159]\n"
"ds_read_b64_tr_b16 a[42:43], v5 offset:1024\n"
"v_mfma_f32_16x16x32_bf16 v[152:155], a[112:115], a[72:75], v[152:155]\n"
"v_mfma_f32_16x16x32_bf16 v[148:151], a[104:107], a[72:75], v[148:151]\n"
"ds_read_b64_tr_b16 a[48:49], v6 offset:0\n"
"v_mfma_f32_16x16x32_bf16 v[144:147], a[96:99], a[72:75], v[144:147]\n"
"v_mfma_f32_16x16x32_bf16 v[160:163], a[96:99], a[80:83], v[160:163]\n"
"ds_read_b64_tr_b16 a[50:51], v6 offset:1024\n"
"buffer_load_dwordx4 v26, s[52:55], s74 offen offset:1024 lds\n"
"v_mfma_f32_16x16x32_bf16 v[164:167], a[104:107], a[80:83], v[164:167]\n"
"ds_read_b64_tr_b16 a[56:57], v7 offset:0\n"
"v_mfma_f32_16x16x32_bf16 v[168:171], a[112:115], a[80:83], v[168:171]\n"
"v_mfma_f32_16x16x32_bf16 v[172:175], a[120:123], a[80:83], v[172:175]\n"
"ds_read_b64_tr_b16 a[58:59], v7 offset:1024\n"
"v_mfma_f32_16x16x32_bf16 v[188:191], a[120:123], a[88:91], v[188:191]\n"
"v_mfma_f32_16x16x32_bf16 v[184:187], a[112:115], a[88:91], v[184:187]\n"
"ds_read_b64_tr_b16 a[36:37], v24 offset:8192\n"
"v_mfma_f32_16x16x32_bf16 v[180:183], a[104:107], a[88:91], v[180:183]\n"
"ds_read_b64_tr_b16 a[38:39], v24 offset:9216\n"
"v_mfma_f32_16x16x32_bf16 v[176:179], a[96:99], a[88:91], v[176:179]\n"
"v_mfma_f32_16x16x32_bf16 v[128:131], a[100:103], a[68:71], v[128:131]\n"
"ds_read_b64_tr_b16 a[44:45], v5 offset:8192\n"
"buffer_load_dwordx4 v27, s[52:55], s74 offen offset:2048 lds\n"
"v_mfma_f32_16x16x32_bf16 v[132:135], a[108:111], a[68:71], v[132:135]\n"
"ds_read_b64_tr_b16 a[46:47], v5 offset:9216\n"
"v_mfma_f32_16x16x32_bf16 v[136:139], a[116:119], a[68:71], v[136:139]\n"
"v_mfma_f32_16x16x32_bf16 v[140:143], a[124:127], a[68:71], v[140:143]\n"
"ds_read_b64_tr_b16 a[52:53], v6 offset:8192\n"
"v_mfma_f32_16x16x32_bf16 v[156:159], a[124:127], a[76:79], v[156:159]\n"
"v_mfma_f32_16x16x32_bf16 v[152:155], a[116:119], a[76:79], v[152:155]\n"
"ds_read_b64_tr_b16 a[54:55], v6 offset:9216\n"
"v_mfma_f32_16x16x32_bf16 v[148:151], a[108:111], a[76:79], v[148:151]\n"
"ds_read_b64_tr_b16 a[60:61], v7 offset:8192\n"
"v_mfma_f32_16x16x32_bf16 v[144:147], a[100:103], a[76:79], v[144:147]\n"
"buffer_load_dwordx4 v28, s[52:55], s74 offen offset:3072 lds\n"
"s_add_u32 s74, s74, 131072\n"
"v_mfma_f32_16x16x32_bf16 v[160:163], a[100:103], a[84:87], v[160:163]\n"
"ds_read_b64_tr_b16 a[62:63], v7 offset:9216\n"
"v_mfma_f32_16x16x32_bf16 v[164:167], a[108:111], a[84:87], v[164:167]\n"
"v_mfma_f32_16x16x32_bf16 v[168:171], a[116:119], a[84:87], v[168:171]\n"
"v_mfma_f32_16x16x32_bf16 v[172:175], a[124:127], a[84:87], v[172:175]\n"
"v_mfma_f32_16x16x32_bf16 v[188:191], a[124:127], a[92:95], v[188:191]\n"
"v_mfma_f32_16x16x32_bf16 v[184:187], a[116:119], a[92:95], v[184:187]\n"
"v_mfma_f32_16x16x32_bf16 v[180:183], a[108:111], a[92:95], v[180:183]\n"
"v_mfma_f32_16x16x32_bf16 v[176:179], a[100:103], a[92:95], v[176:179]\n"
"s_waitcnt vmcnt(8)\n"
"v_cmp_ne_u64 vcc, s[90:91], v[250:251]\n"
"s_cbranch_vccnz SMQ28_%=\n"
"s_add_u32 m0, s76, 0\n"
"s_nop 0\n"
"buffer_load_dwordx4 v0, s[20:23], s72 offen offset:0 sc1 lds\n"
"buffer_load_dwordx4 v0, s[20:23], s72 offen offset:1024 sc1 lds\n"
"s_add_u32 s72, s72, 16384\n"
"s_add_u32 m0, s76, 16384\n"
"s_nop 0\n"
"buffer_load_dwordx4 v0, s[20:23], s72 offen offset:0 sc1 lds\n"
"buffer_load_dwordx4 v0, s[20:23], s72 offen offset:1024 sc1 lds\n"
"s_add_u32 s72, s72, 16384\n"
"s_add_u32 m0, s76, 32768\n"
"s_nop 0\n"
"buffer_load_dwordx4 v0, s[20:23], s72 offen offset:0 sc1 lds\n"
"buffer_load_dwordx4 v0, s[20:23], s72 offen offset:1024 sc1 lds\n"
"s_add_u32 s72, s72, 16384\n"
"s_add_u32 m0, s76, 49152\n"
"s_nop 0\n"
"buffer_load_dwordx4 v0, s[20:23], s72 offen offset:0 sc1 lds\n"
"buffer_load_dwordx4 v0, s[20:23], s72 offen offset:1024 sc1 lds\n"
"s_add_u32 s72, s72, 16384\n"
"v_cvt_pk_bf16_f32 v160, v160, v161\n"
"v_cvt_pk_bf16_f32 v161, v162, v163\n"
"v_cvt_pk_bf16_f32 v162, v164, v165\n"
"v_cvt_pk_bf16_f32 v163, v166, v167\n"
"buffer_store_dwordx4 v[160:163], v0, s[20:23], s85 offen offset:0 sc1\n"
"v_cvt_pk_bf16_f32 v164, v168, v169\n"
"v_cvt_pk_bf16_f32 v165, v170, v171\n"
"v_cvt_pk_bf16_f32 v166, v172, v173\n"
"v_cvt_pk_bf16_f32 v167, v174, v175\n"
"buffer_store_dwordx4 v[164:167], v0, s[20:23], s85 offen offset:1024 sc1\n"
"v_cvt_pk_bf16_f32 v168, v176, v177\n"
"v_cvt_pk_bf16_f32 v169, v178, v179\n"
"v_cvt_pk_bf16_f32 v170, v180, v181\n"
"v_cvt_pk_bf16_f32 v171, v182, v183\n"
"buffer_store_dwordx4 v[168:171], v0, s[20:23], s85 offen offset:2048 sc1\n"
"v_cvt_pk_bf16_f32 v172, v184, v185\n"
"v_cvt_pk_bf16_f32 v173, v186, v187\n"
"v_cvt_pk_bf16_f32 v174, v188, v189\n"
"v_cvt_pk_bf16_f32 v175, v190, v191\n"
"buffer_store_dwordx4 v[172:175], v0, s[20:23], s85 offen offset:3072 sc1\n"
"SBQ29_%=:\n"
"s_waitcnt vmcnt(10)\n"
"s_barrier\n"
"ds_read_b128 a[0:3], v23 offset:0\n"
"ds_read_b128 a[8:11], v23 offset:2048\n"
"ds_read_b128 a[4:7], v4 offset:0\n"
"ds_read_b128 a[12:15], v4 offset:2048\n"
"s_waitcnt vmcnt(8) lgkmcnt(0)\n"
"s_barrier\n"
"v_mfma_f32_16x16x32_bf16 v[192:195], a[32:35], a[0:3], 0\n"
"ds_read_b128 a[64:67], v23 offset:16384\n"
"ds_read_b64_tr_b16 a[96:97], v24 offset:16384\n"
"s_add_u32 m0, s76, 0\n"
"v_mfma_f32_16x16x32_bf16 v[196:199], a[40:43], a[0:3], 0\n"
"ds_read_b64_tr_b16 a[98:99], v24 offset:17408\n"
"ds_read_b64_tr_b16 a[104:105], v5 offset:16384\n"
"buffer_load_dwordx4 v0, s[20:23], s72 offen offset:0 sc1 lds\n"
"v_mfma_f32_16x16x32_bf16 v[200:203], a[48:51], a[0:3], 0\n"
"ds_read_b64_tr_b16 a[106:107], v5 offset:17408\n"
"v_mfma_f32_16x16x32_bf16 v[204:207], a[56:59], a[0:3], 0\n"
"ds_read_b128 a[72:75], v23 offset:18432\n"
"ds_read_b64_tr_b16 a[112:113], v6 offset:16384\n"
"buffer_load_dwordx4 v0, s[20:23], s72 offen offset:1024 sc1 lds\n"
"s_add_u32 s72, s72, 16384\n"
"v_mfma_f32_16x16x32_bf16 v[220:223], a[56:59], a[8:11], 0\n"
"ds_read_b64_tr_b16 a[114:115], v6 offset:17408\n"
"s_add_u32 m0, s77, 0\n"
"v_mfma_f32_16x16x32_bf16 v[216:219], a[48:51], a[8:11], 0\n"
"ds_read_b64_tr_b16 a[120:121], v7 offset:16384\n"
"ds_read_b64_tr_b16 a[122:123], v7 offset:17408\n"
"buffer_load_dwordx4 v25, s[52:55], s74 offen offset:0 lds\n"
"v_mfma_f32_16x16x32_bf16 v[212:215], a[40:43], a[8:11], 0\n"
"ds_read_b128 a[68:71], v4 offset:16384\n"
"v_mfma_f32_16x16x32_bf16 v[208:211], a[32:35], a[8:11], 0\n"
"ds_read_b64_tr_b16 a[100:101], v24 offset:24576\n"
"ds_read_b64_tr_b16 a[102:103], v24 offset:25600\n"
"v_mfma_f32_16x16x32_bf16 v[192:195], a[36:39], a[4:7], v[192:195]\n"
"ds_read_b64_tr_b16 a[108:109], v5 offset:24576\n"
"buffer_load_dwordx4 v26, s[52:55], s74 offen offset:1024 lds\n"
"v_mfma_f32_16x16x32_bf16 v[196:199], a[44:47], a[4:7], v[196:199]\n"
"ds_read_b64_tr_b16 a[110:111], v5 offset:25600\n"
"ds_read_b128 a[76:79], v4 offset:18432\n"
"v_mfma_f32_16x16x32_bf16 v[200:203], a[52:55], a[4:7], v[200:203]\n"
"ds_read_b64_tr_b16 a[116:117], v6 offset:24576\n"
"buffer_load_dwordx4 v27, s[52:55], s74 offen offset:2048 lds\n"
"v_mfma_f32_16x16x32_bf16 v[204:207], a[60:63], a[4:7], v[204:207]\n"
"ds_read_b64_tr_b16 a[118:119], v6 offset:25600\n"
"ds_read_b64_tr_b16 a[124:125], v7 offset:24576\n"
"v_mfma_f32_16x16x32_bf16 v[220:223], a[60:63], a[12:15], v[220:223]\n"
"ds_read_b64_tr_b16 a[126:127], v7 offset:25600\n"
"buffer_load_dwordx4 v28, s[52:55], s74 offen offset:3072 lds\n"
"s_add_u32 s74, s74, 131072\n"
"v_mfma_f32_16x16x32_bf16 v[216:219], a[52:55], a[12:15], v[216:219]\n"
"v_mfma_f32_16x16x32_bf16 v[212:215], a[44:47], a[12:15], v[212:215]\n"
"v_mfma_f32_16x16x32_bf16 v[208:211], a[36:39], a[12:15], v[208:211]\n"
"s_waitcnt vmcnt(12) lgkmcnt(0)\n"
"s_barrier\n"
"v_mfma_f32_16x16x32_bf16 v[192:195], a[96:99], a[64:67], v[192:195]\n"
"ds_read_b128 a[0:3], v23 offset:32768\n"
"ds_read_b64_tr_b16 a[32:33], v24 offset:32768\n"
"s_add_u32 m0, s76, 16384\n"
"v_mfma_f32_16x16x32_bf16 v[196:199], a[104:107], a[64:67], v[196:199]\n"
"ds_read_b64_tr_b16 a[34:35], v24 offset:33792\n"
"ds_read_b64_tr_b16 a[40:41], v5 offset:32768\n"
"buffer_load_dwordx4 v0, s[20:23], s72 offen offset:0 sc1 lds\n"
"v_mfma_f32_16x16x32_bf16 v[200:203], a[112:115], a[64:67], v[200:203]\n"
"ds_read_b64_tr_b16 a[42:43], v5 offset:33792\n"
"v_mfma_f32_16x16x32_bf16 v[204:207], a[120:123], a[64:67], v[204:207]\n"
"ds_read_b128 a[8:11], v23 offset:34816\n"
"ds_read_b64_tr_b16 a[48:49], v6 offset:32768\n"
"buffer_load_dwordx4 v0, s[20:23], s72 offen offset:1024 sc1 lds\n"
"s_add_u32 s72, s72, 16384\n"
"v_mfma_f32_16x16x32_bf16 v[220:223], a[120:123], a[72:75], v[220:223]\n"
"ds_read_b64_tr_b16 a[50:51], v6 offset:33792\n"
"s_add_u32 m0, s77, 16384\n"
"v_mfma_f32_16x16x32_bf16 v[216:219], a[112:115], a[72:75], v[216:219]\n"
"ds_read_b64_tr_b16 a[56:57], v7 offset:32768\n"
"ds_read_b64_tr_b16 a[58:59], v7 offset:33792\n"
"buffer_load_dwordx4 v25, s[52:55], s74 offen offset:0 lds\n"
"v_mfma_f32_16x16x32_bf16 v[212:215], a[104:107], a[72:75], v[212:215]\n"
"ds_read_b128 a[4:7], v4 offset:32768\n"
"v_mfma_f32_16x16x32_bf16 v[208:211], a[96:99], a[72:75], v[208:211]\n"
"ds_read_b64_tr_b16 a[36:37], v24 offset:40960\n"
"ds_read_b64_tr_b16 a[38:39], v24 offset:41984\n"
"v_mfma_f32_16x16x32_bf16 v[192:195], a[100:103], a[68:71], v[192:195]\n"
"ds_read_b64_tr_b16 a[44:45], v5 offset:40960\n"
"buffer_load_dwordx4 v26, s[52:55], s74 offen offset:1024 lds\n"
"v_mfma_f32_16x16x32_bf16 v[196:199], a[108:111], a[68:71], v[196:199]\n"
"ds_read_b64_tr_b16 a[46:47], v5 offset:41984\n"
"ds_read_b128 a[12:15], v4 offset:34816\n"
"v_mfma_f32_16x16x32_bf16 v[200:203], a[116:119], a[68:71], v[200:203]\n"
"ds_read_b64_tr_b16 a[52:53], v6 offset:40960\n"
"buffer_load_dwordx4 v27, s[52:55], s74 offen offset:2048 lds\n"
"v_mfma_f32_16x16x32_bf16 v[204:207], a[124:127], a[68:71], v[204:207]\n"
"ds_read_b64_tr_b16 a[54:55], v6 offset:41984\n"
"ds_read_b64_tr_b16 a[60:61], v7 offset:40960\n"
"v_mfma_f32_16x16x32_bf16 v[220:223], a[124:127], a[76:79], v[220:223]\n"
"ds_read_b64_tr_b16 a[62:63], v7 offset:41984\n"
"buffer_load_dwordx4 v28, s[52:55], s74 offen offset:3072 lds\n"
"s_add_u32 s74, s74, 131072\n"
"v_mfma_f32_16x16x32_bf16 v[216:219], a[116:119], a[76:79], v[216:219]\n"
"v_mfma_f32_16x16x32_bf16 v[212:215], a[108:111], a[76:79], v[212:215]\n"
"v_mfma_f32_16x16x32_bf16 v[208:211], a[100:103], a[76:79], v[208:211]\n"
"s_waitcnt vmcnt(16) lgkmcnt(0)\n"
"s_barrier\n"
"v_mfma_f32_16x16x32_bf16 v[192:195], a[32:35], a[0:3], v[192:195]\n"
"ds_read_b128 a[64:67], v23 offset:49152\n"
"ds_read_b64_tr_b16 a[96:97], v24 offset:49152\n"
"s_add_u32 m0, s76, 32768\n"
"v_mfma_f32_16x16x32_bf16 v[196:199], a[40:43], a[0:3], v[196:199]\n"
"ds_read_b64_tr_b16 a[98:99], v24 offset:50176\n"
"ds_read_b64_tr_b16 a[104:105], v5 offset:49152\n"
"buffer_load_dwordx4 v0, s[20:23], s72 offen offset:0 sc1 lds\n"
"v_mfma_f32_16x16x32_bf16 v[200:203], a[48:51], a[0:3], v[200:203]\n"
"ds_read_b64_tr_b16 a[106:107], v5 offset:50176\n"
"v_mfma_f32_16x16x32_bf16 v[204:207], a[56:59], a[0:3], v[204:207]\n"
"ds_read_b128 a[72:75], v23 offset:51200\n"
"ds_read_b64_tr_b16 a[112:113], v6 offset:49152\n"
"buffer_load_dwordx4 v0, s[20:23], s72 offen offset:1024 sc1 lds\n"
"s_add_u32 s72, s72, 16384\n"
"v_mfma_f32_16x16x32_bf16 v[220:223], a[56:59], a[8:11], v[220:223]\n"
"ds_read_b64_tr_b16 a[114:115], v6 offset:50176\n"
"s_add_u32 m0, s77, 32768\n"
"v_mfma_f32_16x16x32_bf16 v[216:219], a[48:51], a[8:11], v[216:219]\n"
"ds_read_b64_tr_b16 a[120:121], v7 offset:49152\n"
"ds_read_b64_tr_b16 a[122:123], v7 offset:50176\n"
"buffer_load_dwordx4 v25, s[52:55], s74 offen offset:0 lds\n"
"v_mfma_f32_16x16x32_bf16 v[212:215], a[40:43], a[8:11], v[212:215]\n"
"ds_read_b128 a[68:71], v4 offset:49152\n"
"v_mfma_f32_16x16x32_bf16 v[208:211], a[32:35], a[8:11], v[208:211]\n"
"ds_read_b64_tr_b16 a[100:101], v24 offset:57344\n"
"ds_read_b64_tr_b16 a[102:103], v24 offset:58368\n"
"v_mfma_f32_16x16x32_bf16 v[192:195], a[36:39], a[4:7], v[192:195]\n"
"ds_read_b64_tr_b16 a[108:109], v5 offset:57344\n"
"buffer_load_dwordx4 v26, s[52:55], s74 offen offset:1024 lds\n"
"v_mfma_f32_16x16x32_bf16 v[196:199], a[44:47], a[4:7], v[196:199]\n"
"ds_read_b64_tr_b16 a[110:111], v5 offset:58368\n"
"ds_read_b128 a[76:79], v4 offset:51200\n"
"v_mfma_f32_16x16x32_bf16 v[200:203], a[52:55], a[4:7], v[200:203]\n"
"ds_read_b64_tr_b16 a[116:117], v6 offset:57344\n"
"buffer_load_dwordx4 v27, s[52:55], s74 offen offset:2048 lds\n"
"v_mfma_f32_16x16x32_bf16 v[204:207], a[60:63], a[4:7], v[204:207]\n"
"ds_read_b64_tr_b16 a[118:119], v6 offset:58368\n"
"ds_read_b64_tr_b16 a[124:125], v7 offset:57344\n"
"v_mfma_f32_16x16x32_bf16 v[220:223], a[60:63], a[12:15], v[220:223]\n"
"ds_read_b64_tr_b16 a[126:127], v7 offset:58368\n"
"buffer_load_dwordx4 v28, s[52:55], s74 offen offset:3072 lds\n"
"s_add_u32 s74, s74, 131072\n"
"v_mfma_f32_16x16x32_bf16 v[216:219], a[52:55], a[12:15], v[216:219]\n"
"v_mfma_f32_16x16x32_bf16 v[212:215], a[44:47], a[12:15], v[212:215]\n"
"v_mfma_f32_16x16x32_bf16 v[208:211], a[36:39], a[12:15], v[208:211]\n"
"s_waitcnt vmcnt(12) lgkmcnt(0)\n"
"s_barrier\n"
"s_waitcnt vmcnt(18)\n"
"v_mfma_f32_16x16x32_bf16 v[192:195], a[96:99], a[64:67], v[192:195]\n"
"ds_read_b128 a[0:3], v23 offset:0\n"
"ds_read_b64_tr_b16 a[32:33], v24 offset:0\n"
"s_add_u32 m0, s76, 49152\n"
"s_mov_b64 exec, 1\n"
"buffer_store_dwordx2 v[2:3], off, s[20:23], s87 sc1\n"
"s_mov_b64 exec, -1\n"
"v_mfma_f32_16x16x32_bf16 v[196:199], a[104:107], a[64:67], v[196:199]\n"
"ds_read_b64_tr_b16 a[34:35], v24 offset:1024\n"
"ds_read_b64_tr_b16 a[40:41], v5 offset:0\n"
"buffer_load_dwordx4 v0, s[20:23], s72 offen offset:0 sc1 lds\n"
"v_mfma_f32_16x16x32_bf16 v[200:203], a[112:115], a[64:67], v[200:203]\n"
"ds_read_b64_tr_b16 a[42:43], v5 offset:1024\n"
"v_mfma_f32_16x16x32_bf16 v[204:207], a[120:123], a[64:67], v[204:207]\n"
"ds_read_b128 a[8:11], v23 offset:2048\n"
"ds_read_b64_tr_b16 a[48:49], v6 offset:0\n"
"buffer_load_dwordx4 v0, s[20:23], s72 offen offset:1024 sc1 lds\n"
"s_add_u32 s72, s72, 16384\n"
"v_mfma_f32_16x16x32_bf16 v[220:223], a[120:123], a[72:75], v[220:223]\n"
"ds_read_b64_tr_b16 a[50:51], v6 offset:1024\n"
"s_add_u32 m0, s77, 49152\n"
"v_mfma_f32_16x16x32_bf16 v[216:219], a[112:115], a[72:75], v[216:219]\n"
"ds_read_b64_tr_b16 a[56:57], v7 offset:0\n"
"ds_read_b64_tr_b16 a[58:59], v7 offset:1024\n"
"buffer_load_dwordx4 v25, s[52:55], s74 offen offset:0 lds\n"
"v_mfma_f32_16x16x32_bf16 v[212:215], a[104:107], a[72:75], v[212:215]\n"
"ds_read_b128 a[4:7], v4 offset:0\n"
"v_mfma_f32_16x16x32_bf16 v[208:211], a[96:99], a[72:75], v[208:211]\n"
"ds_read_b64_tr_b16 a[36:37], v24 offset:8192\n"
"ds_read_b64_tr_b16 a[38:39], v24 offset:9216\n"
"v_mfma_f32_16x16x32_bf16 v[192:195], a[100:103], a[68:71], v[192:195]\n"
"ds_read_b64_tr_b16 a[44:45], v5 offset:8192\n"
"buffer_load_dwordx4 v26, s[52:55], s74 offen offset:1024 lds\n"
"v_mfma_f32_16x16x32_bf16 v[196:199], a[108:111], a[68:71], v[196:199]\n"
"ds_read_b64_tr_b16 a[46:47], v5 offset:9216\n"
"ds_read_b128 a[12:15], v4 offset:2048\n"
"v_mfma_f32_16x16x32_bf16 v[200:203], a[116:119], a[68:71], v[200:203]\n"
"ds_read_b64_tr_b16 a[52:53], v6 offset:8192\n"
"buffer_load_dwordx4 v27, s[52:55], s74 offen offset:2048 lds\n"
"v_mfma_f32_16x16x32_bf16 v[204:207], a[124:127], a[68:71], v[204:207]\n"
"ds_read_b64_tr_b16 a[54:55], v6 offset:9216\n"
"ds_read_b64_tr_b16 a[60:61], v7 offset:8192\n"
"v_mfma_f32_16x16x32_bf16 v[220:223], a[124:127], a[76:79], v[220:223]\n"
"ds_read_b64_tr_b16 a[62:63], v7 offset:9216\n"
"buffer_load_dwordx4 v28, s[52:55], s74 offen offset:3072 lds\n"
"s_add_u32 s74, s74, 131072\n"
"v_mfma_f32_16x16x32_bf16 v[216:219], a[116:119], a[76:79], v[216:219]\n"
"v_mfma_f32_16x16x32_bf16 v[212:215], a[108:111], a[76:79], v[212:215]\n"
"v_mfma_f32_16x16x32_bf16 v[208:211], a[100:103], a[76:79], v[208:211]\n"
"s_waitcnt vmcnt(13) lgkmcnt(0)\n"
"s_barrier\n"
"v_mfma_f32_16x16x32_bf16 v[192:195], a[32:35], a[0:3], v[192:195]\n"
"ds_read_b128 a[64:67], v23 offset:16384\n"
"ds_read_b64_tr_b16 a[96:97], v24 offset:16384\n"
"s_add_u32 m0, s76, 0\n"
"v_lshrrev_b32 v246, 8, v0\n"
"v_mfma_f32_16x16x32_bf16 v[196:199], a[40:43], a[0:3], v[196:199]\n"
"ds_read_b64_tr_b16 a[98:99], v24 offset:17408\n"
"ds_read_b64_tr_b16 a[104:105], v5 offset:16384\n"
"buffer_load_dwordx4 v0, s[20:23], s72 offen offset:0 sc1 lds\n"
"v_bfe_u32 v247, v0, 6, 2\n"
"v_mfma_f32_16x16x32_bf16 v[200:203], a[48:51], a[0:3], v[200:203]\n"
"ds_read_b64_tr_b16 a[106:107], v5 offset:17408\n"
"v_cmp_eq_u32 vcc, v246, v247\n"
"v_mfma_f32_16x16x32_bf16 v[204:207], a[56:59], a[0:3], v[204:207]\n"
"ds_read_b128 a[72:75], v23 offset:18432\n"
"ds_read_b64_tr_b16 a[112:113], v6 offset:16384\n"
"buffer_load_dwordx4 v0, s[20:23], s72 offen offset:1024 sc1 lds\n"
"s_add_u32 s72, s72, 16384\n"
"v_and_b32 v246, 16, v0\n"
"v_mfma_f32_16x16x32_bf16 v[220:223], a[56:59], a[8:11], v[220:223]\n"
"ds_read_b64_tr_b16 a[114:115], v6 offset:17408\n"
"s_add_u32 m0, s77, 0\n"
"v_mov_b32 v247, 0x3f80\n"
"v_mfma_f32_16x16x32_bf16 v[216:219], a[48:51], a[8:11], v[216:219]\n"
"ds_read_b64_tr_b16 a[120:121], v7 offset:16384\n"
"ds_read_b64_tr_b16 a[122:123], v7 offset:17408\n"
"buffer_load_dwordx4 v25, s[52:55], s74 offen offset:0 lds\n"
"v_lshlrev_b32 v246, v246, v247\n"
"v_mfma_f32_16x16x32_bf16 v[212:215], a[40:43], a[8:11], v[212:215]\n"
"ds_read_b128 a[68:71], v4 offset:16384\n"
"v_cndmask_b32 v246, 0, v246, vcc\n"
"v_mfma_f32_16x16x32_bf16 v[208:211], a[32:35], a[8:11], v[208:211]\n"
"ds_read_b64_tr_b16 a[100:101], v24 offset:24576\n"
"ds_read_b64_tr_b16 a[102:103], v24 offset:25600\n"
"v_and_b32 v247, 32, v0\n"
"v_mfma_f32_16x16x32_bf16 v[192:195], a[36:39], a[4:7], v[192:195]\n"
"ds_read_b64_tr_b16 a[108:109], v5 offset:24576\n"
"buffer_load_dwordx4 v26, s[52:55], s74 offen offset:1024 lds\n"
"v_cmp_eq_u32 vcc, 0, v247\n"
"v_mfma_f32_16x16x32_bf16 v[196:199], a[44:47], a[4:7], v[196:199]\n"
"ds_read_b64_tr_b16 a[110:111], v5 offset:25600\n"
"ds_read_b128 a[76:79], v4 offset:18432\n"
"v_mov_b32 v240, 0\n"
"v_mfma_f32_16x16x32_bf16 v[200:203], a[52:55], a[4:7], v[200:203]\n"
"ds_read_b64_tr_b16 a[116:117], v6 offset:24576\n"
"buffer_load_dwordx4 v27, s[52:55], s74 offen offset:2048 lds\n"
"v_cndmask_b32 v242, 0, v246, vcc\n"
"v_mfma_f32_16x16x32_bf16 v[204:207], a[60:63], a[4:7], v[204:207]\n"
"ds_read_b64_tr_b16 a[118:119], v6 offset:25600\n"
"ds_read_b64_tr_b16 a[124:125], v7 offset:24576\n"
"v_mov_b32 v241, 0\n"
"v_mfma_f32_16x16x32_bf16 v[220:223], a[60:63], a[12:15], v[220:223]\n"
"ds_read_b64_tr_b16 a[126:127], v7 offset:25600\n"
"buffer_load_dwordx4 v28, s[52:55], s74 offen offset:3072 lds\n"
"s_add_u32 s74, s74, 131072\n"
"v_xor_b32 v243, v246, v242\n"
"v_mfma_f32_16x16x32_bf16 v[216:219], a[52:55], a[12:15], v[216:219]\n"
"v_mov_b32 v244, 0\n"
"v_mfma_f32_16x16x32_bf16 v[212:215], a[44:47], a[12:15], v[212:215]\n"
"v_mov_b32 v245, 0\n"
"v_mfma_f32_16x16x32_bf16 v[208:211], a[36:39], a[12:15], v[208:211]\n"
"s_waitcnt vmcnt(13) lgkmcnt(0)\n"
"s_barrier\n"
"v_mfma_f32_16x16x32_bf16 v[192:195], a[96:99], a[64:67], v[192:195]\n"
"ds_read_b128 a[0:3], v23 offset:32768\n"
"ds_read_b64_tr_b16 a[32:33], v24 offset:32768\n"
"s_add_u32 m0, s76, 16384\n"
"v_mov_b32 v8, 0\n"
"buffer_load_dwordx2 v[250:251], v8, s[20:23], s88 offen sc1\n"
"v_mfma_f32_16x16x32_bf16 v[196:199], a[104:107], a[64:67], v[196:199]\n"
"ds_read_b64_tr_b16 a[34:35], v24 offset:33792\n"
"ds_read_b64_tr_b16 a[40:41], v5 offset:32768\n"
"buffer_load_dwordx4 v0, s[20:23], s72 offen offset:0 sc1 lds\n"
"v_accvgpr_read_b32 v32, a144\n"
"v_mfma_f32_16x16x32_bf16 v[200:203], a[112:115], a[64:67], v[200:203]\n"
"ds_read_b64_tr_b16 a[42:43], v5 offset:33792\n"
"v_accvgpr_read_b32 v33, a145\n"
"v_mfma_f32_16x16x32_bf16 v[204:207], a[120:123], a[64:67], v[204:207]\n"
"ds_read_b128 a[8:11], v23 offset:34816\n"
"ds_read_b64_tr_b16 a[48:49], v6 offset:32768\n"
"buffer_load_dwordx4 v0, s[20:23], s72 offen offset:1024 sc1 lds\n"
"s_add_u32 s72, s72, 16384\n"
"v_accvgpr_read_b32 v34, a146\n"
"v_mfma_f32_16x16x32_bf16 v[220:223], a[120:123], a[72:75], v[220:223]\n"
"ds_read_b64_tr_b16 a[50:51], v6 offset:33792\n"
"s_add_u32 m0, s77, 16384\n"
"v_accvgpr_read_b32 v35, a147\n"
"v_mfma_f32_16x16x32_bf16 v[216:219], a[112:115], a[72:75], v[216:219]\n"
"ds_read_b64_tr_b16 a[56:57], v7 offset:32768\n"
"ds_read_b64_tr_b16 a[58:59], v7 offset:33792\n"
"buffer_load_dwordx4 v25, s[52:55], s74 offen offset:0 lds\n"
"v_accvgpr_read_b32 v36, a148\n"
"v_mfma_f32_16x16x32_bf16 v[212:215], a[104:107], a[72:75], v[212:215]\n"
"ds_read_b128 a[4:7], v4 offset:32768\n"
"v_accvgpr_read_b32 v37, a149\n"
"v_mfma_f32_16x16x32_bf16 v[208:211], a[96:99], a[72:75], v[208:211]\n"
"ds_read_b64_tr_b16 a[36:37], v24 offset:40960\n"
"ds_read_b64_tr_b16 a[38:39], v24 offset:41984\n"
"v_accvgpr_read_b32 v38, a150\n"
"v_mfma_f32_16x16x32_bf16 v[192:195], a[100:103], a[68:71], v[192:195]\n"
"ds_read_b64_tr_b16 a[44:45], v5 offset:40960\n"
"buffer_load_dwordx4 v26, s[52:55], s74 offen offset:1024 lds\n"
"v_accvgpr_read_b32 v39, a151\n"
"v_accvgpr_read_b32 v40, a152\n"
"v_mfma_f32_16x16x32_bf16 v[196:199], a[108:111], a[68:71], v[196:199]\n"
"ds_read_b64_tr_b16 a[46:47], v5 offset:41984\n"
"ds_read_b128 a[12:15], v4 offset:34816\n"
"v_accvgpr_read_b32 v41, a153\n"
"v_mfma_f32_16x16x32_bf16 v[200:203], a[116:119], a[68:71], v[200:203]\n"
"ds_read_b64_tr_b16 a[52:53], v6 offset:40960\n"
"buffer_load_dwordx4 v27, s[52:55], s74 offen offset:2048 lds\n"
"v_accvgpr_read_b32 v42, a154\n"
"v_mfma_f32_16x16x32_bf16 v[204:207], a[124:127], a[68:71], v[204:207]\n"
"ds_read_b64_tr_b16 a[54:55], v6 offset:41984\n"
"ds_read_b64_tr_b16 a[60:61], v7 offset:40960\n"
"v_accvgpr_read_b32 v43, a155\n"
"v_mfma_f32_16x16x32_bf16 v[220:223], a[124:127], a[76:79], v[220:223]\n"
"ds_read_b64_tr_b16 a[62:63], v7 offset:41984\n"
"buffer_load_dwordx4 v28, s[52:55], s74 offen offset:3072 lds\n"
"s_add_u32 s74, s74, 131072\n"
"v_accvgpr_read_b32 v44, a156\n"
"v_mfma_f32_16x16x32_bf16 v[216:219], a[116:119], a[76:79], v[216:219]\n"
"v_accvgpr_read_b32 v45, a157\n"
"v_mfma_f32_16x16x32_bf16 v[212:215], a[108:111], a[76:79], v[212:215]\n"
"v_accvgpr_read_b32 v46, a158\n"
"v_mfma_f32_16x16x32_bf16 v[208:211], a[100:103], a[76:79], v[208:211]\n"
"v_accvgpr_read_b32 v47, a159\n"
"s_waitcnt vmcnt(13) lgkmcnt(0)\n"
"s_barrier\n"
"v_mfma_f32_16x16x32_bf16 v[192:195], a[32:35], a[0:3], v[192:195]\n"
"ds_read_b128 a[64:67], v23 offset:49152\n"
"ds_read_b64_tr_b16 a[96:97], v24 offset:49152\n"
"s_add_u32 m0, s76, 32768\n"
"s_mov_b32 exec_lo, 0xffff0000\n"
"s_mov_b32 exec_hi, 0xffff0000\n"
"v_swap_b32 v32, v34\n"
"v_swap_b32 v33, v35\n"
"v_swap_b32 v36, v38\n"
"v_swap_b32 v37, v39\n"
"v_swap_b32 v40, v42\n"
"v_swap_b32 v41, v43\n"
"v_swap_b32 v44, v46\n"
"v_swap_b32 v45, v47\n"
"s_mov_b64 exec, -1\n"
"v_mfma_f32_16x16x32_bf16 v[196:199], a[40:43], a[0:3], v[196:199]\n"
"ds_read_b64_tr_b16 a[98:99], v24 offset:50176\n"
"ds_read_b64_tr_b16 a[104:105], v5 offset:49152\n"
"buffer_load_dwordx4 v0, s[20:23], s72 offen offset:0 sc1 lds\n"
"v_lshlrev_b32 v64, 16, v32\n"
"v_mfma_f32_16x16x32_bf16 v[200:203], a[48:51], a[0:3], v[200:203]\n"
"ds_read_b64_tr_b16 a[106:107], v5 offset:50176\n"
"v_mfma_f32_16x16x32_bf16 v[204:207], a[56:59], a[0:3], v[204:207]\n"
"ds_read_b128 a[72:75], v23 offset:51200\n"
"ds_read_b64_tr_b16 a[112:113], v6 offset:49152\n"
"buffer_load_dwordx4 v0, s[20:23], s72 offen offset:1024 sc1 lds\n"
"s_add_u32 s72, s72, 16384\n"
"v_and_b32 v65, 0xffff0000, v32\n"
"v_mfma_f32_16x16x32_bf16 v[220:223], a[56:59], a[8:11], v[220:223]\n"
"ds_read_b64_tr_b16 a[114:115], v6 offset:50176\n"
"s_add_u32 m0, s77, 32768\n"
"v_mfma_f32_16x16x32_bf16 v[216:219], a[48:51], a[8:11], v[216:219]\n"
"ds_read_b64_tr_b16 a[120:121], v7 offset:49152\n"
"ds_read_b64_tr_b16 a[122:123], v7 offset:50176\n"
"buffer_load_dwordx4 v25, s[52:55], s74 offen offset:0 lds\n"
"v_lshlrev_b32 v66, 16, v33\n"
"v_mfma_f32_16x16x32_bf16 v[212:215], a[40:43], a[8:11], v[212:215]\n"
"ds_read_b128 a[68:71], v4 offset:49152\n"
"v_mfma_f32_16x16x32_bf16 v[208:211], a[32:35], a[8:11], v[208:211]\n"
"ds_read_b64_tr_b16 a[100:101], v24 offset:57344\n"
"ds_read_b64_tr_b16 a[102:103], v24 offset:58368\n"
"v_and_b32 v67, 0xffff0000, v33\n"
"v_mfma_f32_16x16x32_bf16 v[192:195], a[36:39], a[4:7], v[192:195]\n"
"ds_read_b64_tr_b16 a[108:109], v5 offset:57344\n"
"buffer_load_dwordx4 v26, s[52:55], s74 offen offset:1024 lds\n"
"v_lshlrev_b32 v68, 16, v34\n"
"v_mfma_f32_16x16x32_bf16 v[196:199], a[44:47], a[4:7], v[196:199]\n"
"ds_read_b64_tr_b16 a[110:111], v5 offset:58368\n"
"ds_read_b128 a[76:79], v4 offset:51200\n"
"v_mfma_f32_16x16x32_bf16 v[200:203], a[52:55], a[4:7], v[200:203]\n"
"ds_read_b64_tr_b16 a[116:117], v6 offset:57344\n"
"buffer_load_dwordx4 v27, s[52:55], s74 offen offset:2048 lds\n"
"v_and_b32 v69, 0xffff0000, v34\n"
"v_mfma_f32_16x16x32_bf16 v[204:207], a[60:63], a[4:7], v[204:207]\n"
"ds_read_b64_tr_b16 a[118:119], v6 offset:58368\n"
"ds_read_b64_tr_b16 a[124:125], v7 offset:57344\n"
"v_mfma_f32_16x16x32_bf16 v[220:223], a[60:63], a[12:15], v[220:223]\n"
"ds_read_b64_tr_b16 a[126:127], v7 offset:58368\n"
"buffer_load_dwordx4 v28, s[52:55], s74 offen offset:3072 lds\n"
"s_add_u32 s74, s74, 131072\n"
"v_lshlrev_b32 v70, 16, v35\n"
"v_mfma_f32_16x16x32_bf16 v[216:219], a[52:55], a[12:15], v[216:219]\n"
"v_mfma_f32_16x16x32_bf16 v[212:215], a[44:47], a[12:15], v[212:215]\n"
"v_and_b32 v71, 0xffff0000, v35\n"
"v_mfma_f32_16x16x32_bf16 v[208:211], a[36:39], a[12:15], v[208:211]\n"
"s_waitcnt vmcnt(13) lgkmcnt(0)\n"
"s_barrier\n"
"s_waitcnt vmcnt(12)\n"
"v_cmp_ne_u64 vcc, s[90:91], v[250:251]\n"
"s_cbranch_vccnz SPQ21_%=\n"
"BKQ22_%=:\n"
"v_mfma_f32_16x16x32_bf16 v[192:195], a[96:99], a[64:67], v[192:195]\n"
"ds_read_b128 a[0:3], v23 offset:0\n"
"ds_read_b64_tr_b16 a[32:33], v24 offset:0\n"
"s_add_u32 m0, s76, 49152\n"
"buffer_load_dwordx4 v[224:227], v0, s[20:23], s86 offen offset:0 sc1\n"
"v_mfma_f32_16x16x32_bf16 v[196:199], a[104:107], a[64:67], v[196:199]\n"
"ds_read_b64_tr_b16 a[34:35], v24 offset:1024\n"
"ds_read_b64_tr_b16 a[40:41], v5 offset:0\n"
"buffer_load_dwordx4 v0, s[20:23], s72 offen offset:0 sc1 lds\n"
"v_mfma_f32_16x16x32_bf16 v[200:203], a[112:115], a[64:67], v[200:203]\n"
"ds_read_b64_tr_b16 a[42:43], v5 offset:1024\n"
"v_mfma_f32_16x16x32_bf16 v[204:207], a[120:123], a[64:67], v[204:207]\n"
"ds_read_b128 a[8:11], v23 offset:2048\n"
"ds_read_b64_tr_b16 a[48:49], v6 offset:0\n"
"buffer_load_dwordx4 v0, s[20:23], s72 offen offset:1024 sc1 lds\n"
"s_add_u32 s72, s72, 16384\n"
"v_mfma_f32_16x16x32_bf16 v[220:223], a[120:123], a[72:75], v[220:223]\n"
"ds_read_b64_tr_b16 a[50:51], v6 offset:1024\n"
"s_add_u32 m0, s77, 49152\n"
"buffer_load_dwordx4 v[228:231], v0, s[20:23], s86 offen offset:1024 sc1\n"
"v_mfma_f32_16x16x32_bf16 v[216:219], a[112:115], a[72:75], v[216:219]\n"
"ds_read_b64_tr_b16 a[56:57], v7 offset:0\n"
"ds_read_b64_tr_b16 a[58:59], v7 offset:1024\n"
"buffer_load_dwordx4 v25, s[52:55], s74 offen offset:0 lds\n"
"v_mfma_f32_16x16x32_bf16 v[212:215], a[104:107], a[72:75], v[212:215]\n"
"ds_read_b128 a[4:7], v4 offset:0\n"
"v_mfma_f32_16x16x32_bf16 v[208:211], a[96:99], a[72:75], v[208:211]\n"
"ds_read_b64_tr_b16 a[36:37], v24 offset:8192\n"
"ds_read_b64_tr_b16 a[38:39], v24 offset:9216\n"
"v_mfma_f32_16x16x32_bf16 v[192:195], a[100:103], a[68:71], v[192:195]\n"
"ds_read_b64_tr_b16 a[44:45], v5 offset:8192\n"
"buffer_load_dwordx4 v26, s[52:55], s74 offen offset:1024 lds\n"
"buffer_load_dwordx4 v[232:235], v0, s[20:23], s86 offen offset:2048 sc1\n"
"v_mfma_f32_16x16x32_bf16 v[196:199], a[108:111], a[68:71], v[196:199]\n"
"ds_read_b64_tr_b16 a[46:47], v5 offset:9216\n"
"ds_read_b128 a[12:15], v4 offset:2048\n"
"v_mfma_f32_16x16x32_bf16 v[200:203], a[116:119], a[68:71], v[200:203]\n"
"ds_read_b64_tr_b16 a[52:53], v6 offset:8192\n"
"buffer_load_dwordx4 v27, s[52:55], s74 offen offset:2048 lds\n"
"v_mfma_f32_16x16x32_bf16 v[204:207], a[124:127], a[68:71], v[204:207]\n"
"ds_read_b64_tr_b16 a[54:55], v6 offset:9216\n"
"ds_read_b64_tr_b16 a[60:61], v7 offset:8192\n"
"v_mfma_f32_16x16x32_bf16 v[220:223], a[124:127], a[76:79], v[220:223]\n"
"ds_read_b64_tr_b16 a[62:63], v7 offset:9216\n"
"buffer_load_dwordx4 v28, s[52:55], s74 offen offset:3072 lds\n"
"s_add_u32 s74, s74, 131072\n"
"buffer_load_dwordx4 v[236:239], v0, s[20:23], s86 offen offset:3072 sc1\n"
"v_mfma_f32_16x16x32_bf16 v[216:219], a[116:119], a[76:79], v[216:219]\n"
"v_mfma_f32_16x16x32_bf16 v[212:215], a[108:111], a[76:79], v[212:215]\n"
"v_mfma_f32_16x16x32_bf16 v[208:211], a[100:103], a[76:79], v[208:211]\n"
"s_waitcnt vmcnt(16) lgkmcnt(0)\n"
"s_barrier\n"
"v_mfma_f32_16x16x32_bf16 v[192:195], a[32:35], a[0:3], v[192:195]\n"
"ds_read_b128 a[64:67], v23 offset:16384\n"
"ds_read_b64_tr_b16 a[96:97], v24 offset:16384\n"
"s_add_u32 m0, s76, 0\n"
"v_lshlrev_b32 v72, 16, v36\n"
"v_and_b32 v73, 0xffff0000, v36\n"
"v_mfma_f32_16x16x32_bf16 v[196:199], a[40:43], a[0:3], v[196:199]\n"
"ds_read_b64_tr_b16 a[98:99], v24 offset:17408\n"
"ds_read_b64_tr_b16 a[104:105], v5 offset:16384\n"
"buffer_load_dwordx4 v0, s[20:23], s72 offen offset:0 sc1 lds\n"
"v_lshlrev_b32 v74, 16, v37\n"
"v_mfma_f32_16x16x32_bf16 v[200:203], a[48:51], a[0:3], v[200:203]\n"
"ds_read_b64_tr_b16 a[106:107], v5 offset:17408\n"
"v_and_b32 v75, 0xffff0000, v37\n"
"v_lshlrev_b32 v76, 16, v38\n"
"v_mfma_f32_16x16x32_bf16 v[204:207], a[56:59], a[0:3], v[204:207]\n"
"ds_read_b128 a[72:75], v23 offset:18432\n"
"ds_read_b64_tr_b16 a[112:113], v6 offset:16384\n"
"buffer_load_dwordx4 v0, s[20:23], s72 offen offset:1024 sc1 lds\n"
"s_add_u32 s72, s72, 16384\n"
"v_and_b32 v77, 0xffff0000, v38\n"
"v_mfma_f32_16x16x32_bf16 v[220:223], a[56:59], a[8:11], v[220:223]\n"
"ds_read_b64_tr_b16 a[114:115], v6 offset:17408\n"
"s_add_u32 m0, s77, 0\n"
"v_lshlrev_b32 v78, 16, v39\n"
"v_and_b32 v79, 0xffff0000, v39\n"
"v_mfma_f32_16x16x32_bf16 v[216:219], a[48:51], a[8:11], v[216:219]\n"
"ds_read_b64_tr_b16 a[120:121], v7 offset:16384\n"
"ds_read_b64_tr_b16 a[122:123], v7 offset:17408\n"
"buffer_load_dwordx4 v25, s[52:55], s74 offen offset:0 lds\n"
"v_lshlrev_b32 v80, 16, v40\n"
"v_mfma_f32_16x16x32_bf16 v[212:215], a[40:43], a[8:11], v[212:215]\n"
"ds_read_b128 a[68:71], v4 offset:16384\n"
"v_and_b32 v81, 0xffff0000, v40\n"
"v_lshlrev_b32 v82, 16, v41\n"
"v_mfma_f32_16x16x32_bf16 v[208:211], a[32:35], a[8:11], v[208:211]\n"
"ds_read_b64_tr_b16 a[100:101], v24 offset:24576\n"
"ds_read_b64_tr_b16 a[102:103], v24 offset:25600\n"
"v_and_b32 v83, 0xffff0000, v41\n"
"v_mfma_f32_16x16x32_bf16 v[192:195], a[36:39], a[4:7], v[192:195]\n"
"ds_read_b64_tr_b16 a[108:109], v5 offset:24576\n"
"buffer_load_dwordx4 v26, s[52:55], s74 offen offset:1024 lds\n"
"v_lshlrev_b32 v84, 16, v42\n"
"v_and_b32 v85, 0xffff0000, v42\n"
"v_mfma_f32_16x16x32_bf16 v[196:199], a[44:47], a[4:7], v[196:199]\n"
"ds_read_b64_tr_b16 a[110:111], v5 offset:25600\n"
"ds_read_b128 a[76:79], v4 offset:18432\n"
"v_lshlrev_b32 v86, 16, v43\n"
"v_mfma_f32_16x16x32_bf16 v[200:203], a[52:55], a[4:7], v[200:203]\n"
"ds_read_b64_tr_b16 a[116:117], v6 offset:24576\n"
"buffer_load_dwordx4 v27, s[52:55], s74 offen offset:2048 lds\n"
"v_and_b32 v87, 0xffff0000, v43\n"
"v_lshlrev_b32 v88, 16, v44\n"
"v_mfma_f32_16x16x32_bf16 v[204:207], a[60:63], a[4:7], v[204:207]\n"
"ds_read_b64_tr_b16 a[118:119], v6 offset:25600\n"
"ds_read_b64_tr_b16 a[124:125], v7 offset:24576\n"
"v_and_b32 v89, 0xffff0000, v44\n"
"v_mfma_f32_16x16x32_bf16 v[220:223], a[60:63], a[12:15], v[220:223]\n"
"ds_read_b64_tr_b16 a[126:127], v7 offset:25600\n"
"buffer_load_dwordx4 v28, s[52:55], s74 offen offset:3072 lds\n"
"s_add_u32 s74, s74, 131072\n"
"v_lshlrev_b32 v90, 16, v45\n"
"v_and_b32 v91, 0xffff0000, v45\n"
"v_mfma_f32_16x16x32_bf16 v[216:219], a[52:55], a[12:15], v[216:219]\n"
"v_lshlrev_b32 v92, 16, v46\n"
"v_mfma_f32_16x16x32_bf16 v[212:215], a[44:47], a[12:15], v[212:215]\n"
"v_and_b32 v93, 0xffff0000, v46\n"
"v_lshlrev_b32 v94, 16, v47\n"
"v_mfma_f32_16x16x32_bf16 v[208:211], a[36:39], a[12:15], v[208:211]\n"
"v_and_b32 v95, 0xffff0000, v47\n"
"s_waitcnt vmcnt(16) lgkmcnt(0)\n"
"s_barrier\n"
"v_mfma_f32_16x16x32_bf16 v[192:195], a[96:99], a[64:67], v[192:195]\n"
"ds_read_b128 a[0:3], v23 offset:32768\n"
"ds_read_b64_tr_b16 a[32:33], v24 offset:32768\n"
"s_add_u32 m0, s76, 16384\n"
"v_mfma_f32_16x16x32_bf16 v[196:199], a[104:107], a[64:67], v[196:199]\n"
"ds_read_b64_tr_b16 a[34:35], v24 offset:33792\n"
"ds_read_b64_tr_b16 a[40:41], v5 offset:32768\n"
"buffer_load_dwordx4 v0, s[20:23], s72 offen offset:0 sc1 lds\n"
"v_mfma_f32_16x16x32_bf16 v[200:203], a[112:115], a[64:67], v[200:203]\n"
"ds_read_b64_tr_b16 a[42:43], v5 offset:33792\n"
"v_mfma_f32_16x16x32_bf16 v[204:207], a[120:123], a[64:67], v[204:207]\n"
"ds_read_b128 a[8:11], v23 offset:34816\n"
"ds_read_b64_tr_b16 a[48:49], v6 offset:32768\n"
"buffer_load_dwordx4 v0, s[20:23], s72 offen offset:1024 sc1 lds\n"
"s_add_u32 s72, s72, 16384\n"
"v_mfma_f32_16x16x32_bf16 v[220:223], a[120:123], a[72:75], v[220:223]\n"
"ds_read_b64_tr_b16 a[50:51], v6 offset:33792\n"
"s_add_u32 m0, s77, 16384\n"
"v_mfma_f32_16x16x32_bf16 v[216:219], a[112:115], a[72:75], v[216:219]\n"
"ds_read_b64_tr_b16 a[56:57], v7 offset:32768\n"
"ds_read_b64_tr_b16 a[58:59], v7 offset:33792\n"
"buffer_load_dwordx4 v25, s[52:55], s74 offen offset:0 lds\n"
"v_mfma_f32_16x16x32_bf16 v[212:215], a[104:107], a[72:75], v[212:215]\n"
"ds_read_b128 a[4:7], v4 offset:32768\n"
"v_mfma_f32_16x16x32_bf16 v[208:211], a[96:99], a[72:75], v[208:211]\n"
"ds_read_b64_tr_b16 a[36:37], v24 offset:40960\n"
"ds_read_b64_tr_b16 a[38:39], v24 offset:41984\n"
"v_mfma_f32_16x16x32_bf16 v[192:195], a[100:103], a[68:71], v[192:195]\n"
"ds_read_b64_tr_b16 a[44:45], v5 offset:40960\n"
"buffer_load_dwordx4 v26, s[52:55], s74 offen offset:1024 lds\n"
"v_mfma_f32_16x16x32_bf16 v[196:199], a[108:111], a[68:71], v[196:199]\n"
"ds_read_b64_tr_b16 a[46:47], v5 offset:41984\n"
"ds_read_b128 a[12:15], v4 offset:34816\n"
"v_mfma_f32_16x16x32_bf16 v[200:203], a[116:119], a[68:71], v[200:203]\n"
"ds_read_b64_tr_b16 a[52:53], v6 offset:40960\n"
"buffer_load_dwordx4 v27, s[52:55], s74 offen offset:2048 lds\n"
"v_mfma_f32_16x16x32_bf16 v[204:207], a[124:127], a[68:71], v[204:207]\n"
"ds_read_b64_tr_b16 a[54:55], v6 offset:41984\n"
"ds_read_b64_tr_b16 a[60:61], v7 offset:40960\n"
"v_mfma_f32_16x16x32_bf16 v[220:223], a[124:127], a[76:79], v[220:223]\n"
"ds_read_b64_tr_b16 a[62:63], v7 offset:41984\n"
"buffer_load_dwordx4 v28, s[52:55], s74 offen offset:3072 lds\n"
"s_add_u32 s74, s74, 131072\n"
"v_mfma_f32_16x16x32_bf16 v[216:219], a[116:119], a[76:79], v[216:219]\n"
"v_mfma_f32_16x16x32_bf16 v[212:215], a[108:111], a[76:79], v[212:215]\n"
"v_mfma_f32_16x16x32_bf16 v[208:211], a[100:103], a[76:79], v[208:211]\n"
"s_waitcnt vmcnt(13) lgkmcnt(0)\n"
"s_barrier\n"
"s_waitcnt vmcnt(12)\n"
"v_mfma_f32_16x16x32_bf16 v[192:195], a[32:35], a[0:3], v[192:195]\n"
"ds_read_b128 a[64:67], v23 offset:49152\n"
"ds_read_b64_tr_b16 a[96:97], v24 offset:49152\n"
"s_add_u32 m0, s76, 32768\n"
"v_mfma_f32_16x16x32_bf16 v[128:131], v[242:245], v[224:227], v[128:131]\n"
"v_mfma_f32_16x16x32_bf16 v[132:135], v[240:243], v[224:227], v[132:135]\n"
"v_mfma_f32_16x16x32_bf16 v[196:199], a[40:43], a[0:3], v[196:199]\n"
"ds_read_b64_tr_b16 a[98:99], v24 offset:50176\n"
"ds_read_b64_tr_b16 a[104:105], v5 offset:49152\n"
"buffer_load_dwordx4 v0, s[20:23], s72 offen offset:0 sc1 lds\n"
"v_mfma_f32_16x16x32_bf16 v[136:139], v[242:245], v[228:231], v[136:139]\n"
"v_mfma_f32_16x16x32_bf16 v[200:203], a[48:51], a[0:3], v[200:203]\n"
"ds_read_b64_tr_b16 a[106:107], v5 offset:50176\n"
"v_mfma_f32_16x16x32_bf16 v[140:143], v[240:243], v[228:231], v[140:143]\n"
"v_mfma_f32_16x16x32_bf16 v[144:147], v[242:245], v[232:235], v[144:147]\n"
"v_mfma_f32_16x16x32_bf16 v[204:207], a[56:59], a[0:3], v[204:207]\n"
"ds_read_b128 a[72:75], v23 offset:51200\n"
"ds_read_b64_tr_b16 a[112:113], v6 offset:49152\n"
"buffer_load_dwordx4 v0, s[20:23], s72 offen offset:1024 sc1 lds\n"
"s_add_u32 s72, s72, 16384\n"
"v_mfma_f32_16x16x32_bf16 v[148:151], v[240:243], v[232:235], v[148:151]\n"
"v_mfma_f32_16x16x32_bf16 v[220:223], a[56:59], a[8:11], v[220:223]\n"
"ds_read_b64_tr_b16 a[114:115], v6 offset:50176\n"
"s_add_u32 m0, s77, 32768\n"
"v_mfma_f32_16x16x32_bf16 v[152:155], v[242:245], v[236:239], v[152:155]\n"
"v_mfma_f32_16x16x32_bf16 v[156:159], v[240:243], v[236:239], v[156:159]\n"
"v_mfma_f32_16x16x32_bf16 v[216:219], a[48:51], a[8:11], v[216:219]\n"
"ds_read_b64_tr_b16 a[120:121], v7 offset:49152\n"
"ds_read_b64_tr_b16 a[122:123], v7 offset:50176\n"
"buffer_load_dwordx4 v25, s[52:55], s74 offen offset:0 lds\n"
"v_pk_mul_f32 v[96:97], v[128:129], s[92:93] neg_lo:[0,1] neg_hi:[0,1]\n"
"v_mfma_f32_16x16x32_bf16 v[212:215], a[40:43], a[8:11], v[212:215]\n"
"ds_read_b128 a[68:71], v4 offset:49152\n"
"v_pk_mul_f32 v[98:99], v[130:131], s[92:93] neg_lo:[0,1] neg_hi:[0,1]\n"
"v_pk_mul_f32 v[100:101], v[132:133], s[92:93] neg_lo:[0,1] neg_hi:[0,1]\n"
"v_mfma_f32_16x16x32_bf16 v[208:211], a[32:35], a[8:11], v[208:211]\n"
"ds_read_b64_tr_b16 a[100:101], v24 offset:57344\n"
"ds_read_b64_tr_b16 a[102:103], v24 offset:58368\n"
"v_pk_mul_f32 v[102:103], v[134:135], s[92:93] neg_lo:[0,1] neg_hi:[0,1]\n"
"v_mfma_f32_16x16x32_bf16 v[192:195], a[36:39], a[4:7], v[192:195]\n"
"ds_read_b64_tr_b16 a[108:109], v5 offset:57344\n"
"buffer_load_dwordx4 v26, s[52:55], s74 offen offset:1024 lds\n"
"v_exp_f32 v96, v96\n"
"v_exp_f32 v97, v97\n"
"v_mfma_f32_16x16x32_bf16 v[196:199], a[44:47], a[4:7], v[196:199]\n"
"ds_read_b64_tr_b16 a[110:111], v5 offset:58368\n"
"ds_read_b128 a[76:79], v4 offset:51200\n"
"v_exp_f32 v98, v98\n"
"v_mfma_f32_16x16x32_bf16 v[200:203], a[52:55], a[4:7], v[200:203]\n"
"ds_read_b64_tr_b16 a[116:117], v6 offset:57344\n"
"buffer_load_dwordx4 v27, s[52:55], s74 offen offset:2048 lds\n"
"v_exp_f32 v99, v99\n"
"v_exp_f32 v100, v100\n"
"v_mfma_f32_16x16x32_bf16 v[204:207], a[60:63], a[4:7], v[204:207]\n"
"ds_read_b64_tr_b16 a[118:119], v6 offset:58368\n"
"ds_read_b64_tr_b16 a[124:125], v7 offset:57344\n"
"v_exp_f32 v101, v101\n"
"v_mfma_f32_16x16x32_bf16 v[220:223], a[60:63], a[12:15], v[220:223]\n"
"ds_read_b64_tr_b16 a[126:127], v7 offset:58368\n"
"buffer_load_dwordx4 v28, s[52:55], s74 offen offset:3072 lds\n"
"s_add_u32 s74, s74, 131072\n"
"v_exp_f32 v102, v102\n"
"v_exp_f32 v103, v103\n"
"v_mfma_f32_16x16x32_bf16 v[216:219], a[52:55], a[12:15], v[216:219]\n"
"v_pk_add_f32 v[96:97], v[96:97], 1.0 op_sel_hi:[1,0]\n"
"s_nop 1\n"
"v_mfma_f32_16x16x32_bf16 v[212:215], a[44:47], a[12:15], v[212:215]\n"
"v_pk_add_f32 v[98:99], v[98:99], 1.0 op_sel_hi:[1,0]\n"
"v_pk_add_f32 v[100:101], v[100:101], 1.0 op_sel_hi:[1,0]\n"
"s_nop 1\n"
"v_mfma_f32_16x16x32_bf16 v[208:211], a[36:39], a[12:15], v[208:211]\n"
"v_pk_add_f32 v[102:103], v[102:103], 1.0 op_sel_hi:[1,0]\n"
"s_waitcnt vmcnt(12) lgkmcnt(0)\n"
"s_barrier\n"
"v_mfma_f32_16x16x32_bf16 v[192:195], a[96:99], a[64:67], v[192:195]\n"
"ds_read_b128 a[0:3], v23 offset:0\n"
"ds_read_b64_tr_b16 a[32:33], v24 offset:0\n"
"s_add_u32 m0, s76, 49152\n"
"v_rcp_f32 v96, v96\n"
"v_rcp_f32 v97, v97\n"
"v_mfma_f32_16x16x32_bf16 v[196:199], a[104:107], a[64:67], v[196:199]\n"
"ds_read_b64_tr_b16 a[34:35], v24 offset:1024\n"
"ds_read_b64_tr_b16 a[40:41], v5 offset:0\n"
"buffer_load_dwordx4 v0, s[20:23], s72 offen offset:0 sc1 lds\n"
"v_rcp_f32 v98, v98\n"
"v_mfma_f32_16x16x32_bf16 v[200:203], a[112:115], a[64:67], v[200:203]\n"
"ds_read_b64_tr_b16 a[42:43], v5 offset:1024\n"
"v_rcp_f32 v99, v99\n"
"v_rcp_f32 v100, v100\n"
"v_mfma_f32_16x16x32_bf16 v[204:207], a[120:123], a[64:67], v[204:207]\n"
"ds_read_b128 a[8:11], v23 offset:2048\n"
"ds_read_b64_tr_b16 a[48:49], v6 offset:0\n"
"buffer_load_dwordx4 v0, s[20:23], s72 offen offset:1024 sc1 lds\n"
"s_add_u32 s72, s72, 16384\n"
"v_rcp_f32 v101, v101\n"
"v_mfma_f32_16x16x32_bf16 v[220:223], a[120:123], a[72:75], v[220:223]\n"
"ds_read_b64_tr_b16 a[50:51], v6 offset:1024\n"
"s_add_u32 m0, s77, 49152\n"
"v_rcp_f32 v102, v102\n"
"v_rcp_f32 v103, v103\n"
"v_mfma_f32_16x16x32_bf16 v[216:219], a[112:115], a[72:75], v[216:219]\n"
"ds_read_b64_tr_b16 a[56:57], v7 offset:0\n"
"ds_read_b64_tr_b16 a[58:59], v7 offset:1024\n"
"buffer_load_dwordx4 v25, s[52:55], s74 offen offset:0 lds\n"
"v_pk_mul_f32 v[128:129], v[128:129], v[96:97]\n"
"v_mfma_f32_16x16x32_bf16 v[212:215], a[104:107], a[72:75], v[212:215]\n"
"ds_read_b128 a[4:7], v4 offset:0\n"
"v_pk_mul_f32 v[130:131], v[130:131], v[98:99]\n"
"v_pk_mul_f32 v[132:133], v[132:133], v[100:101]\n"
"v_mfma_f32_16x16x32_bf16 v[208:211], a[96:99], a[72:75], v[208:211]\n"
"ds_read_b64_tr_b16 a[36:37], v24 offset:8192\n"
"ds_read_b64_tr_b16 a[38:39], v24 offset:9216\n"
"v_pk_mul_f32 v[134:135], v[134:135], v[102:103]\n"
"v_mfma_f32_16x16x32_bf16 v[192:195], a[100:103], a[68:71], v[192:195]\n"
"ds_read_b64_tr_b16 a[44:45], v5 offset:8192\n"
"buffer_load_dwordx4 v26, s[52:55], s74 offen offset:1024 lds\n"
"v_pk_mul_f32 v[104:105], v[136:137], s[92:93] neg_lo:[0,1] neg_hi:[0,1]\n"
"v_pk_mul_f32 v[106:107], v[138:139], s[92:93] neg_lo:[0,1] neg_hi:[0,1]\n"
"v_mfma_f32_16x16x32_bf16 v[196:199], a[108:111], a[68:71], v[196:199]\n"
"ds_read_b64_tr_b16 a[46:47], v5 offset:9216\n"
"ds_read_b128 a[12:15], v4 offset:2048\n"
"v_pk_mul_f32 v[108:109], v[140:141], s[92:93] neg_lo:[0,1] neg_hi:[0,1]\n"
"v_mfma_f32_16x16x32_bf16 v[200:203], a[116:119], a[68:71], v[200:203]\n"
"ds_read_b64_tr_b16 a[52:53], v6 offset:8192\n"
"buffer_load_dwordx4 v27, s[52:55], s74 offen offset:2048 lds\n"
"v_pk_mul_f32 v[110:111], v[142:143], s[92:93] neg_lo:[0,1] neg_hi:[0,1]\n"
"v_exp_f32 v104, v104\n"
"v_mfma_f32_16x16x32_bf16 v[204:207], a[124:127], a[68:71], v[204:207]\n"
"ds_read_b64_tr_b16 a[54:55], v6 offset:9216\n"
"ds_read_b64_tr_b16 a[60:61], v7 offset:8192\n"
"v_exp_f32 v105, v105\n"
"v_mfma_f32_16x16x32_bf16 v[220:223], a[124:127], a[76:79], v[220:223]\n"
"ds_read_b64_tr_b16 a[62:63], v7 offset:9216\n"
"buffer_load_dwordx4 v28, s[52:55], s74 offen offset:3072 lds\n"
"s_add_u32 s74, s74, 131072\n"
"v_exp_f32 v106, v106\n"
"v_exp_f32 v107, v107\n"
"v_mfma_f32_16x16x32_bf16 v[216:219], a[116:119], a[76:79], v[216:219]\n"
"v_exp_f32 v108, v108\n"
"v_mfma_f32_16x16x32_bf16 v[212:215], a[108:111], a[76:79], v[212:215]\n"
"v_exp_f32 v109, v109\n"
"v_exp_f32 v110, v110\n"
"v_mfma_f32_16x16x32_bf16 v[208:211], a[100:103], a[76:79], v[208:211]\n"
"v_exp_f32 v111, v111\n"
"s_waitcnt vmcnt(12) lgkmcnt(0)\n"
"s_barrier\n"
"v_mfma_f32_16x16x32_bf16 v[192:195], a[32:35], a[0:3], v[192:195]\n"
"ds_read_b128 a[64:67], v23 offset:16384\n"
"ds_read_b64_tr_b16 a[96:97], v24 offset:16384\n"
"v_pk_add_f32 v[104:105], v[104:105], 1.0 op_sel_hi:[1,0]\n"
"v_pk_add_f32 v[106:107], v[106:107], 1.0 op_sel_hi:[1,0]\n"
"s_nop 1\n"
"v_mfma_f32_16x16x32_bf16 v[196:199], a[40:43], a[0:3], v[196:199]\n"
"ds_read_b64_tr_b16 a[98:99], v24 offset:17408\n"
"ds_read_b64_tr_b16 a[104:105], v5 offset:16384\n"
"v_pk_add_f32 v[108:109], v[108:109], 1.0 op_sel_hi:[1,0]\n"
"s_nop 1\n"
"v_mfma_f32_16x16x32_bf16 v[200:203], a[48:51], a[0:3], v[200:203]\n"
"ds_read_b64_tr_b16 a[106:107], v5 offset:17408\n"
"v_pk_add_f32 v[110:111], v[110:111], 1.0 op_sel_hi:[1,0]\n"
"v_rcp_f32 v104, v104\n"
"s_nop 0\n"
"v_mfma_f32_16x16x32_bf16 v[204:207], a[56:59], a[0:3], v[204:207]\n"
"ds_read_b128 a[72:75], v23 offset:18432\n"
"ds_read_b64_tr_b16 a[112:113], v6 offset:16384\n"
"v_rcp_f32 v105, v105\n"
"v_mfma_f32_16x16x32_bf16 v[220:223], a[56:59], a[8:11], v[220:223]\n"
"ds_read_b64_tr_b16 a[114:115], v6 offset:17408\n"
"v_rcp_f32 v106, v106\n"
"v_rcp_f32 v107, v107\n"
"v_mfma_f32_16x16x32_bf16 v[216:219], a[48:51], a[8:11], v[216:219]\n"
"ds_read_b64_tr_b16 a[120:121], v7 offset:16384\n"
"ds_read_b64_tr_b16 a[122:123], v7 offset:17408\n"
"v_rcp_f32 v108, v108\n"
"v_mfma_f32_16x16x32_bf16 v[212:215], a[40:43], a[8:11], v[212:215]\n"
"ds_read_b128 a[68:71], v4 offset:16384\n"
"v_rcp_f32 v109, v109\n"
"v_rcp_f32 v110, v110\n"
"v_mfma_f32_16x16x32_bf16 v[208:211], a[32:35], a[8:11], v[208:211]\n"
"ds_read_b64_tr_b16 a[100:101], v24 offset:24576\n"
"ds_read_b64_tr_b16 a[102:103], v24 offset:25600\n"
"v_rcp_f32 v111, v111\n"
"v_mfma_f32_16x16x32_bf16 v[192:195], a[36:39], a[4:7], v[192:195]\n"
"ds_read_b64_tr_b16 a[108:109], v5 offset:24576\n"
"v_pk_mul_f32 v[136:137], v[136:137], v[104:105]\n"
"v_pk_mul_f32 v[138:139], v[138:139], v[106:107]\n"
"v_mfma_f32_16x16x32_bf16 v[196:199], a[44:47], a[4:7], v[196:199]\n"
"ds_read_b64_tr_b16 a[110:111], v5 offset:25600\n"
"ds_read_b128 a[76:79], v4 offset:18432\n"
"v_pk_mul_f32 v[140:141], v[140:141], v[108:109]\n"
"v_mfma_f32_16x16x32_bf16 v[200:203], a[52:55], a[4:7], v[200:203]\n"
"ds_read_b64_tr_b16 a[116:117], v6 offset:24576\n"
"v_pk_mul_f32 v[142:143], v[142:143], v[110:111]\n"
"v_pk_mul_f32 v[112:113], v[144:145], s[92:93] neg_lo:[0,1] neg_hi:[0,1]\n"
"v_mfma_f32_16x16x32_bf16 v[204:207], a[60:63], a[4:7], v[204:207]\n"
"ds_read_b64_tr_b16 a[118:119], v6 offset:25600\n"
"ds_read_b64_tr_b16 a[124:125], v7 offset:24576\n"
"v_pk_mul_f32 v[114:115], v[146:147], s[92:93] neg_lo:[0,1] neg_hi:[0,1]\n"
"v_mfma_f32_16x16x32_bf16 v[220:223], a[60:63], a[12:15], v[220:223]\n"
"ds_read_b64_tr_b16 a[126:127], v7 offset:25600\n"
"v_pk_mul_f32 v[116:117], v[148:149], s[92:93] neg_lo:[0,1] neg_hi:[0,1]\n"
"v_pk_mul_f32 v[118:119], v[150:151], s[92:93] neg_lo:[0,1] neg_hi:[0,1]\n"
"v_mfma_f32_16x16x32_bf16 v[216:219], a[52:55], a[12:15], v[216:219]\n"
"v_exp_f32 v112, v112\n"
"v_mfma_f32_16x16x32_bf16 v[212:215], a[44:47], a[12:15], v[212:215]\n"
"v_exp_f32 v113, v113\n"
"v_exp_f32 v114, v114\n"
"v_mfma_f32_16x16x32_bf16 v[208:211], a[36:39], a[12:15], v[208:211]\n"
"v_exp_f32 v115, v115\n"
"s_waitcnt vmcnt(6) lgkmcnt(0)\n"
"s_barrier\n"
"v_mfma_f32_16x16x32_bf16 v[192:195], a[96:99], a[64:67], v[192:195]\n"
"ds_read_b128 a[0:3], v23 offset:32768\n"
"ds_read_b64_tr_b16 a[32:33], v24 offset:32768\n"
"v_exp_f32 v116, v116\n"
"v_exp_f32 v117, v117\n"
"v_mfma_f32_16x16x32_bf16 v[196:199], a[104:107], a[64:67], v[196:199]\n"
"ds_read_b64_tr_b16 a[34:35], v24 offset:33792\n"
"ds_read_b64_tr_b16 a[40:41], v5 offset:32768\n"
"v_exp_f32 v118, v118\n"
"v_mfma_f32_16x16x32_bf16 v[200:203], a[112:115], a[64:67], v[200:203]\n"
"ds_read_b64_tr_b16 a[42:43], v5 offset:33792\n"
"v_exp_f32 v119, v119\n"
"v_pk_add_f32 v[112:113], v[112:113], 1.0 op_sel_hi:[1,0]\n"
"s_nop 1\n"
"v_mfma_f32_16x16x32_bf16 v[204:207], a[120:123], a[64:67], v[204:207]\n"
"ds_read_b128 a[8:11], v23 offset:34816\n"
"ds_read_b64_tr_b16 a[48:49], v6 offset:32768\n"
"v_pk_add_f32 v[114:115], v[114:115], 1.0 op_sel_hi:[1,0]\n"
"s_nop 1\n"
"v_mfma_f32_16x16x32_bf16 v[220:223], a[120:123], a[72:75], v[220:223]\n"
"ds_read_b64_tr_b16 a[50:51], v6 offset:33792\n"
"v_pk_add_f32 v[116:117], v[116:117], 1.0 op_sel_hi:[1,0]\n"
"v_pk_add_f32 v[118:119], v[118:119], 1.0 op_sel_hi:[1,0]\n"
"s_nop 1\n"
"v_mfma_f32_16x16x32_bf16 v[216:219], a[112:115], a[72:75], v[216:219]\n"
"ds_read_b64_tr_b16 a[56:57], v7 offset:32768\n"
"ds_read_b64_tr_b16 a[58:59], v7 offset:33792\n"
"v_rcp_f32 v112, v112\n"
"v_mfma_f32_16x16x32_bf16 v[212:215], a[104:107], a[72:75], v[212:215]\n"
"ds_read_b128 a[4:7], v4 offset:32768\n"
"v_rcp_f32 v113, v113\n"
"v_rcp_f32 v114, v114\n"
"v_mfma_f32_16x16x32_bf16 v[208:211], a[96:99], a[72:75], v[208:211]\n"
"ds_read_b64_tr_b16 a[36:37], v24 offset:40960\n"
"ds_read_b64_tr_b16 a[38:39], v24 offset:41984\n"
"v_rcp_f32 v115, v115\n"
"v_mfma_f32_16x16x32_bf16 v[192:195], a[100:103], a[68:71], v[192:195]\n"
"ds_read_b64_tr_b16 a[44:45], v5 offset:40960\n"
"v_rcp_f32 v116, v116\n"
"v_rcp_f32 v117, v117\n"
"v_mfma_f32_16x16x32_bf16 v[196:199], a[108:111], a[68:71], v[196:199]\n"
"ds_read_b64_tr_b16 a[46:47], v5 offset:41984\n"
"ds_read_b128 a[12:15], v4 offset:34816\n"
"v_rcp_f32 v118, v118\n"
"v_mfma_f32_16x16x32_bf16 v[200:203], a[116:119], a[68:71], v[200:203]\n"
"ds_read_b64_tr_b16 a[52:53], v6 offset:40960\n"
"v_rcp_f32 v119, v119\n"
"v_pk_mul_f32 v[144:145], v[144:145], v[112:113]\n"
"v_mfma_f32_16x16x32_bf16 v[204:207], a[124:127], a[68:71], v[204:207]\n"
"ds_read_b64_tr_b16 a[54:55], v6 offset:41984\n"
"ds_read_b64_tr_b16 a[60:61], v7 offset:40960\n"
"v_pk_mul_f32 v[146:147], v[146:147], v[114:115]\n"
"v_mfma_f32_16x16x32_bf16 v[220:223], a[124:127], a[76:79], v[220:223]\n"
"ds_read_b64_tr_b16 a[62:63], v7 offset:41984\n"
"v_pk_mul_f32 v[148:149], v[148:149], v[116:117]\n"
"v_pk_mul_f32 v[150:151], v[150:151], v[118:119]\n"
"v_mfma_f32_16x16x32_bf16 v[216:219], a[116:119], a[76:79], v[216:219]\n"
"v_pk_mul_f32 v[120:121], v[152:153], s[92:93] neg_lo:[0,1] neg_hi:[0,1]\n"
"v_mfma_f32_16x16x32_bf16 v[212:215], a[108:111], a[76:79], v[212:215]\n"
"v_pk_mul_f32 v[122:123], v[154:155], s[92:93] neg_lo:[0,1] neg_hi:[0,1]\n"
"v_pk_mul_f32 v[124:125], v[156:157], s[92:93] neg_lo:[0,1] neg_hi:[0,1]\n"
"v_mfma_f32_16x16x32_bf16 v[208:211], a[100:103], a[76:79], v[208:211]\n"
"v_pk_mul_f32 v[126:127], v[158:159], s[92:93] neg_lo:[0,1] neg_hi:[0,1]\n"
"s_waitcnt vmcnt(0) lgkmcnt(0)\n"
"s_barrier\n"
"v_mfma_f32_16x16x32_bf16 v[192:195], a[32:35], a[0:3], v[192:195]\n"
"ds_read_b128 a[64:67], v23 offset:49152\n"
"ds_read_b64_tr_b16 a[96:97], v24 offset:49152\n"
"v_exp_f32 v120, v120\n"
"v_exp_f32 v121, v121\n"
"v_mfma_f32_16x16x32_bf16 v[196:199], a[40:43], a[0:3], v[196:199]\n"
"ds_read_b64_tr_b16 a[98:99], v24 offset:50176\n"
"ds_read_b64_tr_b16 a[104:105], v5 offset:49152\n"
"v_exp_f32 v122, v122\n"
"v_mfma_f32_16x16x32_bf16 v[200:203], a[48:51], a[0:3], v[200:203]\n"
"ds_read_b64_tr_b16 a[106:107], v5 offset:50176\n"
"v_exp_f32 v123, v123\n"
"v_exp_f32 v124, v124\n"
"v_mfma_f32_16x16x32_bf16 v[204:207], a[56:59], a[0:3], v[204:207]\n"
"ds_read_b128 a[72:75], v23 offset:51200\n"
"ds_read_b64_tr_b16 a[112:113], v6 offset:49152\n"
"v_exp_f32 v125, v125\n"
"v_mfma_f32_16x16x32_bf16 v[220:223], a[56:59], a[8:11], v[220:223]\n"
"ds_read_b64_tr_b16 a[114:115], v6 offset:50176\n"
"v_exp_f32 v126, v126\n"
"v_exp_f32 v127, v127\n"
"v_mfma_f32_16x16x32_bf16 v[216:219], a[48:51], a[8:11], v[216:219]\n"
"ds_read_b64_tr_b16 a[120:121], v7 offset:49152\n"
"ds_read_b64_tr_b16 a[122:123], v7 offset:50176\n"
"v_pk_add_f32 v[120:121], v[120:121], 1.0 op_sel_hi:[1,0]\n"
"s_nop 1\n"
"v_mfma_f32_16x16x32_bf16 v[212:215], a[40:43], a[8:11], v[212:215]\n"
"ds_read_b128 a[68:71], v4 offset:49152\n"
"v_pk_add_f32 v[122:123], v[122:123], 1.0 op_sel_hi:[1,0]\n"
"v_pk_add_f32 v[124:125], v[124:125], 1.0 op_sel_hi:[1,0]\n"
"s_nop 1\n"
"v_mfma_f32_16x16x32_bf16 v[208:211], a[32:35], a[8:11], v[208:211]\n"
"ds_read_b64_tr_b16 a[100:101], v24 offset:57344\n"
"ds_read_b64_tr_b16 a[102:103], v24 offset:58368\n"
"v_pk_add_f32 v[126:127], v[126:127], 1.0 op_sel_hi:[1,0]\n"
"s_nop 1\n"
"v_mfma_f32_16x16x32_bf16 v[192:195], a[36:39], a[4:7], v[192:195]\n"
"ds_read_b64_tr_b16 a[108:109], v5 offset:57344\n"
"v_rcp_f32 v120, v120\n"
"v_rcp_f32 v121, v121\n"
"v_mfma_f32_16x16x32_bf16 v[196:199], a[44:47], a[4:7], v[196:199]\n"
"ds_read_b64_tr_b16 a[110:111], v5 offset:58368\n"
"ds_read_b128 a[76:79], v4 offset:51200\n"
"v_rcp_f32 v122, v122\n"
"v_mfma_f32_16x16x32_bf16 v[200:203], a[52:55], a[4:7], v[200:203]\n"
"ds_read_b64_tr_b16 a[116:117], v6 offset:57344\n"
"v_rcp_f32 v123, v123\n"
"v_rcp_f32 v124, v124\n"
"v_mfma_f32_16x16x32_bf16 v[204:207], a[60:63], a[4:7], v[204:207]\n"
"ds_read_b64_tr_b16 a[118:119], v6 offset:58368\n"
"ds_read_b64_tr_b16 a[124:125], v7 offset:57344\n"
"v_rcp_f32 v125, v125\n"
"v_mfma_f32_16x16x32_bf16 v[220:223], a[60:63], a[12:15], v[220:223]\n"
"ds_read_b64_tr_b16 a[126:127], v7 offset:58368\n"
"v_rcp_f32 v126, v126\n"
"v_rcp_f32 v127, v127\n"
"v_mfma_f32_16x16x32_bf16 v[216:219], a[52:55], a[12:15], v[216:219]\n"
"v_pk_mul_f32 v[152:153], v[152:153], v[120:121]\n"
"v_mfma_f32_16x16x32_bf16 v[212:215], a[44:47], a[12:15], v[212:215]\n"
"v_pk_mul_f32 v[154:155], v[154:155], v[122:123]\n"
"v_pk_mul_f32 v[156:157], v[156:157], v[124:125]\n"
"v_mfma_f32_16x16x32_bf16 v[208:211], a[36:39], a[12:15], v[208:211]\n"
"v_pk_mul_f32 v[158:159], v[158:159], v[126:127]\n"
"s_waitcnt lgkmcnt(0)\n"
"v_mfma_f32_16x16x32_bf16 v[192:195], a[96:99], a[64:67], v[192:195]\n"
"v_mfma_f32_16x16x32_bf16 v[196:199], a[104:107], a[64:67], v[196:199]\n"
"v_mfma_f32_16x16x32_bf16 v[200:203], a[112:115], a[64:67], v[200:203]\n"
"v_mfma_f32_16x16x32_bf16 v[204:207], a[120:123], a[64:67], v[204:207]\n"
"v_mfma_f32_16x16x32_bf16 v[204:207], a[124:127], a[68:71], v[204:207]\n"
"v_mfma_f32_16x16x32_bf16 v[200:203], a[116:119], a[68:71], v[200:203]\n"
"v_mfma_f32_16x16x32_bf16 v[196:199], a[108:111], a[68:71], v[196:199]\n"
"v_mfma_f32_16x16x32_bf16 v[192:195], a[100:103], a[68:71], v[192:195]\n"
"v_mfma_f32_16x16x32_bf16 v[208:211], a[96:99], a[72:75], v[208:211]\n"
"v_mfma_f32_16x16x32_bf16 v[212:215], a[104:107], a[72:75], v[212:215]\n"
"v_mfma_f32_16x16x32_bf16 v[216:219], a[112:115], a[72:75], v[216:219]\n"
"s_nop 1\n"
"v_pk_fma_f32 v[78:79], v[206:207], v[142:143], v[78:79]\n"
"v_pk_fma_f32 v[76:77], v[204:205], v[140:141], v[76:77]\n"
"v_pk_fma_f32 v[74:75], v[202:203], v[138:139], v[74:75]\n"
"v_mfma_f32_16x16x32_bf16 v[220:223], a[120:123], a[72:75], v[220:223]\n"
"v_pk_fma_f32 v[72:73], v[200:201], v[136:137], v[72:73]\n"
"v_pk_fma_f32 v[70:71], v[198:199], v[134:135], v[70:71]\n"
"v_pk_fma_f32 v[68:69], v[196:197], v[132:133], v[68:69]\n"
"v_mfma_f32_16x16x32_bf16 v[220:223], a[124:127], a[76:79], v[220:223]\n"
"v_pk_fma_f32 v[66:67], v[194:195], v[130:131], v[66:67]\n"
"v_pk_fma_f32 v[64:65], v[192:193], v[128:129], v[64:65]\n"
"v_mfma_f32_16x16x32_bf16 v[216:219], a[116:119], a[76:79], v[216:219]\n"
"v_cvt_pk_bf16_f32 v32, v64, v65\n"
"v_cvt_pk_bf16_f32 v33, v66, v67\n"
"v_cvt_pk_bf16_f32 v34, v68, v69\n"
"v_mfma_f32_16x16x32_bf16 v[212:215], a[108:111], a[76:79], v[212:215]\n"
"v_cvt_pk_bf16_f32 v35, v70, v71\n"
"v_cvt_pk_bf16_f32 v36, v72, v73\n"
"v_cvt_pk_bf16_f32 v37, v74, v75\n"
"v_mfma_f32_16x16x32_bf16 v[208:211], a[100:103], a[76:79], v[208:211]\n"
"v_cvt_pk_bf16_f32 v38, v76, v77\n"
"v_cvt_pk_bf16_f32 v39, v78, v79\n"
"s_mov_b32 s95, 32768\n"
"s_mov_b32 exec_lo, 0xffff0000\n"
"s_mov_b32 exec_hi, 0xffff0000\n"
"v_swap_b32 v32, v34\n"
"v_swap_b32 v33, v35\n"
"v_swap_b32 v36, v38\n"
"v_swap_b32 v37, v39\n"
"s_mov_b64 exec, -1\n"
"buffer_store_dwordx4 v[32:35], v22, s[48:51], 0 offen offset:0 sc1\n"
"buffer_store_dwordx4 v[36:39], v22, s[48:51], 0 offen offset:64 sc1\n"
"v_pk_fma_f32 v[94:95], v[222:223], v[158:159], v[94:95]\n"
"v_pk_fma_f32 v[92:93], v[220:221], v[156:157], v[92:93]\n"
"v_pk_fma_f32 v[90:91], v[218:219], v[154:155], v[90:91]\n"
"v_pk_fma_f32 v[88:89], v[216:217], v[152:153], v[88:89]\n"
"v_pk_fma_f32 v[86:87], v[214:215], v[150:151], v[86:87]\n"
"v_pk_fma_f32 v[84:85], v[212:213], v[148:149], v[84:85]\n"
"v_pk_fma_f32 v[82:83], v[210:211], v[146:147], v[82:83]\n"
"v_pk_fma_f32 v[80:81], v[208:209], v[144:145], v[80:81]\n"
"v_cvt_pk_bf16_f32 v40, v80, v81\n"
"v_cvt_pk_bf16_f32 v41, v82, v83\n"
"v_cvt_pk_bf16_f32 v42, v84, v85\n"
"v_cvt_pk_bf16_f32 v43, v86, v87\n"
"v_cvt_pk_bf16_f32 v44, v88, v89\n"
"v_cvt_pk_bf16_f32 v45, v90, v91\n"
"v_cvt_pk_bf16_f32 v46, v92, v93\n"
"v_cvt_pk_bf16_f32 v47, v94, v95\n"
"s_mov_b32 exec_lo, 0xffff0000\n"
"s_mov_b32 exec_hi, 0xffff0000\n"
"v_swap_b32 v40, v42\n"
"v_swap_b32 v41, v43\n"
"v_swap_b32 v44, v46\n"
"v_swap_b32 v45, v47\n"
"s_mov_b64 exec, -1\n"
"buffer_store_dwordx4 v[40:43], v22, s[48:51], s95 offen offset:0 sc1\n"
"buffer_store_dwordx4 v[44:47], v22, s[48:51], s95 offen offset:64 sc1\n"
"s_cmp_eq_u32 s89, 0\n"
"s_cbranch_scc0 EPQ33_%=\n"
"s_mov_b32 s94, 0\n"
"s_mov_b64 exec, 1\n"
"buffer_store_dwordx2 v[2:3], off, s[20:23], s94 sc1\n"
"s_mov_b64 exec, -1\n"
"EPQ33_%=:\n"
"s_branch END_%=\n"
"SDQ2_%=:\n"
"s_cmp_eq_u32 s41, s1\n"
"s_cbranch_scc0 SDQ4_%=\n"
"s_mov_b64 exec, 3\n"
"buffer_store_dwordx4 a[246:249], v0, s[20:23], s4 offen sc1\n"
"buffer_store_dwordx2 a[246:247], off, s[20:23], s64 sc1\n"
"s_mov_b64 exec, -1\n"
"global_load_dword a240, v0, s[18:19]\n"
"s_branch G1Q1_%=\n"
"SDQ4_%=:\n"
"s_cmp_eq_u32 s14, 0\n"
"s_cbranch_scc0 SDQ3_%=\n"
"s_waitcnt vmcnt(18) lgkmcnt(0)\n"
"s_barrier\n"
"v_mfma_f32_16x16x32_bf16 v[32:35], a[32:35], a[0:3], v[32:35]\n"
"ds_read_b128 a[72:75], v7 offset:0\n"
"v_and_b32 v23, 0xf0, v0\n"
"s_add_u32 m0, s40, 0\n"
"v_mfma_f32_16x16x32_bf16 v[64:67], a[36:39], a[0:3], v[64:67]\n"
"v_mov_b32 v25, 0x3f803f80\n"
"buffer_load_dwordx4 v1, s[24:27], s37 offen offset:0 lds\n"
"s_add_u32 s37, s37, 196608\n"
"v_mfma_f32_16x16x32_bf16 v[96:99], a[40:43], a[0:3], v[96:99]\n"
"ds_read_b64_tr_b16 a[104:105], v8 offset:20480\n"
"s_cmp_le_u32 s41, 3\n"
"s_cselect_b32 s54, 7344128, 0\n"
"s_cselect_b32 s58, 2101248, 0\n"
"v_cmp_eq_u32 vcc, 0, v23\n"
"v_mfma_f32_16x16x32_bf16 v[128:131], a[44:47], a[0:3], v[128:131]\n"
"buffer_load_dword a240, v29, s[52:55], 0 offen\n"
"v_cndmask_b32 v24, 0, v25, vcc\n"
"v_mfma_f32_16x16x32_bf16 v[160:163], a[48:51], a[0:3], v[160:163]\n"
"ds_read_b64_tr_b16 a[106:107], v8 offset:20992\n"
"v_mov_b32 v25, v24\n"
"v_mfma_f32_16x16x32_bf16 v[192:195], a[52:55], a[0:3], v[192:195]\n"
"v_add_u32 v29, 2432, v29\n"
"v_mov_b32 v26, v24\n"
"v_mfma_f32_16x16x32_bf16 v[224:227], a[56:59], a[0:3], v[224:227]\n"
"v_mov_b32 v27, v24\n"
"v_mfma_f32_16x16x32_bf16 a[144:147], a[60:63], a[0:3], a[144:147]\n"
"ds_read_b128 a[76:79], v7 offset:2048\n"
"v_mfma_f32_16x16x32_bf16 a[176:179], a[64:67], a[0:3], a[176:179]\n"
"v_mfma_f32_16x16x32_bf16 a[208:211], a[68:71], a[0:3], a[208:211]\n"
"ds_read_b64_tr_b16 a[108:109], v9 offset:20480\n"
"v_mfma_f32_16x16x32_bf16 a[212:215], a[68:71], a[4:7], a[212:215]\n"
"v_mfma_f32_16x16x32_bf16 a[252:255], a[32:35], v[24:27], 0\n"
"v_mfma_f32_16x16x32_bf16 a[180:183], a[64:67], a[4:7], a[180:183]\n"
"ds_read_b64_tr_b16 a[110:111], v9 offset:20992\n"
"v_mfma_f32_16x16x32_bf16 a[148:151], a[60:63], a[4:7], a[148:151]\n"
"v_mfma_f32_16x16x32_bf16 v[228:231], a[56:59], a[4:7], v[228:231]\n"
"v_mfma_f32_16x16x32_bf16 v[196:199], a[52:55], a[4:7], v[196:199]\n"
"ds_read_b64_tr_b16 a[112:113], v10 offset:20480\n"
"v_mfma_f32_16x16x32_bf16 v[164:167], a[48:51], a[4:7], v[164:167]\n"
"v_mfma_f32_16x16x32_bf16 v[132:135], a[44:47], a[4:7], v[132:135]\n"
"ds_read_b128 a[80:83], v7 offset:4096\n"
"buffer_load_dwordx4 v2, s[28:31], s38 offen offset:1024 lds\n"
"v_mfma_f32_16x16x32_bf16 v[100:103], a[40:43], a[4:7], v[100:103]\n"
"v_mfma_f32_16x16x32_bf16 v[68:71], a[36:39], a[4:7], v[68:71]\n"
"ds_read_b64_tr_b16 a[114:115], v10 offset:21504\n"
"v_mfma_f32_16x16x32_bf16 v[36:39], a[32:35], a[4:7], v[36:39]\n"
"v_mov_b32_dpp v24, v24 row_shr:1 row_mask:0xf bank_mask:0xf bound_ctrl:1\n"
"v_mfma_f32_16x16x32_bf16 v[40:43], a[32:35], a[8:11], v[40:43]\n"
"v_mov_b32_dpp v25, v25 row_shr:1 row_mask:0xf bank_mask:0xf bound_ctrl:1\n"
"v_mfma_f32_16x16x32_bf16 v[72:75], a[36:39], a[8:11], v[72:75]\n"
"ds_read_b64_tr_b16 a[116:117], v11 offset:20480\n"
"v_mov_b32_dpp v26, v26 row_shr:1 row_mask:0xf bank_mask:0xf bound_ctrl:1\n"
"v_mfma_f32_16x16x32_bf16 v[104:107], a[40:43], a[8:11], v[104:107]\n"
"v_mov_b32_dpp v27, v27 row_shr:1 row_mask:0xf bank_mask:0xf bound_ctrl:1\n"
"v_mfma_f32_16x16x32_bf16 v[136:139], a[44:47], a[8:11], v[136:139]\n"
"ds_read_b128 a[84:87], v7 offset:6144\n"
"v_mfma_f32_16x16x32_bf16 v[168:171], a[48:51], a[8:11], v[168:171]\n"
"v_mfma_f32_16x16x32_bf16 a[252:255], a[36:39], v[24:27], a[252:255]\n"
"v_mfma_f32_16x16x32_bf16 v[200:203], a[52:55], a[8:11], v[200:203]\n"
"ds_read_b64_tr_b16 a[118:119], v11 offset:21504\n"
"v_mfma_f32_16x16x32_bf16 v[232:235], a[56:59], a[8:11], v[232:235]\n"
"v_mfma_f32_16x16x32_bf16 a[152:155], a[60:63], a[8:11], a[152:155]\n"
"v_mfma_f32_16x16x32_bf16 a[184:187], a[64:67], a[8:11], a[184:187]\n"
"ds_read_b64_tr_b16 a[120:121], v12 offset:20480\n"
"v_mfma_f32_16x16x32_bf16 a[216:219], a[68:71], a[8:11], a[216:219]\n"
"v_mfma_f32_16x16x32_bf16 a[220:223], a[68:71], a[12:15], a[220:223]\n"
"ds_read_b64_tr_b16 a[122:123], v12 offset:21504\n"
"v_mfma_f32_16x16x32_bf16 a[188:191], a[64:67], a[12:15], a[188:191]\n"
"buffer_load_dwordx4 v3, s[28:31], s38 offen offset:2048 lds\n"
"v_mfma_f32_16x16x32_bf16 a[156:159], a[60:63], a[12:15], a[156:159]\n"
"v_mfma_f32_16x16x32_bf16 v[236:239], a[56:59], a[12:15], v[236:239]\n"
"ds_read_b128 a[88:91], v7 offset:8192\n"
"v_mov_b32_dpp v24, v24 row_shr:1 row_mask:0xf bank_mask:0xf bound_ctrl:1\n"
"v_mfma_f32_16x16x32_bf16 v[204:207], a[52:55], a[12:15], v[204:207]\n"
"v_mov_b32_dpp v25, v25 row_shr:1 row_mask:0xf bank_mask:0xf bound_ctrl:1\n"
"v_mfma_f32_16x16x32_bf16 v[172:175], a[48:51], a[12:15], v[172:175]\n"
"ds_read_b64_tr_b16 a[124:125], v13 offset:20480\n"
"v_mov_b32_dpp v26, v26 row_shr:1 row_mask:0xf bank_mask:0xf bound_ctrl:1\n"
"v_mfma_f32_16x16x32_bf16 v[140:143], a[44:47], a[12:15], v[140:143]\n"
"v_mov_b32_dpp v27, v27 row_shr:1 row_mask:0xf bank_mask:0xf bound_ctrl:1\n"
"v_mfma_f32_16x16x32_bf16 v[108:111], a[40:43], a[12:15], v[108:111]\n"
"ds_read_b64_tr_b16 a[126:127], v13 offset:21504\n"
"v_mfma_f32_16x16x32_bf16 v[76:79], a[36:39], a[12:15], v[76:79]\n"
"v_mfma_f32_16x16x32_bf16 a[252:255], a[40:43], v[24:27], a[252:255]\n"
"v_mfma_f32_16x16x32_bf16 v[44:47], a[32:35], a[12:15], v[44:47]\n"
"v_mfma_f32_16x16x32_bf16 v[48:51], a[32:35], a[16:19], v[48:51]\n"
"ds_read_b128 a[92:95], v7 offset:10240\n"
"buffer_load_dword a240, v30, s[56:59], 0 offen\n"
"v_mfma_f32_16x16x32_bf16 v[80:83], a[36:39], a[16:19], v[80:83]\n"
"v_mfma_f32_16x16x32_bf16 v[112:115], a[40:43], a[16:19], v[112:115]\n"
"ds_read_b64_tr_b16 a[128:129], v10 offset:22528\n"
"v_add_u32 v30, 704, v30\n"
"v_mfma_f32_16x16x32_bf16 v[144:147], a[44:47], a[16:19], v[144:147]\n"
"v_mfma_f32_16x16x32_bf16 v[176:179], a[48:51], a[16:19], v[176:179]\n"
"ds_read_b64_tr_b16 a[130:131], v10 offset:23552\n"
"v_mfma_f32_16x16x32_bf16 v[208:211], a[52:55], a[16:19], v[208:211]\n"
"v_mfma_f32_16x16x32_bf16 v[240:243], a[56:59], a[16:19], v[240:243]\n"
"v_mfma_f32_16x16x32_bf16 a[160:163], a[60:63], a[16:19], a[160:163]\n"
"ds_read_b64_tr_b16 a[132:133], v11 offset:22528\n"
"v_mov_b32_dpp v24, v24 row_shr:1 row_mask:0xf bank_mask:0xf bound_ctrl:1\n"
"buffer_load_dwordx4 v4, s[32:35], s38 offen offset:3072 lds\n"
"v_mfma_f32_16x16x32_bf16 a[192:195], a[64:67], a[16:19], a[192:195]\n"
"v_mov_b32_dpp v25, v25 row_shr:1 row_mask:0xf bank_mask:0xf bound_ctrl:1\n"
"v_mfma_f32_16x16x32_bf16 a[224:227], a[68:71], a[16:19], a[224:227]\n"
"ds_read_b128 a[96:99], v7 offset:12288\n"
"v_mov_b32_dpp v26, v26 row_shr:1 row_mask:0xf bank_mask:0xf bound_ctrl:1\n"
"v_mfma_f32_16x16x32_bf16 a[228:231], a[68:71], a[20:23], a[228:231]\n"
"v_mov_b32_dpp v27, v27 row_shr:1 row_mask:0xf bank_mask:0xf bound_ctrl:1\n"
"v_mfma_f32_16x16x32_bf16 a[196:199], a[64:67], a[20:23], a[196:199]\n"
"ds_read_b64_tr_b16 a[134:135], v11 offset:23552\n"
"v_mfma_f32_16x16x32_bf16 a[164:167], a[60:63], a[20:23], a[164:167]\n"
"v_mfma_f32_16x16x32_bf16 a[252:255], a[44:47], v[24:27], a[252:255]\n"
"v_mfma_f32_16x16x32_bf16 v[244:247], a[56:59], a[20:23], v[244:247]\n"
"v_mfma_f32_16x16x32_bf16 v[212:215], a[52:55], a[20:23], v[212:215]\n"
"ds_read_b64_tr_b16 a[136:137], v12 offset:22528\n"
"v_mfma_f32_16x16x32_bf16 v[180:183], a[48:51], a[20:23], v[180:183]\n"
"v_mfma_f32_16x16x32_bf16 v[148:151], a[44:47], a[20:23], v[148:151]\n"
"ds_read_b128 a[100:103], v7 offset:14336\n"
"v_mfma_f32_16x16x32_bf16 v[116:119], a[40:43], a[20:23], v[116:119]\n"
"v_mfma_f32_16x16x32_bf16 v[84:87], a[36:39], a[20:23], v[84:87]\n"
"ds_read_b64_tr_b16 a[138:139], v12 offset:23552\n"
"v_mfma_f32_16x16x32_bf16 v[52:55], a[32:35], a[20:23], v[52:55]\n"
"v_mfma_f32_16x16x32_bf16 v[56:59], a[32:35], a[24:27], v[56:59]\n"
"v_mfma_f32_16x16x32_bf16 v[88:91], a[36:39], a[24:27], v[88:91]\n"
"ds_read_b64_tr_b16 a[140:141], v13 offset:22528\n"
"v_mov_b32_dpp v24, v24 row_shr:1 row_mask:0xf bank_mask:0xf bound_ctrl:1\n"
"s_add_u32 m0, s40, 4096\n"
"v_mfma_f32_16x16x32_bf16 v[120:123], a[40:43], a[24:27], v[120:123]\n"
"v_mov_b32_dpp v25, v25 row_shr:1 row_mask:0xf bank_mask:0xf bound_ctrl:1\n"
"buffer_load_dwordx4 v5, s[32:35], s38 offen offset:0 lds\n"
"s_add_u32 s38, s38, 229376\n"
"v_mfma_f32_16x16x32_bf16 v[152:155], a[44:47], a[24:27], v[152:155]\n"
"ds_read_b64_tr_b16 a[142:143], v13 offset:23552\n"
"v_mov_b32_dpp v26, v26 row_shr:1 row_mask:0xf bank_mask:0xf bound_ctrl:1\n"
"v_mfma_f32_16x16x32_bf16 v[184:187], a[48:51], a[24:27], v[184:187]\n"
"v_mov_b32_dpp v27, v27 row_shr:1 row_mask:0xf bank_mask:0xf bound_ctrl:1\n"
"v_mfma_f32_16x16x32_bf16 v[216:219], a[52:55], a[24:27], v[216:219]\n"
"v_mfma_f32_16x16x32_bf16 v[248:251], a[56:59], a[24:27], v[248:251]\n"
"v_mfma_f32_16x16x32_bf16 a[252:255], a[48:51], v[24:27], a[252:255]\n"
"v_mfma_f32_16x16x32_bf16 a[168:171], a[60:63], a[24:27], a[168:171]\n"
"v_mfma_f32_16x16x32_bf16 a[200:203], a[64:67], a[24:27], a[200:203]\n"
"v_mfma_f32_16x16x32_bf16 a[232:235], a[68:71], a[24:27], a[232:235]\n"
"v_mfma_f32_16x16x32_bf16 a[236:239], a[68:71], a[28:31], a[236:239]\n"
"v_mfma_f32_16x16x32_bf16 a[204:207], a[64:67], a[28:31], a[204:207]\n"
"v_mfma_f32_16x16x32_bf16 a[172:175], a[60:63], a[28:31], a[172:175]\n"
"v_mfma_f32_16x16x32_bf16 v[252:255], a[56:59], a[28:31], v[252:255]\n"
"v_mfma_f32_16x16x32_bf16 v[220:223], a[52:55], a[28:31], v[220:223]\n"
"v_mfma_f32_16x16x32_bf16 v[188:191], a[48:51], a[28:31], v[188:191]\n"
"v_mfma_f32_16x16x32_bf16 v[156:159], a[44:47], a[28:31], v[156:159]\n"
"v_mfma_f32_16x16x32_bf16 v[124:127], a[40:43], a[28:31], v[124:127]\n"
"v_mfma_f32_16x16x32_bf16 v[92:95], a[36:39], a[28:31], v[92:95]\n"
"v_mfma_f32_16x16x32_bf16 v[60:63], a[32:35], a[28:31], v[60:63]\n"
"s_waitcnt vmcnt(12) lgkmcnt(0)\n"
"s_barrier\n"
"v_mfma_f32_16x16x32_bf16 v[32:35], a[104:107], a[72:75], v[32:35]\n"
"ds_read_b128 a[0:3], v6 offset:32768\n"
"s_add_u32 m0, s39, 0\n"
"v_mfma_f32_16x16x32_bf16 v[64:67], a[108:111], a[72:75], v[64:67]\n"
"buffer_load_dwordx4 v20, s[44:47], s36 offen offset:0 lds\n"
"s_add_u32 s36, s36, 15360\n"
"v_mfma_f32_16x16x32_bf16 v[96:99], a[112:115], a[72:75], v[96:99]\n"
"ds_read_b64_tr_b16 a[32:33], v8 offset:40960\n"
"v_mfma_f32_16x16x32_bf16 v[128:131], a[116:119], a[72:75], v[128:131]\n"
"v_mfma_f32_16x16x32_bf16 v[160:163], a[120:123], a[72:75], v[160:163]\n"
"ds_read_b64_tr_b16 a[34:35], v8 offset:41472\n"
"v_mfma_f32_16x16x32_bf16 v[192:195], a[124:127], a[72:75], v[192:195]\n"
"v_mfma_f32_16x16x32_bf16 v[224:227], a[128:131], a[72:75], v[224:227]\n"
"buffer_load_dwordx4 v21, s[44:47], s36 offen offset:1024 lds\n"
"s_add_u32 s36, s36, 15360\n"
"v_mfma_f32_16x16x32_bf16 a[144:147], a[132:135], a[72:75], a[144:147]\n"
"ds_read_b128 a[4:7], v6 offset:34816\n"
"v_mfma_f32_16x16x32_bf16 a[176:179], a[136:139], a[72:75], a[176:179]\n"
"v_mfma_f32_16x16x32_bf16 a[208:211], a[140:143], a[72:75], a[208:211]\n"
"ds_read_b64_tr_b16 a[36:37], v9 offset:40960\n"
"v_mfma_f32_16x16x32_bf16 a[212:215], a[140:143], a[76:79], a[212:215]\n"
"v_mfma_f32_16x16x32_bf16 a[252:255], a[120:123], v[24:27], a[252:255]\n"
"v_mfma_f32_16x16x32_bf16 a[180:183], a[136:139], a[76:79], a[180:183]\n"
"ds_read_b64_tr_b16 a[38:39], v9 offset:41472\n"
"v_mfma_f32_16x16x32_bf16 a[148:151], a[132:135], a[76:79], a[148:151]\n"
"buffer_load_dwordx4 v20, s[44:47], s36 offen offset:2048 lds\n"
"s_add_u32 s36, s36, 15360\n"
"v_mfma_f32_16x16x32_bf16 v[228:231], a[128:131], a[76:79], v[228:231]\n"
"v_mfma_f32_16x16x32_bf16 v[196:199], a[124:127], a[76:79], v[196:199]\n"
"ds_read_b64_tr_b16 a[40:41], v10 offset:40960\n"
"v_mfma_f32_16x16x32_bf16 v[164:167], a[120:123], a[76:79], v[164:167]\n"
"v_mfma_f32_16x16x32_bf16 v[132:135], a[116:119], a[76:79], v[132:135]\n"
"ds_read_b128 a[8:11], v6 offset:36864\n"
"v_mfma_f32_16x16x32_bf16 v[100:103], a[112:115], a[76:79], v[100:103]\n"
"v_mfma_f32_16x16x32_bf16 v[68:71], a[108:111], a[76:79], v[68:71]\n"
"ds_read_b64_tr_b16 a[42:43], v10 offset:41984\n"
"buffer_load_dwordx4 v21, s[44:47], s36 offen offset:3072 lds\n"
"s_add_u32 s36, s36, 19456\n"
"v_mfma_f32_16x16x32_bf16 v[36:39], a[104:107], a[76:79], v[36:39]\n"
"v_mov_b32_dpp v24, v24 row_shl:1 row_mask:0xf bank_mask:0xf bound_ctrl:1\n"
"v_mfma_f32_16x16x32_bf16 v[40:43], a[104:107], a[80:83], v[40:43]\n"
"v_mov_b32_dpp v25, v25 row_shl:1 row_mask:0xf bank_mask:0xf bound_ctrl:1\n"
"v_mfma_f32_16x16x32_bf16 v[72:75], a[108:111], a[80:83], v[72:75]\n"
"ds_read_b64_tr_b16 a[44:45], v11 offset:40960\n"
"v_mov_b32_dpp v26, v26 row_shl:1 row_mask:0xf bank_mask:0xf bound_ctrl:1\n"
"v_mfma_f32_16x16x32_bf16 v[104:107], a[112:115], a[80:83], v[104:107]\n"
"v_mov_b32_dpp v27, v27 row_shl:1 row_mask:0xf bank_mask:0xf bound_ctrl:1\n"
"v_mfma_f32_16x16x32_bf16 v[136:139], a[116:119], a[80:83], v[136:139]\n"
"ds_read_b128 a[12:15], v6 offset:38912\n"
"s_add_u32 m0, s39, 4096\n"
"v_mfma_f32_16x16x32_bf16 v[168:171], a[120:123], a[80:83], v[168:171]\n"
"v_mfma_f32_16x16x32_bf16 a[252:255], a[116:119], v[24:27], a[252:255]\n"
"buffer_load_dwordx4 v20, s[44:47], s36 offen offset:0 lds\n"
"s_add_u32 s36, s36, 15360\n"
"v_mfma_f32_16x16x32_bf16 v[200:203], a[124:127], a[80:83], v[200:203]\n"
"ds_read_b64_tr_b16 a[46:47], v11 offset:41984\n"
"v_mfma_f32_16x16x32_bf16 v[232:235], a[128:131], a[80:83], v[232:235]\n"
"v_mfma_f32_16x16x32_bf16 a[152:155], a[132:135], a[80:83], a[152:155]\n"
"v_mfma_f32_16x16x32_bf16 a[184:187], a[136:139], a[80:83], a[184:187]\n"
"ds_read_b64_tr_b16 a[48:49], v12 offset:40960\n"
"v_mfma_f32_16x16x32_bf16 a[216:219], a[140:143], a[80:83], a[216:219]\n"
"v_mfma_f32_16x16x32_bf16 a[220:223], a[140:143], a[84:87], a[220:223]\n"
"ds_read_b64_tr_b16 a[50:51], v12 offset:41984\n"
"buffer_load_dwordx4 v21, s[44:47], s36 offen offset:1024 lds\n"
"s_add_u32 s36, s36, 15360\n"
"v_mfma_f32_16x16x32_bf16 a[188:191], a[136:139], a[84:87], a[188:191]\n"
"v_mfma_f32_16x16x32_bf16 a[156:159], a[132:135], a[84:87], a[156:159]\n"
"v_mfma_f32_16x16x32_bf16 v[236:239], a[128:131], a[84:87], v[236:239]\n"
"ds_read_b128 a[16:19], v6 offset:40960\n"
"v_mov_b32_dpp v24, v24 row_shl:1 row_mask:0xf bank_mask:0xf bound_ctrl:1\n"
"v_mfma_f32_16x16x32_bf16 v[204:207], a[124:127], a[84:87], v[204:207]\n"
"v_mov_b32_dpp v25, v25 row_shl:1 row_mask:0xf bank_mask:0xf bound_ctrl:1\n"
"v_mfma_f32_16x16x32_bf16 v[172:175], a[120:123], a[84:87], v[172:175]\n"
"ds_read_b64_tr_b16 a[52:53], v13 offset:40960\n"
"v_mov_b32_dpp v26, v26 row_shl:1 row_mask:0xf bank_mask:0xf bound_ctrl:1\n"
"v_mfma_f32_16x16x32_bf16 v[140:143], a[116:119], a[84:87], v[140:143]\n"
"v_mov_b32_dpp v27, v27 row_shl:1 row_mask:0xf bank_mask:0xf bound_ctrl:1\n"
"buffer_load_dwordx4 v20, s[44:47], s36 offen offset:2048 lds\n"
"s_add_u32 s36, s36, 15360\n"
"v_mfma_f32_16x16x32_bf16 v[108:111], a[112:115], a[84:87], v[108:111]\n"
"ds_read_b64_tr_b16 a[54:55], v13 offset:41984\n"
"v_mfma_f32_16x16x32_bf16 v[76:79], a[108:111], a[84:87], v[76:79]\n"
"v_mfma_f32_16x16x32_bf16 a[252:255], a[112:115], v[24:27], a[252:255]\n"
"v_mfma_f32_16x16x32_bf16 v[44:47], a[104:107], a[84:87], v[44:47]\n"
"v_mfma_f32_16x16x32_bf16 v[48:51], a[104:107], a[88:91], v[48:51]\n"
"ds_read_b128 a[20:23], v6 offset:43008\n"
"v_mfma_f32_16x16x32_bf16 v[80:83], a[108:111], a[88:91], v[80:83]\n"
"v_mfma_f32_16x16x32_bf16 v[112:115], a[112:115], a[88:91], v[112:115]\n"
"ds_read_b64_tr_b16 a[56:57], v10 offset:43008\n"
"buffer_load_dwordx4 v21, s[44:47], s36 offen offset:3072 lds\n"
"s_sub_u32 s36, s36, 111488\n"
"v_mfma_f32_16x16x32_bf16 v[144:147], a[116:119], a[88:91], v[144:147]\n"
"v_mfma_f32_16x16x32_bf16 v[176:179], a[120:123], a[88:91], v[176:179]\n"
"ds_read_b64_tr_b16 a[58:59], v10 offset:44032\n"
"v_mfma_f32_16x16x32_bf16 v[208:211], a[124:127], a[88:91], v[208:211]\n"
"v_mfma_f32_16x16x32_bf16 v[240:243], a[128:131], a[88:91], v[240:243]\n"
"v_mfma_f32_16x16x32_bf16 a[160:163], a[132:135], a[88:91], a[160:163]\n"
"ds_read_b64_tr_b16 a[60:61], v11 offset:43008\n"
"v_mov_b32_dpp v24, v24 row_shl:1 row_mask:0xf bank_mask:0xf bound_ctrl:1\n"
"s_add_u32 m0, s40, 20480\n"
"v_mfma_f32_16x16x32_bf16 a[192:195], a[136:139], a[88:91], a[192:195]\n"
"v_mov_b32_dpp v25, v25 row_shl:1 row_mask:0xf bank_mask:0xf bound_ctrl:1\n"
"buffer_load_dwordx4 v1, s[24:27], s37 offen offset:0 lds\n"
"s_add_u32 s37, s37, 196608\n"
"v_mfma_f32_16x16x32_bf16 a[224:227], a[140:143], a[88:91], a[224:227]\n"
"ds_read_b128 a[24:27], v6 offset:45056\n"
"v_mov_b32_dpp v26, v26 row_shl:1 row_mask:0xf bank_mask:0xf bound_ctrl:1\n"
"v_mfma_f32_16x16x32_bf16 a[228:231], a[140:143], a[92:95], a[228:231]\n"
"v_mov_b32_dpp v27, v27 row_shl:1 row_mask:0xf bank_mask:0xf bound_ctrl:1\n"
"v_mfma_f32_16x16x32_bf16 a[196:199], a[136:139], a[92:95], a[196:199]\n"
"ds_read_b64_tr_b16 a[62:63], v11 offset:44032\n"
"v_mfma_f32_16x16x32_bf16 a[164:167], a[132:135], a[92:95], a[164:167]\n"
"v_mfma_f32_16x16x32_bf16 a[252:255], a[108:111], v[24:27], a[252:255]\n"
"v_mfma_f32_16x16x32_bf16 v[244:247], a[128:131], a[92:95], v[244:247]\n"
"v_mfma_f32_16x16x32_bf16 v[212:215], a[124:127], a[92:95], v[212:215]\n"
"ds_read_b64_tr_b16 a[64:65], v12 offset:43008\n"
"buffer_load_dwordx4 v2, s[28:31], s38 offen offset:1024 lds\n"
"v_mfma_f32_16x16x32_bf16 v[180:183], a[120:123], a[92:95], v[180:183]\n"
"v_mfma_f32_16x16x32_bf16 v[148:151], a[116:119], a[92:95], v[148:151]\n"
"ds_read_b128 a[28:31], v6 offset:47104\n"
"v_mfma_f32_16x16x32_bf16 v[116:119], a[112:115], a[92:95], v[116:119]\n"
"v_mfma_f32_16x16x32_bf16 v[84:87], a[108:111], a[92:95], v[84:87]\n"
"ds_read_b64_tr_b16 a[66:67], v12 offset:44032\n"
"v_mfma_f32_16x16x32_bf16 v[52:55], a[104:107], a[92:95], v[52:55]\n"
"v_mfma_f32_16x16x32_bf16 v[56:59], a[104:107], a[96:99], v[56:59]\n"
"buffer_load_dwordx4 v3, s[28:31], s38 offen offset:2048 lds\n"
"v_mfma_f32_16x16x32_bf16 v[88:91], a[108:111], a[96:99], v[88:91]\n"
"ds_read_b64_tr_b16 a[68:69], v13 offset:43008\n"
"v_mov_b32_dpp v24, v24 row_shl:1 row_mask:0xf bank_mask:0xf bound_ctrl:1\n"
"v_mfma_f32_16x16x32_bf16 v[120:123], a[112:115], a[96:99], v[120:123]\n"
"v_mov_b32_dpp v25, v25 row_shl:1 row_mask:0xf bank_mask:0xf bound_ctrl:1\n"
"v_mfma_f32_16x16x32_bf16 v[152:155], a[116:119], a[96:99], v[152:155]\n"
"ds_read_b64_tr_b16 a[70:71], v13 offset:44032\n"
"v_mov_b32_dpp v26, v26 row_shl:1 row_mask:0xf bank_mask:0xf bound_ctrl:1\n"
"v_mfma_f32_16x16x32_bf16 v[184:187], a[120:123], a[96:99], v[184:187]\n"
"v_mov_b32_dpp v27, v27 row_shl:1 row_mask:0xf bank_mask:0xf bound_ctrl:1\n"
"v_mfma_f32_16x16x32_bf16 v[216:219], a[124:127], a[96:99], v[216:219]\n"
"v_mfma_f32_16x16x32_bf16 v[248:251], a[128:131], a[96:99], v[248:251]\n"
"v_mfma_f32_16x16x32_bf16 a[252:255], a[104:107], v[24:27], a[252:255]\n"
"buffer_load_dwordx4 v4, s[32:35], s38 offen offset:3072 lds\n"
"v_mfma_f32_16x16x32_bf16 a[168:171], a[132:135], a[96:99], a[168:171]\n"
"v_mfma_f32_16x16x32_bf16 a[200:203], a[136:139], a[96:99], a[200:203]\n"
"v_and_b32 v23, 0xf0, v0\n"
"v_mfma_f32_16x16x32_bf16 a[232:235], a[140:143], a[96:99], a[232:235]\n"
"v_lshrrev_b32 v22, 8, v0\n"
"v_mfma_f32_16x16x32_bf16 a[236:239], a[140:143], a[100:103], a[236:239]\n"
"v_add_lshl_u32 v22, v22, v23, 6\n"
"v_mfma_f32_16x16x32_bf16 a[204:207], a[136:139], a[100:103], a[204:207]\n"
"v_cmp_lt_u32 vcc, 0x4f, v23\n"
"s_add_u32 m0, s40, 24576\n"
"v_mfma_f32_16x16x32_bf16 a[172:175], a[132:135], a[100:103], a[172:175]\n"
"v_bfrev_b32 v23, 1\n"
"buffer_load_dwordx4 v5, s[32:35], s38 offen offset:0 lds\n"
"s_add_u32 s38, s38, 229376\n"
"v_mfma_f32_16x16x32_bf16 v[252:255], a[128:131], a[100:103], v[252:255]\n"
"s_nop 0\n"
"v_mfma_f32_16x16x32_bf16 v[220:223], a[124:127], a[100:103], v[220:223]\n"
"v_cndmask_b32 v22, v22, v23, vcc\n"
"v_mfma_f32_16x16x32_bf16 v[188:191], a[120:123], a[100:103], v[188:191]\n"
"v_mfma_f32_16x16x32_bf16 v[156:159], a[116:119], a[100:103], v[156:159]\n"
"v_mfma_f32_16x16x32_bf16 v[124:127], a[112:115], a[100:103], v[124:127]\n"
"v_mfma_f32_16x16x32_bf16 v[92:95], a[108:111], a[100:103], v[92:95]\n"
"v_mfma_f32_16x16x32_bf16 v[60:63], a[104:107], a[100:103], v[60:63]\n"
"s_waitcnt vmcnt(20) lgkmcnt(0)\n"
"s_barrier\n"
"v_mfma_f32_16x16x32_bf16 v[32:35], a[32:35], a[0:3], v[32:35]\n"
"ds_read_b128 a[72:75], v7 offset:32768\n"
"s_add_u32 m0, s40, 40960\n"
"v_mfma_f32_16x16x32_bf16 v[64:67], a[36:39], a[0:3], v[64:67]\n"
"buffer_load_dwordx4 v1, s[24:27], s37 offen offset:0 lds\n"
"s_add_u32 s37, s37, 196608\n"
"v_mfma_f32_16x16x32_bf16 v[96:99], a[40:43], a[0:3], v[96:99]\n"
"ds_read_b64_tr_b16 a[104:105], v8 offset:61440\n"
"v_mfma_f32_16x16x32_bf16 v[128:131], a[44:47], a[0:3], v[128:131]\n"
"v_mfma_f32_16x16x32_bf16 v[160:163], a[48:51], a[0:3], v[160:163]\n"
"ds_read_b64_tr_b16 a[106:107], v8 offset:61952\n"
"v_mfma_f32_16x16x32_bf16 v[192:195], a[52:55], a[0:3], v[192:195]\n"
"v_mfma_f32_16x16x32_bf16 v[224:227], a[56:59], a[0:3], v[224:227]\n"
"v_mfma_f32_16x16x32_bf16 a[144:147], a[60:63], a[0:3], a[144:147]\n"
"ds_read_b128 a[76:79], v7 offset:34816\n"
"v_mfma_f32_16x16x32_bf16 a[176:179], a[64:67], a[0:3], a[176:179]\n"
"v_mfma_f32_16x16x32_bf16 a[208:211], a[68:71], a[0:3], a[208:211]\n"
"ds_read_b64_tr_b16 a[108:109], v9 offset:61440\n"
"v_mfma_f32_16x16x32_bf16 a[212:215], a[68:71], a[4:7], a[212:215]\n"
"v_mfma_f32_16x16x32_bf16 a[252:255], a[32:35], v[24:27], a[252:255]\n"
"v_mfma_f32_16x16x32_bf16 a[180:183], a[64:67], a[4:7], a[180:183]\n"
"ds_read_b64_tr_b16 a[110:111], v9 offset:61952\n"
"v_mfma_f32_16x16x32_bf16 a[148:151], a[60:63], a[4:7], a[148:151]\n"
"v_mfma_f32_16x16x32_bf16 v[228:231], a[56:59], a[4:7], v[228:231]\n"
"v_mfma_f32_16x16x32_bf16 v[196:199], a[52:55], a[4:7], v[196:199]\n"
"ds_read_b64_tr_b16 a[112:113], v10 offset:61440\n"
"v_mfma_f32_16x16x32_bf16 v[164:167], a[48:51], a[4:7], v[164:167]\n"
"v_mfma_f32_16x16x32_bf16 v[132:135], a[44:47], a[4:7], v[132:135]\n"
"ds_read_b128 a[80:83], v7 offset:36864\n"
"buffer_load_dwordx4 v2, s[28:31], s38 offen offset:1024 lds\n"
"v_mfma_f32_16x16x32_bf16 v[100:103], a[40:43], a[4:7], v[100:103]\n"
"v_mfma_f32_16x16x32_bf16 v[68:71], a[36:39], a[4:7], v[68:71]\n"
"ds_read_b64_tr_b16 a[114:115], v10 offset:62464\n"
"v_mfma_f32_16x16x32_bf16 v[36:39], a[32:35], a[4:7], v[36:39]\n"
"v_mov_b32_dpp v24, v24 row_shr:1 row_mask:0xf bank_mask:0xf bound_ctrl:1\n"
"v_mfma_f32_16x16x32_bf16 v[40:43], a[32:35], a[8:11], v[40:43]\n"
"v_mov_b32_dpp v25, v25 row_shr:1 row_mask:0xf bank_mask:0xf bound_ctrl:1\n"
"v_mfma_f32_16x16x32_bf16 v[72:75], a[36:39], a[8:11], v[72:75]\n"
"ds_read_b64_tr_b16 a[116:117], v11 offset:61440\n"
"v_mov_b32_dpp v26, v26 row_shr:1 row_mask:0xf bank_mask:0xf bound_ctrl:1\n"
"v_mfma_f32_16x16x32_bf16 v[104:107], a[40:43], a[8:11], v[104:107]\n"
"v_mov_b32_dpp v27, v27 row_shr:1 row_mask:0xf bank_mask:0xf bound_ctrl:1\n"
"v_mfma_f32_16x16x32_bf16 v[136:139], a[44:47], a[8:11], v[136:139]\n"
"ds_read_b128 a[84:87], v7 offset:38912\n"
"v_mfma_f32_16x16x32_bf16 v[168:171], a[48:51], a[8:11], v[168:171]\n"
"v_mfma_f32_16x16x32_bf16 a[252:255], a[36:39], v[24:27], a[252:255]\n"
"v_mfma_f32_16x16x32_bf16 v[200:203], a[52:55], a[8:11], v[200:203]\n"
"ds_read_b64_tr_b16 a[118:119], v11 offset:62464\n"
"v_mfma_f32_16x16x32_bf16 v[232:235], a[56:59], a[8:11], v[232:235]\n"
"v_mfma_f32_16x16x32_bf16 a[152:155], a[60:63], a[8:11], a[152:155]\n"
"v_mfma_f32_16x16x32_bf16 a[184:187], a[64:67], a[8:11], a[184:187]\n"
"ds_read_b64_tr_b16 a[120:121], v12 offset:61440\n"
"v_mfma_f32_16x16x32_bf16 a[216:219], a[68:71], a[8:11], a[216:219]\n"
"v_mfma_f32_16x16x32_bf16 a[220:223], a[68:71], a[12:15], a[220:223]\n"
"ds_read_b64_tr_b16 a[122:123], v12 offset:62464\n"
"v_mfma_f32_16x16x32_bf16 a[188:191], a[64:67], a[12:15], a[188:191]\n"
"buffer_load_dwordx4 v3, s[28:31], s38 offen offset:2048 lds\n"
"v_mfma_f32_16x16x32_bf16 a[156:159], a[60:63], a[12:15], a[156:159]\n"
"v_mfma_f32_16x16x32_bf16 v[236:239], a[56:59], a[12:15], v[236:239]\n"
"ds_read_b128 a[88:91], v7 offset:40960\n"
"v_mov_b32_dpp v24, v24 row_shr:1 row_mask:0xf bank_mask:0xf bound_ctrl:1\n"
"v_mfma_f32_16x16x32_bf16 v[204:207], a[52:55], a[12:15], v[204:207]\n"
"v_mov_b32_dpp v25, v25 row_shr:1 row_mask:0xf bank_mask:0xf bound_ctrl:1\n"
"v_mfma_f32_16x16x32_bf16 v[172:175], a[48:51], a[12:15], v[172:175]\n"
"ds_read_b64_tr_b16 a[124:125], v13 offset:61440\n"
"v_mov_b32_dpp v26, v26 row_shr:1 row_mask:0xf bank_mask:0xf bound_ctrl:1\n"
"v_mfma_f32_16x16x32_bf16 v[140:143], a[44:47], a[12:15], v[140:143]\n"
"v_mov_b32_dpp v27, v27 row_shr:1 row_mask:0xf bank_mask:0xf bound_ctrl:1\n"
"v_mfma_f32_16x16x32_bf16 v[108:111], a[40:43], a[12:15], v[108:111]\n"
"ds_read_b64_tr_b16 a[126:127], v13 offset:62464\n"
"v_mfma_f32_16x16x32_bf16 v[76:79], a[36:39], a[12:15], v[76:79]\n"
"v_mfma_f32_16x16x32_bf16 a[252:255], a[40:43], v[24:27], a[252:255]\n"
"v_mfma_f32_16x16x32_bf16 v[44:47], a[32:35], a[12:15], v[44:47]\n"
"v_mfma_f32_16x16x32_bf16 v[48:51], a[32:35], a[16:19], v[48:51]\n"
"ds_read_b128 a[92:95], v7 offset:43008\n"
"v_mfma_f32_16x16x32_bf16 v[80:83], a[36:39], a[16:19], v[80:83]\n"
"v_mfma_f32_16x16x32_bf16 v[112:115], a[40:43], a[16:19], v[112:115]\n"
"ds_read_b64_tr_b16 a[128:129], v10 offset:63488\n"
"v_mfma_f32_16x16x32_bf16 v[144:147], a[44:47], a[16:19], v[144:147]\n"
"v_mfma_f32_16x16x32_bf16 v[176:179], a[48:51], a[16:19], v[176:179]\n"
"ds_read_b64_tr_b16 a[130:131], v10 offset:64512\n"
"v_mfma_f32_16x16x32_bf16 v[208:211], a[52:55], a[16:19], v[208:211]\n"
"v_mfma_f32_16x16x32_bf16 v[240:243], a[56:59], a[16:19], v[240:243]\n"
"v_mfma_f32_16x16x32_bf16 a[160:163], a[60:63], a[16:19], a[160:163]\n"
"ds_read_b64_tr_b16 a[132:133], v11 offset:63488\n"
"v_mov_b32_dpp v24, v24 row_shr:1 row_mask:0xf bank_mask:0xf bound_ctrl:1\n"
"buffer_load_dwordx4 v4, s[32:35], s38 offen offset:3072 lds\n"
"v_mfma_f32_16x16x32_bf16 a[192:195], a[64:67], a[16:19], a[192:195]\n"
"v_mov_b32_dpp v25, v25 row_shr:1 row_mask:0xf bank_mask:0xf bound_ctrl:1\n"
"v_mfma_f32_16x16x32_bf16 a[224:227], a[68:71], a[16:19], a[224:227]\n"
"ds_read_b128 a[96:99], v7 offset:45056\n"
"v_mov_b32_dpp v26, v26 row_shr:1 row_mask:0xf bank_mask:0xf bound_ctrl:1\n"
"v_mfma_f32_16x16x32_bf16 a[228:231], a[68:71], a[20:23], a[228:231]\n"
"v_mov_b32_dpp v27, v27 row_shr:1 row_mask:0xf bank_mask:0xf bound_ctrl:1\n"
"v_mfma_f32_16x16x32_bf16 a[196:199], a[64:67], a[20:23], a[196:199]\n"
"ds_read_b64_tr_b16 a[134:135], v11 offset:64512\n"
"v_mfma_f32_16x16x32_bf16 a[164:167], a[60:63], a[20:23], a[164:167]\n"
"v_mfma_f32_16x16x32_bf16 a[252:255], a[44:47], v[24:27], a[252:255]\n"
"v_mfma_f32_16x16x32_bf16 v[244:247], a[56:59], a[20:23], v[244:247]\n"
"v_mfma_f32_16x16x32_bf16 v[212:215], a[52:55], a[20:23], v[212:215]\n"
"ds_read_b64_tr_b16 a[136:137], v12 offset:63488\n"
"v_mfma_f32_16x16x32_bf16 v[180:183], a[48:51], a[20:23], v[180:183]\n"
"v_mfma_f32_16x16x32_bf16 v[148:151], a[44:47], a[20:23], v[148:151]\n"
"ds_read_b128 a[100:103], v7 offset:47104\n"
"v_mfma_f32_16x16x32_bf16 v[116:119], a[40:43], a[20:23], v[116:119]\n"
"v_mfma_f32_16x16x32_bf16 v[84:87], a[36:39], a[20:23], v[84:87]\n"
"ds_read_b64_tr_b16 a[138:139], v12 offset:64512\n"
"v_mfma_f32_16x16x32_bf16 v[52:55], a[32:35], a[20:23], v[52:55]\n"
"v_mfma_f32_16x16x32_bf16 v[56:59], a[32:35], a[24:27], v[56:59]\n"
"v_mfma_f32_16x16x32_bf16 v[88:91], a[36:39], a[24:27], v[88:91]\n"
"ds_read_b64_tr_b16 a[140:141], v13 offset:63488\n"
"v_mov_b32_dpp v24, v24 row_shr:1 row_mask:0xf bank_mask:0xf bound_ctrl:1\n"
"s_add_u32 m0, s40, 45056\n"
"v_mfma_f32_16x16x32_bf16 v[120:123], a[40:43], a[24:27], v[120:123]\n"
"v_mov_b32_dpp v25, v25 row_shr:1 row_mask:0xf bank_mask:0xf bound_ctrl:1\n"
"buffer_load_dwordx4 v5, s[32:35], s38 offen offset:0 lds\n"
"s_add_u32 s38, s38, 229376\n"
"v_mfma_f32_16x16x32_bf16 v[152:155], a[44:47], a[24:27], v[152:155]\n"
"ds_read_b64_tr_b16 a[142:143], v13 offset:64512\n"
"v_mov_b32_dpp v26, v26 row_shr:1 row_mask:0xf bank_mask:0xf bound_ctrl:1\n"
"v_mfma_f32_16x16x32_bf16 v[184:187], a[48:51], a[24:27], v[184:187]\n"
"v_mov_b32_dpp v27, v27 row_shr:1 row_mask:0xf bank_mask:0xf bound_ctrl:1\n"
"v_mfma_f32_16x16x32_bf16 v[216:219], a[52:55], a[24:27], v[216:219]\n"
"v_mfma_f32_16x16x32_bf16 v[248:251], a[56:59], a[24:27], v[248:251]\n"
"v_mfma_f32_16x16x32_bf16 a[252:255], a[48:51], v[24:27], a[252:255]\n"
"v_mfma_f32_16x16x32_bf16 a[168:171], a[60:63], a[24:27], a[168:171]\n"
"v_mfma_f32_16x16x32_bf16 a[200:203], a[64:67], a[24:27], a[200:203]\n"
"v_mfma_f32_16x16x32_bf16 a[232:235], a[68:71], a[24:27], a[232:235]\n"
"v_mfma_f32_16x16x32_bf16 a[236:239], a[68:71], a[28:31], a[236:239]\n"
"v_mfma_f32_16x16x32_bf16 a[204:207], a[64:67], a[28:31], a[204:207]\n"
"v_mfma_f32_16x16x32_bf16 a[172:175], a[60:63], a[28:31], a[172:175]\n"
"v_mfma_f32_16x16x32_bf16 v[252:255], a[56:59], a[28:31], v[252:255]\n"
"v_mfma_f32_16x16x32_bf16 v[220:223], a[52:55], a[28:31], v[220:223]\n"
"v_mfma_f32_16x16x32_bf16 v[188:191], a[48:51], a[28:31], v[188:191]\n"
"v_mfma_f32_16x16x32_bf16 v[156:159], a[44:47], a[28:31], v[156:159]\n"
"v_mfma_f32_16x16x32_bf16 v[124:127], a[40:43], a[28:31], v[124:127]\n"
"v_mfma_f32_16x16x32_bf16 v[92:95], a[36:39], a[28:31], v[92:95]\n"
"v_mfma_f32_16x16x32_bf16 v[60:63], a[32:35], a[28:31], v[60:63]\n"
"s_waitcnt vmcnt(10) lgkmcnt(0)\n"
"s_barrier\n"
"v_mfma_f32_16x16x32_bf16 v[32:35], a[104:107], a[72:75], v[32:35]\n"
"ds_read_b128 a[0:3], v6 offset:0\n"
"s_add_u32 m0, s39, 32768\n"
"v_mfma_f32_16x16x32_bf16 v[64:67], a[108:111], a[72:75], v[64:67]\n"
"v_mfma_f32_16x16x32_bf16 a[252:255], a[120:123], v[24:27], a[252:255]\n"
"buffer_load_dwordx4 v20, s[44:47], s36 offen offset:0 lds\n"
"s_add_u32 s36, s36, 15360\n"
"v_mfma_f32_16x16x32_bf16 v[96:99], a[112:115], a[72:75], v[96:99]\n"
"ds_read_b64_tr_b16 a[32:33], v8 offset:0\n"
"v_mov_b32_dpp v24, v24 row_shl:1 row_mask:0xf bank_mask:0xf bound_ctrl:1\n"
"v_mfma_f32_16x16x32_bf16 v[128:131], a[116:119], a[72:75], v[128:131]\n"
"v_mov_b32_dpp v25, v25 row_shl:1 row_mask:0xf bank_mask:0xf bound_ctrl:1\n"
"v_mfma_f32_16x16x32_bf16 v[160:163], a[120:123], a[72:75], v[160:163]\n"
"ds_read_b64_tr_b16 a[34:35], v8 offset:512\n"
"v_mov_b32_dpp v26, v26 row_shl:1 row_mask:0xf bank_mask:0xf bound_ctrl:1\n"
"v_mfma_f32_16x16x32_bf16 v[192:195], a[124:127], a[72:75], v[192:195]\n"
"v_mov_b32_dpp v27, v27 row_shl:1 row_mask:0xf bank_mask:0xf bound_ctrl:1\n"
"v_mfma_f32_16x16x32_bf16 v[224:227], a[128:131], a[72:75], v[224:227]\n"
"buffer_load_dwordx4 v21, s[44:47], s36 offen offset:1024 lds\n"
"s_add_u32 s36, s36, 15360\n"
"v_mfma_f32_16x16x32_bf16 a[144:147], a[132:135], a[72:75], a[144:147]\n"
"ds_read_b128 a[4:7], v6 offset:2048\n"
"v_mfma_f32_16x16x32_bf16 a[252:255], a[116:119], v[24:27], a[252:255]\n"
"v_mfma_f32_16x16x32_bf16 a[176:179], a[136:139], a[72:75], a[176:179]\n"
"v_mov_b32_dpp v24, v24 row_shl:1 row_mask:0xf bank_mask:0xf bound_ctrl:1\n"
"v_mfma_f32_16x16x32_bf16 a[208:211], a[140:143], a[72:75], a[208:211]\n"
"ds_read_b64_tr_b16 a[36:37], v9 offset:0\n"
"v_mov_b32_dpp v25, v25 row_shl:1 row_mask:0xf bank_mask:0xf bound_ctrl:1\n"
"v_mfma_f32_16x16x32_bf16 a[212:215], a[140:143], a[76:79], a[212:215]\n"
"v_mov_b32_dpp v26, v26 row_shl:1 row_mask:0xf bank_mask:0xf bound_ctrl:1\n"
"v_mfma_f32_16x16x32_bf16 a[180:183], a[136:139], a[76:79], a[180:183]\n"
"ds_read_b64_tr_b16 a[38:39], v9 offset:512\n"
"v_mov_b32_dpp v27, v27 row_shl:1 row_mask:0xf bank_mask:0xf bound_ctrl:1\n"
"v_mfma_f32_16x16x32_bf16 a[148:151], a[132:135], a[76:79], a[148:151]\n"
"buffer_load_dwordx4 v20, s[44:47], s36 offen offset:2048 lds\n"
"s_add_u32 s36, s36, 15360\n"
"v_mfma_f32_16x16x32_bf16 v[228:231], a[128:131], a[76:79], v[228:231]\n"
"v_mfma_f32_16x16x32_bf16 a[252:255], a[112:115], v[24:27], a[252:255]\n"
"v_mfma_f32_16x16x32_bf16 v[196:199], a[124:127], a[76:79], v[196:199]\n"
"ds_read_b64_tr_b16 a[40:41], v10 offset:0\n"
"v_mov_b32_dpp v24, v24 row_shl:1 row_mask:0xf bank_mask:0xf bound_ctrl:1\n"
"v_mfma_f32_16x16x32_bf16 v[164:167], a[120:123], a[76:79], v[164:167]\n"
"v_mov_b32_dpp v25, v25 row_shl:1 row_mask:0xf bank_mask:0xf bound_ctrl:1\n"
"v_mfma_f32_16x16x32_bf16 v[132:135], a[116:119], a[76:79], v[132:135]\n"
"ds_read_b128 a[8:11], v6 offset:4096\n"
"v_mov_b32_dpp v26, v26 row_shl:1 row_mask:0xf bank_mask:0xf bound_ctrl:1\n"
"v_mfma_f32_16x16x32_bf16 v[100:103], a[112:115], a[76:79], v[100:103]\n"
"v_mov_b32_dpp v27, v27 row_shl:1 row_mask:0xf bank_mask:0xf bound_ctrl:1\n"
"v_mfma_f32_16x16x32_bf16 v[68:71], a[108:111], a[76:79], v[68:71]\n"
"ds_read_b64_tr_b16 a[42:43], v10 offset:1024\n"
"buffer_load_dwordx4 v21, s[44:47], s36 offen offset:3072 lds\n"
"s_add_u32 s36, s36, 19456\n"
"v_mfma_f32_16x16x32_bf16 v[36:39], a[104:107], a[76:79], v[36:39]\n"
"v_mfma_f32_16x16x32_bf16 a[252:255], a[108:111], v[24:27], a[252:255]\n"
"v_mfma_f32_16x16x32_bf16 v[40:43], a[104:107], a[80:83], v[40:43]\n"
"v_mov_b32_dpp v24, v24 row_shl:1 row_mask:0xf bank_mask:0xf bound_ctrl:1\n"
"v_mfma_f32_16x16x32_bf16 v[72:75], a[108:111], a[80:83], v[72:75]\n"
"ds_read_b64_tr_b16 a[44:45], v11 offset:0\n"
"v_mov_b32_dpp v25, v25 row_shl:1 row_mask:0xf bank_mask:0xf bound_ctrl:1\n"
"v_mfma_f32_16x16x32_bf16 v[104:107], a[112:115], a[80:83], v[104:107]\n"
"v_mov_b32_dpp v26, v26 row_shl:1 row_mask:0xf bank_mask:0xf bound_ctrl:1\n"
"v_mfma_f32_16x16x32_bf16 v[136:139], a[116:119], a[80:83], v[136:139]\n"
"ds_read_b128 a[12:15], v6 offset:6144\n"
"v_mov_b32_dpp v27, v27 row_shl:1 row_mask:0xf bank_mask:0xf bound_ctrl:1\n"
"s_add_u32 m0, s39, 36864\n"
"v_mfma_f32_16x16x32_bf16 v[168:171], a[120:123], a[80:83], v[168:171]\n"
"buffer_load_dwordx4 v20, s[44:47], s36 offen offset:0 lds\n"
"s_add_u32 s36, s36, 15360\n"
"v_mfma_f32_16x16x32_bf16 v[200:203], a[124:127], a[80:83], v[200:203]\n"
"ds_read_b64_tr_b16 a[46:47], v11 offset:1024\n"
"v_mfma_f32_16x16x32_bf16 a[252:255], a[104:107], v[24:27], a[252:255]\n"
"v_mfma_f32_16x16x32_bf16 v[232:235], a[128:131], a[80:83], v[232:235]\n"
"v_mfma_f32_16x16x32_bf16 a[152:155], a[132:135], a[80:83], a[152:155]\n"
"v_mfma_f32_16x16x32_bf16 a[184:187], a[136:139], a[80:83], a[184:187]\n"
"ds_read_b64_tr_b16 a[48:49], v12 offset:0\n"
"v_mfma_f32_16x16x32_bf16 a[216:219], a[140:143], a[80:83], a[216:219]\n"
"v_mfma_f32_16x16x32_bf16 a[220:223], a[140:143], a[84:87], a[220:223]\n"
"ds_read_b64_tr_b16 a[50:51], v12 offset:1024\n"
"buffer_load_dwordx4 v21, s[44:47], s36 offen offset:1024 lds\n"
"s_add_u32 s36, s36, 15360\n"
"v_mfma_f32_16x16x32_bf16 a[188:191], a[136:139], a[84:87], a[188:191]\n"
"v_mfma_f32_16x16x32_bf16 a[156:159], a[132:135], a[84:87], a[156:159]\n"
"v_mfma_f32_16x16x32_bf16 v[236:239], a[128:131], a[84:87], v[236:239]\n"
"ds_read_b128 a[16:19], v6 offset:8192\n"
"v_mfma_f32_16x16x32_bf16 v[204:207], a[124:127], a[84:87], v[204:207]\n"
"v_accvgpr_read_b32 v23, a252\n"
"v_mfma_f32_16x16x32_bf16 v[172:175], a[120:123], a[84:87], v[172:175]\n"
"ds_read_b64_tr_b16 a[52:53], v13 offset:0\n"
"v_cvt_pk_bf16_f32 v24, v23, v23\n"
"v_mfma_f32_16x16x32_bf16 v[140:143], a[116:119], a[84:87], v[140:143]\n"
"v_and_b32 v25, 0xffff0000, v24\n"
"buffer_load_dwordx4 v20, s[44:47], s36 offen offset:2048 lds\n"
"s_add_u32 s36, s36, 15360\n"
"v_mfma_f32_16x16x32_bf16 v[108:111], a[112:115], a[84:87], v[108:111]\n"
"ds_read_b64_tr_b16 a[54:55], v13 offset:1024\n"
"v_sub_f32 v23, v23, v25\n"
"v_mfma_f32_16x16x32_bf16 v[76:79], a[108:111], a[84:87], v[76:79]\n"
"v_cvt_pk_bf16_f32 v25, v23, v23\n"
"v_mfma_f32_16x16x32_bf16 v[44:47], a[104:107], a[84:87], v[44:47]\n"
"buffer_store_dwordx2 v[24:25], v22, s[20:23], s3 offen offset:0 sc1\n"
"v_mfma_f32_16x16x32_bf16 v[48:51], a[104:107], a[88:91], v[48:51]\n"
"ds_read_b128 a[20:23], v6 offset:10240\n"
"v_accvgpr_read_b32 v23, a253\n"
"v_mfma_f32_16x16x32_bf16 v[80:83], a[108:111], a[88:91], v[80:83]\n"
"v_cvt_pk_bf16_f32 v26, v23, v23\n"
"v_mfma_f32_16x16x32_bf16 v[112:115], a[112:115], a[88:91], v[112:115]\n"
"ds_read_b64_tr_b16 a[56:57], v10 offset:2048\n"
"v_and_b32 v27, 0xffff0000, v26\n"
"buffer_load_dwordx4 v21, s[44:47], s36 offen offset:3072 lds\n"
"s_sub_u32 s36, s36, 111488\n"
"v_mfma_f32_16x16x32_bf16 v[144:147], a[116:119], a[88:91], v[144:147]\n"
"v_sub_f32 v23, v23, v27\n"
"v_mfma_f32_16x16x32_bf16 v[176:179], a[120:123], a[88:91], v[176:179]\n"
"ds_read_b64_tr_b16 a[58:59], v10 offset:3072\n"
"v_cvt_pk_bf16_f32 v27, v23, v23\n"
"v_mfma_f32_16x16x32_bf16 v[208:211], a[124:127], a[88:91], v[208:211]\n"
"buffer_store_dwordx2 v[26:27], v22, s[20:23], s3 offen offset:16 sc1\n"
"v_mfma_f32_16x16x32_bf16 v[240:243], a[128:131], a[88:91], v[240:243]\n"
"v_accvgpr_read_b32 v23, a254\n"
"v_mfma_f32_16x16x32_bf16 a[160:163], a[132:135], a[88:91], a[160:163]\n"
"ds_read_b64_tr_b16 a[60:61], v11 offset:2048\n"
"v_cvt_pk_bf16_f32 v24, v23, v23\n"
"s_add_u32 m0, s40, 61440\n"
"v_mfma_f32_16x16x32_bf16 a[192:195], a[136:139], a[88:91], a[192:195]\n"
"v_and_b32 v25, 0xffff0000, v24\n"
"buffer_load_dwordx4 v1, s[24:27], s37 offen offset:0 lds\n"
"s_add_u32 s37, s37, 196608\n"
"v_mfma_f32_16x16x32_bf16 a[224:227], a[140:143], a[88:91], a[224:227]\n"
"ds_read_b128 a[24:27], v6 offset:12288\n"
"v_sub_f32 v23, v23, v25\n"
"v_mfma_f32_16x16x32_bf16 a[228:231], a[140:143], a[92:95], a[228:231]\n"
"v_cvt_pk_bf16_f32 v25, v23, v23\n"
"v_mfma_f32_16x16x32_bf16 a[196:199], a[136:139], a[92:95], a[196:199]\n"
"ds_read_b64_tr_b16 a[62:63], v11 offset:3072\n"
"buffer_store_dwordx2 v[24:25], v22, s[20:23], s3 offen offset:32 sc1\n"
"v_mfma_f32_16x16x32_bf16 a[164:167], a[132:135], a[92:95], a[164:167]\n"
"v_accvgpr_read_b32 v23, a255\n"
"v_mfma_f32_16x16x32_bf16 v[244:247], a[128:131], a[92:95], v[244:247]\n"
"v_cvt_pk_bf16_f32 v26, v23, v23\n"
"v_mfma_f32_16x16x32_bf16 v[212:215], a[124:127], a[92:95], v[212:215]\n"
"ds_read_b64_tr_b16 a[64:65], v12 offset:2048\n"
"v_and_b32 v27, 0xffff0000, v26\n"
"buffer_load_dwordx4 v2, s[28:31], s38 offen offset:1024 lds\n"
"v_mfma_f32_16x16x32_bf16 v[180:183], a[120:123], a[92:95], v[180:183]\n"
"v_sub_f32 v23, v23, v27\n"
"v_mfma_f32_16x16x32_bf16 v[148:151], a[116:119], a[92:95], v[148:151]\n"
"ds_read_b128 a[28:31], v6 offset:14336\n"
"v_cvt_pk_bf16_f32 v27, v23, v23\n"
"v_mfma_f32_16x16x32_bf16 v[116:119], a[112:115], a[92:95], v[116:119]\n"
"buffer_store_dwordx2 v[26:27], v22, s[20:23], s3 offen offset:48 sc1\n"
"v_mfma_f32_16x16x32_bf16 v[84:87], a[108:111], a[92:95], v[84:87]\n"
"ds_read_b64_tr_b16 a[66:67], v12 offset:3072\n"
"v_mfma_f32_16x16x32_bf16 v[52:55], a[104:107], a[92:95], v[52:55]\n"
"v_mfma_f32_16x16x32_bf16 v[56:59], a[104:107], a[96:99], v[56:59]\n"
"buffer_load_dwordx4 v3, s[28:31], s38 offen offset:2048 lds\n"
"v_mfma_f32_16x16x32_bf16 v[88:91], a[108:111], a[96:99], v[88:91]\n"
"ds_read_b64_tr_b16 a[68:69], v13 offset:2048\n"
"v_mfma_f32_16x16x32_bf16 v[120:123], a[112:115], a[96:99], v[120:123]\n"
"v_mfma_f32_16x16x32_bf16 v[152:155], a[116:119], a[96:99], v[152:155]\n"
"ds_read_b64_tr_b16 a[70:71], v13 offset:3072\n"
"v_mfma_f32_16x16x32_bf16 v[184:187], a[120:123], a[96:99], v[184:187]\n"
"v_mfma_f32_16x16x32_bf16 v[216:219], a[124:127], a[96:99], v[216:219]\n"
"v_mfma_f32_16x16x32_bf16 v[248:251], a[128:131], a[96:99], v[248:251]\n"
"buffer_load_dwordx4 v4, s[32:35], s38 offen offset:3072 lds\n"
"v_mfma_f32_16x16x32_bf16 a[168:171], a[132:135], a[96:99], a[168:171]\n"
"v_mfma_f32_16x16x32_bf16 a[200:203], a[136:139], a[96:99], a[200:203]\n"
"v_mfma_f32_16x16x32_bf16 a[232:235], a[140:143], a[96:99], a[232:235]\n"
"v_mfma_f32_16x16x32_bf16 a[236:239], a[140:143], a[100:103], a[236:239]\n"
"v_mfma_f32_16x16x32_bf16 a[204:207], a[136:139], a[100:103], a[204:207]\n"
"s_sub_u32 s41, s41, 1\n"
"s_add_u32 m0, s40, 65536\n"
"v_mfma_f32_16x16x32_bf16 a[172:175], a[132:135], a[100:103], a[172:175]\n"
"buffer_load_dwordx4 v5, s[32:35], s38 offen offset:0 lds\n"
"s_add_u32 s38, s38, 229376\n"
"v_mfma_f32_16x16x32_bf16 v[252:255], a[128:131], a[100:103], v[252:255]\n"
"s_cmp_eq_u32 s2, s41\n"
"v_mfma_f32_16x16x32_bf16 v[220:223], a[124:127], a[100:103], v[220:223]\n"
"s_cselect_b32 s96, 6291456, 0\n"
"v_mfma_f32_16x16x32_bf16 v[188:191], a[120:123], a[100:103], v[188:191]\n"
"s_cselect_b32 s97, 7340032, 0\n"
"v_mfma_f32_16x16x32_bf16 v[156:159], a[116:119], a[100:103], v[156:159]\n"
"s_cselect_b32 s16, 2048, 0\n"
"v_mfma_f32_16x16x32_bf16 v[124:127], a[112:115], a[100:103], v[124:127]\n"
"s_sub_u32 s37, s37, s96\n"
"v_mfma_f32_16x16x32_bf16 v[92:95], a[108:111], a[100:103], v[92:95]\n"
"s_sub_u32 s38, s38, s97\n"
"v_mfma_f32_16x16x32_bf16 v[60:63], a[104:107], a[100:103], v[60:63]\n"
"s_sub_u32 s36, s36, s16\n"
"s_branch G1Q1_%=\n"
"SDQ3_%=:\n"
"s_waitcnt vmcnt(18) lgkmcnt(0)\n"
"s_barrier\n"
"v_mfma_f32_16x16x32_bf16 v[32:35], a[32:35], a[0:3], v[32:35]\n"
"ds_read_b128 a[72:75], v7 offset:0\n"
"v_and_b32 v23, 0xf0, v0\n"
"s_add_u32 m0, s40, 0\n"
"v_mfma_f32_16x16x32_bf16 v[64:67], a[36:39], a[0:3], v[64:67]\n"
"v_mov_b32 v25, 0x3f803f80\n"
"buffer_load_dwordx4 v1, s[24:27], s37 offen offset:0 lds\n"
"s_add_u32 s37, s37, 196608\n"
"v_mfma_f32_16x16x32_bf16 v[96:99], a[40:43], a[0:3], v[96:99]\n"
"ds_read_b64_tr_b16 a[104:105], v8 offset:20480\n"
"s_cmp_le_u32 s41, 3\n"
"s_cselect_b32 s54, 7344128, 0\n"
"s_cselect_b32 s58, 2101248, 0\n"
"v_cmp_eq_u32 vcc, 0, v23\n"
"v_mfma_f32_16x16x32_bf16 v[128:131], a[44:47], a[0:3], v[128:131]\n"
"buffer_load_dword a240, v29, s[52:55], 0 offen\n"
"v_cndmask_b32 v24, 0, v25, vcc\n"
"v_mfma_f32_16x16x32_bf16 v[160:163], a[48:51], a[0:3], v[160:163]\n"
"ds_read_b64_tr_b16 a[106:107], v8 offset:20992\n"
"v_mov_b32 v25, v24\n"
"v_mfma_f32_16x16x32_bf16 v[192:195], a[52:55], a[0:3], v[192:195]\n"
"v_add_u32 v29, 2432, v29\n"
"v_mov_b32 v26, v24\n"
"v_mfma_f32_16x16x32_bf16 v[224:227], a[56:59], a[0:3], v[224:227]\n"
"v_mov_b32 v27, v24\n"
"v_mfma_f32_16x16x32_bf16 a[144:147], a[60:63], a[0:3], a[144:147]\n"
"ds_read_b128 a[76:79], v7 offset:2048\n"
"v_mfma_f32_16x16x32_bf16 a[176:179], a[64:67], a[0:3], a[176:179]\n"
"v_mfma_f32_16x16x32_bf16 a[208:211], a[68:71], a[0:3], a[208:211]\n"
"ds_read_b64_tr_b16 a[108:109], v9 offset:20480\n"
"v_mfma_f32_16x16x32_bf16 a[212:215], a[68:71], a[4:7], a[212:215]\n"
"v_mfma_f32_16x16x32_bf16 a[252:255], a[52:55], v[24:27], 0\n"
"v_mfma_f32_16x16x32_bf16 a[180:183], a[64:67], a[4:7], a[180:183]\n"
"ds_read_b64_tr_b16 a[110:111], v9 offset:20992\n"
"v_mfma_f32_16x16x32_bf16 a[148:151], a[60:63], a[4:7], a[148:151]\n"
"v_mfma_f32_16x16x32_bf16 v[228:231], a[56:59], a[4:7], v[228:231]\n"
"v_mfma_f32_16x16x32_bf16 v[196:199], a[52:55], a[4:7], v[196:199]\n"
"ds_read_b64_tr_b16 a[112:113], v10 offset:20480\n"
"v_mfma_f32_16x16x32_bf16 v[164:167], a[48:51], a[4:7], v[164:167]\n"
"v_mfma_f32_16x16x32_bf16 v[132:135], a[44:47], a[4:7], v[132:135]\n"
"ds_read_b128 a[80:83], v7 offset:4096\n"
"buffer_load_dwordx4 v2, s[28:31], s38 offen offset:1024 lds\n"
"v_mfma_f32_16x16x32_bf16 v[100:103], a[40:43], a[4:7], v[100:103]\n"
"v_mfma_f32_16x16x32_bf16 v[68:71], a[36:39], a[4:7], v[68:71]\n"
"ds_read_b64_tr_b16 a[114:115], v10 offset:21504\n"
"v_mfma_f32_16x16x32_bf16 v[36:39], a[32:35], a[4:7], v[36:39]\n"
"v_mov_b32_dpp v24, v24 row_shr:1 row_mask:0xf bank_mask:0xf bound_ctrl:1\n"
"v_mfma_f32_16x16x32_bf16 v[40:43], a[32:35], a[8:11], v[40:43]\n"
"v_mov_b32_dpp v25, v25 row_shr:1 row_mask:0xf bank_mask:0xf bound_ctrl:1\n"
"v_mfma_f32_16x16x32_bf16 v[72:75], a[36:39], a[8:11], v[72:75]\n"
"ds_read_b64_tr_b16 a[116:117], v11 offset:20480\n"
"v_mov_b32_dpp v26, v26 row_shr:1 row_mask:0xf bank_mask:0xf bound_ctrl:1\n"
"v_mfma_f32_16x16x32_bf16 v[104:107], a[40:43], a[8:11], v[104:107]\n"
"v_mov_b32_dpp v27, v27 row_shr:1 row_mask:0xf bank_mask:0xf bound_ctrl:1\n"
"v_mfma_f32_16x16x32_bf16 v[136:139], a[44:47], a[8:11], v[136:139]\n"
"ds_read_b128 a[84:87], v7 offset:6144\n"
"v_mfma_f32_16x16x32_bf16 v[168:171], a[48:51], a[8:11], v[168:171]\n"
"v_mfma_f32_16x16x32_bf16 a[252:255], a[56:59], v[24:27], a[252:255]\n"
"v_mfma_f32_16x16x32_bf16 v[200:203], a[52:55], a[8:11], v[200:203]\n"
"ds_read_b64_tr_b16 a[118:119], v11 offset:21504\n"
"v_mfma_f32_16x16x32_bf16 v[232:235], a[56:59], a[8:11], v[232:235]\n"
"v_mfma_f32_16x16x32_bf16 a[152:155], a[60:63], a[8:11], a[152:155]\n"
"v_mfma_f32_16x16x32_bf16 a[184:187], a[64:67], a[8:11], a[184:187]\n"
"ds_read_b64_tr_b16 a[120:121], v12 offset:20480\n"
"v_mfma_f32_16x16x32_bf16 a[216:219], a[68:71], a[8:11], a[216:219]\n"
"v_mfma_f32_16x16x32_bf16 a[220:223], a[68:71], a[12:15], a[220:223]\n"
"ds_read_b64_tr_b16 a[122:123], v12 offset:21504\n"
"v_mfma_f32_16x16x32_bf16 a[188:191], a[64:67], a[12:15], a[188:191]\n"
"buffer_load_dwordx4 v3, s[28:31], s38 offen offset:2048 lds\n"
"v_mfma_f32_16x16x32_bf16 a[156:159], a[60:63], a[12:15], a[156:159]\n"
"v_mfma_f32_16x16x32_bf16 v[236:239], a[56:59], a[12:15], v[236:239]\n"
"ds_read_b128 a[88:91], v7 offset:8192\n"
"v_mov_b32_dpp v24, v24 row_shr:1 row_mask:0xf bank_mask:0xf bound_ctrl:1\n"
"v_mfma_f32_16x16x32_bf16 v[204:207], a[52:55], a[12:15], v[204:207]\n"
"v_mov_b32_dpp v25, v25 row_shr:1 row_mask:0xf bank_mask:0xf bound_ctrl:1\n"
"v_mfma_f32_16x16x32_bf16 v[172:175], a[48:51], a[12:15], v[172:175]\n"
"ds_read_b64_tr_b16 a[124:125], v13 offset:20480\n"
"v_mov_b32_dpp v26, v26 row_shr:1 row_mask:0xf bank_mask:0xf bound_ctrl:1\n"
"v_mfma_f32_16x16x32_bf16 v[140:143], a[44:47], a[12:15], v[140:143]\n"
"v_mov_b32_dpp v27, v27 row_shr:1 row_mask:0xf bank_mask:0xf bound_ctrl:1\n"
"v_mfma_f32_16x16x32_bf16 v[108:111], a[40:43], a[12:15], v[108:111]\n"
"ds_read_b64_tr_b16 a[126:127], v13 offset:21504\n"
"v_mfma_f32_16x16x32_bf16 v[76:79], a[36:39], a[12:15], v[76:79]\n"
"v_mfma_f32_16x16x32_bf16 a[252:255], a[60:63], v[24:27], a[252:255]\n"
"v_mfma_f32_16x16x32_bf16 v[44:47], a[32:35], a[12:15], v[44:47]\n"
"v_mfma_f32_16x16x32_bf16 v[48:51], a[32:35], a[16:19], v[48:51]\n"
"ds_read_b128 a[92:95], v7 offset:10240\n"
"buffer_load_dword a240, v30, s[56:59], 0 offen\n"
"v_mfma_f32_16x16x32_bf16 v[80:83], a[36:39], a[16:19], v[80:83]\n"
"v_mfma_f32_16x16x32_bf16 v[112:115], a[40:43], a[16:19], v[112:115]\n"
"ds_read_b64_tr_b16 a[128:129], v10 offset:22528\n"
"v_add_u32 v30, 704, v30\n"
"v_mfma_f32_16x16x32_bf16 v[144:147], a[44:47], a[16:19], v[144:147]\n"
"v_mfma_f32_16x16x32_bf16 v[176:179], a[48:51], a[16:19], v[176:179]\n"
"ds_read_b64_tr_b16 a[130:131], v10 offset:23552\n"
"v_mfma_f32_16x16x32_bf16 v[208:211], a[52:55], a[16:19], v[208:211]\n"
"v_mfma_f32_16x16x32_bf16 v[240:243], a[56:59], a[16:19], v[240:243]\n"
"v_mfma_f32_16x16x32_bf16 a[160:163], a[60:63], a[16:19], a[160:163]\n"
"ds_read_b64_tr_b16 a[132:133], v11 offset:22528\n"
"v_mov_b32_dpp v24, v24 row_shr:1 row_mask:0xf bank_mask:0xf bound_ctrl:1\n"
"buffer_load_dwordx4 v4, s[32:35], s38 offen offset:3072 lds\n"
"v_mfma_f32_16x16x32_bf16 a[192:195], a[64:67], a[16:19], a[192:195]\n"
"v_mov_b32_dpp v25, v25 row_shr:1 row_mask:0xf bank_mask:0xf bound_ctrl:1\n"
"v_mfma_f32_16x16x32_bf16 a[224:227], a[68:71], a[16:19], a[224:227]\n"
"ds_read_b128 a[96:99], v7 offset:12288\n"
"v_mov_b32_dpp v26, v26 row_shr:1 row_mask:0xf bank_mask:0xf bound_ctrl:1\n"
"v_mfma_f32_16x16x32_bf16 a[228:231], a[68:71], a[20:23], a[228:231]\n"
"v_mov_b32_dpp v27, v27 row_shr:1 row_mask:0xf bank_mask:0xf bound_ctrl:1\n"
"v_mfma_f32_16x16x32_bf16 a[196:199], a[64:67], a[20:23], a[196:199]\n"
"ds_read_b64_tr_b16 a[134:135], v11 offset:23552\n"
"v_mfma_f32_16x16x32_bf16 a[164:167], a[60:63], a[20:23], a[164:167]\n"
"v_mfma_f32_16x16x32_bf16 a[252:255], a[64:67], v[24:27], a[252:255]\n"
"v_mfma_f32_16x16x32_bf16 v[244:247], a[56:59], a[20:23], v[244:247]\n"
"v_mfma_f32_16x16x32_bf16 v[212:215], a[52:55], a[20:23], v[212:215]\n"
"ds_read_b64_tr_b16 a[136:137], v12 offset:22528\n"
"v_mfma_f32_16x16x32_bf16 v[180:183], a[48:51], a[20:23], v[180:183]\n"
"v_mfma_f32_16x16x32_bf16 v[148:151], a[44:47], a[20:23], v[148:151]\n"
"ds_read_b128 a[100:103], v7 offset:14336\n"
"v_mfma_f32_16x16x32_bf16 v[116:119], a[40:43], a[20:23], v[116:119]\n"
"v_mfma_f32_16x16x32_bf16 v[84:87], a[36:39], a[20:23], v[84:87]\n"
"ds_read_b64_tr_b16 a[138:139], v12 offset:23552\n"
"v_mfma_f32_16x16x32_bf16 v[52:55], a[32:35], a[20:23], v[52:55]\n"
"v_mfma_f32_16x16x32_bf16 v[56:59], a[32:35], a[24:27], v[56:59]\n"
"v_mfma_f32_16x16x32_bf16 v[88:91], a[36:39], a[24:27], v[88:91]\n"
"ds_read_b64_tr_b16 a[140:141], v13 offset:22528\n"
"v_mov_b32_dpp v24, v24 row_shr:1 row_mask:0xf bank_mask:0xf bound_ctrl:1\n"
"s_add_u32 m0, s40, 4096\n"
"v_mfma_f32_16x16x32_bf16 v[120:123], a[40:43], a[24:27], v[120:123]\n"
"v_mov_b32_dpp v25, v25 row_shr:1 row_mask:0xf bank_mask:0xf bound_ctrl:1\n"
"buffer_load_dwordx4 v5, s[32:35], s38 offen offset:0 lds\n"
"s_add_u32 s38, s38, 229376\n"
"v_mfma_f32_16x16x32_bf16 v[152:155], a[44:47], a[24:27], v[152:155]\n"
"ds_read_b64_tr_b16 a[142:143], v13 offset:23552\n"
"v_mov_b32_dpp v26, v26 row_shr:1 row_mask:0xf bank_mask:0xf bound_ctrl:1\n"
"v_mfma_f32_16x16x32_bf16 v[184:187], a[48:51], a[24:27], v[184:187]\n"
"v_mov_b32_dpp v27, v27 row_shr:1 row_mask:0xf bank_mask:0xf bound_ctrl:1\n"
"v_mfma_f32_16x16x32_bf16 v[216:219], a[52:55], a[24:27], v[216:219]\n"
"v_mfma_f32_16x16x32_bf16 v[248:251], a[56:59], a[24:27], v[248:251]\n"
"v_mfma_f32_16x16x32_bf16 a[252:255], a[68:71], v[24:27], a[252:255]\n"
"v_mfma_f32_16x16x32_bf16 a[168:171], a[60:63], a[24:27], a[168:171]\n"
"v_mfma_f32_16x16x32_bf16 a[200:203], a[64:67], a[24:27], a[200:203]\n"
"v_mfma_f32_16x16x32_bf16 a[232:235], a[68:71], a[24:27], a[232:235]\n"
"v_mfma_f32_16x16x32_bf16 a[236:239], a[68:71], a[28:31], a[236:239]\n"
"v_mfma_f32_16x16x32_bf16 a[204:207], a[64:67], a[28:31], a[204:207]\n"
"v_mfma_f32_16x16x32_bf16 a[172:175], a[60:63], a[28:31], a[172:175]\n"
"v_mfma_f32_16x16x32_bf16 v[252:255], a[56:59], a[28:31], v[252:255]\n"
"v_mfma_f32_16x16x32_bf16 v[220:223], a[52:55], a[28:31], v[220:223]\n"
"v_mfma_f32_16x16x32_bf16 v[188:191], a[48:51], a[28:31], v[188:191]\n"
"v_mfma_f32_16x16x32_bf16 v[156:159], a[44:47], a[28:31], v[156:159]\n"
"v_mfma_f32_16x16x32_bf16 v[124:127], a[40:43], a[28:31], v[124:127]\n"
"v_mfma_f32_16x16x32_bf16 v[92:95], a[36:39], a[28:31], v[92:95]\n"
"v_mfma_f32_16x16x32_bf16 v[60:63], a[32:35], a[28:31], v[60:63]\n"
"s_waitcnt vmcnt(12) lgkmcnt(0)\n"
"s_barrier\n"
"v_mfma_f32_16x16x32_bf16 v[32:35], a[104:107], a[72:75], v[32:35]\n"
"ds_read_b128 a[0:3], v6 offset:32768\n"
"s_add_u32 m0, s39, 0\n"
"v_mfma_f32_16x16x32_bf16 v[64:67], a[108:111], a[72:75], v[64:67]\n"
"buffer_load_dwordx4 v20, s[44:47], s36 offen offset:0 lds\n"
"s_add_u32 s36, s36, 15360\n"
"v_mfma_f32_16x16x32_bf16 v[96:99], a[112:115], a[72:75], v[96:99]\n"
"ds_read_b64_tr_b16 a[32:33], v8 offset:40960\n"
"v_mfma_f32_16x16x32_bf16 v[128:131], a[116:119], a[72:75], v[128:131]\n"
"v_mfma_f32_16x16x32_bf16 v[160:163], a[120:123], a[72:75], v[160:163]\n"
"ds_read_b64_tr_b16 a[34:35], v8 offset:41472\n"
"v_mfma_f32_16x16x32_bf16 v[192:195], a[124:127], a[72:75], v[192:195]\n"
"v_mfma_f32_16x16x32_bf16 v[224:227], a[128:131], a[72:75], v[224:227]\n"
"buffer_load_dwordx4 v21, s[44:47], s36 offen offset:1024 lds\n"
"s_add_u32 s36, s36, 15360\n"
"v_mfma_f32_16x16x32_bf16 a[144:147], a[132:135], a[72:75], a[144:147]\n"
"ds_read_b128 a[4:7], v6 offset:34816\n"
"v_mfma_f32_16x16x32_bf16 a[176:179], a[136:139], a[72:75], a[176:179]\n"
"v_mfma_f32_16x16x32_bf16 a[208:211], a[140:143], a[72:75], a[208:211]\n"
"ds_read_b64_tr_b16 a[36:37], v9 offset:40960\n"
"v_mfma_f32_16x16x32_bf16 a[212:215], a[140:143], a[76:79], a[212:215]\n"
"v_mfma_f32_16x16x32_bf16 a[252:255], a[140:143], v[24:27], a[252:255]\n"
"v_mfma_f32_16x16x32_bf16 a[180:183], a[136:139], a[76:79], a[180:183]\n"
"ds_read_b64_tr_b16 a[38:39], v9 offset:41472\n"
"v_mfma_f32_16x16x32_bf16 a[148:151], a[132:135], a[76:79], a[148:151]\n"
"buffer_load_dwordx4 v20, s[44:47], s36 offen offset:2048 lds\n"
"s_add_u32 s36, s36, 15360\n"
"v_mfma_f32_16x16x32_bf16 v[228:231], a[128:131], a[76:79], v[228:231]\n"
"v_mfma_f32_16x16x32_bf16 v[196:199], a[124:127], a[76:79], v[196:199]\n"
"ds_read_b64_tr_b16 a[40:41], v10 offset:40960\n"
"v_mfma_f32_16x16x32_bf16 v[164:167], a[120:123], a[76:79], v[164:167]\n"
"v_mfma_f32_16x16x32_bf16 v[132:135], a[116:119], a[76:79], v[132:135]\n"
"ds_read_b128 a[8:11], v6 offset:36864\n"
"v_mfma_f32_16x16x32_bf16 v[100:103], a[112:115], a[76:79], v[100:103]\n"
"v_mfma_f32_16x16x32_bf16 v[68:71], a[108:111], a[76:79], v[68:71]\n"
"ds_read_b64_tr_b16 a[42:43], v10 offset:41984\n"
"buffer_load_dwordx4 v21, s[44:47], s36 offen offset:3072 lds\n"
"s_add_u32 s36, s36, 19456\n"
"v_mfma_f32_16x16x32_bf16 v[36:39], a[104:107], a[76:79], v[36:39]\n"
"v_mov_b32_dpp v24, v24 row_shl:1 row_mask:0xf bank_mask:0xf bound_ctrl:1\n"
"v_mfma_f32_16x16x32_bf16 v[40:43], a[104:107], a[80:83], v[40:43]\n"
"v_mov_b32_dpp v25, v25 row_shl:1 row_mask:0xf bank_mask:0xf bound_ctrl:1\n"
"v_mfma_f32_16x16x32_bf16 v[72:75], a[108:111], a[80:83], v[72:75]\n"
"ds_read_b64_tr_b16 a[44:45], v11 offset:40960\n"
"v_mov_b32_dpp v26, v26 row_shl:1 row_mask:0xf bank_mask:0xf bound_ctrl:1\n"
"v_mfma_f32_16x16x32_bf16 v[104:107], a[112:115], a[80:83], v[104:107]\n"
"v_mov_b32_dpp v27, v27 row_shl:1 row_mask:0xf bank_mask:0xf bound_ctrl:1\n"
"v_mfma_f32_16x16x32_bf16 v[136:139], a[116:119], a[80:83], v[136:139]\n"
"ds_read_b128 a[12:15], v6 offset:38912\n"
"s_add_u32 m0, s39, 4096\n"
"v_mfma_f32_16x16x32_bf16 v[168:171], a[120:123], a[80:83], v[168:171]\n"
"v_mfma_f32_16x16x32_bf16 a[252:255], a[136:139], v[24:27], a[252:255]\n"
"buffer_load_dwordx4 v20, s[44:47], s36 offen offset:0 lds\n"
"s_add_u32 s36, s36, 15360\n"
"v_mfma_f32_16x16x32_bf16 v[200:203], a[124:127], a[80:83], v[200:203]\n"
"ds_read_b64_tr_b16 a[46:47], v11 offset:41984\n"
"v_mfma_f32_16x16x32_bf16 v[232:235], a[128:131], a[80:83], v[232:235]\n"
"v_mfma_f32_16x16x32_bf16 a[152:155], a[132:135], a[80:83], a[152:155]\n"
"v_mfma_f32_16x16x32_bf16 a[184:187], a[136:139], a[80:83], a[184:187]\n"
"ds_read_b64_tr_b16 a[48:49], v12 offset:40960\n"
"v_mfma_f32_16x16x32_bf16 a[216:219], a[140:143], a[80:83], a[216:219]\n"
"v_mfma_f32_16x16x32_bf16 a[220:223], a[140:143], a[84:87], a[220:223]\n"
"ds_read_b64_tr_b16 a[50:51], v12 offset:41984\n"
"buffer_load_dwordx4 v21, s[44:47], s36 offen offset:1024 lds\n"
"s_add_u32 s36, s36, 15360\n"
"v_mfma_f32_16x16x32_bf16 a[188:191], a[136:139], a[84:87], a[188:191]\n"
"v_mfma_f32_16x16x32_bf16 a[156:159], a[132:135], a[84:87], a[156:159]\n"
"v_mfma_f32_16x16x32_bf16 v[236:239], a[128:131], a[84:87], v[236:239]\n"
"ds_read_b128 a[16:19], v6 offset:40960\n"
"v_mov_b32_dpp v24, v24 row_shl:1 row_mask:0xf bank_mask:0xf bound_ctrl:1\n"
"v_mfma_f32_16x16x32_bf16 v[204:207], a[124:127], a[84:87], v[204:207]\n"
"v_mov_b32_dpp v25, v25 row_shl:1 row_mask:0xf bank_mask:0xf bound_ctrl:1\n"
"v_mfma_f32_16x16x32_bf16 v[172:175], a[120:123], a[84:87], v[172:175]\n"
"ds_read_b64_tr_b16 a[52:53], v13 offset:40960\n"
"v_mov_b32_dpp v26, v26 row_shl:1 row_mask:0xf bank_mask:0xf bound_ctrl:1\n"
"v_mfma_f32_16x16x32_bf16 v[140:143], a[116:119], a[84:87], v[140:143]\n"
"v_mov_b32_dpp v27, v27 row_shl:1 row_mask:0xf bank_mask:0xf bound_ctrl:1\n"
"buffer_load_dwordx4 v20, s[44:47], s36 offen offset:2048 lds\n"
"s_add_u32 s36, s36, 15360\n"
"v_mfma_f32_16x16x32_bf16 v[108:111], a[112:115], a[84:87], v[108:111]\n"
"ds_read_b64_tr_b16 a[54:55], v13 offset:41984\n"
"v_mfma_f32_16x16x32_bf16 v[76:79], a[108:111], a[84:87], v[76:79]\n"
"v_mfma_f32_16x16x32_bf16 a[252:255], a[132:135], v[24:27], a[252:255]\n"
"v_mfma_f32_16x16x32_bf16 v[44:47], a[104:107], a[84:87], v[44:47]\n"
"v_mfma_f32_16x16x32_bf16 v[48:51], a[104:107], a[88:91], v[48:51]\n"
"ds_read_b128 a[20:23], v6 offset:43008\n"
"v_mfma_f32_16x16x32_bf16 v[80:83], a[108:111], a[88:91], v[80:83]\n"
"v_mfma_f32_16x16x32_bf16 v[112:115], a[112:115], a[88:91], v[112:115]\n"
"ds_read_b64_tr_b16 a[56:57], v10 offset:43008\n"
"buffer_load_dwordx4 v21, s[44:47], s36 offen offset:3072 lds\n"
"s_sub_u32 s36, s36, 111488\n"
"v_mfma_f32_16x16x32_bf16 v[144:147], a[116:119], a[88:91], v[144:147]\n"
"v_mfma_f32_16x16x32_bf16 v[176:179], a[120:123], a[88:91], v[176:179]\n"
"ds_read_b64_tr_b16 a[58:59], v10 offset:44032\n"
"v_mfma_f32_16x16x32_bf16 v[208:211], a[124:127], a[88:91], v[208:211]\n"
"v_mfma_f32_16x16x32_bf16 v[240:243], a[128:131], a[88:91], v[240:243]\n"
"v_mfma_f32_16x16x32_bf16 a[160:163], a[132:135], a[88:91], a[160:163]\n"
"ds_read_b64_tr_b16 a[60:61], v11 offset:43008\n"
"v_mov_b32_dpp v24, v24 row_shl:1 row_mask:0xf bank_mask:0xf bound_ctrl:1\n"
"s_add_u32 m0, s40, 20480\n"
"v_mfma_f32_16x16x32_bf16 a[192:195], a[136:139], a[88:91], a[192:195]\n"
"v_mov_b32_dpp v25, v25 row_shl:1 row_mask:0xf bank_mask:0xf bound_ctrl:1\n"
"buffer_load_dwordx4 v1, s[24:27], s37 offen offset:0 lds\n"
"s_add_u32 s37, s37, 196608\n"
"v_mfma_f32_16x16x32_bf16 a[224:227], a[140:143], a[88:91], a[224:227]\n"
"ds_read_b128 a[24:27], v6 offset:45056\n"
"v_mov_b32_dpp v26, v26 row_shl:1 row_mask:0xf bank_mask:0xf bound_ctrl:1\n"
"v_mfma_f32_16x16x32_bf16 a[228:231], a[140:143], a[92:95], a[228:231]\n"
"v_mov_b32_dpp v27, v27 row_shl:1 row_mask:0xf bank_mask:0xf bound_ctrl:1\n"
"v_mfma_f32_16x16x32_bf16 a[196:199], a[136:139], a[92:95], a[196:199]\n"
"ds_read_b64_tr_b16 a[62:63], v11 offset:44032\n"
"v_mfma_f32_16x16x32_bf16 a[164:167], a[132:135], a[92:95], a[164:167]\n"
"v_mfma_f32_16x16x32_bf16 a[252:255], a[128:131], v[24:27], a[252:255]\n"
"v_mfma_f32_16x16x32_bf16 v[244:247], a[128:131], a[92:95], v[244:247]\n"
"v_mfma_f32_16x16x32_bf16 v[212:215], a[124:127], a[92:95], v[212:215]\n"
"ds_read_b64_tr_b16 a[64:65], v12 offset:43008\n"
"buffer_load_dwordx4 v2, s[28:31], s38 offen offset:1024 lds\n"
"v_mfma_f32_16x16x32_bf16 v[180:183], a[120:123], a[92:95], v[180:183]\n"
"v_mfma_f32_16x16x32_bf16 v[148:151], a[116:119], a[92:95], v[148:151]\n"
"ds_read_b128 a[28:31], v6 offset:47104\n"
"v_mfma_f32_16x16x32_bf16 v[116:119], a[112:115], a[92:95], v[116:119]\n"
"v_mfma_f32_16x16x32_bf16 v[84:87], a[108:111], a[92:95], v[84:87]\n"
"ds_read_b64_tr_b16 a[66:67], v12 offset:44032\n"
"v_mfma_f32_16x16x32_bf16 v[52:55], a[104:107], a[92:95], v[52:55]\n"
"v_mfma_f32_16x16x32_bf16 v[56:59], a[104:107], a[96:99], v[56:59]\n"
"buffer_load_dwordx4 v3, s[28:31], s38 offen offset:2048 lds\n"
"v_mfma_f32_16x16x32_bf16 v[88:91], a[108:111], a[96:99], v[88:91]\n"
"ds_read_b64_tr_b16 a[68:69], v13 offset:43008\n"
"v_mov_b32_dpp v24, v24 row_shl:1 row_mask:0xf bank_mask:0xf bound_ctrl:1\n"
"v_mfma_f32_16x16x32_bf16 v[120:123], a[112:115], a[96:99], v[120:123]\n"
"v_mov_b32_dpp v25, v25 row_shl:1 row_mask:0xf bank_mask:0xf bound_ctrl:1\n"
"v_mfma_f32_16x16x32_bf16 v[152:155], a[116:119], a[96:99], v[152:155]\n"
"ds_read_b64_tr_b16 a[70:71], v13 offset:44032\n"
"v_mov_b32_dpp v26, v26 row_shl:1 row_mask:0xf bank_mask:0xf bound_ctrl:1\n"
"v_mfma_f32_16x16x32_bf16 v[184:187], a[120:123], a[96:99], v[184:187]\n"
"v_mov_b32_dpp v27, v27 row_shl:1 row_mask:0xf bank_mask:0xf bound_ctrl:1\n"
"v_mfma_f32_16x16x32_bf16 v[216:219], a[124:127], a[96:99], v[216:219]\n"
"v_mfma_f32_16x16x32_bf16 v[248:251], a[128:131], a[96:99], v[248:251]\n"
"v_mfma_f32_16x16x32_bf16 a[252:255], a[124:127], v[24:27], a[252:255]\n"
"buffer_load_dwordx4 v4, s[32:35], s38 offen offset:3072 lds\n"
"v_mfma_f32_16x16x32_bf16 a[168:171], a[132:135], a[96:99], a[168:171]\n"
"v_mfma_f32_16x16x32_bf16 a[200:203], a[136:139], a[96:99], a[200:203]\n"
"v_and_b32 v23, 0xf0, v0\n"
"v_mfma_f32_16x16x32_bf16 a[232:235], a[140:143], a[96:99], a[232:235]\n"
"v_lshrrev_b32 v22, 8, v0\n"
"v_mfma_f32_16x16x32_bf16 a[236:239], a[140:143], a[100:103], a[236:239]\n"
"v_add_lshl_u32 v22, v22, v23, 6\n"
"v_mfma_f32_16x16x32_bf16 a[204:207], a[136:139], a[100:103], a[204:207]\n"
"v_cmp_lt_u32 vcc, 0x4f, v23\n"
"s_add_u32 m0, s40, 24576\n"
"v_mfma_f32_16x16x32_bf16 a[172:175], a[132:135], a[100:103], a[172:175]\n"
"v_bfrev_b32 v23, 1\n"
"buffer_load_dwordx4 v5, s[32:35], s38 offen offset:0 lds\n"
"s_add_u32 s38, s38, 229376\n"
"v_mfma_f32_16x16x32_bf16 v[252:255], a[128:131], a[100:103], v[252:255]\n"
"s_nop 0\n"
"v_mfma_f32_16x16x32_bf16 v[220:223], a[124:127], a[100:103], v[220:223]\n"
"v_cndmask_b32 v22, v22, v23, vcc\n"
"v_mfma_f32_16x16x32_bf16 v[188:191], a[120:123], a[100:103], v[188:191]\n"
"v_mfma_f32_16x16x32_bf16 v[156:159], a[116:119], a[100:103], v[156:159]\n"
"v_mfma_f32_16x16x32_bf16 v[124:127], a[112:115], a[100:103], v[124:127]\n"
"v_mfma_f32_16x16x32_bf16 v[92:95], a[108:111], a[100:103], v[92:95]\n"
"v_mfma_f32_16x16x32_bf16 v[60:63], a[104:107], a[100:103], v[60:63]\n"
"s_waitcnt vmcnt(20) lgkmcnt(0)\n"
"s_barrier\n"
"v_mfma_f32_16x16x32_bf16 v[32:35], a[32:35], a[0:3], v[32:35]\n"
"ds_read_b128 a[72:75], v7 offset:32768\n"
"s_add_u32 m0, s40, 40960\n"
"v_mfma_f32_16x16x32_bf16 v[64:67], a[36:39], a[0:3], v[64:67]\n"
"buffer_load_dwordx4 v1, s[24:27], s37 offen offset:0 lds\n"
"s_add_u32 s37, s37, 196608\n"
"v_mfma_f32_16x16x32_bf16 v[96:99], a[40:43], a[0:3], v[96:99]\n"
"ds_read_b64_tr_b16 a[104:105], v8 offset:61440\n"
"v_mfma_f32_16x16x32_bf16 v[128:131], a[44:47], a[0:3], v[128:131]\n"
"v_mfma_f32_16x16x32_bf16 v[160:163], a[48:51], a[0:3], v[160:163]\n"
"ds_read_b64_tr_b16 a[106:107], v8 offset:61952\n"
"v_mfma_f32_16x16x32_bf16 v[192:195], a[52:55], a[0:3], v[192:195]\n"
"v_mfma_f32_16x16x32_bf16 v[224:227], a[56:59], a[0:3], v[224:227]\n"
"v_mfma_f32_16x16x32_bf16 a[144:147], a[60:63], a[0:3], a[144:147]\n"
"ds_read_b128 a[76:79], v7 offset:34816\n"
"v_mfma_f32_16x16x32_bf16 a[176:179], a[64:67], a[0:3], a[176:179]\n"
"v_mfma_f32_16x16x32_bf16 a[208:211], a[68:71], a[0:3], a[208:211]\n"
"ds_read_b64_tr_b16 a[108:109], v9 offset:61440\n"
"v_mfma_f32_16x16x32_bf16 a[212:215], a[68:71], a[4:7], a[212:215]\n"
"v_mfma_f32_16x16x32_bf16 a[252:255], a[52:55], v[24:27], a[252:255]\n"
"v_mfma_f32_16x16x32_bf16 a[180:183], a[64:67], a[4:7], a[180:183]\n"
"ds_read_b64_tr_b16 a[110:111], v9 offset:61952\n"
"v_mfma_f32_16x16x32_bf16 a[148:151], a[60:63], a[4:7], a[148:151]\n"
"v_mfma_f32_16x16x32_bf16 v[228:231], a[56:59], a[4:7], v[228:231]\n"
"v_mfma_f32_16x16x32_bf16 v[196:199], a[52:55], a[4:7], v[196:199]\n"
"ds_read_b64_tr_b16 a[112:113], v10 offset:61440\n"
"v_mfma_f32_16x16x32_bf16 v[164:167], a[48:51], a[4:7], v[164:167]\n"
"v_mfma_f32_16x16x32_bf16 v[132:135], a[44:47], a[4:7], v[132:135]\n"
"ds_read_b128 a[80:83], v7 offset:36864\n"
"buffer_load_dwordx4 v2, s[28:31], s38 offen offset:1024 lds\n"
"v_mfma_f32_16x16x32_bf16 v[100:103], a[40:43], a[4:7], v[100:103]\n"
"v_mfma_f32_16x16x32_bf16 v[68:71], a[36:39], a[4:7], v[68:71]\n"
"ds_read_b64_tr_b16 a[114:115], v10 offset:62464\n"
"v_mfma_f32_16x16x32_bf16 v[36:39], a[32:35], a[4:7], v[36:39]\n"
"v_mov_b32_dpp v24, v24 row_shr:1 row_mask:0xf bank_mask:0xf bound_ctrl:1\n"
"v_mfma_f32_16x16x32_bf16 v[40:43], a[32:35], a[8:11], v[40:43]\n"
"v_mov_b32_dpp v25, v25 row_shr:1 row_mask:0xf bank_mask:0xf bound_ctrl:1\n"
"v_mfma_f32_16x16x32_bf16 v[72:75], a[36:39], a[8:11], v[72:75]\n"
"ds_read_b64_tr_b16 a[116:117], v11 offset:61440\n"
"v_mov_b32_dpp v26, v26 row_shr:1 row_mask:0xf bank_mask:0xf bound_ctrl:1\n"
"v_mfma_f32_16x16x32_bf16 v[104:107], a[40:43], a[8:11], v[104:107]\n"
"v_mov_b32_dpp v27, v27 row_shr:1 row_mask:0xf bank_mask:0xf bound_ctrl:1\n"
"v_mfma_f32_16x16x32_bf16 v[136:139], a[44:47], a[8:11], v[136:139]\n"
"ds_read_b128 a[84:87], v7 offset:38912\n"
"v_mfma_f32_16x16x32_bf16 v[168:171], a[48:51], a[8:11], v[168:171]\n"
"v_mfma_f32_16x16x32_bf16 a[252:255], a[56:59], v[24:27], a[252:255]\n"
"v_mfma_f32_16x16x32_bf16 v[200:203], a[52:55], a[8:11], v[200:203]\n"
"ds_read_b64_tr_b16 a[118:119], v11 offset:62464\n"
"v_mfma_f32_16x16x32_bf16 v[232:235], a[56:59], a[8:11], v[232:235]\n"
"v_mfma_f32_16x16x32_bf16 a[152:155], a[60:63], a[8:11], a[152:155]\n"
"v_mfma_f32_16x16x32_bf16 a[184:187], a[64:67], a[8:11], a[184:187]\n"
"ds_read_b64_tr_b16 a[120:121], v12 offset:61440\n"
"v_mfma_f32_16x16x32_bf16 a[216:219], a[68:71], a[8:11], a[216:219]\n"
"v_mfma_f32_16x16x32_bf16 a[220:223], a[68:71], a[12:15], a[220:223]\n"
"ds_read_b64_tr_b16 a[122:123], v12 offset:62464\n"
"v_mfma_f32_16x16x32_bf16 a[188:191], a[64:67], a[12:15], a[188:191]\n"
"buffer_load_dwordx4 v3, s[28:31], s38 offen offset:2048 lds\n"
"v_mfma_f32_16x16x32_bf16 a[156:159], a[60:63], a[12:15], a[156:159]\n"
"v_mfma_f32_16x16x32_bf16 v[236:239], a[56:59], a[12:15], v[236:239]\n"
"ds_read_b128 a[88:91], v7 offset:40960\n"
"v_mov_b32_dpp v24, v24 row_shr:1 row_mask:0xf bank_mask:0xf bound_ctrl:1\n"
"v_mfma_f32_16x16x32_bf16 v[204:207], a[52:55], a[12:15], v[204:207]\n"
"v_mov_b32_dpp v25, v25 row_shr:1 row_mask:0xf bank_mask:0xf bound_ctrl:1\n"
"v_mfma_f32_16x16x32_bf16 v[172:175], a[48:51], a[12:15], v[172:175]\n"
"ds_read_b64_tr_b16 a[124:125], v13 offset:61440\n"
"v_mov_b32_dpp v26, v26 row_shr:1 row_mask:0xf bank_mask:0xf bound_ctrl:1\n"
"v_mfma_f32_16x16x32_bf16 v[140:143], a[44:47], a[12:15], v[140:143]\n"
"v_mov_b32_dpp v27, v27 row_shr:1 row_mask:0xf bank_mask:0xf bound_ctrl:1\n"
"v_mfma_f32_16x16x32_bf16 v[108:111], a[40:43], a[12:15], v[108:111]\n"
"ds_read_b64_tr_b16 a[126:127], v13 offset:62464\n"
"v_mfma_f32_16x16x32_bf16 v[76:79], a[36:39], a[12:15], v[76:79]\n"
"v_mfma_f32_16x16x32_bf16 a[252:255], a[60:63], v[24:27], a[252:255]\n"
"v_mfma_f32_16x16x32_bf16 v[44:47], a[32:35], a[12:15], v[44:47]\n"
"v_mfma_f32_16x16x32_bf16 v[48:51], a[32:35], a[16:19], v[48:51]\n"
"ds_read_b128 a[92:95], v7 offset:43008\n"
"v_mfma_f32_16x16x32_bf16 v[80:83], a[36:39], a[16:19], v[80:83]\n"
"v_mfma_f32_16x16x32_bf16 v[112:115], a[40:43], a[16:19], v[112:115]\n"
"ds_read_b64_tr_b16 a[128:129], v10 offset:63488\n"
"v_mfma_f32_16x16x32_bf16 v[144:147], a[44:47], a[16:19], v[144:147]\n"
"v_mfma_f32_16x16x32_bf16 v[176:179], a[48:51], a[16:19], v[176:179]\n"
"ds_read_b64_tr_b16 a[130:131], v10 offset:64512\n"
"v_mfma_f32_16x16x32_bf16 v[208:211], a[52:55], a[16:19], v[208:211]\n"
"v_mfma_f32_16x16x32_bf16 v[240:243], a[56:59], a[16:19], v[240:243]\n"
"v_mfma_f32_16x16x32_bf16 a[160:163], a[60:63], a[16:19], a[160:163]\n"
"ds_read_b64_tr_b16 a[132:133], v11 offset:63488\n"
"v_mov_b32_dpp v24, v24 row_shr:1 row_mask:0xf bank_mask:0xf bound_ctrl:1\n"
"buffer_load_dwordx4 v4, s[32:35], s38 offen offset:3072 lds\n"
"v_mfma_f32_16x16x32_bf16 a[192:195], a[64:67], a[16:19], a[192:195]\n"
"v_mov_b32_dpp v25, v25 row_shr:1 row_mask:0xf bank_mask:0xf bound_ctrl:1\n"
"v_mfma_f32_16x16x32_bf16 a[224:227], a[68:71], a[16:19], a[224:227]\n"
"ds_read_b128 a[96:99], v7 offset:45056\n"
"v_mov_b32_dpp v26, v26 row_shr:1 row_mask:0xf bank_mask:0xf bound_ctrl:1\n"
"v_mfma_f32_16x16x32_bf16 a[228:231], a[68:71], a[20:23], a[228:231]\n"
"v_mov_b32_dpp v27, v27 row_shr:1 row_mask:0xf bank_mask:0xf bound_ctrl:1\n"
"v_mfma_f32_16x16x32_bf16 a[196:199], a[64:67], a[20:23], a[196:199]\n"
"ds_read_b64_tr_b16 a[134:135], v11 offset:64512\n"
"v_mfma_f32_16x16x32_bf16 a[164:167], a[60:63], a[20:23], a[164:167]\n"
"v_mfma_f32_16x16x32_bf16 a[252:255], a[64:67], v[24:27], a[252:255]\n"
"v_mfma_f32_16x16x32_bf16 v[244:247], a[56:59], a[20:23], v[244:247]\n"
"v_mfma_f32_16x16x32_bf16 v[212:215], a[52:55], a[20:23], v[212:215]\n"
"ds_read_b64_tr_b16 a[136:137], v12 offset:63488\n"
"v_mfma_f32_16x16x32_bf16 v[180:183], a[48:51], a[20:23], v[180:183]\n"
"v_mfma_f32_16x16x32_bf16 v[148:151], a[44:47], a[20:23], v[148:151]\n"
"ds_read_b128 a[100:103], v7 offset:47104\n"
"v_mfma_f32_16x16x32_bf16 v[116:119], a[40:43], a[20:23], v[116:119]\n"
"v_mfma_f32_16x16x32_bf16 v[84:87], a[36:39], a[20:23], v[84:87]\n"
"ds_read_b64_tr_b16 a[138:139], v12 offset:64512\n"
"v_mfma_f32_16x16x32_bf16 v[52:55], a[32:35], a[20:23], v[52:55]\n"
"v_mfma_f32_16x16x32_bf16 v[56:59], a[32:35], a[24:27], v[56:59]\n"
"v_mfma_f32_16x16x32_bf16 v[88:91], a[36:39], a[24:27], v[88:91]\n"
"ds_read_b64_tr_b16 a[140:141], v13 offset:63488\n"
"v_mov_b32_dpp v24, v24 row_shr:1 row_mask:0xf bank_mask:0xf bound_ctrl:1\n"
"s_add_u32 m0, s40, 45056\n"
"v_mfma_f32_16x16x32_bf16 v[120:123], a[40:43], a[24:27], v[120:123]\n"
"v_mov_b32_dpp v25, v25 row_shr:1 row_mask:0xf bank_mask:0xf bound_ctrl:1\n"
"buffer_load_dwordx4 v5, s[32:35], s38 offen offset:0 lds\n"
"s_add_u32 s38, s38, 229376\n"
"v_mfma_f32_16x16x32_bf16 v[152:155], a[44:47], a[24:27], v[152:155]\n"
"ds_read_b64_tr_b16 a[142:143], v13 offset:64512\n"
"v_mov_b32_dpp v26, v26 row_shr:1 row_mask:0xf bank_mask:0xf bound_ctrl:1\n"
"v_mfma_f32_16x16x32_bf16 v[184:187], a[48:51], a[24:27], v[184:187]\n"
"v_mov_b32_dpp v27, v27 row_shr:1 row_mask:0xf bank_mask:0xf bound_ctrl:1\n"
"v_mfma_f32_16x16x32_bf16 v[216:219], a[52:55], a[24:27], v[216:219]\n"
"v_mfma_f32_16x16x32_bf16 v[248:251], a[56:59], a[24:27], v[248:251]\n"
"v_mfma_f32_16x16x32_bf16 a[252:255], a[68:71], v[24:27], a[252:255]\n"
"v_mfma_f32_16x16x32_bf16 a[168:171], a[60:63], a[24:27], a[168:171]\n"
"v_mfma_f32_16x16x32_bf16 a[200:203], a[64:67], a[24:27], a[200:203]\n"
"v_mfma_f32_16x16x32_bf16 a[232:235], a[68:71], a[24:27], a[232:235]\n"
"v_mfma_f32_16x16x32_bf16 a[236:239], a[68:71], a[28:31], a[236:239]\n"
"v_mfma_f32_16x16x32_bf16 a[204:207], a[64:67], a[28:31], a[204:207]\n"
"v_mfma_f32_16x16x32_bf16 a[172:175], a[60:63], a[28:31], a[172:175]\n"
"v_mfma_f32_16x16x32_bf16 v[252:255], a[56:59], a[28:31], v[252:255]\n"
"v_mfma_f32_16x16x32_bf16 v[220:223], a[52:55], a[28:31], v[220:223]\n"
"v_mfma_f32_16x16x32_bf16 v[188:191], a[48:51], a[28:31], v[188:191]\n"
"v_mfma_f32_16x16x32_bf16 v[156:159], a[44:47], a[28:31], v[156:159]\n"
"v_mfma_f32_16x16x32_bf16 v[124:127], a[40:43], a[28:31], v[124:127]\n"
"v_mfma_f32_16x16x32_bf16 v[92:95], a[36:39], a[28:31], v[92:95]\n"
"v_mfma_f32_16x16x32_bf16 v[60:63], a[32:35], a[28:31], v[60:63]\n"
"s_waitcnt vmcnt(10) lgkmcnt(0)\n"
"s_barrier\n"
"v_mfma_f32_16x16x32_bf16 v[32:35], a[104:107], a[72:75], v[32:35]\n"
"ds_read_b128 a[0:3], v6 offset:0\n"
"s_add_u32 m0, s39, 32768\n"
"v_mfma_f32_16x16x32_bf16 v[64:67], a[108:111], a[72:75], v[64:67]\n"
"v_mfma_f32_16x16x32_bf16 a[252:255], a[140:143], v[24:27], a[252:255]\n"
"buffer_load_dwordx4 v20, s[44:47], s36 offen offset:0 lds\n"
"s_add_u32 s36, s36, 15360\n"
"v_mfma_f32_16x16x32_bf16 v[96:99], a[112:115], a[72:75], v[96:99]\n"
"ds_read_b64_tr_b16 a[32:33], v8 offset:0\n"
"v_mov_b32_dpp v24, v24 row_shl:1 row_mask:0xf bank_mask:0xf bound_ctrl:1\n"
"v_mfma_f32_16x16x32_bf16 v[128:131], a[116:119], a[72:75], v[128:131]\n"
"v_mov_b32_dpp v25, v25 row_shl:1 row_mask:0xf bank_mask:0xf bound_ctrl:1\n"
"v_mfma_f32_16x16x32_bf16 v[160:163], a[120:123], a[72:75], v[160:163]\n"
"ds_read_b64_tr_b16 a[34:35], v8 offset:512\n"
"v_mov_b32_dpp v26, v26 row_shl:1 row_mask:0xf bank_mask:0xf bound_ctrl:1\n"
"v_mfma_f32_16x16x32_bf16 v[192:195], a[124:127], a[72:75], v[192:195]\n"
"v_mov_b32_dpp v27, v27 row_shl:1 row_mask:0xf bank_mask:0xf bound_ctrl:1\n"
"v_mfma_f32_16x16x32_bf16 v[224:227], a[128:131], a[72:75], v[224:227]\n"
"buffer_load_dwordx4 v21, s[44:47], s36 offen offset:1024 lds\n"
"s_add_u32 s36, s36, 15360\n"
"v_mfma_f32_16x16x32_bf16 a[144:147], a[132:135], a[72:75], a[144:147]\n"
"ds_read_b128 a[4:7], v6 offset:2048\n"
"v_mfma_f32_16x16x32_bf16 a[252:255], a[136:139], v[24:27], a[252:255]\n"
"v_mfma_f32_16x16x32_bf16 a[176:179], a[136:139], a[72:75], a[176:179]\n"
"v_mov_b32_dpp v24, v24 row_shl:1 row_mask:0xf bank_mask:0xf bound_ctrl:1\n"
"v_mfma_f32_16x16x32_bf16 a[208:211], a[140:143], a[72:75], a[208:211]\n"
"ds_read_b64_tr_b16 a[36:37], v9 offset:0\n"
"v_mov_b32_dpp v25, v25 row_shl:1 row_mask:0xf bank_mask:0xf bound_ctrl:1\n"
"v_mfma_f32_16x16x32_bf16 a[212:215], a[140:143], a[76:79], a[212:215]\n"
"v_mov_b32_dpp v26, v26 row_shl:1 row_mask:0xf bank_mask:0xf bound_ctrl:1\n"
"v_mfma_f32_16x16x32_bf16 a[180:183], a[136:139], a[76:79], a[180:183]\n"
"ds_read_b64_tr_b16 a[38:39], v9 offset:512\n"
"v_mov_b32_dpp v27, v27 row_shl:1 row_mask:0xf bank_mask:0xf bound_ctrl:1\n"
"v_mfma_f32_16x16x32_bf16 a[148:151], a[132:135], a[76:79], a[148:151]\n"
"buffer_load_dwordx4 v20, s[44:47], s36 offen offset:2048 lds\n"
"s_add_u32 s36, s36, 15360\n"
"v_mfma_f32_16x16x32_bf16 v[228:231], a[128:131], a[76:79], v[228:231]\n"
"v_mfma_f32_16x16x32_bf16 a[252:255], a[132:135], v[24:27], a[252:255]\n"
"v_mfma_f32_16x16x32_bf16 v[196:199], a[124:127], a[76:79], v[196:199]\n"
"ds_read_b64_tr_b16 a[40:41], v10 offset:0\n"
"v_mov_b32_dpp v24, v24 row_shl:1 row_mask:0xf bank_mask:0xf bound_ctrl:1\n"
"v_mfma_f32_16x16x32_bf16 v[164:167], a[120:123], a[76:79], v[164:167]\n"
"v_mov_b32_dpp v25, v25 row_shl:1 row_mask:0xf bank_mask:0xf bound_ctrl:1\n"
"v_mfma_f32_16x16x32_bf16 v[132:135], a[116:119], a[76:79], v[132:135]\n"
"ds_read_b128 a[8:11], v6 offset:4096\n"
"v_mov_b32_dpp v26, v26 row_shl:1 row_mask:0xf bank_mask:0xf bound_ctrl:1\n"
"v_mfma_f32_16x16x32_bf16 v[100:103], a[112:115], a[76:79], v[100:103]\n"
"v_mov_b32_dpp v27, v27 row_shl:1 row_mask:0xf bank_mask:0xf bound_ctrl:1\n"
"v_mfma_f32_16x16x32_bf16 v[68:71], a[108:111], a[76:79], v[68:71]\n"
"ds_read_b64_tr_b16 a[42:43], v10 offset:1024\n"
"buffer_load_dwordx4 v21, s[44:47], s36 offen offset:3072 lds\n"
"s_add_u32 s36, s36, 19456\n"
"v_mfma_f32_16x16x32_bf16 v[36:39], a[104:107], a[76:79], v[36:39]\n"
"v_mfma_f32_16x16x32_bf16 a[252:255], a[128:131], v[24:27], a[252:255]\n"
"v_mfma_f32_16x16x32_bf16 v[40:43], a[104:107], a[80:83], v[40:43]\n"
"v_mov_b32_dpp v24, v24 row_shl:1 row_mask:0xf bank_mask:0xf bound_ctrl:1\n"
"v_mfma_f32_16x16x32_bf16 v[72:75], a[108:111], a[80:83], v[72:75]\n"
"ds_read_b64_tr_b16 a[44:45], v11 offset:0\n"
"v_mov_b32_dpp v25, v25 row_shl:1 row_mask:0xf bank_mask:0xf bound_ctrl:1\n"
"v_mfma_f32_16x16x32_bf16 v[104:107], a[112:115], a[80:83], v[104:107]\n"
"v_mov_b32_dpp v26, v26 row_shl:1 row_mask:0xf bank_mask:0xf bound_ctrl:1\n"
"v_mfma_f32_16x16x32_bf16 v[136:139], a[116:119], a[80:83], v[136:139]\n"
"ds_read_b128 a[12:15], v6 offset:6144\n"
"v_mov_b32_dpp v27, v27 row_shl:1 row_mask:0xf bank_mask:0xf bound_ctrl:1\n"
"s_add_u32 m0, s39, 36864\n"
"v_mfma_f32_16x16x32_bf16 v[168:171], a[120:123], a[80:83], v[168:171]\n"
"buffer_load_dwordx4 v20, s[44:47], s36 offen offset:0 lds\n"
"s_add_u32 s36, s36, 15360\n"
"v_mfma_f32_16x16x32_bf16 v[200:203], a[124:127], a[80:83], v[200:203]\n"
"ds_read_b64_tr_b16 a[46:47], v11 offset:1024\n"
"v_mfma_f32_16x16x32_bf16 a[252:255], a[124:127], v[24:27], a[252:255]\n"
"v_mfma_f32_16x16x32_bf16 v[232:235], a[128:131], a[80:83], v[232:235]\n"
"v_mfma_f32_16x16x32_bf16 a[152:155], a[132:135], a[80:83], a[152:155]\n"
"v_mfma_f32_16x16x32_bf16 a[184:187], a[136:139], a[80:83], a[184:187]\n"
"ds_read_b64_tr_b16 a[48:49], v12 offset:0\n"
"v_mfma_f32_16x16x32_bf16 a[216:219], a[140:143], a[80:83], a[216:219]\n"
"v_mfma_f32_16x16x32_bf16 a[220:223], a[140:143], a[84:87], a[220:223]\n"
"ds_read_b64_tr_b16 a[50:51], v12 offset:1024\n"
"buffer_load_dwordx4 v21, s[44:47], s36 offen offset:1024 lds\n"
"s_add_u32 s36, s36, 15360\n"
"v_mfma_f32_16x16x32_bf16 a[188:191], a[136:139], a[84:87], a[188:191]\n"
"v_mfma_f32_16x16x32_bf16 a[156:159], a[132:135], a[84:87], a[156:159]\n"
"v_mfma_f32_16x16x32_bf16 v[236:239], a[128:131], a[84:87], v[236:239]\n"
"ds_read_b128 a[16:19], v6 offset:8192\n"
"v_mfma_f32_16x16x32_bf16 v[204:207], a[124:127], a[84:87], v[204:207]\n"
"v_accvgpr_read_b32 v23, a252\n"
"v_mfma_f32_16x16x32_bf16 v[172:175], a[120:123], a[84:87], v[172:175]\n"
"ds_read_b64_tr_b16 a[52:53], v13 offset:0\n"
"v_cvt_pk_bf16_f32 v24, v23, v23\n"
"v_mfma_f32_16x16x32_bf16 v[140:143], a[116:119], a[84:87], v[140:143]\n"
"v_and_b32 v25, 0xffff0000, v24\n"
"buffer_load_dwordx4 v20, s[44:47], s36 offen offset:2048 lds\n"
"s_add_u32 s36, s36, 15360\n"
"v_mfma_f32_16x16x32_bf16 v[108:111], a[112:115], a[84:87], v[108:111]\n"
"ds_read_b64_tr_b16 a[54:55], v13 offset:1024\n"
"v_sub_f32 v23, v23, v25\n"
"v_mfma_f32_16x16x32_bf16 v[76:79], a[108:111], a[84:87], v[76:79]\n"
"v_cvt_pk_bf16_f32 v25, v23, v23\n"
"v_mfma_f32_16x16x32_bf16 v[44:47], a[104:107], a[84:87], v[44:47]\n"
"buffer_store_dwordx2 v[24:25], v22, s[20:23], s3 offen offset:0 sc1\n"
"v_mfma_f32_16x16x32_bf16 v[48:51], a[104:107], a[88:91], v[48:51]\n"
"ds_read_b128 a[20:23], v6 offset:10240\n"
"v_accvgpr_read_b32 v23, a253\n"
"v_mfma_f32_16x16x32_bf16 v[80:83], a[108:111], a[88:91], v[80:83]\n"
"v_cvt_pk_bf16_f32 v26, v23, v23\n"
"v_mfma_f32_16x16x32_bf16 v[112:115], a[112:115], a[88:91], v[112:115]\n"
"ds_read_b64_tr_b16 a[56:57], v10 offset:2048\n"
"v_and_b32 v27, 0xffff0000, v26\n"
"buffer_load_dwordx4 v21, s[44:47], s36 offen offset:3072 lds\n"
"s_sub_u32 s36, s36, 111488\n"
"v_mfma_f32_16x16x32_bf16 v[144:147], a[116:119], a[88:91], v[144:147]\n"
"v_sub_f32 v23, v23, v27\n"
"v_mfma_f32_16x16x32_bf16 v[176:179], a[120:123], a[88:91], v[176:179]\n"
"ds_read_b64_tr_b16 a[58:59], v10 offset:3072\n"
"v_cvt_pk_bf16_f32 v27, v23, v23\n"
"v_mfma_f32_16x16x32_bf16 v[208:211], a[124:127], a[88:91], v[208:211]\n"
"buffer_store_dwordx2 v[26:27], v22, s[20:23], s3 offen offset:16 sc1\n"
"v_mfma_f32_16x16x32_bf16 v[240:243], a[128:131], a[88:91], v[240:243]\n"
"v_accvgpr_read_b32 v23, a254\n"
"v_mfma_f32_16x16x32_bf16 a[160:163], a[132:135], a[88:91], a[160:163]\n"
"ds_read_b64_tr_b16 a[60:61], v11 offset:2048\n"
"v_cvt_pk_bf16_f32 v24, v23, v23\n"
"s_add_u32 m0, s40, 61440\n"
"v_mfma_f32_16x16x32_bf16 a[192:195], a[136:139], a[88:91], a[192:195]\n"
"v_and_b32 v25, 0xffff0000, v24\n"
"buffer_load_dwordx4 v1, s[24:27], s37 offen offset:0 lds\n"
"s_add_u32 s37, s37, 196608\n"
"v_mfma_f32_16x16x32_bf16 a[224:227], a[140:143], a[88:91], a[224:227]\n"
"ds_read_b128 a[24:27], v6 offset:12288\n"
"v_sub_f32 v23, v23, v25\n"
"v_mfma_f32_16x16x32_bf16 a[228:231], a[140:143], a[92:95], a[228:231]\n"
"v_cvt_pk_bf16_f32 v25, v23, v23\n"
"v_mfma_f32_16x16x32_bf16 a[196:199], a[136:139], a[92:95], a[196:199]\n"
"ds_read_b64_tr_b16 a[62:63], v11 offset:3072\n"
"buffer_store_dwordx2 v[24:25], v22, s[20:23], s3 offen offset:32 sc1\n"
"v_mfma_f32_16x16x32_bf16 a[164:167], a[132:135], a[92:95], a[164:167]\n"
"v_accvgpr_read_b32 v23, a255\n"
"v_mfma_f32_16x16x32_bf16 v[244:247], a[128:131], a[92:95], v[244:247]\n"
"v_cvt_pk_bf16_f32 v26, v23, v23\n"
"v_mfma_f32_16x16x32_bf16 v[212:215], a[124:127], a[92:95], v[212:215]\n"
"ds_read_b64_tr_b16 a[64:65], v12 offset:2048\n"
"v_and_b32 v27, 0xffff0000, v26\n"
"buffer_load_dwordx4 v2, s[28:31], s38 offen offset:1024 lds\n"
"v_mfma_f32_16x16x32_bf16 v[180:183], a[120:123], a[92:95], v[180:183]\n"
"v_sub_f32 v23, v23, v27\n"
"v_mfma_f32_16x16x32_bf16 v[148:151], a[116:119], a[92:95], v[148:151]\n"
"ds_read_b128 a[28:31], v6 offset:14336\n"
"v_cvt_pk_bf16_f32 v27, v23, v23\n"
"v_mfma_f32_16x16x32_bf16 v[116:119], a[112:115], a[92:95], v[116:119]\n"
"buffer_store_dwordx2 v[26:27], v22, s[20:23], s3 offen offset:48 sc1\n"
"v_mfma_f32_16x16x32_bf16 v[84:87], a[108:111], a[92:95], v[84:87]\n"
"ds_read_b64_tr_b16 a[66:67], v12 offset:3072\n"
"v_mfma_f32_16x16x32_bf16 v[52:55], a[104:107], a[92:95], v[52:55]\n"
"v_mfma_f32_16x16x32_bf16 v[56:59], a[104:107], a[96:99], v[56:59]\n"
"buffer_load_dwordx4 v3, s[28:31], s38 offen offset:2048 lds\n"
"v_mfma_f32_16x16x32_bf16 v[88:91], a[108:111], a[96:99], v[88:91]\n"
"ds_read_b64_tr_b16 a[68:69], v13 offset:2048\n"
"v_mfma_f32_16x16x32_bf16 v[120:123], a[112:115], a[96:99], v[120:123]\n"
"v_mfma_f32_16x16x32_bf16 v[152:155], a[116:119], a[96:99], v[152:155]\n"
"ds_read_b64_tr_b16 a[70:71], v13 offset:3072\n"
"v_mfma_f32_16x16x32_bf16 v[184:187], a[120:123], a[96:99], v[184:187]\n"
"v_mfma_f32_16x16x32_bf16 v[216:219], a[124:127], a[96:99], v[216:219]\n"
"v_mfma_f32_16x16x32_bf16 v[248:251], a[128:131], a[96:99], v[248:251]\n"
"buffer_load_dwordx4 v4, s[32:35], s38 offen offset:3072 lds\n"
"v_mfma_f32_16x16x32_bf16 a[168:171], a[132:135], a[96:99], a[168:171]\n"
"v_mfma_f32_16x16x32_bf16 a[200:203], a[136:139], a[96:99], a[200:203]\n"
"v_mfma_f32_16x16x32_bf16 a[232:235], a[140:143], a[96:99], a[232:235]\n"
"v_mfma_f32_16x16x32_bf16 a[236:239], a[140:143], a[100:103], a[236:239]\n"
"v_mfma_f32_16x16x32_bf16 a[204:207], a[136:139], a[100:103], a[204:207]\n"
"s_sub_u32 s41, s41, 1\n"
"s_add_u32 m0, s40, 65536\n"
"v_mfma_f32_16x16x32_bf16 a[172:175], a[132:135], a[100:103], a[172:175]\n"
"buffer_load_dwordx4 v5, s[32:35], s38 offen offset:0 lds\n"
"s_add_u32 s38, s38, 229376\n"
"v_mfma_f32_16x16x32_bf16 v[252:255], a[128:131], a[100:103], v[252:255]\n"
"s_cmp_eq_u32 s2, s41\n"
"v_mfma_f32_16x16x32_bf16 v[220:223], a[124:127], a[100:103], v[220:223]\n"
"s_cselect_b32 s96, 6291456, 0\n"
"v_mfma_f32_16x16x32_bf16 v[188:191], a[120:123], a[100:103], v[188:191]\n"
"s_cselect_b32 s97, 7340032, 0\n"
"v_mfma_f32_16x16x32_bf16 v[156:159], a[116:119], a[100:103], v[156:159]\n"
"s_cselect_b32 s16, 2048, 0\n"
"v_mfma_f32_16x16x32_bf16 v[124:127], a[112:115], a[100:103], v[124:127]\n"
"s_sub_u32 s37, s37, s96\n"
"v_mfma_f32_16x16x32_bf16 v[92:95], a[108:111], a[100:103], v[92:95]\n"
"s_sub_u32 s38, s38, s97\n"
"v_mfma_f32_16x16x32_bf16 v[60:63], a[104:107], a[100:103], v[60:63]\n"
"s_sub_u32 s36, s36, s16\n"
"s_branch G1Q1_%=\n"
"SPQ5_%=:\n"
"s_mov_b32 s94, 1048576\n"
"LPQ7_%=:\n"
"buffer_load_dwordx4 v[24:27], v0, s[20:23], s65 offen sc1\n"
"s_waitcnt vmcnt(0)\n"
"v_cmp_ne_u64 vcc, s[90:91], v[24:25]\n"
"v_cmp_ne_u64_e64 s[96:97], s[90:91], v[26:27]\n"
"s_nop 0\n"
"s_or_b64 vcc, vcc, s[96:97]\n"
"s_cbranch_vccz BKQ6_%=\n"
"s_sub_u32 s94, s94, 1\n"
"s_cbranch_scc0 LPQ7_%=\n"
"s_branch BKQ6_%=\n"
"SPQ8_%=:\n"
"s_mov_b32 s94, 1048576\n"
"LPQ10_%=:\n"
"buffer_load_dwordx4 v[20:23], v0, s[20:23], s5 offen sc1\n"
"s_waitcnt vmcnt(0)\n"
"v_cmp_ne_u64 vcc, s[90:91], v[20:21]\n"
"v_cmp_ne_u64_e64 s[96:97], s[90:91], v[22:23]\n"
"s_nop 0\n"
"s_or_b64 vcc, vcc, s[96:97]\n"
"s_cbranch_vccz BKQ9_%=\n"
"s_sub_u32 s94, s94, 1\n"
"s_cbranch_scc0 LPQ10_%=\n"
"s_branch BKQ9_%=\n"
"SPQ12_%=:\n"
"s_mov_b32 s94, 1048576\n"
"LPQ14_%=:\n"
"buffer_load_dwordx2 v[14:15], v9, s[20:23], s78 offen sc1\n"
"s_waitcnt vmcnt(0)\n"
"v_cmp_ne_u64 vcc, s[90:91], v[14:15]\n"
"s_cbranch_vccz BKQ13_%=\n"
"s_sub_u32 s94, s94, 1\n"
"s_cbranch_scc0 LPQ14_%=\n"
"s_branch BKQ13_%=\n"
"SPQ15_%=:\n"
"s_mov_b32 s94, 1048576\n"
"LPQ17_%=:\n"
"buffer_load_dwordx2 v[14:15], v9, s[20:23], s70 offen sc1\n"
"s_waitcnt vmcnt(0)\n"
"v_cmp_ne_u64 vcc, s[90:91], v[14:15]\n"
"s_cbranch_vccz BKQ16_%=\n"
"s_sub_u32 s94, s94, 1\n"
"s_cbranch_scc0 LPQ17_%=\n"
"s_branch BKQ16_%=\n"
"SPQ18_%=:\n"
"s_mov_b32 s94, 1048576\n"
"LPQ20_%=:\n"
"buffer_load_dwordx2 v[250:251], v9, s[20:23], s75 offen sc1\n"
"s_waitcnt vmcnt(0)\n"
"v_cmp_ne_u64 vcc, s[90:91], v[250:251]\n"
"s_cbranch_vccz BKQ19_%=\n"
"s_sub_u32 s94, s94, 1\n"
"s_cbranch_scc0 LPQ20_%=\n"
"s_branch BKQ19_%=\n"
"SPQ21_%=:\n"
"s_mov_b32 s94, 1048576\n"
"LPQ23_%=:\n"
"buffer_load_dwordx2 v[250:251], v8, s[20:23], s88 offen sc1\n"
"s_waitcnt vmcnt(0)\n"
"v_cmp_ne_u64 vcc, s[90:91], v[250:251]\n"
"s_cbranch_vccz BKQ22_%=\n"
"s_sub_u32 s94, s94, 1\n"
"s_cbranch_scc0 LPQ23_%=\n"
"s_branch BKQ22_%=\n"
"LXQ24_%=:\n"
"s_mov_b32 s94, 1048576\n"
"LLQ26_%=:\n"
"buffer_load_dwordx2 v[14:15], v9, s[20:23], s69 offen sc1\n"
"s_waitcnt vmcnt(0)\n"
"v_cmp_ne_u64 vcc, s[90:91], v[14:15]\n"
"s_cbranch_vccz LOQ27_%=\n"
"s_sub_u32 s94, s94, 1\n"
"s_cbranch_scc0 LLQ26_%=\n"
"LOQ27_%=:\n"
"s_add_u32 m0, s76, 0\n"
"s_nop 0\n"
"buffer_load_dwordx4 v0, s[20:23], s72 offen offset:0 sc1 lds\n"
"buffer_load_dwordx4 v0, s[20:23], s72 offen offset:1024 sc1 lds\n"
"buffer_load_dwordx4 v0, s[20:23], s72 offen offset:2048 sc1 lds\n"
"buffer_load_dwordx4 v0, s[20:23], s72 offen offset:3072 sc1 lds\n"
"s_add_u32 s72, s72, 16384\n"
"s_add_u32 m0, s76, 16384\n"
"s_nop 0\n"
"buffer_load_dwordx4 v0, s[20:23], s72 offen offset:0 sc1 lds\n"
"buffer_load_dwordx4 v0, s[20:23], s72 offen offset:1024 sc1 lds\n"
"buffer_load_dwordx4 v0, s[20:23], s72 offen offset:2048 sc1 lds\n"
"buffer_load_dwordx4 v0, s[20:23], s72 offen offset:3072 sc1 lds\n"
"s_add_u32 s72, s72, 65536\n"
"s_add_u32 m0, s76, 32768\n"
"s_nop 0\n"
"buffer_load_dwordx4 v0, s[20:23], s72 offen offset:0 sc1 lds\n"
"buffer_load_dwordx4 v0, s[20:23], s72 offen offset:1024 sc1 lds\n"
"buffer_load_dwordx4 v0, s[20:23], s72 offen offset:2048 sc1 lds\n"
"buffer_load_dwordx4 v0, s[20:23], s72 offen offset:3072 sc1 lds\n"
"s_add_u32 s72, s72, 16384\n"
"s_add_u32 m0, s76, 49152\n"
"s_nop 0\n"
"buffer_load_dwordx4 v0, s[20:23], s72 offen offset:0 sc1 lds\n"
"buffer_load_dwordx4 v0, s[20:23], s72 offen offset:1024 sc1 lds\n"
"buffer_load_dwordx4 v0, s[20:23], s72 offen offset:2048 sc1 lds\n"
"buffer_load_dwordx4 v0, s[20:23], s72 offen offset:3072 sc1 lds\n"
"s_add_u32 s72, s72, 16384\n"
"s_branch LBQ25_%=\n"
"SMQ28_%=:\n"
"buffer_load_dword a240, v16, s[56:59], s0 offen offset:0\n"
"s_nop 15\n"
"s_nop 7\n"
"v_cvt_pk_bf16_f32 v160, v160, v161\n"
"v_cvt_pk_bf16_f32 v161, v162, v163\n"
"v_cvt_pk_bf16_f32 v162, v164, v165\n"
"v_cvt_pk_bf16_f32 v163, v166, v167\n"
"buffer_store_dwordx4 v[160:163], v0, s[20:23], s85 offen offset:0 sc1\n"
"v_cvt_pk_bf16_f32 v164, v168, v169\n"
"v_cvt_pk_bf16_f32 v165, v170, v171\n"
"v_cvt_pk_bf16_f32 v166, v172, v173\n"
"v_cvt_pk_bf16_f32 v167, v174, v175\n"
"buffer_store_dwordx4 v[164:167], v0, s[20:23], s85 offen offset:1024 sc1\n"
"v_cvt_pk_bf16_f32 v168, v176, v177\n"
"v_cvt_pk_bf16_f32 v169, v178, v179\n"
"v_cvt_pk_bf16_f32 v170, v180, v181\n"
"v_cvt_pk_bf16_f32 v171, v182, v183\n"
"buffer_store_dwordx4 v[168:171], v0, s[20:23], s85 offen offset:2048 sc1\n"
"v_cvt_pk_bf16_f32 v172, v184, v185\n"
"v_cvt_pk_bf16_f32 v173, v186, v187\n"
"v_cvt_pk_bf16_f32 v174, v188, v189\n"
"v_cvt_pk_bf16_f32 v175, v190, v191\n"
"buffer_store_dwordx4 v[172:175], v0, s[20:23], s85 offen offset:3072 sc1\n"
"v_mov_b32 v8, 0\n"
"s_mov_b32 s3, 0\n"
"s_mov_b32 s10, 0\n"
"s_waitcnt vmcnt(0)\n"
"s_mov_b64 exec, 1\n"
"buffer_store_dwordx2 v[2:3], off, s[20:23], s87 sc1\n"
"s_mov_b64 exec, -1\n"
"s_mov_b32 s94, 1048576\n"
"SLQ30_%=:\n"
"buffer_load_dwordx2 v[250:251], v10, s[20:23], s84 offen sc1\n"
"buffer_load_dwordx2 v[14:15], v8, s[20:23], s88 offen sc1\n"
"s_waitcnt vmcnt(0)\n"
"v_cmp_ne_u64 vcc, s[90:91], v[250:251]\n"
"s_cbranch_vccz SOQ31_%=\n"
"v_cmp_ne_u64 vcc, s[90:91], v[14:15]\n"
"s_cmp_lg_u32 s3, 0\n"
"s_cbranch_scc1 SKQ32_%=\n"
"s_cbranch_vccnz SKQ32_%=\n"
"buffer_load_dword a240, v17, s[20:23], s86 offen offset:0 sc1\n"
"s_mov_b32 s3, 1\n"
"SKQ32_%=:\n"
"s_sub_u32 s94, s94, 1\n"
"s_cbranch_scc0 SLQ30_%=\n"
"SOQ31_%=:\n"
"s_add_u32 m0, s76, 0\n"
"s_nop 0\n"
"buffer_load_dwordx4 v0, s[20:23], s72 offen offset:0 sc1 lds\n"
"buffer_load_dwordx4 v0, s[20:23], s72 offen offset:1024 sc1 lds\n"
"s_add_u32 s72, s72, 16384\n"
"s_add_u32 m0, s76, 16384\n"
"s_nop 0\n"
"buffer_load_dwordx4 v0, s[20:23], s72 offen offset:0 sc1 lds\n"
"buffer_load_dwordx4 v0, s[20:23], s72 offen offset:1024 sc1 lds\n"
"s_add_u32 s72, s72, 16384\n"
"s_add_u32 m0, s76, 32768\n"
"s_nop 0\n"
"buffer_load_dwordx4 v0, s[20:23], s72 offen offset:0 sc1 lds\n"
"buffer_load_dwordx4 v0, s[20:23], s72 offen offset:1024 sc1 lds\n"
"s_add_u32 s72, s72, 16384\n"
"s_add_u32 m0, s76, 49152\n"
"s_nop 0\n"
"buffer_load_dwordx4 v0, s[20:23], s72 offen offset:0 sc1 lds\n"
"buffer_load_dwordx4 v0, s[20:23], s72 offen offset:1024 sc1 lds\n"
"s_add_u32 s72, s72, 16384\n"
"s_waitcnt vmcnt(0)\n"
"s_branch SBQ29_%=\n"
"KVF_%=:\n"
".Lfz_kv:\n"
"v_xor_b32 v36, 64, v33\n"
"s_add_u32 m0, s40, 20480\n"
"s_nop 0\n"
"buffer_load_dwordx4 v1, s[24:27], s37 offen offset:0 lds\n"
"buffer_load_dwordx4 v2, s[24:27], s37 offen offset:1024 lds\n"
"s_add_u32 s37, s37, 196608\n"
"v_accvgpr_write_b32 a241, v20\n"
"buffer_load_dwordx4 v3, s[28:31], s38 offen offset:2048 lds\n"
"v_accvgpr_write_b32 a242, v21\n"
"buffer_load_dwordx4 v4, s[28:31], s38 offen offset:3072 lds\n"
"v_accvgpr_write_b32 a243, v22\n"
"s_add_u32 m0, s40, 24576\n"
"s_nop 0\n"
"buffer_load_dwordx4 v5, s[28:31], s38 offen offset:0 lds\n"
"s_add_u32 s38, s38, 229376\n"
"v_accvgpr_write_b32 a244, v23\n"
"s_add_u32 m0, s39, 32768\n"
"s_nop 0\n"
"buffer_load_dwordx4 v33, s[44:47], s36 offen offset:0 lds\n"
"s_add_u32 s36, s36, 15360\n"
"v_accvgpr_write_b32 a245, v24\n"
"buffer_load_dwordx4 v36, s[44:47], s36 offen offset:1024 lds\n"
"s_add_u32 s36, s36, 15360\n"
"v_accvgpr_write_b32 a250, v25\n"
"buffer_load_dwordx4 v33, s[44:47], s36 offen offset:2048 lds\n"
"s_add_u32 s36, s36, 15360\n"
"v_accvgpr_write_b32 a251, v27\n"
"buffer_load_dwordx4 v36, s[44:47], s36 offen offset:3072 lds\n"
"s_add_u32 s36, s36, 19456\n"
"v_accvgpr_write_b32 a252, v16\n"
"s_add_u32 m0, s39, 36864\n"
"s_nop 0\n"
"buffer_load_dwordx4 v33, s[44:47], s36 offen offset:0 lds\n"
"s_add_u32 s36, s36, 15360\n"
"buffer_load_dwordx4 v36, s[44:47], s36 offen offset:1024 lds\n"
"s_add_u32 s36, s36, 15360\n"
"v_accvgpr_write_b32 a253, v17\n"
"buffer_load_dwordx4 v33, s[44:47], s36 offen offset:2048 lds\n"
"s_add_u32 s36, s36, 15360\n"
"v_accvgpr_write_b32 a254, v18\n"
"buffer_load_dwordx4 v36, s[44:47], s36 offen offset:3072 lds\n"
"s_sub_u32 s36, s36, 111488\n"
"v_accvgpr_write_b32 a255, v19\n"
"s_add_u32 m0, s40, 40960\n"
"s_nop 0\n"
"buffer_load_dwordx4 v1, s[24:27], s37 offen offset:0 lds\n"
"v_accvgpr_write_b32 a240, v28\n"
"buffer_load_dwordx4 v2, s[24:27], s37 offen offset:1024 lds\n"
"s_add_u32 s37, s37, 196608\n"
"v_mov_b32 v20, v33\n"
"buffer_load_dwordx4 v3, s[28:31], s38 offen offset:2048 lds\n"
"v_mov_b32 v21, v36\n"
"buffer_load_dwordx4 v4, s[28:31], s38 offen offset:3072 lds\n"
"v_mov_b32 v30, v34\n"
"s_add_u32 m0, s40, 45056\n"
"s_nop 0\n"
"buffer_load_dwordx4 v5, s[28:31], s38 offen offset:0 lds\n"
"s_add_u32 s38, s38, 229376\n"
"s_getpc_b64 s[18:19]\n"
"PCKV_%=:\n"
"s_sub_u32 s18, s18, PCKV_%=-CB_%=\n"
"s_subb_u32 s19, s19, 0\n"
"s_waitcnt vmcnt(18)\n"
"s_barrier\n"
"ds_read_b128 a[0:3], v6 offset:0\n"
"ds_read_b64_tr_b16 a[32:33], v8 offset:0\n"
"ds_read_b64_tr_b16 a[34:35], v8 offset:512\n"
"ds_read_b128 a[4:7], v6 offset:2048\n"
"ds_read_b64_tr_b16 a[36:37], v9 offset:0\n"
"ds_read_b64_tr_b16 a[38:39], v9 offset:512\n"
"ds_read_b64_tr_b16 a[40:41], v8 offset:1024\n"
"ds_read_b128 a[8:11], v6 offset:4096\n"
"ds_read_b64_tr_b16 a[42:43], v8 offset:1536\n"
"ds_read_b64_tr_b16 a[44:45], v9 offset:1024\n"
"ds_read_b128 a[12:15], v6 offset:6144\n"
"ds_read_b64_tr_b16 a[46:47], v9 offset:1536\n"
"ds_read_b64_tr_b16 a[48:49], v10 offset:0\n"
"ds_read_b64_tr_b16 a[50:51], v10 offset:768\n"
"ds_read_b128 a[16:19], v6 offset:8192\n"
"ds_read_b64_tr_b16 a[52:53], v11 offset:0\n"
"ds_read_b64_tr_b16 a[54:55], v11 offset:768\n"
"ds_read_b128 a[20:23], v6 offset:10240\n"
"ds_read_b64_tr_b16 a[56:57], v12 offset:0\n"
"ds_read_b64_tr_b16 a[58:59], v12 offset:768\n"
"ds_read_b64_tr_b16 a[60:61], v13 offset:0\n"
"ds_read_b128 a[24:27], v6 offset:12288\n"
"ds_read_b64_tr_b16 a[62:63], v13 offset:768\n"
"ds_read_b64_tr_b16 a[64:65], v14 offset:0\n"
"ds_read_b128 a[28:31], v6 offset:14336\n"
"ds_read_b64_tr_b16 a[66:67], v14 offset:768\n"
"ds_read_b64_tr_b16 a[68:69], v15 offset:0\n"
"ds_read_b64_tr_b16 a[70:71], v15 offset:768\n"
"s_waitcnt vmcnt(13) lgkmcnt(0)\n"
"s_barrier\n"
"v_mfma_f32_16x16x32_bf16 v[32:35], a[32:35], a[0:3], 0\n"
"ds_read_b128 a[72:75], v7 offset:0\n"
"s_add_u32 m0, s40, 61440\n"
"v_mfma_f32_16x16x32_bf16 v[64:67], a[36:39], a[0:3], 0\n"
"buffer_load_dwordx4 v1, s[24:27], s37 offen offset:0 lds\n"
"v_mfma_f32_16x16x32_bf16 v[96:99], a[0:3], a[40:43], 0\n"
"ds_read_b64_tr_b16 a[104:105], v8 offset:20480\n"
"v_mfma_f32_16x16x32_bf16 v[128:131], a[0:3], a[44:47], 0\n"
"v_mfma_f32_16x16x32_bf16 v[160:163], a[48:51], a[0:3], 0\n"
"ds_read_b64_tr_b16 a[106:107], v8 offset:20992\n"
"v_mfma_f32_16x16x32_bf16 v[192:195], a[52:55], a[0:3], 0\n"
"v_mfma_f32_16x16x32_bf16 v[224:227], a[56:59], a[0:3], 0\n"
"buffer_load_dwordx4 v2, s[24:27], s37 offen offset:1024 lds\n"
"s_add_u32 s37, s37, 196608\n"
"v_mfma_f32_16x16x32_bf16 a[144:147], a[60:63], a[0:3], 0\n"
"ds_read_b128 a[76:79], v7 offset:2048\n"
"v_mfma_f32_16x16x32_bf16 a[176:179], a[64:67], a[0:3], 0\n"
"v_mfma_f32_16x16x32_bf16 a[208:211], a[68:71], a[0:3], 0\n"
"ds_read_b64_tr_b16 a[108:109], v9 offset:20480\n"
"v_mfma_f32_16x16x32_bf16 a[212:215], a[68:71], a[4:7], 0\n"
"v_mfma_f32_16x16x32_bf16 a[180:183], a[64:67], a[4:7], 0\n"
"ds_read_b64_tr_b16 a[110:111], v9 offset:20992\n"
"buffer_load_dwordx4 v3, s[28:31], s38 offen offset:2048 lds\n"
"v_mfma_f32_16x16x32_bf16 a[148:151], a[60:63], a[4:7], 0\n"
"v_mfma_f32_16x16x32_bf16 v[228:231], a[56:59], a[4:7], 0\n"
"v_mfma_f32_16x16x32_bf16 v[196:199], a[52:55], a[4:7], 0\n"
"ds_read_b64_tr_b16 a[112:113], v8 offset:21504\n"
"v_mfma_f32_16x16x32_bf16 v[164:167], a[48:51], a[4:7], 0\n"
"v_mfma_f32_16x16x32_bf16 v[132:135], a[4:7], a[44:47], 0\n"
"ds_read_b128 a[80:83], v7 offset:4096\n"
"buffer_load_dwordx4 v4, s[28:31], s38 offen offset:3072 lds\n"
"v_mfma_f32_16x16x32_bf16 v[100:103], a[4:7], a[40:43], 0\n"
"v_mfma_f32_16x16x32_bf16 v[68:71], a[36:39], a[4:7], 0\n"
"ds_read_b64_tr_b16 a[114:115], v8 offset:22016\n"
"v_mfma_f32_16x16x32_bf16 v[36:39], a[32:35], a[4:7], 0\n"
"v_mfma_f32_16x16x32_bf16 v[40:43], a[32:35], a[8:11], 0\n"
"s_add_u32 m0, s40, 65536\n"
"v_mfma_f32_16x16x32_bf16 v[72:75], a[36:39], a[8:11], 0\n"
"ds_read_b64_tr_b16 a[116:117], v9 offset:21504\n"
"buffer_load_dwordx4 v5, s[28:31], s38 offen offset:0 lds\n"
"s_add_u32 s38, s38, 229376\n"
"v_mfma_f32_16x16x32_bf16 v[104:107], a[8:11], a[40:43], 0\n"
"v_mfma_f32_16x16x32_bf16 v[136:139], a[8:11], a[44:47], 0\n"
"ds_read_b128 a[84:87], v7 offset:6144\n"
"v_mfma_f32_16x16x32_bf16 v[168:171], a[48:51], a[8:11], 0\n"
"v_mfma_f32_16x16x32_bf16 v[200:203], a[52:55], a[8:11], 0\n"
"ds_read_b64_tr_b16 a[118:119], v9 offset:22016\n"
"v_mfma_f32_16x16x32_bf16 v[232:235], a[56:59], a[8:11], 0\n"
"buffer_load_dwordx2 v[28:29], off, s[20:23], 0 sc1\n"
"s_cmp_eq_u32 s2, 7\n"
"s_cselect_b32 s96, 6291456, 0\n"
"s_cselect_b32 s97, 7340032, 0\n"
"s_cselect_b32 s16, 2048, 0\n"
"v_mfma_f32_16x16x32_bf16 a[152:155], a[60:63], a[8:11], 0\n"
"v_mfma_f32_16x16x32_bf16 a[184:187], a[64:67], a[8:11], 0\n"
"ds_read_b64_tr_b16 a[120:121], v10 offset:20480\n"
"v_mfma_f32_16x16x32_bf16 a[216:219], a[68:71], a[8:11], 0\n"
"v_mfma_f32_16x16x32_bf16 a[220:223], a[68:71], a[12:15], 0\n"
"ds_read_b64_tr_b16 a[122:123], v10 offset:21248\n"
"v_mfma_f32_16x16x32_bf16 a[188:191], a[64:67], a[12:15], 0\n"
"buffer_load_dwordx4 v[16:19], v0, s[44:47], s41 offen offset:0\n"
"s_sub_u32 s37, s37, s96\n"
"s_sub_u32 s38, s38, s97\n"
"v_mfma_f32_16x16x32_bf16 a[156:159], a[60:63], a[12:15], 0\n"
"v_mfma_f32_16x16x32_bf16 v[236:239], a[56:59], a[12:15], 0\n"
"ds_read_b128 a[88:91], v7 offset:8192\n"
"v_mfma_f32_16x16x32_bf16 v[204:207], a[52:55], a[12:15], 0\n"
"v_mfma_f32_16x16x32_bf16 v[172:175], a[48:51], a[12:15], 0\n"
"ds_read_b64_tr_b16 a[124:125], v11 offset:20480\n"
"v_mfma_f32_16x16x32_bf16 v[140:143], a[12:15], a[44:47], 0\n"
"buffer_load_dwordx4 v[24:27], v0, s[44:47], s41 offen offset:1024\n"
"s_sub_u32 s36, s36, s16\n"
"v_mfma_f32_16x16x32_bf16 v[108:111], a[12:15], a[40:43], 0\n"
"ds_read_b64_tr_b16 a[126:127], v11 offset:21248\n"
"v_mfma_f32_16x16x32_bf16 v[76:79], a[36:39], a[12:15], 0\n"
"v_mfma_f32_16x16x32_bf16 v[44:47], a[32:35], a[12:15], 0\n"
"v_mfma_f32_16x16x32_bf16 v[48:51], a[32:35], a[16:19], 0\n"
"ds_read_b128 a[92:95], v7 offset:10240\n"
"v_mfma_f32_16x16x32_bf16 v[80:83], a[36:39], a[16:19], 0\n"
"s_add_u32 m0, s40, 0\n"
"v_mfma_f32_16x16x32_bf16 v[112:115], a[16:19], a[40:43], 0\n"
"ds_read_b64_tr_b16 a[128:129], v12 offset:20480\n"
"buffer_load_dwordx4 v1, s[24:27], s37 offen offset:0 lds\n"
"v_mfma_f32_16x16x32_bf16 v[144:147], a[16:19], a[44:47], 0\n"
"v_mfma_f32_16x16x32_bf16 v[176:179], a[48:51], a[16:19], 0\n"
"ds_read_b64_tr_b16 a[130:131], v12 offset:21248\n"
"v_mfma_f32_16x16x32_bf16 v[208:211], a[52:55], a[16:19], 0\n"
"v_mfma_f32_16x16x32_bf16 v[240:243], a[56:59], a[16:19], 0\n"
"v_mfma_f32_16x16x32_bf16 a[160:163], a[60:63], a[16:19], 0\n"
"ds_read_b64_tr_b16 a[132:133], v13 offset:20480\n"
"buffer_load_dwordx4 v2, s[24:27], s37 offen offset:1024 lds\n"
"s_add_u32 s37, s37, 196608\n"
"v_mfma_f32_16x16x32_bf16 a[192:195], a[64:67], a[16:19], 0\n"
"v_mfma_f32_16x16x32_bf16 a[224:227], a[68:71], a[16:19], 0\n"
"ds_read_b128 a[96:99], v7 offset:12288\n"
"v_mfma_f32_16x16x32_bf16 a[228:231], a[68:71], a[20:23], 0\n"
"v_mfma_f32_16x16x32_bf16 a[196:199], a[64:67], a[20:23], 0\n"
"ds_read_b64_tr_b16 a[134:135], v13 offset:21248\n"
"v_mfma_f32_16x16x32_bf16 a[164:167], a[60:63], a[20:23], 0\n"
"buffer_load_dwordx4 v3, s[28:31], s38 offen offset:2048 lds\n"
"v_mfma_f32_16x16x32_bf16 v[244:247], a[56:59], a[20:23], 0\n"
"v_mfma_f32_16x16x32_bf16 v[212:215], a[52:55], a[20:23], 0\n"
"ds_read_b64_tr_b16 a[136:137], v14 offset:20480\n"
"v_mfma_f32_16x16x32_bf16 v[180:183], a[48:51], a[20:23], 0\n"
"v_mfma_f32_16x16x32_bf16 v[148:151], a[20:23], a[44:47], 0\n"
"ds_read_b128 a[100:103], v7 offset:14336\n"
"v_mfma_f32_16x16x32_bf16 v[116:119], a[20:23], a[40:43], 0\n"
"buffer_load_dwordx4 v4, s[28:31], s38 offen offset:3072 lds\n"
"v_mfma_f32_16x16x32_bf16 v[84:87], a[36:39], a[20:23], 0\n"
"ds_read_b64_tr_b16 a[138:139], v14 offset:21248\n"
"v_mfma_f32_16x16x32_bf16 v[52:55], a[32:35], a[20:23], 0\n"
"v_mfma_f32_16x16x32_bf16 v[56:59], a[32:35], a[24:27], 0\n"
"v_mfma_f32_16x16x32_bf16 v[88:91], a[36:39], a[24:27], 0\n"
"ds_read_b64_tr_b16 a[140:141], v15 offset:20480\n"
"s_add_u32 m0, s40, 4096\n"
"v_mfma_f32_16x16x32_bf16 v[120:123], a[24:27], a[40:43], 0\n"
"buffer_load_dwordx4 v5, s[28:31], s38 offen offset:0 lds\n"
"s_add_u32 s38, s38, 229376\n"
"v_mfma_f32_16x16x32_bf16 v[152:155], a[24:27], a[44:47], 0\n"
"ds_read_b64_tr_b16 a[142:143], v15 offset:21248\n"
"v_mfma_f32_16x16x32_bf16 v[184:187], a[48:51], a[24:27], 0\n"
"v_mfma_f32_16x16x32_bf16 v[216:219], a[52:55], a[24:27], 0\n"
"v_mfma_f32_16x16x32_bf16 v[248:251], a[56:59], a[24:27], 0\n"
"s_add_u32 m0, s67, 0\n"
"v_mfma_f32_16x16x32_bf16 a[168:171], a[60:63], a[24:27], 0\n"
"buffer_load_dwordx4 v30, s[60:63], s66 offen offset:0 lds\n"
"v_mfma_f32_16x16x32_bf16 a[200:203], a[64:67], a[24:27], 0\n"
"v_mfma_f32_16x16x32_bf16 a[232:235], a[68:71], a[24:27], 0\n"
"v_mfma_f32_16x16x32_bf16 a[236:239], a[68:71], a[28:31], 0\n"
"v_mfma_f32_16x16x32_bf16 a[204:207], a[64:67], a[28:31], 0\n"
"v_mfma_f32_16x16x32_bf16 a[172:175], a[60:63], a[28:31], 0\n"
"buffer_load_dwordx4 v30, s[60:63], s66 offen offset:1024 lds\n"
"v_mfma_f32_16x16x32_bf16 v[252:255], a[56:59], a[28:31], 0\n"
"v_mfma_f32_16x16x32_bf16 v[220:223], a[52:55], a[28:31], 0\n"
"v_mfma_f32_16x16x32_bf16 v[188:191], a[48:51], a[28:31], 0\n"
"v_mfma_f32_16x16x32_bf16 v[156:159], a[28:31], a[44:47], 0\n"
"v_mfma_f32_16x16x32_bf16 v[124:127], a[28:31], a[40:43], 0\n"
"v_mfma_f32_16x16x32_bf16 v[92:95], a[36:39], a[28:31], 0\n"
"v_mfma_f32_16x16x32_bf16 v[60:63], a[32:35], a[28:31], 0\n"
"s_waitcnt vmcnt(15) lgkmcnt(0)\n"
"s_barrier\n"
"v_mfma_f32_16x16x32_bf16 v[32:35], a[104:107], a[72:75], v[32:35]\n"
"ds_read_b128 a[0:3], v6 offset:32768\n"
"s_add_u32 m0, s39, 0\n"
"v_mfma_f32_16x16x32_bf16 v[64:67], a[108:111], a[72:75], v[64:67]\n"
"buffer_load_dwordx4 v20, s[44:47], s36 offen offset:0 lds\n"
"s_add_u32 s36, s36, 15360\n"
"v_mfma_f32_16x16x32_bf16 v[96:99], a[72:75], a[112:115], v[96:99]\n"
"ds_read_b64_tr_b16 a[32:33], v8 offset:40960\n"
"v_mfma_f32_16x16x32_bf16 v[128:131], a[72:75], a[116:119], v[128:131]\n"
"v_mfma_f32_16x16x32_bf16 v[160:163], a[120:123], a[72:75], v[160:163]\n"
"ds_read_b64_tr_b16 a[34:35], v8 offset:41472\n"
"v_mfma_f32_16x16x32_bf16 v[192:195], a[124:127], a[72:75], v[192:195]\n"
"v_mfma_f32_16x16x32_bf16 v[224:227], a[128:131], a[72:75], v[224:227]\n"
"buffer_load_dwordx4 v21, s[44:47], s36 offen offset:1024 lds\n"
"s_add_u32 s36, s36, 15360\n"
"v_mfma_f32_16x16x32_bf16 a[144:147], a[132:135], a[72:75], a[144:147]\n"
"ds_read_b128 a[4:7], v6 offset:34816\n"
"v_mfma_f32_16x16x32_bf16 a[176:179], a[136:139], a[72:75], a[176:179]\n"
"v_mfma_f32_16x16x32_bf16 a[208:211], a[140:143], a[72:75], a[208:211]\n"
"ds_read_b64_tr_b16 a[36:37], v9 offset:40960\n"
"v_mfma_f32_16x16x32_bf16 a[212:215], a[140:143], a[76:79], a[212:215]\n"
"v_mfma_f32_16x16x32_bf16 a[180:183], a[136:139], a[76:79], a[180:183]\n"
"ds_read_b64_tr_b16 a[38:39], v9 offset:41472\n"
"v_mfma_f32_16x16x32_bf16 a[148:151], a[132:135], a[76:79], a[148:151]\n"
"buffer_load_dwordx4 v20, s[44:47], s36 offen offset:2048 lds\n"
"s_add_u32 s36, s36, 15360\n"
"v_mfma_f32_16x16x32_bf16 v[228:231], a[128:131], a[76:79], v[228:231]\n"
"v_mfma_f32_16x16x32_bf16 v[196:199], a[124:127], a[76:79], v[196:199]\n"
"ds_read_b64_tr_b16 a[40:41], v8 offset:41984\n"
"v_mfma_f32_16x16x32_bf16 v[164:167], a[120:123], a[76:79], v[164:167]\n"
"v_mfma_f32_16x16x32_bf16 v[132:135], a[76:79], a[116:119], v[132:135]\n"
"ds_read_b128 a[8:11], v6 offset:36864\n"
"v_mfma_f32_16x16x32_bf16 v[100:103], a[76:79], a[112:115], v[100:103]\n"
"v_mfma_f32_16x16x32_bf16 v[68:71], a[108:111], a[76:79], v[68:71]\n"
"ds_read_b64_tr_b16 a[42:43], v8 offset:42496\n"
"buffer_load_dwordx4 v21, s[44:47], s36 offen offset:3072 lds\n"
"s_add_u32 s36, s36, 19456\n"
"v_mfma_f32_16x16x32_bf16 v[36:39], a[104:107], a[76:79], v[36:39]\n"
"v_mfma_f32_16x16x32_bf16 v[40:43], a[104:107], a[80:83], v[40:43]\n"
"v_mfma_f32_16x16x32_bf16 v[72:75], a[108:111], a[80:83], v[72:75]\n"
"ds_read_b64_tr_b16 a[44:45], v9 offset:41984\n"
"v_mfma_f32_16x16x32_bf16 v[104:107], a[80:83], a[112:115], v[104:107]\n"
"v_mfma_f32_16x16x32_bf16 v[136:139], a[80:83], a[116:119], v[136:139]\n"
"ds_read_b128 a[12:15], v6 offset:38912\n"
"s_add_u32 m0, s39, 4096\n"
"v_mfma_f32_16x16x32_bf16 v[168:171], a[120:123], a[80:83], v[168:171]\n"
"buffer_load_dwordx4 v20, s[44:47], s36 offen offset:0 lds\n"
"s_add_u32 s36, s36, 15360\n"
"v_mfma_f32_16x16x32_bf16 v[200:203], a[124:127], a[80:83], v[200:203]\n"
"ds_read_b64_tr_b16 a[46:47], v9 offset:42496\n"
"v_mfma_f32_16x16x32_bf16 v[232:235], a[128:131], a[80:83], v[232:235]\n"
"v_mfma_f32_16x16x32_bf16 a[152:155], a[132:135], a[80:83], a[152:155]\n"
"v_mfma_f32_16x16x32_bf16 a[184:187], a[136:139], a[80:83], a[184:187]\n"
"ds_read_b64_tr_b16 a[48:49], v10 offset:40960\n"
"v_mfma_f32_16x16x32_bf16 a[216:219], a[140:143], a[80:83], a[216:219]\n"
"v_mfma_f32_16x16x32_bf16 a[220:223], a[140:143], a[84:87], a[220:223]\n"
"ds_read_b64_tr_b16 a[50:51], v10 offset:41728\n"
"s_waitcnt vmcnt(12)\n"
"v_lshlrev_b32 v30, 16, v16\n"
"buffer_load_dwordx4 v21, s[44:47], s36 offen offset:1024 lds\n"
"s_add_u32 s36, s36, 15360\n"
"v_mfma_f32_16x16x32_bf16 a[188:191], a[136:139], a[84:87], a[188:191]\n"
"v_and_b32 v16, 0xffff0000, v16\n"
"v_mfma_f32_16x16x32_bf16 a[156:159], a[132:135], a[84:87], a[156:159]\n"
"v_add_f32 v22, v30, v16\n"
"v_mfma_f32_16x16x32_bf16 v[236:239], a[128:131], a[84:87], v[236:239]\n"
"ds_read_b128 a[16:19], v6 offset:40960\n"
"v_mul_f32 v23, v30, v30\n"
"v_mfma_f32_16x16x32_bf16 v[204:207], a[124:127], a[84:87], v[204:207]\n"
"v_fmac_f32 v23, v16, v16\n"
"v_mfma_f32_16x16x32_bf16 v[172:175], a[120:123], a[84:87], v[172:175]\n"
"ds_read_b64_tr_b16 a[52:53], v11 offset:40960\n"
"v_lshlrev_b32 v30, 16, v17\n"
"v_mfma_f32_16x16x32_bf16 v[140:143], a[84:87], a[116:119], v[140:143]\n"
"v_and_b32 v17, 0xffff0000, v17\n"
"buffer_load_dwordx4 v20, s[44:47], s36 offen offset:2048 lds\n"
"s_add_u32 s36, s36, 15360\n"
"v_mfma_f32_16x16x32_bf16 v[108:111], a[84:87], a[112:115], v[108:111]\n"
"ds_read_b64_tr_b16 a[54:55], v11 offset:41728\n"
"v_add_f32 v22, v22, v30\n"
"v_mfma_f32_16x16x32_bf16 v[76:79], a[108:111], a[84:87], v[76:79]\n"
"v_fmac_f32 v23, v30, v30\n"
"v_mfma_f32_16x16x32_bf16 v[44:47], a[104:107], a[84:87], v[44:47]\n"
"v_add_f32 v22, v22, v17\n"
"v_mfma_f32_16x16x32_bf16 v[48:51], a[104:107], a[88:91], v[48:51]\n"
"ds_read_b128 a[20:23], v6 offset:43008\n"
"v_fmac_f32 v23, v17, v17\n"
"v_mfma_f32_16x16x32_bf16 v[80:83], a[108:111], a[88:91], v[80:83]\n"
"v_lshlrev_b32 v30, 16, v18\n"
"v_mfma_f32_16x16x32_bf16 v[112:115], a[88:91], a[112:115], v[112:115]\n"
"ds_read_b64_tr_b16 a[56:57], v12 offset:40960\n"
"v_and_b32 v18, 0xffff0000, v18\n"
"buffer_load_dwordx4 v21, s[44:47], s36 offen offset:3072 lds\n"
"s_sub_u32 s36, s36, 111488\n"
"v_mfma_f32_16x16x32_bf16 v[144:147], a[88:91], a[116:119], v[144:147]\n"
"v_add_f32 v22, v22, v30\n"
"v_mfma_f32_16x16x32_bf16 v[176:179], a[120:123], a[88:91], v[176:179]\n"
"ds_read_b64_tr_b16 a[58:59], v12 offset:41728\n"
"v_fmac_f32 v23, v30, v30\n"
"v_mfma_f32_16x16x32_bf16 v[208:211], a[124:127], a[88:91], v[208:211]\n"
"v_add_f32 v22, v22, v18\n"
"v_mfma_f32_16x16x32_bf16 v[240:243], a[128:131], a[88:91], v[240:243]\n"
"v_fmac_f32 v23, v18, v18\n"
"v_mfma_f32_16x16x32_bf16 a[160:163], a[132:135], a[88:91], a[160:163]\n"
"ds_read_b64_tr_b16 a[60:61], v13 offset:40960\n"
"v_lshlrev_b32 v30, 16, v19\n"
"s_add_u32 m0, s40, 20480\n"
"v_mfma_f32_16x16x32_bf16 a[192:195], a[136:139], a[88:91], a[192:195]\n"
"v_and_b32 v19, 0xffff0000, v19\n"
"buffer_load_dwordx4 v1, s[24:27], s37 offen offset:0 lds\n"
"v_mfma_f32_16x16x32_bf16 a[224:227], a[140:143], a[88:91], a[224:227]\n"
"ds_read_b128 a[24:27], v6 offset:45056\n"
"v_add_f32 v22, v22, v30\n"
"v_mfma_f32_16x16x32_bf16 a[228:231], a[140:143], a[92:95], a[228:231]\n"
"v_fmac_f32 v23, v30, v30\n"
"v_mfma_f32_16x16x32_bf16 a[196:199], a[136:139], a[92:95], a[196:199]\n"
"ds_read_b64_tr_b16 a[62:63], v13 offset:41728\n"
"v_add_f32 v22, v22, v19\n"
"v_mfma_f32_16x16x32_bf16 a[164:167], a[132:135], a[92:95], a[164:167]\n"
"v_fmac_f32 v23, v19, v19\n"
"v_mfma_f32_16x16x32_bf16 v[244:247], a[128:131], a[92:95], v[244:247]\n"
"buffer_load_dwordx4 v[16:19], v0, s[44:47], s41 offen offset:2048\n"
"v_mfma_f32_16x16x32_bf16 v[212:215], a[124:127], a[92:95], v[212:215]\n"
"ds_read_b64_tr_b16 a[64:65], v14 offset:40960\n"
"v_lshlrev_b32 v30, 16, v24\n"
"v_and_b32 v24, 0xffff0000, v24\n"
"buffer_load_dwordx4 v2, s[24:27], s37 offen offset:1024 lds\n"
"s_add_u32 s37, s37, 196608\n"
"v_mfma_f32_16x16x32_bf16 v[180:183], a[120:123], a[92:95], v[180:183]\n"
"v_add_f32 v22, v22, v30\n"
"v_mfma_f32_16x16x32_bf16 v[148:151], a[92:95], a[116:119], v[148:151]\n"
"ds_read_b128 a[28:31], v6 offset:47104\n"
"v_fmac_f32 v23, v30, v30\n"
"v_mfma_f32_16x16x32_bf16 v[116:119], a[92:95], a[112:115], v[116:119]\n"
"v_add_f32 v22, v22, v24\n"
"v_mfma_f32_16x16x32_bf16 v[84:87], a[108:111], a[92:95], v[84:87]\n"
"ds_read_b64_tr_b16 a[66:67], v14 offset:41728\n"
"v_fmac_f32 v23, v24, v24\n"
"v_mfma_f32_16x16x32_bf16 v[52:55], a[104:107], a[92:95], v[52:55]\n"
"v_lshlrev_b32 v30, 16, v25\n"
"v_mfma_f32_16x16x32_bf16 v[56:59], a[104:107], a[96:99], v[56:59]\n"
"v_and_b32 v25, 0xffff0000, v25\n"
"buffer_load_dwordx4 v3, s[28:31], s38 offen offset:2048 lds\n"
"v_mfma_f32_16x16x32_bf16 v[88:91], a[108:111], a[96:99], v[88:91]\n"
"ds_read_b64_tr_b16 a[68:69], v15 offset:40960\n"
"v_add_f32 v22, v22, v30\n"
"v_mfma_f32_16x16x32_bf16 v[120:123], a[96:99], a[112:115], v[120:123]\n"
"v_fmac_f32 v23, v30, v30\n"
"v_mfma_f32_16x16x32_bf16 v[152:155], a[96:99], a[116:119], v[152:155]\n"
"ds_read_b64_tr_b16 a[70:71], v15 offset:41728\n"
"v_add_f32 v22, v22, v25\n"
"v_mfma_f32_16x16x32_bf16 v[184:187], a[120:123], a[96:99], v[184:187]\n"
"v_fmac_f32 v23, v25, v25\n"
"v_mfma_f32_16x16x32_bf16 v[216:219], a[124:127], a[96:99], v[216:219]\n"
"v_lshlrev_b32 v30, 16, v26\n"
"v_mfma_f32_16x16x32_bf16 v[248:251], a[128:131], a[96:99], v[248:251]\n"
"v_and_b32 v26, 0xffff0000, v26\n"
"buffer_load_dwordx4 v4, s[28:31], s38 offen offset:3072 lds\n"
"v_mfma_f32_16x16x32_bf16 a[168:171], a[132:135], a[96:99], a[168:171]\n"
"v_add_f32 v22, v22, v30\n"
"v_mfma_f32_16x16x32_bf16 a[200:203], a[136:139], a[96:99], a[200:203]\n"
"v_fmac_f32 v23, v30, v30\n"
"v_mfma_f32_16x16x32_bf16 a[232:235], a[140:143], a[96:99], a[232:235]\n"
"v_add_f32 v22, v22, v26\n"
"v_mfma_f32_16x16x32_bf16 a[236:239], a[140:143], a[100:103], a[236:239]\n"
"v_fmac_f32 v23, v26, v26\n"
"v_mfma_f32_16x16x32_bf16 a[204:207], a[136:139], a[100:103], a[204:207]\n"
"v_lshlrev_b32 v30, 16, v27\n"
"s_add_u32 m0, s40, 24576\n"
"v_mfma_f32_16x16x32_bf16 a[172:175], a[132:135], a[100:103], a[172:175]\n"
"v_and_b32 v27, 0xffff0000, v27\n"
"buffer_load_dwordx4 v5, s[28:31], s38 offen offset:0 lds\n"
"s_add_u32 s38, s38, 229376\n"
"v_mfma_f32_16x16x32_bf16 v[252:255], a[128:131], a[100:103], v[252:255]\n"
"v_add_f32 v22, v22, v30\n"
"v_mfma_f32_16x16x32_bf16 v[220:223], a[124:127], a[100:103], v[220:223]\n"
"v_fmac_f32 v23, v30, v30\n"
"v_mfma_f32_16x16x32_bf16 v[188:191], a[120:123], a[100:103], v[188:191]\n"
"v_add_f32 v22, v22, v27\n"
"v_mfma_f32_16x16x32_bf16 v[156:159], a[100:103], a[116:119], v[156:159]\n"
"v_fmac_f32 v23, v27, v27\n"
"v_mfma_f32_16x16x32_bf16 v[124:127], a[100:103], a[112:115], v[124:127]\n"
"buffer_load_dwordx4 v[24:27], v0, s[44:47], s41 offen offset:3072\n"
"v_mfma_f32_16x16x32_bf16 v[92:95], a[108:111], a[100:103], v[92:95]\n"
"v_mfma_f32_16x16x32_bf16 v[60:63], a[104:107], a[100:103], v[60:63]\n"
"s_waitcnt vmcnt(25) lgkmcnt(0)\n"
"s_barrier\n"
"v_mfma_f32_16x16x32_bf16 v[32:35], a[32:35], a[0:3], v[32:35]\n"
"ds_read_b128 a[72:75], v7 offset:32768\n"
"s_add_u32 m0, s40, 40960\n"
"v_mfma_f32_16x16x32_bf16 v[64:67], a[36:39], a[0:3], v[64:67]\n"
"buffer_load_dwordx4 v1, s[24:27], s37 offen offset:0 lds\n"
"v_mfma_f32_16x16x32_bf16 v[96:99], a[0:3], a[40:43], v[96:99]\n"
"ds_read_b64_tr_b16 a[104:105], v8 offset:61440\n"
"v_mfma_f32_16x16x32_bf16 v[128:131], a[0:3], a[44:47], v[128:131]\n"
"v_mfma_f32_16x16x32_bf16 v[160:163], a[48:51], a[0:3], v[160:163]\n"
"ds_read_b64_tr_b16 a[106:107], v8 offset:61952\n"
"v_mfma_f32_16x16x32_bf16 v[192:195], a[52:55], a[0:3], v[192:195]\n"
"v_mfma_f32_16x16x32_bf16 v[224:227], a[56:59], a[0:3], v[224:227]\n"
"v_mfma_f32_16x16x32_bf16 a[144:147], a[60:63], a[0:3], a[144:147]\n"
"ds_read_b128 a[76:79], v7 offset:34816\n"
"v_mfma_f32_16x16x32_bf16 a[176:179], a[64:67], a[0:3], a[176:179]\n"
"v_mfma_f32_16x16x32_bf16 a[208:211], a[68:71], a[0:3], a[208:211]\n"
"ds_read_b64_tr_b16 a[108:109], v9 offset:61440\n"
"v_mfma_f32_16x16x32_bf16 a[212:215], a[68:71], a[4:7], a[212:215]\n"
"s_min_u32 s90, s90, END_%=-CB_%=-1024\n"
"v_mfma_f32_16x16x32_bf16 a[180:183], a[64:67], a[4:7], a[180:183]\n"
"ds_read_b64_tr_b16 a[110:111], v9 offset:61952\n"
"v_mfma_f32_16x16x32_bf16 a[148:151], a[60:63], a[4:7], a[148:151]\n"
"s_add_u32 s18, s18, s90\n"
"s_addc_u32 s19, s19, 0\n"
"v_mfma_f32_16x16x32_bf16 v[228:231], a[56:59], a[4:7], v[228:231]\n"
"v_mfma_f32_16x16x32_bf16 v[196:199], a[52:55], a[4:7], v[196:199]\n"
"ds_read_b64_tr_b16 a[112:113], v8 offset:62464\n"
"v_mfma_f32_16x16x32_bf16 v[164:167], a[48:51], a[4:7], v[164:167]\n"
"s_sub_u32 s95, s95, 7296\n"
"v_mfma_f32_16x16x32_bf16 v[132:135], a[4:7], a[44:47], v[132:135]\n"
"ds_read_b128 a[80:83], v7 offset:36864\n"
"buffer_load_dwordx4 v2, s[24:27], s37 offen offset:1024 lds\n"
"s_add_u32 s37, s37, 196608\n"
"v_mfma_f32_16x16x32_bf16 v[100:103], a[4:7], a[40:43], v[100:103]\n"
"s_sub_u32 s94, s94, 2112\n"
"v_mfma_f32_16x16x32_bf16 v[68:71], a[36:39], a[4:7], v[68:71]\n"
"ds_read_b64_tr_b16 a[114:115], v8 offset:62976\n"
"v_mfma_f32_16x16x32_bf16 v[36:39], a[32:35], a[4:7], v[36:39]\n"
"v_mfma_f32_16x16x32_bf16 v[40:43], a[32:35], a[8:11], v[40:43]\n"
"v_readfirstlane_b32 s90, v28\n"
"v_mfma_f32_16x16x32_bf16 v[72:75], a[36:39], a[8:11], v[72:75]\n"
"ds_read_b64_tr_b16 a[116:117], v9 offset:62464\n"
"v_mfma_f32_16x16x32_bf16 v[104:107], a[8:11], a[40:43], v[104:107]\n"
"v_readfirstlane_b32 s91, v29\n"
"v_mfma_f32_16x16x32_bf16 v[136:139], a[8:11], a[44:47], v[136:139]\n"
"ds_read_b128 a[84:87], v7 offset:38912\n"
"v_mfma_f32_16x16x32_bf16 v[168:171], a[48:51], a[8:11], v[168:171]\n"
"v_mfma_f32_16x16x32_bf16 v[200:203], a[52:55], a[8:11], v[200:203]\n"
"ds_read_b64_tr_b16 a[118:119], v9 offset:62976\n"
"v_mfma_f32_16x16x32_bf16 v[232:235], a[56:59], a[8:11], v[232:235]\n"
"v_mfma_f32_16x16x32_bf16 a[152:155], a[60:63], a[8:11], a[152:155]\n"
"s_add_u32 s90, s90, 1\n"
"v_mfma_f32_16x16x32_bf16 a[184:187], a[64:67], a[8:11], a[184:187]\n"
"ds_read_b64_tr_b16 a[120:121], v10 offset:61440\n"
"v_mfma_f32_16x16x32_bf16 a[216:219], a[68:71], a[8:11], a[216:219]\n"
"v_mfma_f32_16x16x32_bf16 a[220:223], a[68:71], a[12:15], a[220:223]\n"
"ds_read_b64_tr_b16 a[122:123], v10 offset:62208\n"
"v_accvgpr_write_b32 a246, s90\n"
"v_mfma_f32_16x16x32_bf16 a[188:191], a[64:67], a[12:15], a[188:191]\n"
"buffer_load_dwordx4 v3, s[28:31], s38 offen offset:2048 lds\n"
"v_mfma_f32_16x16x32_bf16 a[156:159], a[60:63], a[12:15], a[156:159]\n"
"v_accvgpr_write_b32 a247, s91\n"
"v_mfma_f32_16x16x32_bf16 v[236:239], a[56:59], a[12:15], v[236:239]\n"
"ds_read_b128 a[88:91], v7 offset:40960\n"
"v_mfma_f32_16x16x32_bf16 v[204:207], a[52:55], a[12:15], v[204:207]\n"
"v_mfma_f32_16x16x32_bf16 v[172:175], a[48:51], a[12:15], v[172:175]\n"
"ds_read_b64_tr_b16 a[124:125], v11 offset:61440\n"
"v_accvgpr_write_b32 a248, s90\n"
"v_mfma_f32_16x16x32_bf16 v[140:143], a[12:15], a[44:47], v[140:143]\n"
"v_mfma_f32_16x16x32_bf16 v[108:111], a[12:15], a[40:43], v[108:111]\n"
"ds_read_b64_tr_b16 a[126:127], v11 offset:62208\n"
"v_accvgpr_write_b32 a249, s91\n"
"v_mfma_f32_16x16x32_bf16 v[76:79], a[36:39], a[12:15], v[76:79]\n"
"v_mfma_f32_16x16x32_bf16 v[44:47], a[32:35], a[12:15], v[44:47]\n"
"v_mfma_f32_16x16x32_bf16 v[48:51], a[32:35], a[16:19], v[48:51]\n"
"ds_read_b128 a[92:95], v7 offset:43008\n"
"v_mfma_f32_16x16x32_bf16 v[80:83], a[36:39], a[16:19], v[80:83]\n"
"v_mfma_f32_16x16x32_bf16 v[112:115], a[16:19], a[40:43], v[112:115]\n"
"ds_read_b64_tr_b16 a[128:129], v12 offset:61440\n"
"v_mfma_f32_16x16x32_bf16 v[144:147], a[16:19], a[44:47], v[144:147]\n"
"v_mfma_f32_16x16x32_bf16 v[176:179], a[48:51], a[16:19], v[176:179]\n"
"ds_read_b64_tr_b16 a[130:131], v12 offset:62208\n"
"v_mfma_f32_16x16x32_bf16 v[208:211], a[52:55], a[16:19], v[208:211]\n"
"v_mfma_f32_16x16x32_bf16 v[240:243], a[56:59], a[16:19], v[240:243]\n"
"v_mfma_f32_16x16x32_bf16 a[160:163], a[60:63], a[16:19], a[160:163]\n"
"ds_read_b64_tr_b16 a[132:133], v13 offset:61440\n"
"buffer_load_dwordx4 v4, s[28:31], s38 offen offset:3072 lds\n"
"v_mfma_f32_16x16x32_bf16 a[192:195], a[64:67], a[16:19], a[192:195]\n"
"v_mfma_f32_16x16x32_bf16 a[224:227], a[68:71], a[16:19], a[224:227]\n"
"ds_read_b128 a[96:99], v7 offset:45056\n"
"v_mfma_f32_16x16x32_bf16 a[228:231], a[68:71], a[20:23], a[228:231]\n"
"v_mfma_f32_16x16x32_bf16 a[196:199], a[64:67], a[20:23], a[196:199]\n"
"ds_read_b64_tr_b16 a[134:135], v13 offset:62208\n"
"v_mfma_f32_16x16x32_bf16 a[164:167], a[60:63], a[20:23], a[164:167]\n"
"v_mfma_f32_16x16x32_bf16 v[244:247], a[56:59], a[20:23], v[244:247]\n"
"v_mfma_f32_16x16x32_bf16 v[212:215], a[52:55], a[20:23], v[212:215]\n"
"ds_read_b64_tr_b16 a[136:137], v14 offset:61440\n"
"v_mfma_f32_16x16x32_bf16 v[180:183], a[48:51], a[20:23], v[180:183]\n"
"v_mfma_f32_16x16x32_bf16 v[148:151], a[20:23], a[44:47], v[148:151]\n"
"ds_read_b128 a[100:103], v7 offset:47104\n"
"v_mfma_f32_16x16x32_bf16 v[116:119], a[20:23], a[40:43], v[116:119]\n"
"v_mfma_f32_16x16x32_bf16 v[84:87], a[36:39], a[20:23], v[84:87]\n"
"ds_read_b64_tr_b16 a[138:139], v14 offset:62208\n"
"v_mfma_f32_16x16x32_bf16 v[52:55], a[32:35], a[20:23], v[52:55]\n"
"v_mfma_f32_16x16x32_bf16 v[56:59], a[32:35], a[24:27], v[56:59]\n"
"v_mfma_f32_16x16x32_bf16 v[88:91], a[36:39], a[24:27], v[88:91]\n"
"ds_read_b64_tr_b16 a[140:141], v15 offset:61440\n"
"s_add_u32 m0, s40, 45056\n"
"v_mfma_f32_16x16x32_bf16 v[120:123], a[24:27], a[40:43], v[120:123]\n"
"buffer_load_dwordx4 v5, s[28:31], s38 offen offset:0 lds\n"
"s_add_u32 s38, s38, 229376\n"
"v_mfma_f32_16x16x32_bf16 v[152:155], a[24:27], a[44:47], v[152:155]\n"
"ds_read_b64_tr_b16 a[142:143], v15 offset:62208\n"
"v_mfma_f32_16x16x32_bf16 v[184:187], a[48:51], a[24:27], v[184:187]\n"
"v_mfma_f32_16x16x32_bf16 v[216:219], a[52:55], a[24:27], v[216:219]\n"
"v_mfma_f32_16x16x32_bf16 v[248:251], a[56:59], a[24:27], v[248:251]\n"
"v_mfma_f32_16x16x32_bf16 a[168:171], a[60:63], a[24:27], a[168:171]\n"
"v_mfma_f32_16x16x32_bf16 a[200:203], a[64:67], a[24:27], a[200:203]\n"
"v_mfma_f32_16x16x32_bf16 a[232:235], a[68:71], a[24:27], a[232:235]\n"
"v_mfma_f32_16x16x32_bf16 a[236:239], a[68:71], a[28:31], a[236:239]\n"
"v_mfma_f32_16x16x32_bf16 a[204:207], a[64:67], a[28:31], a[204:207]\n"
"v_mfma_f32_16x16x32_bf16 a[172:175], a[60:63], a[28:31], a[172:175]\n"
"v_mfma_f32_16x16x32_bf16 v[252:255], a[56:59], a[28:31], v[252:255]\n"
"v_mfma_f32_16x16x32_bf16 v[220:223], a[52:55], a[28:31], v[220:223]\n"
"v_mfma_f32_16x16x32_bf16 v[188:191], a[48:51], a[28:31], v[188:191]\n"
"v_mfma_f32_16x16x32_bf16 v[156:159], a[28:31], a[44:47], v[156:159]\n"
"v_mfma_f32_16x16x32_bf16 v[124:127], a[28:31], a[40:43], v[124:127]\n"
"v_mfma_f32_16x16x32_bf16 v[92:95], a[36:39], a[28:31], v[92:95]\n"
"v_mfma_f32_16x16x32_bf16 v[60:63], a[32:35], a[28:31], v[60:63]\n"
"s_waitcnt vmcnt(12) lgkmcnt(0)\n"
"s_barrier\n"
"v_mfma_f32_16x16x32_bf16 v[32:35], a[104:107], a[72:75], v[32:35]\n"
"ds_read_b128 a[0:3], v6 offset:0\n"
"s_add_u32 m0, s39, 32768\n"
"v_mfma_f32_16x16x32_bf16 v[64:67], a[108:111], a[72:75], v[64:67]\n"
"buffer_load_dwordx4 v20, s[44:47], s36 offen offset:0 lds\n"
"s_add_u32 s36, s36, 15360\n"
"v_mfma_f32_16x16x32_bf16 v[96:99], a[72:75], a[112:115], v[96:99]\n"
"ds_read_b64_tr_b16 a[32:33], v8 offset:0\n"
"v_mfma_f32_16x16x32_bf16 v[128:131], a[72:75], a[116:119], v[128:131]\n"
"v_mfma_f32_16x16x32_bf16 v[160:163], a[120:123], a[72:75], v[160:163]\n"
"ds_read_b64_tr_b16 a[34:35], v8 offset:512\n"
"v_mfma_f32_16x16x32_bf16 v[192:195], a[124:127], a[72:75], v[192:195]\n"
"v_mfma_f32_16x16x32_bf16 v[224:227], a[128:131], a[72:75], v[224:227]\n"
"s_waitcnt vmcnt(6)\n"
"v_lshlrev_b32 v30, 16, v16\n"
"buffer_load_dwordx4 v21, s[44:47], s36 offen offset:1024 lds\n"
"s_add_u32 s36, s36, 15360\n"
"v_mfma_f32_16x16x32_bf16 a[144:147], a[132:135], a[72:75], a[144:147]\n"
"ds_read_b128 a[4:7], v6 offset:2048\n"
"v_and_b32 v16, 0xffff0000, v16\n"
"v_mfma_f32_16x16x32_bf16 a[176:179], a[136:139], a[72:75], a[176:179]\n"
"v_add_f32 v28, v30, v16\n"
"v_mul_f32 v29, v30, v30\n"
"v_mfma_f32_16x16x32_bf16 a[208:211], a[140:143], a[72:75], a[208:211]\n"
"ds_read_b64_tr_b16 a[36:37], v9 offset:0\n"
"v_fmac_f32 v29, v16, v16\n"
"v_mfma_f32_16x16x32_bf16 a[212:215], a[140:143], a[76:79], a[212:215]\n"
"v_lshlrev_b32 v30, 16, v17\n"
"v_and_b32 v17, 0xffff0000, v17\n"
"v_mfma_f32_16x16x32_bf16 a[180:183], a[136:139], a[76:79], a[180:183]\n"
"ds_read_b64_tr_b16 a[38:39], v9 offset:512\n"
"v_add_f32 v28, v28, v30\n"
"v_mfma_f32_16x16x32_bf16 a[148:151], a[132:135], a[76:79], a[148:151]\n"
"v_fmac_f32 v29, v30, v30\n"
"buffer_load_dwordx4 v20, s[44:47], s36 offen offset:2048 lds\n"
"s_add_u32 s36, s36, 15360\n"
"v_mfma_f32_16x16x32_bf16 v[228:231], a[128:131], a[76:79], v[228:231]\n"
"v_add_f32 v28, v28, v17\n"
"v_fmac_f32 v29, v17, v17\n"
"v_mfma_f32_16x16x32_bf16 v[196:199], a[124:127], a[76:79], v[196:199]\n"
"ds_read_b64_tr_b16 a[40:41], v8 offset:1024\n"
"v_lshlrev_b32 v30, 16, v18\n"
"v_mfma_f32_16x16x32_bf16 v[164:167], a[120:123], a[76:79], v[164:167]\n"
"v_and_b32 v18, 0xffff0000, v18\n"
"v_add_f32 v28, v28, v30\n"
"v_mfma_f32_16x16x32_bf16 v[132:135], a[76:79], a[116:119], v[132:135]\n"
"ds_read_b128 a[8:11], v6 offset:4096\n"
"v_fmac_f32 v29, v30, v30\n"
"v_mfma_f32_16x16x32_bf16 v[100:103], a[76:79], a[112:115], v[100:103]\n"
"v_add_f32 v28, v28, v18\n"
"v_mfma_f32_16x16x32_bf16 v[68:71], a[108:111], a[76:79], v[68:71]\n"
"ds_read_b64_tr_b16 a[42:43], v8 offset:1536\n"
"v_fmac_f32 v29, v18, v18\n"
"v_lshlrev_b32 v30, 16, v19\n"
"buffer_load_dwordx4 v21, s[44:47], s36 offen offset:3072 lds\n"
"s_add_u32 s36, s36, 19456\n"
"v_mfma_f32_16x16x32_bf16 v[36:39], a[104:107], a[76:79], v[36:39]\n"
"v_and_b32 v19, 0xffff0000, v19\n"
"v_mfma_f32_16x16x32_bf16 v[40:43], a[104:107], a[80:83], v[40:43]\n"
"v_add_f32 v28, v28, v30\n"
"v_fmac_f32 v29, v30, v30\n"
"v_mfma_f32_16x16x32_bf16 v[72:75], a[108:111], a[80:83], v[72:75]\n"
"ds_read_b64_tr_b16 a[44:45], v9 offset:1024\n"
"v_add_f32 v28, v28, v19\n"
"v_mfma_f32_16x16x32_bf16 v[104:107], a[80:83], a[112:115], v[104:107]\n"
"v_fmac_f32 v29, v19, v19\n"
"v_mfma_f32_16x16x32_bf16 v[136:139], a[80:83], a[116:119], v[136:139]\n"
"ds_read_b128 a[12:15], v6 offset:6144\n"
"v_lshlrev_b32 v30, 16, v24\n"
"v_and_b32 v24, 0xffff0000, v24\n"
"s_add_u32 m0, s39, 36864\n"
"v_mfma_f32_16x16x32_bf16 v[168:171], a[120:123], a[80:83], v[168:171]\n"
"v_add_f32 v28, v28, v30\n"
"buffer_load_dwordx4 v20, s[44:47], s36 offen offset:0 lds\n"
"s_add_u32 s36, s36, 15360\n"
"v_mfma_f32_16x16x32_bf16 v[200:203], a[124:127], a[80:83], v[200:203]\n"
"ds_read_b64_tr_b16 a[46:47], v9 offset:1536\n"
"v_fmac_f32 v29, v30, v30\n"
"v_add_f32 v28, v28, v24\n"
"v_mfma_f32_16x16x32_bf16 v[232:235], a[128:131], a[80:83], v[232:235]\n"
"v_fmac_f32 v29, v24, v24\n"
"v_mfma_f32_16x16x32_bf16 a[152:155], a[132:135], a[80:83], a[152:155]\n"
"v_lshlrev_b32 v30, 16, v25\n"
"v_and_b32 v25, 0xffff0000, v25\n"
"v_mfma_f32_16x16x32_bf16 a[184:187], a[136:139], a[80:83], a[184:187]\n"
"ds_read_b64_tr_b16 a[48:49], v10 offset:0\n"
"v_add_f32 v28, v28, v30\n"
"v_mfma_f32_16x16x32_bf16 a[216:219], a[140:143], a[80:83], a[216:219]\n"
"v_fmac_f32 v29, v30, v30\n"
"v_mfma_f32_16x16x32_bf16 a[220:223], a[140:143], a[84:87], a[220:223]\n"
"ds_read_b64_tr_b16 a[50:51], v10 offset:768\n"
"v_add_f32 v28, v28, v25\n"
"v_fmac_f32 v29, v25, v25\n"
"buffer_load_dwordx4 v21, s[44:47], s36 offen offset:1024 lds\n"
"s_add_u32 s36, s36, 15360\n"
"v_mfma_f32_16x16x32_bf16 a[188:191], a[136:139], a[84:87], a[188:191]\n"
"v_lshlrev_b32 v30, 16, v26\n"
"v_mfma_f32_16x16x32_bf16 a[156:159], a[132:135], a[84:87], a[156:159]\n"
"v_and_b32 v26, 0xffff0000, v26\n"
"v_add_f32 v28, v28, v30\n"
"v_mfma_f32_16x16x32_bf16 v[236:239], a[128:131], a[84:87], v[236:239]\n"
"ds_read_b128 a[16:19], v6 offset:8192\n"
"v_fmac_f32 v29, v30, v30\n"
"v_mfma_f32_16x16x32_bf16 v[204:207], a[124:127], a[84:87], v[204:207]\n"
"v_add_f32 v28, v28, v26\n"
"v_mfma_f32_16x16x32_bf16 v[172:175], a[120:123], a[84:87], v[172:175]\n"
"ds_read_b64_tr_b16 a[52:53], v11 offset:0\n"
"v_fmac_f32 v29, v26, v26\n"
"v_lshlrev_b32 v30, 16, v27\n"
"v_mfma_f32_16x16x32_bf16 v[140:143], a[84:87], a[116:119], v[140:143]\n"
"v_and_b32 v27, 0xffff0000, v27\n"
"buffer_load_dwordx4 v20, s[44:47], s36 offen offset:2048 lds\n"
"s_add_u32 s36, s36, 15360\n"
"v_mfma_f32_16x16x32_bf16 v[108:111], a[84:87], a[112:115], v[108:111]\n"
"ds_read_b64_tr_b16 a[54:55], v11 offset:768\n"
"v_add_f32 v28, v28, v30\n"
"v_fmac_f32 v29, v30, v30\n"
"v_mfma_f32_16x16x32_bf16 v[76:79], a[108:111], a[84:87], v[76:79]\n"
"v_add_f32 v28, v28, v27\n"
"v_mfma_f32_16x16x32_bf16 v[44:47], a[104:107], a[84:87], v[44:47]\n"
"v_fmac_f32 v29, v27, v27\n"
"v_mfma_f32_16x16x32_bf16 v[48:51], a[104:107], a[88:91], v[48:51]\n"
"ds_read_b128 a[20:23], v6 offset:10240\n"
"v_permlane32_swap_b32 v22, v28\n"
"v_permlane32_swap_b32 v23, v29\n"
"v_mfma_f32_16x16x32_bf16 v[80:83], a[108:111], a[88:91], v[80:83]\n"
"v_add_f32 v22, v22, v28\n"
"v_mfma_f32_16x16x32_bf16 v[112:115], a[88:91], a[112:115], v[112:115]\n"
"ds_read_b64_tr_b16 a[56:57], v12 offset:0\n"
"v_add_f32 v23, v23, v29\n"
"buffer_load_dwordx4 v21, s[44:47], s36 offen offset:3072 lds\n"
"s_sub_u32 s36, s36, 111488\n"
"v_mfma_f32_16x16x32_bf16 v[144:147], a[88:91], a[116:119], v[144:147]\n"
"v_add_f32_dpp v22, v22, v22 row_ror:8 row_mask:0xf bank_mask:0xf\n"
"v_add_f32_dpp v23, v23, v23 row_ror:8 row_mask:0xf bank_mask:0xf\n"
"v_mfma_f32_16x16x32_bf16 v[176:179], a[120:123], a[88:91], v[176:179]\n"
"ds_read_b64_tr_b16 a[58:59], v12 offset:768\n"
"v_add_f32_dpp v22, v22, v22 row_ror:4 row_mask:0xf bank_mask:0xf\n"
"v_mfma_f32_16x16x32_bf16 v[208:211], a[124:127], a[88:91], v[208:211]\n"
"v_add_f32_dpp v23, v23, v23 row_ror:4 row_mask:0xf bank_mask:0xf\n"
"v_mfma_f32_16x16x32_bf16 v[240:243], a[128:131], a[88:91], v[240:243]\n"
"v_add_f32_dpp v22, v22, v22 row_ror:2 row_mask:0xf bank_mask:0xf\n"
"v_mfma_f32_16x16x32_bf16 a[160:163], a[132:135], a[88:91], a[160:163]\n"
"ds_read_b64_tr_b16 a[60:61], v13 offset:0\n"
"v_add_f32_dpp v23, v23, v23 row_ror:2 row_mask:0xf bank_mask:0xf\n"
"v_add_f32_dpp v22, v22, v22 row_ror:1 row_mask:0xf bank_mask:0xf\n"
"s_add_u32 m0, s40, 61440\n"
"v_mfma_f32_16x16x32_bf16 a[192:195], a[136:139], a[88:91], a[192:195]\n"
"v_add_f32_dpp v23, v23, v23 row_ror:1 row_mask:0xf bank_mask:0xf\n"
"buffer_load_dwordx4 v1, s[24:27], s37 offen offset:0 lds\n"
"v_mfma_f32_16x16x32_bf16 a[224:227], a[140:143], a[88:91], a[224:227]\n"
"ds_read_b128 a[24:27], v6 offset:12288\n"
"v_mov_b32 v28, v22\n"
"v_mfma_f32_16x16x32_bf16 a[228:231], a[140:143], a[92:95], a[228:231]\n"
"v_mov_b32 v29, v23\n"
"v_mfma_f32_16x16x32_bf16 a[196:199], a[136:139], a[92:95], a[196:199]\n"
"ds_read_b64_tr_b16 a[62:63], v13 offset:768\n"
"v_lshrrev_b32 v30, 4, v0\n"
"v_permlane16_swap_b32 v28, v22\n"
"v_mfma_f32_16x16x32_bf16 a[164:167], a[132:135], a[92:95], a[164:167]\n"
"v_permlane16_swap_b32 v29, v23\n"
"v_mfma_f32_16x16x32_bf16 v[244:247], a[128:131], a[92:95], v[244:247]\n"
"v_add_f32 v22, v22, v28\n"
"v_mfma_f32_16x16x32_bf16 v[212:215], a[124:127], a[92:95], v[212:215]\n"
"ds_read_b64_tr_b16 a[64:65], v14 offset:0\n"
"v_add_f32 v23, v23, v29\n"
"buffer_load_dwordx4 v2, s[24:27], s37 offen offset:1024 lds\n"
"s_add_u32 s37, s37, 196608\n"
"v_mfma_f32_16x16x32_bf16 v[180:183], a[120:123], a[92:95], v[180:183]\n"
"v_mul_f32 v22, 0xba800000, v22\n"
"v_mul_f32 v23, 0x3a800000, v23\n"
"v_mfma_f32_16x16x32_bf16 v[148:151], a[92:95], a[116:119], v[148:151]\n"
"ds_read_b128 a[28:31], v6 offset:14336\n"
"v_cvt_pk_bf16_f32 v28, v22, v22\n"
"v_mfma_f32_16x16x32_bf16 v[116:119], a[92:95], a[112:115], v[116:119]\n"
"v_fma_f32 v23, -v22, v22, v23\n"
"v_mfma_f32_16x16x32_bf16 v[84:87], a[108:111], a[92:95], v[84:87]\n"
"ds_read_b64_tr_b16 a[66:67], v14 offset:768\n"
"v_and_b32 v28, 0xffff0000, v28\n"
"v_max_f32 v23, 0, v23\n"
"v_mfma_f32_16x16x32_bf16 v[52:55], a[104:107], a[92:95], v[52:55]\n"
"v_sub_f32 v28, v22, v28\n"
"v_mfma_f32_16x16x32_bf16 v[56:59], a[104:107], a[96:99], v[56:59]\n"
"v_add_f32 v23, 0x3727c5ac, v23\n"
"buffer_load_dwordx4 v3, s[28:31], s38 offen offset:2048 lds\n"
"v_mfma_f32_16x16x32_bf16 v[88:91], a[108:111], a[96:99], v[88:91]\n"
"ds_read_b64_tr_b16 a[68:69], v15 offset:0\n"
"v_and_b32 v30, 32, v30\n"
"v_mfma_f32_16x16x32_bf16 v[120:123], a[96:99], a[112:115], v[120:123]\n"
"v_rsq_f32 v26, v23\n"
"v_cvt_pk_bf16_f32 v16, v22, v28\n"
"v_mfma_f32_16x16x32_bf16 v[152:155], a[96:99], a[116:119], v[152:155]\n"
"ds_read_b64_tr_b16 a[70:71], v15 offset:768\n"
"v_cvt_pk_bf16_f32 v17, v22, v28\n"
"v_mfma_f32_16x16x32_bf16 v[184:187], a[120:123], a[96:99], v[184:187]\n"
"v_cvt_pk_bf16_f32 v18, v22, v28\n"
"v_mfma_f32_16x16x32_bf16 v[216:219], a[124:127], a[96:99], v[216:219]\n"
"v_cvt_pk_bf16_f32 v19, v22, v28\n"
"v_mfma_f32_16x16x32_bf16 v[248:251], a[128:131], a[96:99], v[248:251]\n"
"v_mul_f32 v24, s92, v26\n"
"v_mul_f32 v25, v23, v26\n"
"buffer_load_dwordx4 v4, s[28:31], s38 offen offset:3072 lds\n"
"v_mfma_f32_16x16x32_bf16 a[168:171], a[132:135], a[96:99], a[168:171]\n"
"v_mov_b32 v27, v26\n"
"v_mfma_f32_16x16x32_bf16 a[200:203], a[136:139], a[96:99], a[200:203]\n"
"v_lshrrev_b32 v29, 3, v30\n"
"v_mfma_f32_16x16x32_bf16 a[232:235], a[140:143], a[96:99], a[232:235]\n"
"s_mov_b32 exec_lo, 1\n"
"s_mov_b32 exec_hi, 1\n"
"buffer_store_dwordx4 v[16:19], v30, s[20:23], s9 offen sc1\n"
"buffer_store_dwordx4 v[24:27], v30, s[20:23], s9 offen offset:16 sc1\n"
"buffer_store_dword v26, v29, s[20:23], s10 offen sc1\n"
"s_mov_b64 exec, -1\n"
"v_mfma_f32_16x16x32_bf16 a[236:239], a[140:143], a[100:103], a[236:239]\n"
"v_lshl_add_u32 v29, v0, 2, s95\n"
"v_lshl_add_u32 v30, v0, 2, s94\n"
"v_mfma_f32_16x16x32_bf16 a[204:207], a[136:139], a[100:103], a[204:207]\n"
"v_cmp_gt_u32 vcc, 608, v0\n"
"s_add_u32 m0, s40, 65536\n"
"v_mfma_f32_16x16x32_bf16 a[172:175], a[132:135], a[100:103], a[172:175]\n"
"v_bfrev_b32 v23, 3\n"
"buffer_load_dwordx4 v5, s[28:31], s38 offen offset:0 lds\n"
"s_add_u32 s38, s38, 229376\n"
"v_mfma_f32_16x16x32_bf16 v[252:255], a[128:131], a[100:103], v[252:255]\n"
"v_cndmask_b32 v29, v23, v29, vcc\n"
"v_cmp_gt_u32 vcc, 176, v0\n"
"s_cmp_eq_u32 s2, 6\n"
"v_mfma_f32_16x16x32_bf16 v[220:223], a[124:127], a[100:103], v[220:223]\n"
"s_cselect_b32 s96, 6291456, 0\n"
"v_mfma_f32_16x16x32_bf16 v[188:191], a[120:123], a[100:103], v[188:191]\n"
"v_cndmask_b32 v30, v23, v30, vcc\n"
"s_cselect_b32 s97, 7340032, 0\n"
"v_mfma_f32_16x16x32_bf16 v[156:159], a[100:103], a[116:119], v[156:159]\n"
"v_accvgpr_read_b32 v16, a252\n"
"v_accvgpr_read_b32 v17, a253\n"
"s_cselect_b32 s16, 2048, 0\n"
"v_mfma_f32_16x16x32_bf16 v[124:127], a[100:103], a[112:115], v[124:127]\n"
"v_accvgpr_read_b32 v18, a254\n"
"s_sub_u32 s37, s37, s96\n"
"v_mfma_f32_16x16x32_bf16 v[92:95], a[108:111], a[100:103], v[92:95]\n"
"v_accvgpr_read_b32 v19, a255\n"
"s_sub_u32 s38, s38, s97\n"
"v_mfma_f32_16x16x32_bf16 v[60:63], a[104:107], a[100:103], v[60:63]\n"
"v_accvgpr_read_b32 v28, a240\n"
"s_sub_u32 s36, s36, s16\n"
"s_mov_b32 s41, 6\n"
"s_bitcmp1_b32 s0, 6\n"
"s_cbranch_scc1 SDKV2_%=\n"
"G1KV1_%=:\n"
"s_waitcnt vmcnt(18) lgkmcnt(0)\n"
"s_barrier\n"
"v_mfma_f32_16x16x32_bf16 v[32:35], a[32:35], a[0:3], v[32:35]\n"
"ds_read_b128 a[72:75], v7 offset:0\n"
"s_add_u32 m0, s40, 0\n"
"v_mfma_f32_16x16x32_bf16 v[64:67], a[36:39], a[0:3], v[64:67]\n"
"buffer_load_dwordx4 v1, s[24:27], s37 offen offset:0 lds\n"
"v_mfma_f32_16x16x32_bf16 v[96:99], a[0:3], a[40:43], v[96:99]\n"
"ds_read_b64_tr_b16 a[104:105], v8 offset:20480\n"
"s_cmp_le_u32 s41, 3\n"
"s_cselect_b32 s54, 7344128, 0\n"
"s_cselect_b32 s58, 2101248, 0\n"
"v_mfma_f32_16x16x32_bf16 v[128:131], a[0:3], a[44:47], v[128:131]\n"
"buffer_load_dword a240, v29, s[52:55], 0 offen\n"
"v_mfma_f32_16x16x32_bf16 v[160:163], a[48:51], a[0:3], v[160:163]\n"
"ds_read_b64_tr_b16 a[106:107], v8 offset:20992\n"
"v_mfma_f32_16x16x32_bf16 v[192:195], a[52:55], a[0:3], v[192:195]\n"
"v_add_u32 v29, 2432, v29\n"
"v_mfma_f32_16x16x32_bf16 v[224:227], a[56:59], a[0:3], v[224:227]\n"
"v_mfma_f32_16x16x32_bf16 a[144:147], a[60:63], a[0:3], a[144:147]\n"
"ds_read_b128 a[76:79], v7 offset:2048\n"
"v_mfma_f32_16x16x32_bf16 a[176:179], a[64:67], a[0:3], a[176:179]\n"
"v_mfma_f32_16x16x32_bf16 a[208:211], a[68:71], a[0:3], a[208:211]\n"
"ds_read_b64_tr_b16 a[108:109], v9 offset:20480\n"
"v_mfma_f32_16x16x32_bf16 a[212:215], a[68:71], a[4:7], a[212:215]\n"
"v_mfma_f32_16x16x32_bf16 a[180:183], a[64:67], a[4:7], a[180:183]\n"
"ds_read_b64_tr_b16 a[110:111], v9 offset:20992\n"
"v_mfma_f32_16x16x32_bf16 a[148:151], a[60:63], a[4:7], a[148:151]\n"
"v_mfma_f32_16x16x32_bf16 v[228:231], a[56:59], a[4:7], v[228:231]\n"
"v_mfma_f32_16x16x32_bf16 v[196:199], a[52:55], a[4:7], v[196:199]\n"
"ds_read_b64_tr_b16 a[112:113], v8 offset:21504\n"
"v_mfma_f32_16x16x32_bf16 v[164:167], a[48:51], a[4:7], v[164:167]\n"
"v_mfma_f32_16x16x32_bf16 v[132:135], a[4:7], a[44:47], v[132:135]\n"
"ds_read_b128 a[80:83], v7 offset:4096\n"
"buffer_load_dwordx4 v2, s[24:27], s37 offen offset:1024 lds\n"
"s_add_u32 s37, s37, 196608\n"
"v_mfma_f32_16x16x32_bf16 v[100:103], a[4:7], a[40:43], v[100:103]\n"
"v_mfma_f32_16x16x32_bf16 v[68:71], a[36:39], a[4:7], v[68:71]\n"
"ds_read_b64_tr_b16 a[114:115], v8 offset:22016\n"
"v_mfma_f32_16x16x32_bf16 v[36:39], a[32:35], a[4:7], v[36:39]\n"
"v_mfma_f32_16x16x32_bf16 v[40:43], a[32:35], a[8:11], v[40:43]\n"
"v_mfma_f32_16x16x32_bf16 v[72:75], a[36:39], a[8:11], v[72:75]\n"
"ds_read_b64_tr_b16 a[116:117], v9 offset:21504\n"
"v_mfma_f32_16x16x32_bf16 v[104:107], a[8:11], a[40:43], v[104:107]\n"
"v_mfma_f32_16x16x32_bf16 v[136:139], a[8:11], a[44:47], v[136:139]\n"
"ds_read_b128 a[84:87], v7 offset:6144\n"
"v_mfma_f32_16x16x32_bf16 v[168:171], a[48:51], a[8:11], v[168:171]\n"
"v_mfma_f32_16x16x32_bf16 v[200:203], a[52:55], a[8:11], v[200:203]\n"
"ds_read_b64_tr_b16 a[118:119], v9 offset:22016\n"
"v_mfma_f32_16x16x32_bf16 v[232:235], a[56:59], a[8:11], v[232:235]\n"
"v_mfma_f32_16x16x32_bf16 a[152:155], a[60:63], a[8:11], a[152:155]\n"
"v_mfma_f32_16x16x32_bf16 a[184:187], a[64:67], a[8:11], a[184:187]\n"
"ds_read_b64_tr_b16 a[120:121], v10 offset:20480\n"
"v_mfma_f32_16x16x32_bf16 a[216:219], a[68:71], a[8:11], a[216:219]\n"
"v_mfma_f32_16x16x32_bf16 a[220:223], a[68:71], a[12:15], a[220:223]\n"
"ds_read_b64_tr_b16 a[122:123], v10 offset:21248\n"
"v_mfma_f32_16x16x32_bf16 a[188:191], a[64:67], a[12:15], a[188:191]\n"
"buffer_load_dwordx4 v3, s[28:31], s38 offen offset:2048 lds\n"
"v_mfma_f32_16x16x32_bf16 a[156:159], a[60:63], a[12:15], a[156:159]\n"
"v_mfma_f32_16x16x32_bf16 v[236:239], a[56:59], a[12:15], v[236:239]\n"
"ds_read_b128 a[88:91], v7 offset:8192\n"
"v_mfma_f32_16x16x32_bf16 v[204:207], a[52:55], a[12:15], v[204:207]\n"
"v_mfma_f32_16x16x32_bf16 v[172:175], a[48:51], a[12:15], v[172:175]\n"
"ds_read_b64_tr_b16 a[124:125], v11 offset:20480\n"
"v_mfma_f32_16x16x32_bf16 v[140:143], a[12:15], a[44:47], v[140:143]\n"
"v_mfma_f32_16x16x32_bf16 v[108:111], a[12:15], a[40:43], v[108:111]\n"
"ds_read_b64_tr_b16 a[126:127], v11 offset:21248\n"
"v_mfma_f32_16x16x32_bf16 v[76:79], a[36:39], a[12:15], v[76:79]\n"
"v_mfma_f32_16x16x32_bf16 v[44:47], a[32:35], a[12:15], v[44:47]\n"
"v_mfma_f32_16x16x32_bf16 v[48:51], a[32:35], a[16:19], v[48:51]\n"
"ds_read_b128 a[92:95], v7 offset:10240\n"
"buffer_load_dword a240, v30, s[56:59], 0 offen\n"
"v_mfma_f32_16x16x32_bf16 v[80:83], a[36:39], a[16:19], v[80:83]\n"
"v_mfma_f32_16x16x32_bf16 v[112:115], a[16:19], a[40:43], v[112:115]\n"
"ds_read_b64_tr_b16 a[128:129], v12 offset:20480\n"
"v_add_u32 v30, 704, v30\n"
"v_mfma_f32_16x16x32_bf16 v[144:147], a[16:19], a[44:47], v[144:147]\n"
"v_mfma_f32_16x16x32_bf16 v[176:179], a[48:51], a[16:19], v[176:179]\n"
"ds_read_b64_tr_b16 a[130:131], v12 offset:21248\n"
"v_mfma_f32_16x16x32_bf16 v[208:211], a[52:55], a[16:19], v[208:211]\n"
"v_mfma_f32_16x16x32_bf16 v[240:243], a[56:59], a[16:19], v[240:243]\n"
"v_mfma_f32_16x16x32_bf16 a[160:163], a[60:63], a[16:19], a[160:163]\n"
"ds_read_b64_tr_b16 a[132:133], v13 offset:20480\n"
"buffer_load_dwordx4 v4, s[28:31], s38 offen offset:3072 lds\n"
"v_mfma_f32_16x16x32_bf16 a[192:195], a[64:67], a[16:19], a[192:195]\n"
"v_mfma_f32_16x16x32_bf16 a[224:227], a[68:71], a[16:19], a[224:227]\n"
"ds_read_b128 a[96:99], v7 offset:12288\n"
"v_mfma_f32_16x16x32_bf16 a[228:231], a[68:71], a[20:23], a[228:231]\n"
"v_mfma_f32_16x16x32_bf16 a[196:199], a[64:67], a[20:23], a[196:199]\n"
"ds_read_b64_tr_b16 a[134:135], v13 offset:21248\n"
"v_mfma_f32_16x16x32_bf16 a[164:167], a[60:63], a[20:23], a[164:167]\n"
"v_mfma_f32_16x16x32_bf16 v[244:247], a[56:59], a[20:23], v[244:247]\n"
"v_mfma_f32_16x16x32_bf16 v[212:215], a[52:55], a[20:23], v[212:215]\n"
"ds_read_b64_tr_b16 a[136:137], v14 offset:20480\n"
"v_mfma_f32_16x16x32_bf16 v[180:183], a[48:51], a[20:23], v[180:183]\n"
"v_mfma_f32_16x16x32_bf16 v[148:151], a[20:23], a[44:47], v[148:151]\n"
"ds_read_b128 a[100:103], v7 offset:14336\n"
"v_mfma_f32_16x16x32_bf16 v[116:119], a[20:23], a[40:43], v[116:119]\n"
"v_mfma_f32_16x16x32_bf16 v[84:87], a[36:39], a[20:23], v[84:87]\n"
"ds_read_b64_tr_b16 a[138:139], v14 offset:21248\n"
"v_mfma_f32_16x16x32_bf16 v[52:55], a[32:35], a[20:23], v[52:55]\n"
"v_mfma_f32_16x16x32_bf16 v[56:59], a[32:35], a[24:27], v[56:59]\n"
"v_mfma_f32_16x16x32_bf16 v[88:91], a[36:39], a[24:27], v[88:91]\n"
"ds_read_b64_tr_b16 a[140:141], v15 offset:20480\n"
"s_add_u32 m0, s40, 4096\n"
"v_mfma_f32_16x16x32_bf16 v[120:123], a[24:27], a[40:43], v[120:123]\n"
"buffer_load_dwordx4 v5, s[28:31], s38 offen offset:0 lds\n"
"s_add_u32 s38, s38, 229376\n"
"v_mfma_f32_16x16x32_bf16 v[152:155], a[24:27], a[44:47], v[152:155]\n"
"ds_read_b64_tr_b16 a[142:143], v15 offset:21248\n"
"v_mfma_f32_16x16x32_bf16 v[184:187], a[48:51], a[24:27], v[184:187]\n"
"v_mfma_f32_16x16x32_bf16 v[216:219], a[52:55], a[24:27], v[216:219]\n"
"v_mfma_f32_16x16x32_bf16 v[248:251], a[56:59], a[24:27], v[248:251]\n"
"v_mfma_f32_16x16x32_bf16 a[168:171], a[60:63], a[24:27], a[168:171]\n"
"v_mfma_f32_16x16x32_bf16 a[200:203], a[64:67], a[24:27], a[200:203]\n"
"v_mfma_f32_16x16x32_bf16 a[232:235], a[68:71], a[24:27], a[232:235]\n"
"v_mfma_f32_16x16x32_bf16 a[236:239], a[68:71], a[28:31], a[236:239]\n"
"v_mfma_f32_16x16x32_bf16 a[204:207], a[64:67], a[28:31], a[204:207]\n"
"v_mfma_f32_16x16x32_bf16 a[172:175], a[60:63], a[28:31], a[172:175]\n"
"v_mfma_f32_16x16x32_bf16 v[252:255], a[56:59], a[28:31], v[252:255]\n"
"v_mfma_f32_16x16x32_bf16 v[220:223], a[52:55], a[28:31], v[220:223]\n"
"v_mfma_f32_16x16x32_bf16 v[188:191], a[48:51], a[28:31], v[188:191]\n"
"v_mfma_f32_16x16x32_bf16 v[156:159], a[28:31], a[44:47], v[156:159]\n"
"v_mfma_f32_16x16x32_bf16 v[124:127], a[28:31], a[40:43], v[124:127]\n"
"v_mfma_f32_16x16x32_bf16 v[92:95], a[36:39], a[28:31], v[92:95]\n"
"v_mfma_f32_16x16x32_bf16 v[60:63], a[32:35], a[28:31], v[60:63]\n"
"s_waitcnt vmcnt(12) lgkmcnt(0)\n"
"s_barrier\n"
"v_mfma_f32_16x16x32_bf16 v[32:35], a[104:107], a[72:75], v[32:35]\n"
"ds_read_b128 a[0:3], v6 offset:32768\n"
"s_add_u32 m0, s39, 0\n"
"v_mfma_f32_16x16x32_bf16 v[64:67], a[108:111], a[72:75], v[64:67]\n"
"buffer_load_dwordx4 v20, s[44:47], s36 offen offset:0 lds\n"
"s_add_u32 s36, s36, 15360\n"
"v_mfma_f32_16x16x32_bf16 v[96:99], a[72:75], a[112:115], v[96:99]\n"
"ds_read_b64_tr_b16 a[32:33], v8 offset:40960\n"
"v_mfma_f32_16x16x32_bf16 v[128:131], a[72:75], a[116:119], v[128:131]\n"
"v_mfma_f32_16x16x32_bf16 v[160:163], a[120:123], a[72:75], v[160:163]\n"
"ds_read_b64_tr_b16 a[34:35], v8 offset:41472\n"
"v_mfma_f32_16x16x32_bf16 v[192:195], a[124:127], a[72:75], v[192:195]\n"
"v_mfma_f32_16x16x32_bf16 v[224:227], a[128:131], a[72:75], v[224:227]\n"
"buffer_load_dwordx4 v21, s[44:47], s36 offen offset:1024 lds\n"
"s_add_u32 s36, s36, 15360\n"
"v_mfma_f32_16x16x32_bf16 a[144:147], a[132:135], a[72:75], a[144:147]\n"
"ds_read_b128 a[4:7], v6 offset:34816\n"
"v_mfma_f32_16x16x32_bf16 a[176:179], a[136:139], a[72:75], a[176:179]\n"
"v_mfma_f32_16x16x32_bf16 a[208:211], a[140:143], a[72:75], a[208:211]\n"
"ds_read_b64_tr_b16 a[36:37], v9 offset:40960\n"
"v_mfma_f32_16x16x32_bf16 a[212:215], a[140:143], a[76:79], a[212:215]\n"
"v_mfma_f32_16x16x32_bf16 a[180:183], a[136:139], a[76:79], a[180:183]\n"
"ds_read_b64_tr_b16 a[38:39], v9 offset:41472\n"
"v_mfma_f32_16x16x32_bf16 a[148:151], a[132:135], a[76:79], a[148:151]\n"
"buffer_load_dwordx4 v20, s[44:47], s36 offen offset:2048 lds\n"
"s_add_u32 s36, s36, 15360\n"
"v_mfma_f32_16x16x32_bf16 v[228:231], a[128:131], a[76:79], v[228:231]\n"
"v_mfma_f32_16x16x32_bf16 v[196:199], a[124:127], a[76:79], v[196:199]\n"
"ds_read_b64_tr_b16 a[40:41], v8 offset:41984\n"
"v_mfma_f32_16x16x32_bf16 v[164:167], a[120:123], a[76:79], v[164:167]\n"
"v_mfma_f32_16x16x32_bf16 v[132:135], a[76:79], a[116:119], v[132:135]\n"
"ds_read_b128 a[8:11], v6 offset:36864\n"
"v_mfma_f32_16x16x32_bf16 v[100:103], a[76:79], a[112:115], v[100:103]\n"
"v_mfma_f32_16x16x32_bf16 v[68:71], a[108:111], a[76:79], v[68:71]\n"
"ds_read_b64_tr_b16 a[42:43], v8 offset:42496\n"
"buffer_load_dwordx4 v21, s[44:47], s36 offen offset:3072 lds\n"
"s_add_u32 s36, s36, 19456\n"
"v_mfma_f32_16x16x32_bf16 v[36:39], a[104:107], a[76:79], v[36:39]\n"
"v_mfma_f32_16x16x32_bf16 v[40:43], a[104:107], a[80:83], v[40:43]\n"
"v_mfma_f32_16x16x32_bf16 v[72:75], a[108:111], a[80:83], v[72:75]\n"
"ds_read_b64_tr_b16 a[44:45], v9 offset:41984\n"
"v_mfma_f32_16x16x32_bf16 v[104:107], a[80:83], a[112:115], v[104:107]\n"
"v_mfma_f32_16x16x32_bf16 v[136:139], a[80:83], a[116:119], v[136:139]\n"
"ds_read_b128 a[12:15], v6 offset:38912\n"
"s_add_u32 m0, s39, 4096\n"
"v_mfma_f32_16x16x32_bf16 v[168:171], a[120:123], a[80:83], v[168:171]\n"
"buffer_load_dwordx4 v20, s[44:47], s36 offen offset:0 lds\n"
"s_add_u32 s36, s36, 15360\n"
"v_mfma_f32_16x16x32_bf16 v[200:203], a[124:127], a[80:83], v[200:203]\n"
"ds_read_b64_tr_b16 a[46:47], v9 offset:42496\n"
"v_mfma_f32_16x16x32_bf16 v[232:235], a[128:131], a[80:83], v[232:235]\n"
"v_mfma_f32_16x16x32_bf16 a[152:155], a[132:135], a[80:83], a[152:155]\n"
"v_mfma_f32_16x16x32_bf16 a[184:187], a[136:139], a[80:83], a[184:187]\n"
"ds_read_b64_tr_b16 a[48:49], v10 offset:40960\n"
"v_mfma_f32_16x16x32_bf16 a[216:219], a[140:143], a[80:83], a[216:219]\n"
"v_mfma_f32_16x16x32_bf16 a[220:223], a[140:143], a[84:87], a[220:223]\n"
"ds_read_b64_tr_b16 a[50:51], v10 offset:41728\n"
"buffer_load_dwordx4 v21, s[44:47], s36 offen offset:1024 lds\n"
"s_add_u32 s36, s36, 15360\n"
"v_mfma_f32_16x16x32_bf16 a[188:191], a[136:139], a[84:87], a[188:191]\n"
"v_mfma_f32_16x16x32_bf16 a[156:159], a[132:135], a[84:87], a[156:159]\n"
"v_mfma_f32_16x16x32_bf16 v[236:239], a[128:131], a[84:87], v[236:239]\n"
"ds_read_b128 a[16:19], v6 offset:40960\n"
"v_mfma_f32_16x16x32_bf16 v[204:207], a[124:127], a[84:87], v[204:207]\n"
"v_mfma_f32_16x16x32_bf16 v[172:175], a[120:123], a[84:87], v[172:175]\n"
"ds_read_b64_tr_b16 a[52:53], v11 offset:40960\n"
"v_mfma_f32_16x16x32_bf16 v[140:143], a[84:87], a[116:119], v[140:143]\n"
"buffer_load_dwordx4 v20, s[44:47], s36 offen offset:2048 lds\n"
"s_add_u32 s36, s36, 15360\n"
"v_mfma_f32_16x16x32_bf16 v[108:111], a[84:87], a[112:115], v[108:111]\n"
"ds_read_b64_tr_b16 a[54:55], v11 offset:41728\n"
"v_mfma_f32_16x16x32_bf16 v[76:79], a[108:111], a[84:87], v[76:79]\n"
"v_mfma_f32_16x16x32_bf16 v[44:47], a[104:107], a[84:87], v[44:47]\n"
"v_mfma_f32_16x16x32_bf16 v[48:51], a[104:107], a[88:91], v[48:51]\n"
"ds_read_b128 a[20:23], v6 offset:43008\n"
"v_mfma_f32_16x16x32_bf16 v[80:83], a[108:111], a[88:91], v[80:83]\n"
"v_mfma_f32_16x16x32_bf16 v[112:115], a[88:91], a[112:115], v[112:115]\n"
"ds_read_b64_tr_b16 a[56:57], v12 offset:40960\n"
"buffer_load_dwordx4 v21, s[44:47], s36 offen offset:3072 lds\n"
"s_sub_u32 s36, s36, 111488\n"
"v_mfma_f32_16x16x32_bf16 v[144:147], a[88:91], a[116:119], v[144:147]\n"
"v_mfma_f32_16x16x32_bf16 v[176:179], a[120:123], a[88:91], v[176:179]\n"
"ds_read_b64_tr_b16 a[58:59], v12 offset:41728\n"
"v_mfma_f32_16x16x32_bf16 v[208:211], a[124:127], a[88:91], v[208:211]\n"
"v_mfma_f32_16x16x32_bf16 v[240:243], a[128:131], a[88:91], v[240:243]\n"
"v_mfma_f32_16x16x32_bf16 a[160:163], a[132:135], a[88:91], a[160:163]\n"
"ds_read_b64_tr_b16 a[60:61], v13 offset:40960\n"
"s_add_u32 m0, s40, 20480\n"
"v_mfma_f32_16x16x32_bf16 a[192:195], a[136:139], a[88:91], a[192:195]\n"
"buffer_load_dwordx4 v1, s[24:27], s37 offen offset:0 lds\n"
"v_mfma_f32_16x16x32_bf16 a[224:227], a[140:143], a[88:91], a[224:227]\n"
"ds_read_b128 a[24:27], v6 offset:45056\n"
"v_mfma_f32_16x16x32_bf16 a[228:231], a[140:143], a[92:95], a[228:231]\n"
"v_mfma_f32_16x16x32_bf16 a[196:199], a[136:139], a[92:95], a[196:199]\n"
"ds_read_b64_tr_b16 a[62:63], v13 offset:41728\n"
"v_mfma_f32_16x16x32_bf16 a[164:167], a[132:135], a[92:95], a[164:167]\n"
"v_mfma_f32_16x16x32_bf16 v[244:247], a[128:131], a[92:95], v[244:247]\n"
"v_mfma_f32_16x16x32_bf16 v[212:215], a[124:127], a[92:95], v[212:215]\n"
"ds_read_b64_tr_b16 a[64:65], v14 offset:40960\n"
"buffer_load_dwordx4 v2, s[24:27], s37 offen offset:1024 lds\n"
"s_add_u32 s37, s37, 196608\n"
"v_mfma_f32_16x16x32_bf16 v[180:183], a[120:123], a[92:95], v[180:183]\n"
"v_mfma_f32_16x16x32_bf16 v[148:151], a[92:95], a[116:119], v[148:151]\n"
"ds_read_b128 a[28:31], v6 offset:47104\n"
"v_mfma_f32_16x16x32_bf16 v[116:119], a[92:95], a[112:115], v[116:119]\n"
"v_mfma_f32_16x16x32_bf16 v[84:87], a[108:111], a[92:95], v[84:87]\n"
"ds_read_b64_tr_b16 a[66:67], v14 offset:41728\n"
"v_mfma_f32_16x16x32_bf16 v[52:55], a[104:107], a[92:95], v[52:55]\n"
"v_mfma_f32_16x16x32_bf16 v[56:59], a[104:107], a[96:99], v[56:59]\n"
"buffer_load_dwordx4 v3, s[28:31], s38 offen offset:2048 lds\n"
"v_mfma_f32_16x16x32_bf16 v[88:91], a[108:111], a[96:99], v[88:91]\n"
"ds_read_b64_tr_b16 a[68:69], v15 offset:40960\n"
"v_mfma_f32_16x16x32_bf16 v[120:123], a[96:99], a[112:115], v[120:123]\n"
"v_mfma_f32_16x16x32_bf16 v[152:155], a[96:99], a[116:119], v[152:155]\n"
"ds_read_b64_tr_b16 a[70:71], v15 offset:41728\n"
"v_mfma_f32_16x16x32_bf16 v[184:187], a[120:123], a[96:99], v[184:187]\n"
"v_mfma_f32_16x16x32_bf16 v[216:219], a[124:127], a[96:99], v[216:219]\n"
"v_mfma_f32_16x16x32_bf16 v[248:251], a[128:131], a[96:99], v[248:251]\n"
"buffer_load_dwordx4 v4, s[28:31], s38 offen offset:3072 lds\n"
"v_mfma_f32_16x16x32_bf16 a[168:171], a[132:135], a[96:99], a[168:171]\n"
"v_mfma_f32_16x16x32_bf16 a[200:203], a[136:139], a[96:99], a[200:203]\n"
"v_mfma_f32_16x16x32_bf16 a[232:235], a[140:143], a[96:99], a[232:235]\n"
"v_mfma_f32_16x16x32_bf16 a[236:239], a[140:143], a[100:103], a[236:239]\n"
"v_mfma_f32_16x16x32_bf16 a[204:207], a[136:139], a[100:103], a[204:207]\n"
"s_add_u32 m0, s40, 24576\n"
"v_mfma_f32_16x16x32_bf16 a[172:175], a[132:135], a[100:103], a[172:175]\n"
"buffer_load_dwordx4 v5, s[28:31], s38 offen offset:0 lds\n"
"s_add_u32 s38, s38, 229376\n"
"v_mfma_f32_16x16x32_bf16 v[252:255], a[128:131], a[100:103], v[252:255]\n"
"v_mfma_f32_16x16x32_bf16 v[220:223], a[124:127], a[100:103], v[220:223]\n"
"v_mfma_f32_16x16x32_bf16 v[188:191], a[120:123], a[100:103], v[188:191]\n"
"v_mfma_f32_16x16x32_bf16 v[156:159], a[100:103], a[116:119], v[156:159]\n"
"v_mfma_f32_16x16x32_bf16 v[124:127], a[100:103], a[112:115], v[124:127]\n"
"v_mfma_f32_16x16x32_bf16 v[92:95], a[108:111], a[100:103], v[92:95]\n"
"v_mfma_f32_16x16x32_bf16 v[60:63], a[104:107], a[100:103], v[60:63]\n"
"s_waitcnt vmcnt(20) lgkmcnt(0)\n"
"s_barrier\n"
"v_mfma_f32_16x16x32_bf16 v[32:35], a[32:35], a[0:3], v[32:35]\n"
"ds_read_b128 a[72:75], v7 offset:32768\n"
"s_add_u32 m0, s40, 40960\n"
"v_mfma_f32_16x16x32_bf16 v[64:67], a[36:39], a[0:3], v[64:67]\n"
"buffer_load_dwordx4 v1, s[24:27], s37 offen offset:0 lds\n"
"v_mfma_f32_16x16x32_bf16 v[96:99], a[0:3], a[40:43], v[96:99]\n"
"ds_read_b64_tr_b16 a[104:105], v8 offset:61440\n"
"v_mfma_f32_16x16x32_bf16 v[128:131], a[0:3], a[44:47], v[128:131]\n"
"v_mfma_f32_16x16x32_bf16 v[160:163], a[48:51], a[0:3], v[160:163]\n"
"ds_read_b64_tr_b16 a[106:107], v8 offset:61952\n"
"v_mfma_f32_16x16x32_bf16 v[192:195], a[52:55], a[0:3], v[192:195]\n"
"v_mfma_f32_16x16x32_bf16 v[224:227], a[56:59], a[0:3], v[224:227]\n"
"v_mfma_f32_16x16x32_bf16 a[144:147], a[60:63], a[0:3], a[144:147]\n"
"ds_read_b128 a[76:79], v7 offset:34816\n"
"v_mfma_f32_16x16x32_bf16 a[176:179], a[64:67], a[0:3], a[176:179]\n"
"v_mfma_f32_16x16x32_bf16 a[208:211], a[68:71], a[0:3], a[208:211]\n"
"ds_read_b64_tr_b16 a[108:109], v9 offset:61440\n"
"v_mfma_f32_16x16x32_bf16 a[212:215], a[68:71], a[4:7], a[212:215]\n"
"v_mfma_f32_16x16x32_bf16 a[180:183], a[64:67], a[4:7], a[180:183]\n"
"ds_read_b64_tr_b16 a[110:111], v9 offset:61952\n"
"v_mfma_f32_16x16x32_bf16 a[148:151], a[60:63], a[4:7], a[148:151]\n"
"v_mfma_f32_16x16x32_bf16 v[228:231], a[56:59], a[4:7], v[228:231]\n"
"v_mfma_f32_16x16x32_bf16 v[196:199], a[52:55], a[4:7], v[196:199]\n"
"ds_read_b64_tr_b16 a[112:113], v8 offset:62464\n"
"v_mfma_f32_16x16x32_bf16 v[164:167], a[48:51], a[4:7], v[164:167]\n"
"v_mfma_f32_16x16x32_bf16 v[132:135], a[4:7], a[44:47], v[132:135]\n"
"ds_read_b128 a[80:83], v7 offset:36864\n"
"buffer_load_dwordx4 v2, s[24:27], s37 offen offset:1024 lds\n"
"s_add_u32 s37, s37, 196608\n"
"v_mfma_f32_16x16x32_bf16 v[100:103], a[4:7], a[40:43], v[100:103]\n"
"v_mfma_f32_16x16x32_bf16 v[68:71], a[36:39], a[4:7], v[68:71]\n"
"ds_read_b64_tr_b16 a[114:115], v8 offset:62976\n"
"v_mfma_f32_16x16x32_bf16 v[36:39], a[32:35], a[4:7], v[36:39]\n"
"v_mfma_f32_16x16x32_bf16 v[40:43], a[32:35], a[8:11], v[40:43]\n"
"v_mfma_f32_16x16x32_bf16 v[72:75], a[36:39], a[8:11], v[72:75]\n"
"ds_read_b64_tr_b16 a[116:117], v9 offset:62464\n"
"v_mfma_f32_16x16x32_bf16 v[104:107], a[8:11], a[40:43], v[104:107]\n"
"v_mfma_f32_16x16x32_bf16 v[136:139], a[8:11], a[44:47], v[136:139]\n"
"ds_read_b128 a[84:87], v7 offset:38912\n"
"v_mfma_f32_16x16x32_bf16 v[168:171], a[48:51], a[8:11], v[168:171]\n"
"v_mfma_f32_16x16x32_bf16 v[200:203], a[52:55], a[8:11], v[200:203]\n"
"ds_read_b64_tr_b16 a[118:119], v9 offset:62976\n"
"v_mfma_f32_16x16x32_bf16 v[232:235], a[56:59], a[8:11], v[232:235]\n"
"v_mfma_f32_16x16x32_bf16 a[152:155], a[60:63], a[8:11], a[152:155]\n"
"v_mfma_f32_16x16x32_bf16 a[184:187], a[64:67], a[8:11], a[184:187]\n"
"ds_read_b64_tr_b16 a[120:121], v10 offset:61440\n"
"v_mfma_f32_16x16x32_bf16 a[216:219], a[68:71], a[8:11], a[216:219]\n"
"v_mfma_f32_16x16x32_bf16 a[220:223], a[68:71], a[12:15], a[220:223]\n"
"ds_read_b64_tr_b16 a[122:123], v10 offset:62208\n"
"v_mfma_f32_16x16x32_bf16 a[188:191], a[64:67], a[12:15], a[188:191]\n"
"buffer_load_dwordx4 v3, s[28:31], s38 offen offset:2048 lds\n"
"v_mfma_f32_16x16x32_bf16 a[156:159], a[60:63], a[12:15], a[156:159]\n"
"v_mfma_f32_16x16x32_bf16 v[236:239], a[56:59], a[12:15], v[236:239]\n"
"ds_read_b128 a[88:91], v7 offset:40960\n"
"v_mfma_f32_16x16x32_bf16 v[204:207], a[52:55], a[12:15], v[204:207]\n"
"v_mfma_f32_16x16x32_bf16 v[172:175], a[48:51], a[12:15], v[172:175]\n"
"ds_read_b64_tr_b16 a[124:125], v11 offset:61440\n"
"v_mfma_f32_16x16x32_bf16 v[140:143], a[12:15], a[44:47], v[140:143]\n"
"v_mfma_f32_16x16x32_bf16 v[108:111], a[12:15], a[40:43], v[108:111]\n"
"ds_read_b64_tr_b16 a[126:127], v11 offset:62208\n"
"v_mfma_f32_16x16x32_bf16 v[76:79], a[36:39], a[12:15], v[76:79]\n"
"v_mfma_f32_16x16x32_bf16 v[44:47], a[32:35], a[12:15], v[44:47]\n"
"v_mfma_f32_16x16x32_bf16 v[48:51], a[32:35], a[16:19], v[48:51]\n"
"ds_read_b128 a[92:95], v7 offset:43008\n"
"v_mfma_f32_16x16x32_bf16 v[80:83], a[36:39], a[16:19], v[80:83]\n"
"v_mfma_f32_16x16x32_bf16 v[112:115], a[16:19], a[40:43], v[112:115]\n"
"ds_read_b64_tr_b16 a[128:129], v12 offset:61440\n"
"v_mfma_f32_16x16x32_bf16 v[144:147], a[16:19], a[44:47], v[144:147]\n"
"v_mfma_f32_16x16x32_bf16 v[176:179], a[48:51], a[16:19], v[176:179]\n"
"ds_read_b64_tr_b16 a[130:131], v12 offset:62208\n"
"v_mfma_f32_16x16x32_bf16 v[208:211], a[52:55], a[16:19], v[208:211]\n"
"v_mfma_f32_16x16x32_bf16 v[240:243], a[56:59], a[16:19], v[240:243]\n"
"v_mfma_f32_16x16x32_bf16 a[160:163], a[60:63], a[16:19], a[160:163]\n"
"ds_read_b64_tr_b16 a[132:133], v13 offset:61440\n"
"buffer_load_dwordx4 v4, s[28:31], s38 offen offset:3072 lds\n"
"v_mfma_f32_16x16x32_bf16 a[192:195], a[64:67], a[16:19], a[192:195]\n"
"v_mfma_f32_16x16x32_bf16 a[224:227], a[68:71], a[16:19], a[224:227]\n"
"ds_read_b128 a[96:99], v7 offset:45056\n"
"v_mfma_f32_16x16x32_bf16 a[228:231], a[68:71], a[20:23], a[228:231]\n"
"v_mfma_f32_16x16x32_bf16 a[196:199], a[64:67], a[20:23], a[196:199]\n"
"ds_read_b64_tr_b16 a[134:135], v13 offset:62208\n"
"v_mfma_f32_16x16x32_bf16 a[164:167], a[60:63], a[20:23], a[164:167]\n"
"v_mfma_f32_16x16x32_bf16 v[244:247], a[56:59], a[20:23], v[244:247]\n"
"v_mfma_f32_16x16x32_bf16 v[212:215], a[52:55], a[20:23], v[212:215]\n"
"ds_read_b64_tr_b16 a[136:137], v14 offset:61440\n"
"v_mfma_f32_16x16x32_bf16 v[180:183], a[48:51], a[20:23], v[180:183]\n"
"v_mfma_f32_16x16x32_bf16 v[148:151], a[20:23], a[44:47], v[148:151]\n"
"ds_read_b128 a[100:103], v7 offset:47104\n"
"v_mfma_f32_16x16x32_bf16 v[116:119], a[20:23], a[40:43], v[116:119]\n"
"v_mfma_f32_16x16x32_bf16 v[84:87], a[36:39], a[20:23], v[84:87]\n"
"ds_read_b64_tr_b16 a[138:139], v14 offset:62208\n"
"v_mfma_f32_16x16x32_bf16 v[52:55], a[32:35], a[20:23], v[52:55]\n"
"v_mfma_f32_16x16x32_bf16 v[56:59], a[32:35], a[24:27], v[56:59]\n"
"v_mfma_f32_16x16x32_bf16 v[88:91], a[36:39], a[24:27], v[88:91]\n"
"ds_read_b64_tr_b16 a[140:141], v15 offset:61440\n"
"s_add_u32 m0, s40, 45056\n"
"v_mfma_f32_16x16x32_bf16 v[120:123], a[24:27], a[40:43], v[120:123]\n"
"buffer_load_dwordx4 v5, s[28:31], s38 offen offset:0 lds\n"
"s_add_u32 s38, s38, 229376\n"
"v_mfma_f32_16x16x32_bf16 v[152:155], a[24:27], a[44:47], v[152:155]\n"
"ds_read_b64_tr_b16 a[142:143], v15 offset:62208\n"
"v_mfma_f32_16x16x32_bf16 v[184:187], a[48:51], a[24:27], v[184:187]\n"
"v_mfma_f32_16x16x32_bf16 v[216:219], a[52:55], a[24:27], v[216:219]\n"
"v_mfma_f32_16x16x32_bf16 v[248:251], a[56:59], a[24:27], v[248:251]\n"
"v_mfma_f32_16x16x32_bf16 a[168:171], a[60:63], a[24:27], a[168:171]\n"
"v_mfma_f32_16x16x32_bf16 a[200:203], a[64:67], a[24:27], a[200:203]\n"
"v_mfma_f32_16x16x32_bf16 a[232:235], a[68:71], a[24:27], a[232:235]\n"
"v_mfma_f32_16x16x32_bf16 a[236:239], a[68:71], a[28:31], a[236:239]\n"
"v_mfma_f32_16x16x32_bf16 a[204:207], a[64:67], a[28:31], a[204:207]\n"
"v_mfma_f32_16x16x32_bf16 a[172:175], a[60:63], a[28:31], a[172:175]\n"
"v_mfma_f32_16x16x32_bf16 v[252:255], a[56:59], a[28:31], v[252:255]\n"
"v_mfma_f32_16x16x32_bf16 v[220:223], a[52:55], a[28:31], v[220:223]\n"
"v_mfma_f32_16x16x32_bf16 v[188:191], a[48:51], a[28:31], v[188:191]\n"
"v_mfma_f32_16x16x32_bf16 v[156:159], a[28:31], a[44:47], v[156:159]\n"
"v_mfma_f32_16x16x32_bf16 v[124:127], a[28:31], a[40:43], v[124:127]\n"
"v_mfma_f32_16x16x32_bf16 v[92:95], a[36:39], a[28:31], v[92:95]\n"
"v_mfma_f32_16x16x32_bf16 v[60:63], a[32:35], a[28:31], v[60:63]\n"
"s_waitcnt vmcnt(10) lgkmcnt(0)\n"
"s_barrier\n"
"v_mfma_f32_16x16x32_bf16 v[32:35], a[104:107], a[72:75], v[32:35]\n"
"ds_read_b128 a[0:3], v6 offset:0\n"
"s_add_u32 m0, s39, 32768\n"
"v_mfma_f32_16x16x32_bf16 v[64:67], a[108:111], a[72:75], v[64:67]\n"
"buffer_load_dwordx4 v20, s[44:47], s36 offen offset:0 lds\n"
"s_add_u32 s36, s36, 15360\n"
"v_mfma_f32_16x16x32_bf16 v[96:99], a[72:75], a[112:115], v[96:99]\n"
"ds_read_b64_tr_b16 a[32:33], v8 offset:0\n"
"v_mfma_f32_16x16x32_bf16 v[128:131], a[72:75], a[116:119], v[128:131]\n"
"v_mfma_f32_16x16x32_bf16 v[160:163], a[120:123], a[72:75], v[160:163]\n"
"ds_read_b64_tr_b16 a[34:35], v8 offset:512\n"
"v_mfma_f32_16x16x32_bf16 v[192:195], a[124:127], a[72:75], v[192:195]\n"
"v_mfma_f32_16x16x32_bf16 v[224:227], a[128:131], a[72:75], v[224:227]\n"
"buffer_load_dwordx4 v21, s[44:47], s36 offen offset:1024 lds\n"
"s_add_u32 s36, s36, 15360\n"
"v_mfma_f32_16x16x32_bf16 a[144:147], a[132:135], a[72:75], a[144:147]\n"
"ds_read_b128 a[4:7], v6 offset:2048\n"
"v_mfma_f32_16x16x32_bf16 a[176:179], a[136:139], a[72:75], a[176:179]\n"
"v_mfma_f32_16x16x32_bf16 a[208:211], a[140:143], a[72:75], a[208:211]\n"
"ds_read_b64_tr_b16 a[36:37], v9 offset:0\n"
"v_mfma_f32_16x16x32_bf16 a[212:215], a[140:143], a[76:79], a[212:215]\n"
"v_mfma_f32_16x16x32_bf16 a[180:183], a[136:139], a[76:79], a[180:183]\n"
"ds_read_b64_tr_b16 a[38:39], v9 offset:512\n"
"v_mfma_f32_16x16x32_bf16 a[148:151], a[132:135], a[76:79], a[148:151]\n"
"buffer_load_dwordx4 v20, s[44:47], s36 offen offset:2048 lds\n"
"s_add_u32 s36, s36, 15360\n"
"v_mfma_f32_16x16x32_bf16 v[228:231], a[128:131], a[76:79], v[228:231]\n"
"v_mfma_f32_16x16x32_bf16 v[196:199], a[124:127], a[76:79], v[196:199]\n"
"ds_read_b64_tr_b16 a[40:41], v8 offset:1024\n"
"v_mfma_f32_16x16x32_bf16 v[164:167], a[120:123], a[76:79], v[164:167]\n"
"v_mfma_f32_16x16x32_bf16 v[132:135], a[76:79], a[116:119], v[132:135]\n"
"ds_read_b128 a[8:11], v6 offset:4096\n"
"v_mfma_f32_16x16x32_bf16 v[100:103], a[76:79], a[112:115], v[100:103]\n"
"v_mfma_f32_16x16x32_bf16 v[68:71], a[108:111], a[76:79], v[68:71]\n"
"ds_read_b64_tr_b16 a[42:43], v8 offset:1536\n"
"buffer_load_dwordx4 v21, s[44:47], s36 offen offset:3072 lds\n"
"s_add_u32 s36, s36, 19456\n"
"v_mfma_f32_16x16x32_bf16 v[36:39], a[104:107], a[76:79], v[36:39]\n"
"v_mfma_f32_16x16x32_bf16 v[40:43], a[104:107], a[80:83], v[40:43]\n"
"v_mfma_f32_16x16x32_bf16 v[72:75], a[108:111], a[80:83], v[72:75]\n"
"ds_read_b64_tr_b16 a[44:45], v9 offset:1024\n"
"v_mfma_f32_16x16x32_bf16 v[104:107], a[80:83], a[112:115], v[104:107]\n"
"v_mfma_f32_16x16x32_bf16 v[136:139], a[80:83], a[116:119], v[136:139]\n"
"ds_read_b128 a[12:15], v6 offset:6144\n"
"s_add_u32 m0, s39, 36864\n"
"v_mfma_f32_16x16x32_bf16 v[168:171], a[120:123], a[80:83], v[168:171]\n"
"buffer_load_dwordx4 v20, s[44:47], s36 offen offset:0 lds\n"
"s_add_u32 s36, s36, 15360\n"
"v_mfma_f32_16x16x32_bf16 v[200:203], a[124:127], a[80:83], v[200:203]\n"
"ds_read_b64_tr_b16 a[46:47], v9 offset:1536\n"
"v_mfma_f32_16x16x32_bf16 v[232:235], a[128:131], a[80:83], v[232:235]\n"
"v_mfma_f32_16x16x32_bf16 a[152:155], a[132:135], a[80:83], a[152:155]\n"
"v_mfma_f32_16x16x32_bf16 a[184:187], a[136:139], a[80:83], a[184:187]\n"
"ds_read_b64_tr_b16 a[48:49], v10 offset:0\n"
"v_mfma_f32_16x16x32_bf16 a[216:219], a[140:143], a[80:83], a[216:219]\n"
"v_mfma_f32_16x16x32_bf16 a[220:223], a[140:143], a[84:87], a[220:223]\n"
"ds_read_b64_tr_b16 a[50:51], v10 offset:768\n"
"buffer_load_dwordx4 v21, s[44:47], s36 offen offset:1024 lds\n"
"s_add_u32 s36, s36, 15360\n"
"v_mfma_f32_16x16x32_bf16 a[188:191], a[136:139], a[84:87], a[188:191]\n"
"v_mfma_f32_16x16x32_bf16 a[156:159], a[132:135], a[84:87], a[156:159]\n"
"v_mfma_f32_16x16x32_bf16 v[236:239], a[128:131], a[84:87], v[236:239]\n"
"ds_read_b128 a[16:19], v6 offset:8192\n"
"v_mfma_f32_16x16x32_bf16 v[204:207], a[124:127], a[84:87], v[204:207]\n"
"v_mfma_f32_16x16x32_bf16 v[172:175], a[120:123], a[84:87], v[172:175]\n"
"ds_read_b64_tr_b16 a[52:53], v11 offset:0\n"
"v_mfma_f32_16x16x32_bf16 v[140:143], a[84:87], a[116:119], v[140:143]\n"
"buffer_load_dwordx4 v20, s[44:47], s36 offen offset:2048 lds\n"
"s_add_u32 s36, s36, 15360\n"
"v_mfma_f32_16x16x32_bf16 v[108:111], a[84:87], a[112:115], v[108:111]\n"
"ds_read_b64_tr_b16 a[54:55], v11 offset:768\n"
"v_mfma_f32_16x16x32_bf16 v[76:79], a[108:111], a[84:87], v[76:79]\n"
"v_mfma_f32_16x16x32_bf16 v[44:47], a[104:107], a[84:87], v[44:47]\n"
"v_mfma_f32_16x16x32_bf16 v[48:51], a[104:107], a[88:91], v[48:51]\n"
"ds_read_b128 a[20:23], v6 offset:10240\n"
"v_mfma_f32_16x16x32_bf16 v[80:83], a[108:111], a[88:91], v[80:83]\n"
"v_mfma_f32_16x16x32_bf16 v[112:115], a[88:91], a[112:115], v[112:115]\n"
"ds_read_b64_tr_b16 a[56:57], v12 offset:0\n"
"buffer_load_dwordx4 v21, s[44:47], s36 offen offset:3072 lds\n"
"s_sub_u32 s36, s36, 111488\n"
"v_mfma_f32_16x16x32_bf16 v[144:147], a[88:91], a[116:119], v[144:147]\n"
"v_mfma_f32_16x16x32_bf16 v[176:179], a[120:123], a[88:91], v[176:179]\n"
"ds_read_b64_tr_b16 a[58:59], v12 offset:768\n"
"v_mfma_f32_16x16x32_bf16 v[208:211], a[124:127], a[88:91], v[208:211]\n"
"v_mfma_f32_16x16x32_bf16 v[240:243], a[128:131], a[88:91], v[240:243]\n"
"v_mfma_f32_16x16x32_bf16 a[160:163], a[132:135], a[88:91], a[160:163]\n"
"ds_read_b64_tr_b16 a[60:61], v13 offset:0\n"
"s_add_u32 m0, s40, 61440\n"
"v_mfma_f32_16x16x32_bf16 a[192:195], a[136:139], a[88:91], a[192:195]\n"
"buffer_load_dwordx4 v1, s[24:27], s37 offen offset:0 lds\n"
"v_mfma_f32_16x16x32_bf16 a[224:227], a[140:143], a[88:91], a[224:227]\n"
"ds_read_b128 a[24:27], v6 offset:12288\n"
"v_mfma_f32_16x16x32_bf16 a[228:231], a[140:143], a[92:95], a[228:231]\n"
"v_mfma_f32_16x16x32_bf16 a[196:199], a[136:139], a[92:95], a[196:199]\n"
"ds_read_b64_tr_b16 a[62:63], v13 offset:768\n"
"v_mfma_f32_16x16x32_bf16 a[164:167], a[132:135], a[92:95], a[164:167]\n"
"v_mfma_f32_16x16x32_bf16 v[244:247], a[128:131], a[92:95], v[244:247]\n"
"v_mfma_f32_16x16x32_bf16 v[212:215], a[124:127], a[92:95], v[212:215]\n"
"ds_read_b64_tr_b16 a[64:65], v14 offset:0\n"
"buffer_load_dwordx4 v2, s[24:27], s37 offen offset:1024 lds\n"
"s_add_u32 s37, s37, 196608\n"
"v_mfma_f32_16x16x32_bf16 v[180:183], a[120:123], a[92:95], v[180:183]\n"
"v_mfma_f32_16x16x32_bf16 v[148:151], a[92:95], a[116:119], v[148:151]\n"
"ds_read_b128 a[28:31], v6 offset:14336\n"
"v_mfma_f32_16x16x32_bf16 v[116:119], a[92:95], a[112:115], v[116:119]\n"
"v_mfma_f32_16x16x32_bf16 v[84:87], a[108:111], a[92:95], v[84:87]\n"
"ds_read_b64_tr_b16 a[66:67], v14 offset:768\n"
"v_mfma_f32_16x16x32_bf16 v[52:55], a[104:107], a[92:95], v[52:55]\n"
"v_mfma_f32_16x16x32_bf16 v[56:59], a[104:107], a[96:99], v[56:59]\n"
"buffer_load_dwordx4 v3, s[28:31], s38 offen offset:2048 lds\n"
"v_mfma_f32_16x16x32_bf16 v[88:91], a[108:111], a[96:99], v[88:91]\n"
"ds_read_b64_tr_b16 a[68:69], v15 offset:0\n"
"v_mfma_f32_16x16x32_bf16 v[120:123], a[96:99], a[112:115], v[120:123]\n"
"v_mfma_f32_16x16x32_bf16 v[152:155], a[96:99], a[116:119], v[152:155]\n"
"ds_read_b64_tr_b16 a[70:71], v15 offset:768\n"
"v_mfma_f32_16x16x32_bf16 v[184:187], a[120:123], a[96:99], v[184:187]\n"
"v_mfma_f32_16x16x32_bf16 v[216:219], a[124:127], a[96:99], v[216:219]\n"
"v_mfma_f32_16x16x32_bf16 v[248:251], a[128:131], a[96:99], v[248:251]\n"
"buffer_load_dwordx4 v4, s[28:31], s38 offen offset:3072 lds\n"
"v_mfma_f32_16x16x32_bf16 a[168:171], a[132:135], a[96:99], a[168:171]\n"
"v_mfma_f32_16x16x32_bf16 a[200:203], a[136:139], a[96:99], a[200:203]\n"
"v_mfma_f32_16x16x32_bf16 a[232:235], a[140:143], a[96:99], a[232:235]\n"
"v_mfma_f32_16x16x32_bf16 a[236:239], a[140:143], a[100:103], a[236:239]\n"
"v_mfma_f32_16x16x32_bf16 a[204:207], a[136:139], a[100:103], a[204:207]\n"
"s_sub_u32 s41, s41, 1\n"
"s_add_u32 m0, s40, 65536\n"
"v_mfma_f32_16x16x32_bf16 a[172:175], a[132:135], a[100:103], a[172:175]\n"
"buffer_load_dwordx4 v5, s[28:31], s38 offen offset:0 lds\n"
"s_add_u32 s38, s38, 229376\n"
"v_mfma_f32_16x16x32_bf16 v[252:255], a[128:131], a[100:103], v[252:255]\n"
"s_cmp_eq_u32 s2, s41\n"
"v_mfma_f32_16x16x32_bf16 v[220:223], a[124:127], a[100:103], v[220:223]\n"
"s_cselect_b32 s96, 6291456, 0\n"
"v_mfma_f32_16x16x32_bf16 v[188:191], a[120:123], a[100:103], v[188:191]\n"
"s_cselect_b32 s97, 7340032, 0\n"
"v_mfma_f32_16x16x32_bf16 v[156:159], a[100:103], a[116:119], v[156:159]\n"
"s_cselect_b32 s16, 2048, 0\n"
"v_mfma_f32_16x16x32_bf16 v[124:127], a[100:103], a[112:115], v[124:127]\n"
"s_sub_u32 s37, s37, s96\n"
"v_mfma_f32_16x16x32_bf16 v[92:95], a[108:111], a[100:103], v[92:95]\n"
"s_sub_u32 s38, s38, s97\n"
"v_mfma_f32_16x16x32_bf16 v[60:63], a[104:107], a[100:103], v[60:63]\n"
"s_sub_u32 s36, s36, s16\n"
"s_bitcmp0_b32 s0, s41\n"
"s_cbranch_scc1 G1KV1_%=\n"
"s_cmp_lg_u32 s41, 0\n"
"s_cbranch_scc1 SDKV2_%=\n"
"s_waitcnt vmcnt(18) lgkmcnt(0)\n"
"s_barrier\n"
"v_mfma_f32_16x16x32_bf16 v[32:35], a[32:35], a[0:3], v[32:35]\n"
"ds_read_b128 a[72:75], v7 offset:0\n"
"buffer_load_dwordx4 v[24:27], v0, s[20:23], s65 offen sc1\n"
"v_mfma_f32_16x16x32_bf16 v[64:67], a[36:39], a[0:3], v[64:67]\n"
"buffer_load_dwordx4 v[20:23], v0, s[20:23], s5 offen sc1\n"
"v_mfma_f32_16x16x32_bf16 v[96:99], a[0:3], a[40:43], v[96:99]\n"
"ds_read_b64_tr_b16 a[104:105], v8 offset:20480\n"
"v_mfma_f32_16x16x32_bf16 v[128:131], a[0:3], a[44:47], v[128:131]\n"
"v_mfma_f32_16x16x32_bf16 v[160:163], a[48:51], a[0:3], v[160:163]\n"
"ds_read_b64_tr_b16 a[106:107], v8 offset:20992\n"
"v_mfma_f32_16x16x32_bf16 v[192:195], a[52:55], a[0:3], v[192:195]\n"
"v_mfma_f32_16x16x32_bf16 v[224:227], a[56:59], a[0:3], v[224:227]\n"
"v_mfma_f32_16x16x32_bf16 a[144:147], a[60:63], a[0:3], a[144:147]\n"
"ds_read_b128 a[76:79], v7 offset:2048\n"
"v_mfma_f32_16x16x32_bf16 a[176:179], a[64:67], a[0:3], a[176:179]\n"
"v_mfma_f32_16x16x32_bf16 a[208:211], a[68:71], a[0:3], a[208:211]\n"
"ds_read_b64_tr_b16 a[108:109], v9 offset:20480\n"
"v_mfma_f32_16x16x32_bf16 a[212:215], a[68:71], a[4:7], a[212:215]\n"
"v_mfma_f32_16x16x32_bf16 a[180:183], a[64:67], a[4:7], a[180:183]\n"
"ds_read_b64_tr_b16 a[110:111], v9 offset:20992\n"
"v_mfma_f32_16x16x32_bf16 a[148:151], a[60:63], a[4:7], a[148:151]\n"
"v_mfma_f32_16x16x32_bf16 v[228:231], a[56:59], a[4:7], v[228:231]\n"
"v_mfma_f32_16x16x32_bf16 v[196:199], a[52:55], a[4:7], v[196:199]\n"
"ds_read_b64_tr_b16 a[112:113], v8 offset:21504\n"
"v_mfma_f32_16x16x32_bf16 v[164:167], a[48:51], a[4:7], v[164:167]\n"
"v_mfma_f32_16x16x32_bf16 v[132:135], a[4:7], a[44:47], v[132:135]\n"
"ds_read_b128 a[80:83], v7 offset:4096\n"
"v_mfma_f32_16x16x32_bf16 v[100:103], a[4:7], a[40:43], v[100:103]\n"
"v_mfma_f32_16x16x32_bf16 v[68:71], a[36:39], a[4:7], v[68:71]\n"
"ds_read_b64_tr_b16 a[114:115], v8 offset:22016\n"
"v_mfma_f32_16x16x32_bf16 v[36:39], a[32:35], a[4:7], v[36:39]\n"
"v_mfma_f32_16x16x32_bf16 v[40:43], a[32:35], a[8:11], v[40:43]\n"
"v_mfma_f32_16x16x32_bf16 v[72:75], a[36:39], a[8:11], v[72:75]\n"
"ds_read_b64_tr_b16 a[116:117], v9 offset:21504\n"
"v_mfma_f32_16x16x32_bf16 v[104:107], a[8:11], a[40:43], v[104:107]\n"
"v_mfma_f32_16x16x32_bf16 v[136:139], a[8:11], a[44:47], v[136:139]\n"
"ds_read_b128 a[84:87], v7 offset:6144\n"
"v_mfma_f32_16x16x32_bf16 v[168:171], a[48:51], a[8:11], v[168:171]\n"
"v_mfma_f32_16x16x32_bf16 v[200:203], a[52:55], a[8:11], v[200:203]\n"
"ds_read_b64_tr_b16 a[118:119], v9 offset:22016\n"
"v_mfma_f32_16x16x32_bf16 v[232:235], a[56:59], a[8:11], v[232:235]\n"
"v_mfma_f32_16x16x32_bf16 a[152:155], a[60:63], a[8:11], a[152:155]\n"
"v_mfma_f32_16x16x32_bf16 a[184:187], a[64:67], a[8:11], a[184:187]\n"
"ds_read_b64_tr_b16 a[120:121], v10 offset:20480\n"
"v_mfma_f32_16x16x32_bf16 a[216:219], a[68:71], a[8:11], a[216:219]\n"
"v_mfma_f32_16x16x32_bf16 a[220:223], a[68:71], a[12:15], a[220:223]\n"
"ds_read_b64_tr_b16 a[122:123], v10 offset:21248\n"
"v_lshrrev_b32 v30, 2, v0\n"
"v_mfma_f32_16x16x32_bf16 a[188:191], a[64:67], a[12:15], a[188:191]\n"
"v_mfma_f32_16x16x32_bf16 a[156:159], a[60:63], a[12:15], a[156:159]\n"
"v_mfma_f32_16x16x32_bf16 v[236:239], a[56:59], a[12:15], v[236:239]\n"
"ds_read_b128 a[88:91], v7 offset:8192\n"
"v_mfma_f32_16x16x32_bf16 v[204:207], a[52:55], a[12:15], v[204:207]\n"
"v_mfma_f32_16x16x32_bf16 v[172:175], a[48:51], a[12:15], v[172:175]\n"
"ds_read_b64_tr_b16 a[124:125], v11 offset:20480\n"
"v_mfma_f32_16x16x32_bf16 v[140:143], a[12:15], a[44:47], v[140:143]\n"
"v_mfma_f32_16x16x32_bf16 v[108:111], a[12:15], a[40:43], v[108:111]\n"
"ds_read_b64_tr_b16 a[126:127], v11 offset:21248\n"
"v_mfma_f32_16x16x32_bf16 v[76:79], a[36:39], a[12:15], v[76:79]\n"
"v_mfma_f32_16x16x32_bf16 v[44:47], a[32:35], a[12:15], v[44:47]\n"
"v_mfma_f32_16x16x32_bf16 v[48:51], a[32:35], a[16:19], v[48:51]\n"
"ds_read_b128 a[92:95], v7 offset:10240\n"
"v_mfma_f32_16x16x32_bf16 v[80:83], a[36:39], a[16:19], v[80:83]\n"
"v_mfma_f32_16x16x32_bf16 v[112:115], a[16:19], a[40:43], v[112:115]\n"
"ds_read_b64_tr_b16 a[128:129], v12 offset:20480\n"
"v_mfma_f32_16x16x32_bf16 v[144:147], a[16:19], a[44:47], v[144:147]\n"
"v_mfma_f32_16x16x32_bf16 v[176:179], a[48:51], a[16:19], v[176:179]\n"
"ds_read_b64_tr_b16 a[130:131], v12 offset:21248\n"
"v_mfma_f32_16x16x32_bf16 v[208:211], a[52:55], a[16:19], v[208:211]\n"
"v_mfma_f32_16x16x32_bf16 v[240:243], a[56:59], a[16:19], v[240:243]\n"
"v_mfma_f32_16x16x32_bf16 a[160:163], a[60:63], a[16:19], a[160:163]\n"
"ds_read_b64_tr_b16 a[132:133], v13 offset:20480\n"
"v_mfma_f32_16x16x32_bf16 a[192:195], a[64:67], a[16:19], a[192:195]\n"
"v_mfma_f32_16x16x32_bf16 a[224:227], a[68:71], a[16:19], a[224:227]\n"
"ds_read_b128 a[96:99], v7 offset:12288\n"
"v_mfma_f32_16x16x32_bf16 a[228:231], a[68:71], a[20:23], a[228:231]\n"
"v_mfma_f32_16x16x32_bf16 a[196:199], a[64:67], a[20:23], a[196:199]\n"
"ds_read_b64_tr_b16 a[134:135], v13 offset:21248\n"
"v_mfma_f32_16x16x32_bf16 a[164:167], a[60:63], a[20:23], a[164:167]\n"
"v_mfma_f32_16x16x32_bf16 v[244:247], a[56:59], a[20:23], v[244:247]\n"
"v_mfma_f32_16x16x32_bf16 v[212:215], a[52:55], a[20:23], v[212:215]\n"
"ds_read_b64_tr_b16 a[136:137], v14 offset:20480\n"
"v_mfma_f32_16x16x32_bf16 v[180:183], a[48:51], a[20:23], v[180:183]\n"
"v_mfma_f32_16x16x32_bf16 v[148:151], a[20:23], a[44:47], v[148:151]\n"
"ds_read_b128 a[100:103], v7 offset:14336\n"
"v_mfma_f32_16x16x32_bf16 v[116:119], a[20:23], a[40:43], v[116:119]\n"
"v_mfma_f32_16x16x32_bf16 v[84:87], a[36:39], a[20:23], v[84:87]\n"
"ds_read_b64_tr_b16 a[138:139], v14 offset:21248\n"
"v_mfma_f32_16x16x32_bf16 v[52:55], a[32:35], a[20:23], v[52:55]\n"
"v_mfma_f32_16x16x32_bf16 v[56:59], a[32:35], a[24:27], v[56:59]\n"
"v_mfma_f32_16x16x32_bf16 v[88:91], a[36:39], a[24:27], v[88:91]\n"
"ds_read_b64_tr_b16 a[140:141], v15 offset:20480\n"
"v_mfma_f32_16x16x32_bf16 v[120:123], a[24:27], a[40:43], v[120:123]\n"
"v_mfma_f32_16x16x32_bf16 v[152:155], a[24:27], a[44:47], v[152:155]\n"
"ds_read_b64_tr_b16 a[142:143], v15 offset:21248\n"
"v_mfma_f32_16x16x32_bf16 v[184:187], a[48:51], a[24:27], v[184:187]\n"
"v_mfma_f32_16x16x32_bf16 v[216:219], a[52:55], a[24:27], v[216:219]\n"
"v_mfma_f32_16x16x32_bf16 v[248:251], a[56:59], a[24:27], v[248:251]\n"
"v_mfma_f32_16x16x32_bf16 a[168:171], a[60:63], a[24:27], a[168:171]\n"
"v_mfma_f32_16x16x32_bf16 a[200:203], a[64:67], a[24:27], a[200:203]\n"
"v_mfma_f32_16x16x32_bf16 a[232:235], a[68:71], a[24:27], a[232:235]\n"
"v_mfma_f32_16x16x32_bf16 a[236:239], a[68:71], a[28:31], a[236:239]\n"
"v_mfma_f32_16x16x32_bf16 a[204:207], a[64:67], a[28:31], a[204:207]\n"
"v_mfma_f32_16x16x32_bf16 a[172:175], a[60:63], a[28:31], a[172:175]\n"
"v_mfma_f32_16x16x32_bf16 v[252:255], a[56:59], a[28:31], v[252:255]\n"
"v_mfma_f32_16x16x32_bf16 v[220:223], a[52:55], a[28:31], v[220:223]\n"
"v_mfma_f32_16x16x32_bf16 v[188:191], a[48:51], a[28:31], v[188:191]\n"
"v_mfma_f32_16x16x32_bf16 v[156:159], a[28:31], a[44:47], v[156:159]\n"
"v_mfma_f32_16x16x32_bf16 v[124:127], a[28:31], a[40:43], v[124:127]\n"
"v_mfma_f32_16x16x32_bf16 v[92:95], a[36:39], a[28:31], v[92:95]\n"
"v_mfma_f32_16x16x32_bf16 v[60:63], a[32:35], a[28:31], v[60:63]\n"
"s_waitcnt vmcnt(7) lgkmcnt(0)\n"
"s_barrier\n"
"v_mfma_f32_16x16x32_bf16 v[32:35], a[104:107], a[72:75], v[32:35]\n"
"ds_read_b128 a[0:3], v6 offset:32768\n"
"v_mfma_f32_16x16x32_bf16 v[64:67], a[108:111], a[72:75], v[64:67]\n"
"v_mfma_f32_16x16x32_bf16 v[96:99], a[72:75], a[112:115], v[96:99]\n"
"ds_read_b64_tr_b16 a[32:33], v8 offset:40960\n"
"v_mfma_f32_16x16x32_bf16 v[128:131], a[72:75], a[116:119], v[128:131]\n"
"v_mfma_f32_16x16x32_bf16 v[160:163], a[120:123], a[72:75], v[160:163]\n"
"ds_read_b64_tr_b16 a[34:35], v8 offset:41472\n"
"s_waitcnt vmcnt(1)\n"
"v_cmp_ne_u64 vcc, s[90:91], v[24:25]\n"
"v_cmp_ne_u64_e64 s[96:97], s[90:91], v[26:27]\n"
"s_nop 0\n"
"s_or_b64 vcc, vcc, s[96:97]\n"
"s_cbranch_vccnz SPKV5_%=\n"
"BKKV6_%=:\n"
"v_mfma_f32_16x16x32_bf16 v[192:195], a[124:127], a[72:75], v[192:195]\n"
"v_mfma_f32_16x16x32_bf16 v[224:227], a[128:131], a[72:75], v[224:227]\n"
"v_mfma_f32_16x16x32_bf16 a[144:147], a[132:135], a[72:75], a[144:147]\n"
"ds_read_b128 a[4:7], v6 offset:34816\n"
"v_mfma_f32_16x16x32_bf16 a[176:179], a[136:139], a[72:75], a[176:179]\n"
"s_waitcnt vmcnt(0)\n"
"v_cmp_ne_u64 vcc, s[90:91], v[20:21]\n"
"v_cmp_ne_u64_e64 s[96:97], s[90:91], v[22:23]\n"
"s_nop 0\n"
"s_or_b64 vcc, vcc, s[96:97]\n"
"s_cbranch_vccnz SPKV8_%=\n"
"BKKV9_%=:\n"
"v_mfma_f32_16x16x32_bf16 a[208:211], a[140:143], a[72:75], a[208:211]\n"
"ds_read_b64_tr_b16 a[36:37], v9 offset:40960\n"
"v_mfma_f32_16x16x32_bf16 a[212:215], a[140:143], a[76:79], a[212:215]\n"
"v_mfma_f32_16x16x32_bf16 a[180:183], a[136:139], a[76:79], a[180:183]\n"
"ds_read_b64_tr_b16 a[38:39], v9 offset:41472\n"
"s_add_u32 m0, s11, 0\n"
"v_mfma_f32_16x16x32_bf16 a[148:151], a[132:135], a[76:79], a[148:151]\n"
"buffer_load_dwordx4 v0, s[20:23], s6 offen offset:0 sc1 lds\n"
"v_mfma_f32_16x16x32_bf16 v[228:231], a[128:131], a[76:79], v[228:231]\n"
"v_mfma_f32_16x16x32_bf16 v[196:199], a[124:127], a[76:79], v[196:199]\n"
"ds_read_b64_tr_b16 a[40:41], v8 offset:41984\n"
"v_mfma_f32_16x16x32_bf16 v[164:167], a[120:123], a[76:79], v[164:167]\n"
"v_mfma_f32_16x16x32_bf16 v[132:135], a[76:79], a[116:119], v[132:135]\n"
"ds_read_b128 a[8:11], v6 offset:36864\n"
"buffer_load_dwordx4 v0, s[20:23], s6 offen offset:1024 sc1 lds\n"
"v_mfma_f32_16x16x32_bf16 v[100:103], a[76:79], a[112:115], v[100:103]\n"
"v_mfma_f32_16x16x32_bf16 v[68:71], a[108:111], a[76:79], v[68:71]\n"
"ds_read_b64_tr_b16 a[42:43], v8 offset:42496\n"
"v_mfma_f32_16x16x32_bf16 v[36:39], a[104:107], a[76:79], v[36:39]\n"
"v_mfma_f32_16x16x32_bf16 v[40:43], a[104:107], a[80:83], v[40:43]\n"
"v_mfma_f32_16x16x32_bf16 v[72:75], a[108:111], a[80:83], v[72:75]\n"
"ds_read_b64_tr_b16 a[44:45], v9 offset:41984\n"
"buffer_load_dwordx4 v0, s[20:23], s6 offen offset:2048 sc1 lds\n"
"v_mfma_f32_16x16x32_bf16 v[104:107], a[80:83], a[112:115], v[104:107]\n"
"v_mfma_f32_16x16x32_bf16 v[136:139], a[80:83], a[116:119], v[136:139]\n"
"ds_read_b128 a[12:15], v6 offset:38912\n"
"v_mfma_f32_16x16x32_bf16 v[168:171], a[120:123], a[80:83], v[168:171]\n"
"v_mfma_f32_16x16x32_bf16 v[200:203], a[124:127], a[80:83], v[200:203]\n"
"ds_read_b64_tr_b16 a[46:47], v9 offset:42496\n"
"v_mfma_f32_16x16x32_bf16 v[232:235], a[128:131], a[80:83], v[232:235]\n"
"buffer_load_dwordx4 v0, s[20:23], s6 offen offset:3072 sc1 lds\n"
"s_add_u32 s6, s6, 4096\n"
"v_mfma_f32_16x16x32_bf16 a[152:155], a[132:135], a[80:83], a[152:155]\n"
"v_mfma_f32_16x16x32_bf16 a[184:187], a[136:139], a[80:83], a[184:187]\n"
"ds_read_b64_tr_b16 a[48:49], v10 offset:40960\n"
"v_mfma_f32_16x16x32_bf16 a[216:219], a[140:143], a[80:83], a[216:219]\n"
"v_mfma_f32_16x16x32_bf16 a[220:223], a[140:143], a[84:87], a[220:223]\n"
"ds_read_b64_tr_b16 a[50:51], v10 offset:41728\n"
"s_add_u32 m0, s11, 4096\n"
"v_mfma_f32_16x16x32_bf16 a[188:191], a[136:139], a[84:87], a[188:191]\n"
"buffer_load_dwordx4 v0, s[20:23], s6 offen offset:0 sc1 lds\n"
"v_mfma_f32_16x16x32_bf16 a[156:159], a[132:135], a[84:87], a[156:159]\n"
"v_mfma_f32_16x16x32_bf16 v[236:239], a[128:131], a[84:87], v[236:239]\n"
"ds_read_b128 a[16:19], v6 offset:40960\n"
"v_mfma_f32_16x16x32_bf16 v[204:207], a[124:127], a[84:87], v[204:207]\n"
"s_add_u32 m0, s12, 0\n"
"v_mfma_f32_16x16x32_bf16 v[172:175], a[120:123], a[84:87], v[172:175]\n"
"ds_read_b64_tr_b16 a[52:53], v11 offset:40960\n"
"buffer_load_dwordx4 v0, s[20:23], s7 offen offset:0 sc1 lds\n"
"v_mfma_f32_16x16x32_bf16 v[140:143], a[84:87], a[116:119], v[140:143]\n"
"v_mfma_f32_16x16x32_bf16 v[108:111], a[84:87], a[112:115], v[108:111]\n"
"ds_read_b64_tr_b16 a[54:55], v11 offset:41728\n"
"v_mfma_f32_16x16x32_bf16 v[76:79], a[108:111], a[84:87], v[76:79]\n"
"v_mfma_f32_16x16x32_bf16 v[44:47], a[104:107], a[84:87], v[44:47]\n"
"v_mfma_f32_16x16x32_bf16 v[48:51], a[104:107], a[88:91], v[48:51]\n"
"ds_read_b128 a[20:23], v6 offset:43008\n"
"buffer_load_dwordx4 v0, s[20:23], s7 offen offset:1024 sc1 lds\n"
"v_mfma_f32_16x16x32_bf16 v[80:83], a[108:111], a[88:91], v[80:83]\n"
"v_mfma_f32_16x16x32_bf16 v[112:115], a[88:91], a[112:115], v[112:115]\n"
"ds_read_b64_tr_b16 a[56:57], v12 offset:40960\n"
"v_mfma_f32_16x16x32_bf16 v[144:147], a[88:91], a[116:119], v[144:147]\n"
"v_mfma_f32_16x16x32_bf16 v[176:179], a[120:123], a[88:91], v[176:179]\n"
"ds_read_b64_tr_b16 a[58:59], v12 offset:41728\n"
"s_add_u32 m0, s13, 0\n"
"v_mfma_f32_16x16x32_bf16 v[208:211], a[124:127], a[88:91], v[208:211]\n"
"buffer_load_dword v30, s[20:23], s8 offen sc1 lds\n"
"v_mfma_f32_16x16x32_bf16 v[240:243], a[128:131], a[88:91], v[240:243]\n"
"v_mfma_f32_16x16x32_bf16 a[160:163], a[132:135], a[88:91], a[160:163]\n"
"ds_read_b64_tr_b16 a[60:61], v13 offset:40960\n"
"v_mfma_f32_16x16x32_bf16 a[192:195], a[136:139], a[88:91], a[192:195]\n"
"v_mfma_f32_16x16x32_bf16 a[224:227], a[140:143], a[88:91], a[224:227]\n"
"ds_read_b128 a[24:27], v6 offset:45056\n"
"v_mfma_f32_16x16x32_bf16 a[228:231], a[140:143], a[92:95], a[228:231]\n"
"v_mfma_f32_16x16x32_bf16 a[196:199], a[136:139], a[92:95], a[196:199]\n"
"ds_read_b64_tr_b16 a[62:63], v13 offset:41728\n"
"v_mfma_f32_16x16x32_bf16 a[164:167], a[132:135], a[92:95], a[164:167]\n"
"v_mfma_f32_16x16x32_bf16 v[244:247], a[128:131], a[92:95], v[244:247]\n"
"v_mfma_f32_16x16x32_bf16 v[212:215], a[124:127], a[92:95], v[212:215]\n"
"ds_read_b64_tr_b16 a[64:65], v14 offset:40960\n"
"v_mfma_f32_16x16x32_bf16 v[180:183], a[120:123], a[92:95], v[180:183]\n"
"v_mfma_f32_16x16x32_bf16 v[148:151], a[92:95], a[116:119], v[148:151]\n"
"ds_read_b128 a[28:31], v6 offset:47104\n"
"v_mfma_f32_16x16x32_bf16 v[116:119], a[92:95], a[112:115], v[116:119]\n"
"v_mfma_f32_16x16x32_bf16 v[84:87], a[108:111], a[92:95], v[84:87]\n"
"ds_read_b64_tr_b16 a[66:67], v14 offset:41728\n"
"v_mfma_f32_16x16x32_bf16 v[52:55], a[104:107], a[92:95], v[52:55]\n"
"v_mfma_f32_16x16x32_bf16 v[56:59], a[104:107], a[96:99], v[56:59]\n"
"v_mfma_f32_16x16x32_bf16 v[88:91], a[108:111], a[96:99], v[88:91]\n"
"ds_read_b64_tr_b16 a[68:69], v15 offset:40960\n"
"v_mfma_f32_16x16x32_bf16 v[120:123], a[96:99], a[112:115], v[120:123]\n"
"v_mfma_f32_16x16x32_bf16 v[152:155], a[96:99], a[116:119], v[152:155]\n"
"ds_read_b64_tr_b16 a[70:71], v15 offset:41728\n"
"v_mfma_f32_16x16x32_bf16 v[184:187], a[120:123], a[96:99], v[184:187]\n"
"v_mfma_f32_16x16x32_bf16 v[216:219], a[124:127], a[96:99], v[216:219]\n"
"v_mfma_f32_16x16x32_bf16 v[248:251], a[128:131], a[96:99], v[248:251]\n"
"v_mfma_f32_16x16x32_bf16 a[168:171], a[132:135], a[96:99], a[168:171]\n"
"v_mfma_f32_16x16x32_bf16 a[200:203], a[136:139], a[96:99], a[200:203]\n"
"v_mfma_f32_16x16x32_bf16 a[232:235], a[140:143], a[96:99], a[232:235]\n"
"v_mfma_f32_16x16x32_bf16 a[236:239], a[140:143], a[100:103], a[236:239]\n"
"v_mfma_f32_16x16x32_bf16 a[204:207], a[136:139], a[100:103], a[204:207]\n"
"v_mfma_f32_16x16x32_bf16 a[172:175], a[132:135], a[100:103], a[172:175]\n"
"v_mfma_f32_16x16x32_bf16 v[252:255], a[128:131], a[100:103], v[252:255]\n"
"v_mfma_f32_16x16x32_bf16 v[220:223], a[124:127], a[100:103], v[220:223]\n"
"v_mfma_f32_16x16x32_bf16 v[188:191], a[120:123], a[100:103], v[188:191]\n"
"v_mfma_f32_16x16x32_bf16 v[156:159], a[100:103], a[116:119], v[156:159]\n"
"v_mfma_f32_16x16x32_bf16 v[124:127], a[100:103], a[112:115], v[124:127]\n"
"v_mfma_f32_16x16x32_bf16 v[92:95], a[108:111], a[100:103], v[92:95]\n"
"v_mfma_f32_16x16x32_bf16 v[60:63], a[104:107], a[100:103], v[60:63]\n"
"s_waitcnt vmcnt(10) lgkmcnt(0)\n"
"s_barrier\n"
"v_mfma_f32_16x16x32_bf16 v[32:35], a[32:35], a[0:3], v[32:35]\n"
"ds_read_b128 a[72:75], v7 offset:32768\n"
"v_mfma_f32_16x16x32_bf16 v[64:67], a[36:39], a[0:3], v[64:67]\n"
"v_mfma_f32_16x16x32_bf16 v[96:99], a[0:3], a[40:43], v[96:99]\n"
"ds_read_b64_tr_b16 a[104:105], v8 offset:61440\n"
"v_mfma_f32_16x16x32_bf16 v[128:131], a[0:3], a[44:47], v[128:131]\n"
"v_mfma_f32_16x16x32_bf16 v[160:163], a[48:51], a[0:3], v[160:163]\n"
"ds_read_b64_tr_b16 a[106:107], v8 offset:61952\n"
"v_mfma_f32_16x16x32_bf16 v[192:195], a[52:55], a[0:3], v[192:195]\n"
"v_mfma_f32_16x16x32_bf16 v[224:227], a[56:59], a[0:3], v[224:227]\n"
"v_mfma_f32_16x16x32_bf16 a[144:147], a[60:63], a[0:3], a[144:147]\n"
"ds_read_b128 a[76:79], v7 offset:34816\n"
"v_mfma_f32_16x16x32_bf16 a[176:179], a[64:67], a[0:3], a[176:179]\n"
"v_mfma_f32_16x16x32_bf16 a[208:211], a[68:71], a[0:3], a[208:211]\n"
"ds_read_b64_tr_b16 a[108:109], v9 offset:61440\n"
"v_mfma_f32_16x16x32_bf16 a[212:215], a[68:71], a[4:7], a[212:215]\n"
"v_mfma_f32_16x16x32_bf16 a[180:183], a[64:67], a[4:7], a[180:183]\n"
"ds_read_b64_tr_b16 a[110:111], v9 offset:61952\n"
"v_mfma_f32_16x16x32_bf16 a[148:151], a[60:63], a[4:7], a[148:151]\n"
"v_mfma_f32_16x16x32_bf16 v[228:231], a[56:59], a[4:7], v[228:231]\n"
"v_mfma_f32_16x16x32_bf16 v[196:199], a[52:55], a[4:7], v[196:199]\n"
"ds_read_b64_tr_b16 a[112:113], v8 offset:62464\n"
"v_mfma_f32_16x16x32_bf16 v[164:167], a[48:51], a[4:7], v[164:167]\n"
"v_mfma_f32_16x16x32_bf16 v[132:135], a[4:7], a[44:47], v[132:135]\n"
"ds_read_b128 a[80:83], v7 offset:36864\n"
"v_mfma_f32_16x16x32_bf16 v[100:103], a[4:7], a[40:43], v[100:103]\n"
"v_mfma_f32_16x16x32_bf16 v[68:71], a[36:39], a[4:7], v[68:71]\n"
"ds_read_b64_tr_b16 a[114:115], v8 offset:62976\n"
"v_mfma_f32_16x16x32_bf16 v[36:39], a[32:35], a[4:7], v[36:39]\n"
"v_mfma_f32_16x16x32_bf16 v[40:43], a[32:35], a[8:11], v[40:43]\n"
"v_mfma_f32_16x16x32_bf16 v[72:75], a[36:39], a[8:11], v[72:75]\n"
"ds_read_b64_tr_b16 a[116:117], v9 offset:62464\n"
"v_mfma_f32_16x16x32_bf16 v[104:107], a[8:11], a[40:43], v[104:107]\n"
"v_mfma_f32_16x16x32_bf16 v[136:139], a[8:11], a[44:47], v[136:139]\n"
"ds_read_b128 a[84:87], v7 offset:38912\n"
"v_mfma_f32_16x16x32_bf16 v[168:171], a[48:51], a[8:11], v[168:171]\n"
"v_mfma_f32_16x16x32_bf16 v[200:203], a[52:55], a[8:11], v[200:203]\n"
"ds_read_b64_tr_b16 a[118:119], v9 offset:62976\n"
"v_mfma_f32_16x16x32_bf16 v[232:235], a[56:59], a[8:11], v[232:235]\n"
"v_mfma_f32_16x16x32_bf16 a[152:155], a[60:63], a[8:11], a[152:155]\n"
"v_mfma_f32_16x16x32_bf16 a[184:187], a[64:67], a[8:11], a[184:187]\n"
"ds_read_b64_tr_b16 a[120:121], v10 offset:61440\n"
"v_mfma_f32_16x16x32_bf16 a[216:219], a[68:71], a[8:11], a[216:219]\n"
"v_mfma_f32_16x16x32_bf16 a[220:223], a[68:71], a[12:15], a[220:223]\n"
"ds_read_b64_tr_b16 a[122:123], v10 offset:62208\n"
"v_mfma_f32_16x16x32_bf16 a[188:191], a[64:67], a[12:15], a[188:191]\n"
"v_mfma_f32_16x16x32_bf16 a[156:159], a[60:63], a[12:15], a[156:159]\n"
"v_mfma_f32_16x16x32_bf16 v[236:239], a[56:59], a[12:15], v[236:239]\n"
"ds_read_b128 a[88:91], v7 offset:40960\n"
"v_mfma_f32_16x16x32_bf16 v[204:207], a[52:55], a[12:15], v[204:207]\n"
"v_mfma_f32_16x16x32_bf16 v[172:175], a[48:51], a[12:15], v[172:175]\n"
"ds_read_b64_tr_b16 a[124:125], v11 offset:61440\n"
"v_mfma_f32_16x16x32_bf16 v[140:143], a[12:15], a[44:47], v[140:143]\n"
"v_mfma_f32_16x16x32_bf16 v[108:111], a[12:15], a[40:43], v[108:111]\n"
"ds_read_b64_tr_b16 a[126:127], v11 offset:62208\n"
"v_mfma_f32_16x16x32_bf16 v[76:79], a[36:39], a[12:15], v[76:79]\n"
"v_mfma_f32_16x16x32_bf16 v[44:47], a[32:35], a[12:15], v[44:47]\n"
"v_mfma_f32_16x16x32_bf16 v[48:51], a[32:35], a[16:19], v[48:51]\n"
"ds_read_b128 a[92:95], v7 offset:43008\n"
"v_accvgpr_read_b32 v20, a241\n"
"v_mfma_f32_16x16x32_bf16 v[80:83], a[36:39], a[16:19], v[80:83]\n"
"v_mfma_f32_16x16x32_bf16 v[112:115], a[16:19], a[40:43], v[112:115]\n"
"ds_read_b64_tr_b16 a[128:129], v12 offset:61440\n"
"v_accvgpr_read_b32 v21, a242\n"
"v_mfma_f32_16x16x32_bf16 v[144:147], a[16:19], a[44:47], v[144:147]\n"
"v_mfma_f32_16x16x32_bf16 v[176:179], a[48:51], a[16:19], v[176:179]\n"
"ds_read_b64_tr_b16 a[130:131], v12 offset:62208\n"
"v_accvgpr_read_b32 v22, a243\n"
"v_mfma_f32_16x16x32_bf16 v[208:211], a[52:55], a[16:19], v[208:211]\n"
"v_mfma_f32_16x16x32_bf16 v[240:243], a[56:59], a[16:19], v[240:243]\n"
"v_accvgpr_read_b32 v23, a244\n"
"v_mfma_f32_16x16x32_bf16 a[160:163], a[60:63], a[16:19], a[160:163]\n"
"ds_read_b64_tr_b16 a[132:133], v13 offset:61440\n"
"v_mfma_f32_16x16x32_bf16 a[192:195], a[64:67], a[16:19], a[192:195]\n"
"v_accvgpr_read_b32 v24, a245\n"
"v_mfma_f32_16x16x32_bf16 a[224:227], a[68:71], a[16:19], a[224:227]\n"
"ds_read_b128 a[96:99], v7 offset:45056\n"
"v_mfma_f32_16x16x32_bf16 a[228:231], a[68:71], a[20:23], a[228:231]\n"
"v_accvgpr_read_b32 v25, a250\n"
"v_mfma_f32_16x16x32_bf16 a[196:199], a[64:67], a[20:23], a[196:199]\n"
"ds_read_b64_tr_b16 a[134:135], v13 offset:62208\n"
"v_mfma_f32_16x16x32_bf16 a[164:167], a[60:63], a[20:23], a[164:167]\n"
"v_accvgpr_read_b32 v27, a251\n"
"v_mfma_f32_16x16x32_bf16 v[244:247], a[56:59], a[20:23], v[244:247]\n"
"v_mfma_f32_16x16x32_bf16 v[212:215], a[52:55], a[20:23], v[212:215]\n"
"ds_read_b64_tr_b16 a[136:137], v14 offset:61440\n"
"v_mfma_f32_16x16x32_bf16 v[180:183], a[48:51], a[20:23], v[180:183]\n"
"v_mfma_f32_16x16x32_bf16 v[148:151], a[20:23], a[44:47], v[148:151]\n"
"ds_read_b128 a[100:103], v7 offset:47104\n"
"v_add_u32 v26, 7168, v25\n"
"v_mfma_f32_16x16x32_bf16 v[116:119], a[20:23], a[40:43], v[116:119]\n"
"v_mfma_f32_16x16x32_bf16 v[84:87], a[36:39], a[20:23], v[84:87]\n"
"ds_read_b64_tr_b16 a[138:139], v14 offset:62208\n"
"v_mfma_f32_16x16x32_bf16 v[52:55], a[32:35], a[20:23], v[52:55]\n"
"v_mfma_f32_16x16x32_bf16 v[56:59], a[32:35], a[24:27], v[56:59]\n"
"v_mfma_f32_16x16x32_bf16 v[88:91], a[36:39], a[24:27], v[88:91]\n"
"ds_read_b64_tr_b16 a[140:141], v15 offset:61440\n"
"v_mfma_f32_16x16x32_bf16 v[120:123], a[24:27], a[40:43], v[120:123]\n"
"v_mfma_f32_16x16x32_bf16 v[152:155], a[24:27], a[44:47], v[152:155]\n"
"ds_read_b64_tr_b16 a[142:143], v15 offset:62208\n"
"v_mfma_f32_16x16x32_bf16 v[184:187], a[48:51], a[24:27], v[184:187]\n"
"v_mfma_f32_16x16x32_bf16 v[216:219], a[52:55], a[24:27], v[216:219]\n"
"v_mfma_f32_16x16x32_bf16 v[248:251], a[56:59], a[24:27], v[248:251]\n"
"v_add_u32 v29, s15, v0\n"
"s_lshl_b32 s94, s14, 12\n"
"s_lshl_b32 s95, s14, 9\n"
"v_mfma_f32_16x16x32_bf16 a[168:171], a[60:63], a[24:27], a[168:171]\n"
"v_and_b32 v30, 0xf0, v0\n"
"s_add_u32 s94, s94, 20480\n"
"s_add_u32 s95, s95, 28672\n"
"v_mfma_f32_16x16x32_bf16 a[200:203], a[64:67], a[24:27], a[200:203]\n"
"v_lshl_add_u32 v30, v30, 1, s94\n"
"v_mfma_f32_16x16x32_bf16 a[232:235], a[68:71], a[24:27], a[232:235]\n"
"v_lshrrev_b32 v1, 8, v0\n"
"v_mfma_f32_16x16x32_bf16 a[236:239], a[68:71], a[28:31], a[236:239]\n"
"v_lshl_add_u32 v1, v1, 4, s95\n"
"v_mfma_f32_16x16x32_bf16 a[204:207], a[64:67], a[28:31], a[204:207]\n"
"v_mfma_f32_16x16x32_bf16 a[172:175], a[60:63], a[28:31], a[172:175]\n"
"v_mfma_f32_16x16x32_bf16 v[252:255], a[56:59], a[28:31], v[252:255]\n"
"v_mfma_f32_16x16x32_bf16 v[220:223], a[52:55], a[28:31], v[220:223]\n"
"v_mfma_f32_16x16x32_bf16 v[188:191], a[48:51], a[28:31], v[188:191]\n"
"v_mfma_f32_16x16x32_bf16 v[156:159], a[28:31], a[44:47], v[156:159]\n"
"v_mfma_f32_16x16x32_bf16 v[124:127], a[28:31], a[40:43], v[124:127]\n"
"v_mfma_f32_16x16x32_bf16 v[92:95], a[36:39], a[28:31], v[92:95]\n"
"v_mfma_f32_16x16x32_bf16 v[60:63], a[32:35], a[28:31], v[60:63]\n"
"s_waitcnt vmcnt(0) lgkmcnt(0)\n"
"s_barrier\n"
"v_mfma_f32_16x16x32_bf16 v[32:35], a[104:107], a[72:75], v[32:35]\n"
"ds_read_b64_tr_b16 a[0:1], v16 offset:0\n"
"s_add_u32 m0, s77, 0\n"
"v_mfma_f32_16x16x32_bf16 v[64:67], a[108:111], a[72:75], v[64:67]\n"
"ds_read_b128 a[8:11], v29 offset:0\n"
"buffer_load_dwordx4 v25, s[52:55], s74 offen offset:0 lds\n"
"v_mfma_f32_16x16x32_bf16 v[96:99], a[72:75], a[112:115], v[96:99]\n"
"v_mfma_f32_16x16x32_bf16 v[128:131], a[72:75], a[116:119], v[128:131]\n"
"v_mfma_f32_16x16x32_bf16 v[160:163], a[120:123], a[72:75], v[160:163]\n"
"ds_read_b64_tr_b16 a[2:3], v16 offset:256\n"
"ds_read_b128 a[12:15], v29 offset:1024\n"
"v_mfma_f32_16x16x32_bf16 v[192:195], a[124:127], a[72:75], v[192:195]\n"
"buffer_load_dwordx4 v26, s[52:55], s74 offen offset:1024 lds\n"
"v_mfma_f32_16x16x32_bf16 v[224:227], a[128:131], a[72:75], v[224:227]\n"
"v_mfma_f32_16x16x32_bf16 a[144:147], a[132:135], a[72:75], a[144:147]\n"
"ds_read_b128 a[64:67], v29 offset:2048\n"
"v_mfma_f32_16x16x32_bf16 a[176:179], a[136:139], a[72:75], a[176:179]\n"
"ds_read_b64_tr_b16 a[4:5], v16 offset:32\n"
"v_mfma_f32_16x16x32_bf16 a[208:211], a[140:143], a[72:75], a[208:211]\n"
"v_mfma_f32_16x16x32_bf16 a[212:215], a[140:143], a[76:79], a[212:215]\n"
"ds_read_b128 a[68:71], v29 offset:3072\n"
"buffer_load_dwordx4 v27, s[52:55], s74 offen offset:2048 lds\n"
"v_mfma_f32_16x16x32_bf16 a[180:183], a[136:139], a[76:79], a[180:183]\n"
"v_mfma_f32_16x16x32_bf16 a[148:151], a[132:135], a[76:79], a[148:151]\n"
"ds_read_b64_tr_b16 a[6:7], v16 offset:288\n"
"v_mfma_f32_16x16x32_bf16 v[228:231], a[128:131], a[76:79], v[228:231]\n"
"ds_read_b128 a[16:19], v30 offset:0\n"
"v_mfma_f32_16x16x32_bf16 v[196:199], a[124:127], a[76:79], v[196:199]\n"
"v_mfma_f32_16x16x32_bf16 v[164:167], a[120:123], a[76:79], v[164:167]\n"
"buffer_load_dwordx4 v28, s[52:55], s74 offen offset:3072 lds\n"
"s_add_u32 s74, s74, 131072\n"
"v_mfma_f32_16x16x32_bf16 v[132:135], a[76:79], a[116:119], v[132:135]\n"
"ds_read_b128 a[20:23], v30 offset:512\n"
"v_mfma_f32_16x16x32_bf16 v[100:103], a[76:79], a[112:115], v[100:103]\n"
"v_mfma_f32_16x16x32_bf16 v[68:71], a[108:111], a[76:79], v[68:71]\n"
"v_mfma_f32_16x16x32_bf16 v[36:39], a[104:107], a[76:79], v[36:39]\n"
"ds_read_b128 a[24:27], v30 offset:1024\n"
"s_add_u32 m0, s77, 16384\n"
"v_mfma_f32_16x16x32_bf16 v[40:43], a[104:107], a[80:83], v[40:43]\n"
"v_and_b32 v2, 0xf0, v0\n"
"buffer_load_dwordx4 v25, s[52:55], s74 offen offset:0 lds\n"
"v_mfma_f32_16x16x32_bf16 v[72:75], a[108:111], a[80:83], v[72:75]\n"
"v_mfma_f32_16x16x32_bf16 v[104:107], a[80:83], a[112:115], v[104:107]\n"
"v_mov_b32 v4, 0x3f803f80\n"
"ds_read_b128 a[28:31], v30 offset:1536\n"
"v_mfma_f32_16x16x32_bf16 v[136:139], a[80:83], a[116:119], v[136:139]\n"
"v_mfma_f32_16x16x32_bf16 v[168:171], a[120:123], a[80:83], v[168:171]\n"
"v_cmp_eq_u32 vcc, 0, v2\n"
"v_mfma_f32_16x16x32_bf16 v[200:203], a[124:127], a[80:83], v[200:203]\n"
"buffer_load_dwordx4 v26, s[52:55], s74 offen offset:1024 lds\n"
"v_mfma_f32_16x16x32_bf16 v[232:235], a[128:131], a[80:83], v[232:235]\n"
"v_cndmask_b32 v3, 0, v4, vcc\n"
"ds_read_b128 a[32:35], v30 offset:2048\n"
"v_mfma_f32_16x16x32_bf16 a[152:155], a[132:135], a[80:83], a[152:155]\n"
"v_mfma_f32_16x16x32_bf16 a[184:187], a[136:139], a[80:83], a[184:187]\n"
"v_accvgpr_write_b32 a48, v3\n"
"v_mfma_f32_16x16x32_bf16 a[216:219], a[140:143], a[80:83], a[216:219]\n"
"ds_read_b128 a[36:39], v30 offset:2560\n"
"buffer_load_dwordx4 v27, s[52:55], s74 offen offset:2048 lds\n"
"v_mfma_f32_16x16x32_bf16 a[220:223], a[140:143], a[84:87], a[220:223]\n"
"v_mfma_f32_16x16x32_bf16 a[188:191], a[136:139], a[84:87], a[188:191]\n"
"v_accvgpr_write_b32 a49, v3\n"
"v_mfma_f32_16x16x32_bf16 a[156:159], a[132:135], a[84:87], a[156:159]\n"
"ds_read_b128 a[40:43], v30 offset:3072\n"
"v_mfma_f32_16x16x32_bf16 v[236:239], a[128:131], a[84:87], v[236:239]\n"
"v_accvgpr_write_b32 a50, v3\n"
"v_mfma_f32_16x16x32_bf16 v[204:207], a[124:127], a[84:87], v[204:207]\n"
"buffer_load_dwordx4 v28, s[52:55], s74 offen offset:3072 lds\n"
"s_add_u32 s74, s74, 524288\n"
"v_mfma_f32_16x16x32_bf16 v[172:175], a[120:123], a[84:87], v[172:175]\n"
"v_accvgpr_write_b32 a51, v3\n"
"ds_read_b128 a[44:47], v30 offset:3584\n"
"v_mfma_f32_16x16x32_bf16 v[140:143], a[84:87], a[116:119], v[140:143]\n"
"v_mfma_f32_16x16x32_bf16 v[108:111], a[84:87], a[112:115], v[108:111]\n"
"v_cmp_eq_u32 vcc, 16, v2\n"
"v_mfma_f32_16x16x32_bf16 v[76:79], a[108:111], a[84:87], v[76:79]\n"
"ds_read_b128 v[8:11], v1 offset:0\n"
"s_add_u32 m0, s77, 32768\n"
"v_mfma_f32_16x16x32_bf16 v[44:47], a[104:107], a[84:87], v[44:47]\n"
"buffer_load_dwordx4 v25, s[52:55], s74 offen offset:0 lds\n"
"v_mfma_f32_16x16x32_bf16 v[48:51], a[104:107], a[88:91], v[48:51]\n"
"v_cndmask_b32 v3, 0, v4, vcc\n"
"v_mfma_f32_16x16x32_bf16 v[80:83], a[108:111], a[88:91], v[80:83]\n"
"ds_read_b128 v[12:15], v1 offset:64\n"
"v_mfma_f32_16x16x32_bf16 v[112:115], a[88:91], a[112:115], v[112:115]\n"
"v_accvgpr_write_b32 a52, v3\n"
"v_mfma_f32_16x16x32_bf16 v[144:147], a[88:91], a[116:119], v[144:147]\n"
"v_mfma_f32_16x16x32_bf16 v[176:179], a[120:123], a[88:91], v[176:179]\n"
"v_accvgpr_write_b32 a53, v3\n"
"ds_read_b64 a[240:241], v30 offset:16\n"
"buffer_load_dwordx4 v26, s[52:55], s74 offen offset:1024 lds\n"
"v_mfma_f32_16x16x32_bf16 v[208:211], a[124:127], a[88:91], v[208:211]\n"
"v_mfma_f32_16x16x32_bf16 v[240:243], a[128:131], a[88:91], v[240:243]\n"
"v_accvgpr_write_b32 a54, v3\n"
"v_mfma_f32_16x16x32_bf16 a[160:163], a[132:135], a[88:91], a[160:163]\n"
"v_mfma_f32_16x16x32_bf16 a[192:195], a[136:139], a[88:91], a[192:195]\n"
"ds_read_b64 a[242:243], v30 offset:528\n"
"v_mfma_f32_16x16x32_bf16 a[224:227], a[140:143], a[88:91], a[224:227]\n"
"v_accvgpr_write_b32 a55, v3\n"
"buffer_load_dwordx4 v27, s[52:55], s74 offen offset:2048 lds\n"
"v_mfma_f32_16x16x32_bf16 a[228:231], a[140:143], a[92:95], a[228:231]\n"
"v_mfma_f32_16x16x32_bf16 a[196:199], a[136:139], a[92:95], a[196:199]\n"
"v_cmp_eq_u32 vcc, 32, v2\n"
"ds_read_b64 a[244:245], v30 offset:1040\n"
"v_mfma_f32_16x16x32_bf16 a[164:167], a[132:135], a[92:95], a[164:167]\n"
"v_mfma_f32_16x16x32_bf16 v[244:247], a[128:131], a[92:95], v[244:247]\n"
"v_cndmask_b32 v3, 0, v4, vcc\n"
"buffer_load_dwordx4 v28, s[52:55], s74 offen offset:3072 lds\n"
"s_add_u32 s74, s74, 131072\n"
"v_mfma_f32_16x16x32_bf16 v[212:215], a[124:127], a[92:95], v[212:215]\n"
"ds_read_b64 a[246:247], v30 offset:1552\n"
"v_mfma_f32_16x16x32_bf16 v[180:183], a[120:123], a[92:95], v[180:183]\n"
"v_accvgpr_write_b32 a56, v3\n"
"v_mfma_f32_16x16x32_bf16 v[148:151], a[92:95], a[116:119], v[148:151]\n"
"v_mfma_f32_16x16x32_bf16 v[116:119], a[92:95], a[112:115], v[116:119]\n"
"v_accvgpr_write_b32 a57, v3\n"
"ds_read_b64 a[248:249], v30 offset:2064\n"
"s_add_u32 m0, s77, 49152\n"
"v_mfma_f32_16x16x32_bf16 v[84:87], a[108:111], a[92:95], v[84:87]\n"
"buffer_load_dwordx4 v25, s[52:55], s74 offen offset:0 lds\n"
"v_mfma_f32_16x16x32_bf16 v[52:55], a[104:107], a[92:95], v[52:55]\n"
"v_mfma_f32_16x16x32_bf16 v[56:59], a[104:107], a[96:99], v[56:59]\n"
"v_accvgpr_write_b32 a58, v3\n"
"ds_read_b64 a[250:251], v30 offset:2576\n"
"v_mfma_f32_16x16x32_bf16 v[88:91], a[108:111], a[96:99], v[88:91]\n"
"v_mfma_f32_16x16x32_bf16 v[120:123], a[96:99], a[112:115], v[120:123]\n"
"v_accvgpr_write_b32 a59, v3\n"
"v_mfma_f32_16x16x32_bf16 v[152:155], a[96:99], a[116:119], v[152:155]\n"
"ds_read_b64 a[252:253], v30 offset:3088\n"
"buffer_load_dwordx4 v26, s[52:55], s74 offen offset:1024 lds\n"
"v_mfma_f32_16x16x32_bf16 v[184:187], a[120:123], a[96:99], v[184:187]\n"
"v_cmp_eq_u32 vcc, 48, v2\n"
"v_mfma_f32_16x16x32_bf16 v[216:219], a[124:127], a[96:99], v[216:219]\n"
"v_mfma_f32_16x16x32_bf16 v[248:251], a[128:131], a[96:99], v[248:251]\n"
"v_cndmask_b32 v3, 0, v4, vcc\n"
"ds_read_b64 a[254:255], v30 offset:3600\n"
"v_mfma_f32_16x16x32_bf16 a[168:171], a[132:135], a[96:99], a[168:171]\n"
"v_mfma_f32_16x16x32_bf16 a[200:203], a[136:139], a[96:99], a[200:203]\n"
"buffer_load_dwordx4 v27, s[52:55], s74 offen offset:2048 lds\n"
"v_mfma_f32_16x16x32_bf16 a[232:235], a[140:143], a[96:99], a[232:235]\n"
"v_accvgpr_write_b32 a60, v3\n"
"v_mfma_f32_16x16x32_bf16 a[236:239], a[140:143], a[100:103], a[236:239]\n"
"v_mfma_f32_16x16x32_bf16 a[204:207], a[136:139], a[100:103], a[204:207]\n"
"v_accvgpr_write_b32 a61, v3\n"
"v_mfma_f32_16x16x32_bf16 a[172:175], a[132:135], a[100:103], a[172:175]\n"
"v_mfma_f32_16x16x32_bf16 v[252:255], a[128:131], a[100:103], v[252:255]\n"
"v_accvgpr_write_b32 a62, v3\n"
"buffer_load_dwordx4 v28, s[52:55], s74 offen offset:3072 lds\n"
"s_add_u32 s74, s74, 131072\n"
"v_mfma_f32_16x16x32_bf16 v[220:223], a[124:127], a[100:103], v[220:223]\n"
"v_mfma_f32_16x16x32_bf16 v[188:191], a[120:123], a[100:103], v[188:191]\n"
"v_accvgpr_write_b32 a63, v3\n"
"v_mfma_f32_16x16x32_bf16 v[156:159], a[100:103], a[116:119], v[156:159]\n"
"v_mfma_f32_16x16x32_bf16 v[124:127], a[100:103], a[112:115], v[124:127]\n"
"v_mfma_f32_16x16x32_bf16 v[92:95], a[108:111], a[100:103], v[92:95]\n"
"v_mfma_f32_16x16x32_bf16 v[60:63], a[104:107], a[100:103], v[60:63]\n"
"s_waitcnt lgkmcnt(0)\n"
"ds_read_b128 v[4:7], v1 offset:128\n"
"v_mfma_f32_16x16x32_bf16 v[96:99], a[16:19], a[64:67], v[96:99]\n"
"v_mfma_f32_16x16x32_bf16 v[128:131], a[16:19], a[68:71], v[128:131]\n"
"v_mfma_f32_16x16x32_bf16 v[32:35], a[8:11], a[16:19], v[32:35]\n"
"v_mfma_f32_16x16x32_bf16 v[64:67], a[12:15], a[16:19], v[64:67]\n"
"v_mfma_f32_16x16x32_bf16 v[100:103], a[20:23], a[64:67], v[100:103]\n"
"ds_read_b128 a[116:119], v29 offset:4096\n"
"v_mfma_f32_16x16x32_bf16 v[132:135], a[20:23], a[68:71], v[132:135]\n"
"ds_read_b128 a[120:123], v29 offset:5120\n"
"ds_read_b128 a[124:127], v29 offset:6144\n"
"v_mfma_f32_16x16x32_bf16 v[36:39], a[8:11], a[20:23], v[36:39]\n"
"ds_read_b128 a[128:131], v29 offset:7168\n"
"v_mfma_f32_16x16x32_bf16 v[68:71], a[12:15], a[20:23], v[68:71]\n"
"ds_read_b128 a[132:135], v29 offset:8192\n"
"ds_read_b128 a[136:139], v29 offset:9216\n"
"v_mfma_f32_16x16x32_bf16 v[104:107], a[24:27], a[64:67], v[104:107]\n"
"v_pk_mul_f32 v[96:97], v[96:97], v[8:9]\n"
"v_pk_mul_f32 v[98:99], v[98:99], v[10:11]\n"
"v_mfma_f32_16x16x32_bf16 v[136:139], a[24:27], a[68:71], v[136:139]\n"
"v_pk_mul_f32 v[128:129], v[128:129], v[8:9]\n"
"v_pk_mul_f32 v[130:131], v[130:131], v[10:11]\n"
"v_mfma_f32_16x16x32_bf16 v[40:43], a[8:11], a[24:27], v[40:43]\n"
"ds_read_b128 v[8:11], v1 offset:192\n"
"v_cvt_pk_bf16_f32 v32, v32, v33\n"
"v_mfma_f32_16x16x32_bf16 v[72:75], a[12:15], a[24:27], v[72:75]\n"
"v_cvt_pk_bf16_f32 v33, v34, v35\n"
"v_cvt_pk_bf16_f32 v34, v64, v65\n"
"v_cvt_pk_bf16_f32 v35, v66, v67\n"
"v_mfma_f32_16x16x32_bf16 v[108:111], a[28:31], a[64:67], v[108:111]\n"
"v_pk_mul_f32 v[100:101], v[100:101], v[12:13]\n"
"v_pk_mul_f32 v[102:103], v[102:103], v[14:15]\n"
"v_mfma_f32_16x16x32_bf16 v[140:143], a[28:31], a[68:71], v[140:143]\n"
"v_pk_mul_f32 v[132:133], v[132:133], v[12:13]\n"
"v_pk_mul_f32 v[134:135], v[134:135], v[14:15]\n"
"v_mfma_f32_16x16x32_bf16 v[44:47], a[8:11], a[28:31], v[44:47]\n"
"ds_read_b128 v[12:15], v1 offset:256\n"
"v_cvt_pk_bf16_f32 v36, v36, v37\n"
"v_mfma_f32_16x16x32_bf16 v[76:79], a[12:15], a[28:31], v[76:79]\n"
"v_cvt_pk_bf16_f32 v37, v38, v39\n"
"v_cvt_pk_bf16_f32 v38, v68, v69\n"
"v_cvt_pk_bf16_f32 v39, v70, v71\n"
"v_mfma_f32_16x16x32_bf16 v[112:115], a[32:35], a[64:67], v[112:115]\n"
"s_waitcnt lgkmcnt(8)\n"
"v_pk_mul_f32 v[104:105], v[104:105], v[4:5]\n"
"v_mfma_f32_16x16x32_bf16 v[144:147], a[32:35], a[68:71], v[144:147]\n"
"v_pk_mul_f32 v[106:107], v[106:107], v[6:7]\n"
"v_pk_mul_f32 v[136:137], v[136:137], v[4:5]\n"
"v_pk_mul_f32 v[138:139], v[138:139], v[6:7]\n"
"v_mfma_f32_16x16x32_bf16 v[48:51], a[8:11], a[32:35], v[48:51]\n"
"ds_read_b128 v[4:7], v1 offset:320\n"
"v_cvt_pk_bf16_f32 v40, v40, v41\n"
"v_mfma_f32_16x16x32_bf16 v[80:83], a[12:15], a[32:35], v[80:83]\n"
"v_cvt_pk_bf16_f32 v41, v42, v43\n"
"v_cvt_pk_bf16_f32 v42, v72, v73\n"
"v_cvt_pk_bf16_f32 v43, v74, v75\n"
"v_mfma_f32_16x16x32_bf16 v[116:119], a[36:39], a[64:67], v[116:119]\n"
"s_waitcnt lgkmcnt(2)\n"
"v_pk_mul_f32 v[108:109], v[108:109], v[8:9]\n"
"v_mfma_f32_16x16x32_bf16 v[148:151], a[36:39], a[68:71], v[148:151]\n"
"v_pk_mul_f32 v[110:111], v[110:111], v[10:11]\n"
"v_pk_mul_f32 v[140:141], v[140:141], v[8:9]\n"
"v_pk_mul_f32 v[142:143], v[142:143], v[10:11]\n"
"v_mfma_f32_16x16x32_bf16 v[52:55], a[8:11], a[36:39], v[52:55]\n"
"ds_read_b128 v[8:11], v1 offset:384\n"
"v_cvt_pk_bf16_f32 v44, v44, v45\n"
"v_mfma_f32_16x16x32_bf16 v[84:87], a[12:15], a[36:39], v[84:87]\n"
"v_cvt_pk_bf16_f32 v45, v46, v47\n"
"v_cvt_pk_bf16_f32 v46, v76, v77\n"
"v_cvt_pk_bf16_f32 v47, v78, v79\n"
"v_mfma_f32_16x16x32_bf16 v[120:123], a[40:43], a[64:67], v[120:123]\n"
"s_waitcnt lgkmcnt(2)\n"
"v_pk_mul_f32 v[112:113], v[112:113], v[12:13]\n"
"v_mfma_f32_16x16x32_bf16 v[152:155], a[40:43], a[68:71], v[152:155]\n"
"v_pk_mul_f32 v[114:115], v[114:115], v[14:15]\n"
"v_pk_mul_f32 v[144:145], v[144:145], v[12:13]\n"
"v_pk_mul_f32 v[146:147], v[146:147], v[14:15]\n"
"v_mfma_f32_16x16x32_bf16 v[56:59], a[8:11], a[40:43], v[56:59]\n"
"ds_read_b128 v[12:15], v1 offset:448\n"
"v_cvt_pk_bf16_f32 v48, v48, v49\n"
"v_mfma_f32_16x16x32_bf16 v[88:91], a[12:15], a[40:43], v[88:91]\n"
"v_cvt_pk_bf16_f32 v49, v50, v51\n"
"v_cvt_pk_bf16_f32 v50, v80, v81\n"
"v_cvt_pk_bf16_f32 v51, v82, v83\n"
"v_mfma_f32_16x16x32_bf16 v[124:127], a[44:47], a[64:67], v[124:127]\n"
"s_waitcnt lgkmcnt(2)\n"
"v_pk_mul_f32 v[116:117], v[116:117], v[4:5]\n"
"v_mfma_f32_16x16x32_bf16 v[156:159], a[44:47], a[68:71], v[156:159]\n"
"v_pk_mul_f32 v[118:119], v[118:119], v[6:7]\n"
"v_pk_mul_f32 v[148:149], v[148:149], v[4:5]\n"
"v_mfma_f32_16x16x32_bf16 v[60:63], a[8:11], a[44:47], v[60:63]\n"
"v_pk_mul_f32 v[150:151], v[150:151], v[6:7]\n"
"v_cvt_pk_bf16_f32 v52, v52, v53\n"
"v_mfma_f32_16x16x32_bf16 v[92:95], a[12:15], a[44:47], v[92:95]\n"
"v_cvt_pk_bf16_f32 v53, v54, v55\n"
"v_cvt_pk_bf16_f32 v54, v84, v85\n"
"v_cvt_pk_bf16_f32 v55, v86, v87\n"
"s_waitcnt lgkmcnt(1)\n"
"v_pk_mul_f32 v[120:121], v[120:121], v[8:9]\n"
"v_pk_mul_f32 v[122:123], v[122:123], v[10:11]\n"
"v_pk_mul_f32 v[152:153], v[152:153], v[8:9]\n"
"v_pk_mul_f32 v[154:155], v[154:155], v[10:11]\n"
"v_cvt_pk_bf16_f32 v56, v56, v57\n"
"v_cvt_pk_bf16_f32 v57, v58, v59\n"
"v_cvt_pk_bf16_f32 v58, v88, v89\n"
"v_cvt_pk_bf16_f32 v59, v90, v91\n"
"s_waitcnt lgkmcnt(0)\n"
"v_pk_mul_f32 v[124:125], v[124:125], v[12:13]\n"
"v_pk_mul_f32 v[126:127], v[126:127], v[14:15]\n"
"v_pk_mul_f32 v[156:157], v[156:157], v[12:13]\n"
"v_pk_mul_f32 v[158:159], v[158:159], v[14:15]\n"
"v_cvt_pk_bf16_f32 v60, v60, v61\n"
"v_cvt_pk_bf16_f32 v61, v62, v63\n"
"v_cvt_pk_bf16_f32 v62, v92, v93\n"
"v_cvt_pk_bf16_f32 v63, v94, v95\n"
"v_mfma_f32_16x16x32_bf16 v[64:67], a[0:3], v[32:35], 0\n"
"v_mfma_f32_16x16x32_bf16 v[68:71], a[4:7], v[32:35], 0\n"
"v_mfma_f32_16x16x32_bf16 v[72:75], a[0:3], v[36:39], 0\n"
"v_mfma_f32_16x16x32_bf16 v[76:79], a[4:7], v[36:39], 0\n"
"v_mfma_f32_16x16x32_bf16 v[80:83], a[0:3], v[40:43], 0\n"
"v_mfma_f32_16x16x32_bf16 v[84:87], a[4:7], v[40:43], 0\n"
"v_mfma_f32_16x16x32_bf16 v[88:91], a[0:3], v[44:47], 0\n"
"v_mfma_f32_16x16x32_bf16 v[92:95], a[4:7], v[44:47], 0\n"
"v_cvt_pk_bf16_f32 v96, v96, v97\n"
"v_cvt_pk_bf16_f32 v97, v98, v99\n"
"v_cvt_pk_bf16_f32 v98, v100, v101\n"
"v_cvt_pk_bf16_f32 v99, v102, v103\n"
"v_cvt_pk_bf16_f32 v100, v104, v105\n"
"v_cvt_pk_bf16_f32 v101, v106, v107\n"
"v_cvt_pk_bf16_f32 v102, v108, v109\n"
"v_cvt_pk_bf16_f32 v103, v110, v111\n"
"v_cvt_pk_bf16_f32 v104, v112, v113\n"
"v_cvt_pk_bf16_f32 v105, v114, v115\n"
"v_cvt_pk_bf16_f32 v106, v116, v117\n"
"v_cvt_pk_bf16_f32 v107, v118, v119\n"
"v_cvt_pk_bf16_f32 v108, v120, v121\n"
"v_cvt_pk_bf16_f32 v109, v122, v123\n"
"v_cvt_pk_bf16_f32 v110, v124, v125\n"
"v_cvt_pk_bf16_f32 v111, v126, v127\n"
"v_cvt_pk_bf16_f32 v112, v128, v129\n"
"v_cvt_pk_bf16_f32 v113, v130, v131\n"
"v_cvt_pk_bf16_f32 v114, v132, v133\n"
"v_cvt_pk_bf16_f32 v115, v134, v135\n"
"v_cvt_pk_bf16_f32 v116, v136, v137\n"
"v_cvt_pk_bf16_f32 v117, v138, v139\n"
"v_cvt_pk_bf16_f32 v118, v140, v141\n"
"v_cvt_pk_bf16_f32 v119, v142, v143\n"
"v_cvt_pk_bf16_f32 v120, v144, v145\n"
"v_cvt_pk_bf16_f32 v121, v146, v147\n"
"v_cvt_pk_bf16_f32 v122, v148, v149\n"
"v_cvt_pk_bf16_f32 v123, v150, v151\n"
"v_cvt_pk_bf16_f32 v124, v152, v153\n"
"v_cvt_pk_bf16_f32 v125, v154, v155\n"
"v_cvt_pk_bf16_f32 v126, v156, v157\n"
"v_cvt_pk_bf16_f32 v127, v158, v159\n"
"v_xor_b32 v29, 64, v17\n"
"v_xor_b32 v30, 64, v18\n"
"s_barrier\n"
"v_accvgpr_read_b32 v4, a240\n"
"v_accvgpr_read_b32 v5, a242\n"
"v_max3_f32 v32, v64, v65, v66\n"
"v_max3_f32 v34, -v64, -v65, -v66\n"
"v_max3_f32 v36, v72, v73, v74\n"
"v_max3_f32 v38, -v72, -v73, -v74\n"
"v_max3_f32 v32, v32, v67, v68\n"
"v_max3_f32 v34, v34, -v67, -v68\n"
"v_max3_f32 v36, v36, v75, v76\n"
"v_max3_f32 v38, v38, -v75, -v76\n"
"v_max3_f32 v32, v32, v69, v70\n"
"v_max3_f32 v34, v34, -v69, -v70\n"
"v_max3_f32 v36, v36, v77, v78\n"
"v_max3_f32 v38, v38, -v77, -v78\n"
"v_max_f32 v32, v32, v71\n"
"v_max_f32_e64 v34, v34, -v71\n"
"v_max_f32 v36, v36, v79\n"
"v_max_f32_e64 v38, v38, -v79\n"
"v_permlane32_swap_b32 v32, v34\n"
"v_accvgpr_read_b32 v6, a244\n"
"v_permlane32_swap_b32 v36, v38\n"
"v_accvgpr_read_b32 v7, a246\n"
"v_max_f32 v32, v32, v34\n"
"v_max3_f32 v40, v80, v81, v82\n"
"v_max_f32 v36, v36, v38\n"
"v_max3_f32 v42, -v80, -v81, -v82\n"
"s_nop 0\n"
"v_permlane16_swap_b32 v32, v36\n"
"v_max3_f32 v44, v88, v89, v90\n"
"v_max_f32 v32, v32, v36\n"
"v_max3_f32 v46, -v88, -v89, -v90\n"
"v_mov_b32 v36, v32\n"
"v_max3_f32 v40, v40, v83, v84\n"
"s_nop 0\n"
"v_permlane16_swap_b32 v32, v36\n"
"v_max3_f32 v42, v42, -v83, -v84\n"
"v_mov_b32 v34, v32\n"
"v_max3_f32 v44, v44, v91, v92\n"
"v_mov_b32 v38, v36\n"
"v_max3_f32 v46, v46, -v91, -v92\n"
"v_permlane32_swap_b32 v32, v34\n"
"v_max3_f32 v40, v40, v85, v86\n"
"v_permlane32_swap_b32 v36, v38\n"
"v_max3_f32 v42, v42, -v85, -v86\n"
"v_mul_f32_e64 v32, -v4, v32\n"
"v_max3_f32 v44, v44, v93, v94\n"
"v_mul_f32_e64 v34, -v4, v34\n"
"v_max3_f32 v46, v46, -v93, -v94\n"
"v_mul_f32_e64 v36, -v5, v36\n"
"v_max_f32 v40, v40, v87\n"
"v_mul_f32_e64 v38, -v5, v38\n"
"v_max_f32_e64 v42, v42, -v87\n"
"v_max_f32 v44, v44, v95\n"
"v_max_f32_e64 v46, v46, -v95\n"
"v_pk_fma_f32 v[128:129], v[64:65], v[4:5], v[32:33] op_sel:[0,0,0] op_sel_hi:[1,0,0]\n"
"v_permlane32_swap_b32 v40, v42\n"
"v_pk_fma_f32 v[130:131], v[66:67], v[4:5], v[32:33] op_sel:[0,0,0] op_sel_hi:[1,0,0]\n"
"v_permlane32_swap_b32 v44, v46\n"
"v_pk_fma_f32 v[132:133], v[68:69], v[4:5], v[32:33] op_sel:[0,0,0] op_sel_hi:[1,0,0]\n"
"v_max_f32 v40, v40, v42\n"
"v_pk_fma_f32 v[134:135], v[70:71], v[4:5], v[32:33] op_sel:[0,0,0] op_sel_hi:[1,0,0]\n"
"v_max_f32 v44, v44, v46\n"
"v_pk_fma_f32 v[64:65], v[64:65], v[4:5], v[34:35] op_sel:[0,0,0] op_sel_hi:[1,0,0] neg_lo:[0,1,0] neg_hi:[0,1,0]\n"
"s_nop 0\n"
"v_permlane16_swap_b32 v40, v44\n"
"v_pk_fma_f32 v[66:67], v[66:67], v[4:5], v[34:35] op_sel:[0,0,0] op_sel_hi:[1,0,0] neg_lo:[0,1,0] neg_hi:[0,1,0]\n"
"v_max_f32 v40, v40, v44\n"
"v_pk_fma_f32 v[68:69], v[68:69], v[4:5], v[34:35] op_sel:[0,0,0] op_sel_hi:[1,0,0] neg_lo:[0,1,0] neg_hi:[0,1,0]\n"
"v_mov_b32 v44, v40\n"
"v_pk_fma_f32 v[70:71], v[70:71], v[4:5], v[34:35] op_sel:[0,0,0] op_sel_hi:[1,0,0] neg_lo:[0,1,0] neg_hi:[0,1,0]\n"
"s_nop 0\n"
"v_permlane16_swap_b32 v40, v44\n"
"v_pk_fma_f32 v[136:137], v[72:73], v[4:5], v[36:37] op_sel:[0,1,0] op_sel_hi:[1,1,0]\n"
"v_mov_b32 v42, v40\n"
"v_pk_fma_f32 v[138:139], v[74:75], v[4:5], v[36:37] op_sel:[0,1,0] op_sel_hi:[1,1,0]\n"
"v_mov_b32 v46, v44\n"
"v_pk_fma_f32 v[140:141], v[76:77], v[4:5], v[36:37] op_sel:[0,1,0] op_sel_hi:[1,1,0]\n"
"v_permlane32_swap_b32 v40, v42\n"
"v_pk_fma_f32 v[142:143], v[78:79], v[4:5], v[36:37] op_sel:[0,1,0] op_sel_hi:[1,1,0]\n"
"v_permlane32_swap_b32 v44, v46\n"
"v_pk_fma_f32 v[72:73], v[72:73], v[4:5], v[38:39] op_sel:[0,1,0] op_sel_hi:[1,1,0] neg_lo:[0,1,0] neg_hi:[0,1,0]\n"
"v_mul_f32_e64 v40, -v6, v40\n"
"v_pk_fma_f32 v[74:75], v[74:75], v[4:5], v[38:39] op_sel:[0,1,0] op_sel_hi:[1,1,0] neg_lo:[0,1,0] neg_hi:[0,1,0]\n"
"v_mul_f32_e64 v42, -v6, v42\n"
"v_pk_fma_f32 v[76:77], v[76:77], v[4:5], v[38:39] op_sel:[0,1,0] op_sel_hi:[1,1,0] neg_lo:[0,1,0] neg_hi:[0,1,0]\n"
"v_mul_f32_e64 v44, -v7, v44\n"
"v_pk_fma_f32 v[78:79], v[78:79], v[4:5], v[38:39] op_sel:[0,1,0] op_sel_hi:[1,1,0] neg_lo:[0,1,0] neg_hi:[0,1,0]\n"
"v_mul_f32_e64 v46, -v7, v46\n"
"v_exp_f32 v128, v128\n"
"v_mfma_f32_16x16x32_bf16 v[160:163], a[116:119], a[16:19], v[160:163]\n"
"v_exp_f32 v129, v129\n"
"v_mfma_f32_16x16x32_bf16 v[192:195], a[120:123], a[16:19], v[192:195]\n"
"v_exp_f32 v130, v130\n"
"v_mfma_f32_16x16x32_bf16 v[224:227], a[124:127], a[16:19], v[224:227]\n"
"v_exp_f32 v131, v131\n"
"v_exp_f32 v132, v132\n"
"v_mfma_f32_16x16x32_bf16 a[144:147], a[128:131], a[16:19], a[144:147]\n"
"v_exp_f32 v133, v133\n"
"v_mfma_f32_16x16x32_bf16 a[176:179], a[132:135], a[16:19], a[176:179]\n"
"v_exp_f32 v134, v134\n"
"v_mfma_f32_16x16x32_bf16 a[208:211], a[136:139], a[16:19], a[208:211]\n"
"v_exp_f32 v135, v135\n"
"v_exp_f32 v64, v64\n"
"v_mfma_f32_16x16x32_bf16 v[164:167], a[116:119], a[20:23], v[164:167]\n"
"v_exp_f32 v65, v65\n"
"v_mfma_f32_16x16x32_bf16 v[196:199], a[120:123], a[20:23], v[196:199]\n"
"v_exp_f32 v66, v66\n"
"v_exp_f32 v67, v67\n"
"v_mfma_f32_16x16x32_bf16 v[228:231], a[124:127], a[20:23], v[228:231]\n"
"v_exp_f32 v68, v68\n"
"v_mfma_f32_16x16x32_bf16 a[148:151], a[128:131], a[20:23], a[148:151]\n"
"v_exp_f32 v69, v69\n"
"v_mfma_f32_16x16x32_bf16 a[180:183], a[132:135], a[20:23], a[180:183]\n"
"v_exp_f32 v70, v70\n"
"v_exp_f32 v71, v71\n"
"v_mfma_f32_16x16x32_bf16 a[212:215], a[136:139], a[20:23], a[212:215]\n"
"v_exp_f32 v136, v136\n"
"v_mfma_f32_16x16x32_bf16 v[168:171], a[116:119], a[24:27], v[168:171]\n"
"v_exp_f32 v137, v137\n"
"v_mfma_f32_16x16x32_bf16 v[200:203], a[120:123], a[24:27], v[200:203]\n"
"v_exp_f32 v138, v138\n"
"v_exp_f32 v139, v139\n"
"v_mfma_f32_16x16x32_bf16 v[232:235], a[124:127], a[24:27], v[232:235]\n"
"v_exp_f32 v140, v140\n"
"v_mfma_f32_16x16x32_bf16 a[152:155], a[128:131], a[24:27], a[152:155]\n"
"v_exp_f32 v141, v141\n"
"v_exp_f32 v142, v142\n"
"v_mfma_f32_16x16x32_bf16 a[184:187], a[132:135], a[24:27], a[184:187]\n"
"v_exp_f32 v143, v143\n"
"v_mfma_f32_16x16x32_bf16 a[216:219], a[136:139], a[24:27], a[216:219]\n"
"v_exp_f32 v72, v72\n"
"v_mfma_f32_16x16x32_bf16 v[172:175], a[116:119], a[28:31], v[172:175]\n"
"v_exp_f32 v73, v73\n"
"v_exp_f32 v74, v74\n"
"v_mfma_f32_16x16x32_bf16 v[204:207], a[120:123], a[28:31], v[204:207]\n"
"v_exp_f32 v75, v75\n"
"v_mfma_f32_16x16x32_bf16 v[236:239], a[124:127], a[28:31], v[236:239]\n"
"v_exp_f32 v76, v76\n"
"v_mfma_f32_16x16x32_bf16 a[156:159], a[128:131], a[28:31], a[156:159]\n"
"v_exp_f32 v77, v77\n"
"v_exp_f32 v78, v78\n"
"v_mfma_f32_16x16x32_bf16 a[188:191], a[132:135], a[28:31], a[188:191]\n"
"v_exp_f32 v79, v79\n"
"v_mfma_f32_16x16x32_bf16 a[220:223], a[136:139], a[28:31], a[220:223]\n"
"v_pk_fma_f32 v[144:145], v[80:81], v[6:7], v[40:41] op_sel:[0,0,0] op_sel_hi:[1,0,0]\n"
"v_pk_fma_f32 v[146:147], v[82:83], v[6:7], v[40:41] op_sel:[0,0,0] op_sel_hi:[1,0,0]\n"
"v_pk_fma_f32 v[148:149], v[84:85], v[6:7], v[40:41] op_sel:[0,0,0] op_sel_hi:[1,0,0]\n"
"v_pk_fma_f32 v[150:151], v[86:87], v[6:7], v[40:41] op_sel:[0,0,0] op_sel_hi:[1,0,0]\n"
"v_pk_fma_f32 v[80:81], v[80:81], v[6:7], v[42:43] op_sel:[0,0,0] op_sel_hi:[1,0,0] neg_lo:[0,1,0] neg_hi:[0,1,0]\n"
"v_pk_fma_f32 v[82:83], v[82:83], v[6:7], v[42:43] op_sel:[0,0,0] op_sel_hi:[1,0,0] neg_lo:[0,1,0] neg_hi:[0,1,0]\n"
"v_pk_fma_f32 v[84:85], v[84:85], v[6:7], v[42:43] op_sel:[0,0,0] op_sel_hi:[1,0,0] neg_lo:[0,1,0] neg_hi:[0,1,0]\n"
"v_pk_fma_f32 v[86:87], v[86:87], v[6:7], v[42:43] op_sel:[0,0,0] op_sel_hi:[1,0,0] neg_lo:[0,1,0] neg_hi:[0,1,0]\n"
"v_pk_fma_f32 v[152:153], v[88:89], v[6:7], v[44:45] op_sel:[0,1,0] op_sel_hi:[1,1,0]\n"
"v_pk_fma_f32 v[154:155], v[90:91], v[6:7], v[44:45] op_sel:[0,1,0] op_sel_hi:[1,1,0]\n"
"v_pk_fma_f32 v[156:157], v[92:93], v[6:7], v[44:45] op_sel:[0,1,0] op_sel_hi:[1,1,0]\n"
"v_pk_fma_f32 v[158:159], v[94:95], v[6:7], v[44:45] op_sel:[0,1,0] op_sel_hi:[1,1,0]\n"
"v_pk_fma_f32 v[88:89], v[88:89], v[6:7], v[46:47] op_sel:[0,1,0] op_sel_hi:[1,1,0] neg_lo:[0,1,0] neg_hi:[0,1,0]\n"
"v_pk_fma_f32 v[90:91], v[90:91], v[6:7], v[46:47] op_sel:[0,1,0] op_sel_hi:[1,1,0] neg_lo:[0,1,0] neg_hi:[0,1,0]\n"
"v_pk_fma_f32 v[92:93], v[92:93], v[6:7], v[46:47] op_sel:[0,1,0] op_sel_hi:[1,1,0] neg_lo:[0,1,0] neg_hi:[0,1,0]\n"
"v_pk_fma_f32 v[94:95], v[94:95], v[6:7], v[46:47] op_sel:[0,1,0] op_sel_hi:[1,1,0] neg_lo:[0,1,0] neg_hi:[0,1,0]\n"
"v_pk_add_f32 v[8:9], v[128:129], v[130:131]\n"
"v_pk_add_f32 v[10:11], v[64:65], v[66:67]\n"
"v_pk_add_f32 v[12:13], v[136:137], v[138:139]\n"
"v_pk_add_f32 v[14:15], v[72:73], v[74:75]\n"
"v_pk_add_f32 v[8:9], v[8:9], v[132:133]\n"
"v_pk_add_f32 v[10:11], v[10:11], v[68:69]\n"
"v_pk_add_f32 v[12:13], v[12:13], v[140:141]\n"
"v_pk_add_f32 v[14:15], v[14:15], v[76:77]\n"
"v_pk_add_f32 v[8:9], v[8:9], v[134:135]\n"
"v_pk_add_f32 v[10:11], v[10:11], v[70:71]\n"
"v_pk_add_f32 v[12:13], v[12:13], v[142:143]\n"
"v_pk_add_f32 v[14:15], v[14:15], v[78:79]\n"
"v_add_f32 v32, v8, v9\n"
"v_add_f32 v34, v10, v11\n"
"v_add_f32 v36, v12, v13\n"
"v_add_f32 v38, v14, v15\n"
"v_permlane32_swap_b32 v32, v34\n"
"v_exp_f32 v144, v144\n"
"v_permlane32_swap_b32 v36, v38\n"
"v_exp_f32 v145, v145\n"
"v_mfma_f32_16x16x32_bf16 v[176:179], a[116:119], a[32:35], v[176:179]\n"
"v_add_f32 v32, v32, v34\n"
"v_exp_f32 v146, v146\n"
"v_mfma_f32_16x16x32_bf16 v[208:211], a[120:123], a[32:35], v[208:211]\n"
"v_add_f32 v36, v36, v38\n"
"v_exp_f32 v147, v147\n"
"v_mfma_f32_16x16x32_bf16 v[240:243], a[124:127], a[32:35], v[240:243]\n"
"v_permlane16_swap_b32 v32, v36\n"
"v_exp_f32 v148, v148\n"
"v_add_f32 v32, v32, v36\n"
"v_exp_f32 v149, v149\n"
"v_mfma_f32_16x16x32_bf16 a[160:163], a[128:131], a[32:35], a[160:163]\n"
"v_rcp_f32 v32, v32\n"
"v_mfma_f32_16x16x32_bf16 a[192:195], a[132:135], a[32:35], a[192:195]\n"
"v_exp_f32 v150, v150\n"
"v_mfma_f32_16x16x32_bf16 a[224:227], a[136:139], a[32:35], a[224:227]\n"
"v_mov_b32 v36, v32\n"
"v_exp_f32 v151, v151\n"
"s_nop 0\n"
"v_permlane16_swap_b32 v32, v36\n"
"v_exp_f32 v80, v80\n"
"v_mfma_f32_16x16x32_bf16 v[180:183], a[116:119], a[36:39], v[180:183]\n"
"v_mov_b32 v34, v32\n"
"v_exp_f32 v81, v81\n"
"v_mfma_f32_16x16x32_bf16 v[212:215], a[120:123], a[36:39], v[212:215]\n"
"v_mov_b32 v38, v36\n"
"v_exp_f32 v82, v82\n"
"v_permlane32_swap_b32 v32, v34\n"
"v_exp_f32 v83, v83\n"
"v_mfma_f32_16x16x32_bf16 v[244:247], a[124:127], a[36:39], v[244:247]\n"
"v_permlane32_swap_b32 v36, v38\n"
"v_exp_f32 v84, v84\n"
"v_mfma_f32_16x16x32_bf16 a[164:167], a[128:131], a[36:39], a[164:167]\n"
"v_exp_f32 v85, v85\n"
"v_mfma_f32_16x16x32_bf16 a[196:199], a[132:135], a[36:39], a[196:199]\n"
"v_exp_f32 v86, v86\n"
"v_exp_f32 v87, v87\n"
"v_mfma_f32_16x16x32_bf16 a[228:231], a[136:139], a[36:39], a[228:231]\n"
"v_exp_f32 v152, v152\n"
"v_mfma_f32_16x16x32_bf16 v[184:187], a[116:119], a[40:43], v[184:187]\n"
"v_exp_f32 v153, v153\n"
"v_mfma_f32_16x16x32_bf16 v[216:219], a[120:123], a[40:43], v[216:219]\n"
"v_exp_f32 v154, v154\n"
"v_exp_f32 v155, v155\n"
"v_mfma_f32_16x16x32_bf16 v[248:251], a[124:127], a[40:43], v[248:251]\n"
"v_exp_f32 v156, v156\n"
"v_mfma_f32_16x16x32_bf16 a[168:171], a[128:131], a[40:43], a[168:171]\n"
"v_exp_f32 v157, v157\n"
"v_exp_f32 v158, v158\n"
"v_mfma_f32_16x16x32_bf16 a[200:203], a[132:135], a[40:43], a[200:203]\n"
"v_exp_f32 v159, v159\n"
"v_mfma_f32_16x16x32_bf16 a[232:235], a[136:139], a[40:43], a[232:235]\n"
"v_exp_f32 v88, v88\n"
"v_mfma_f32_16x16x32_bf16 v[188:191], a[116:119], a[44:47], v[188:191]\n"
"v_exp_f32 v89, v89\n"
"v_exp_f32 v90, v90\n"
"v_mfma_f32_16x16x32_bf16 v[220:223], a[120:123], a[44:47], v[220:223]\n"
"v_exp_f32 v91, v91\n"
"v_mfma_f32_16x16x32_bf16 v[252:255], a[124:127], a[44:47], v[252:255]\n"
"v_exp_f32 v92, v92\n"
"v_mfma_f32_16x16x32_bf16 a[172:175], a[128:131], a[44:47], a[172:175]\n"
"v_exp_f32 v93, v93\n"
"v_exp_f32 v94, v94\n"
"v_mfma_f32_16x16x32_bf16 a[204:207], a[132:135], a[44:47], a[204:207]\n"
"v_exp_f32 v95, v95\n"
"v_mfma_f32_16x16x32_bf16 a[236:239], a[136:139], a[44:47], a[236:239]\n"
"v_pk_add_f32 v[8:9], v[144:145], v[146:147]\n"
"v_pk_add_f32 v[10:11], v[80:81], v[82:83]\n"
"v_pk_add_f32 v[12:13], v[152:153], v[154:155]\n"
"v_pk_add_f32 v[14:15], v[88:89], v[90:91]\n"
"v_pk_add_f32 v[8:9], v[8:9], v[148:149]\n"
"v_pk_add_f32 v[10:11], v[10:11], v[84:85]\n"
"v_pk_add_f32 v[12:13], v[12:13], v[156:157]\n"
"v_pk_add_f32 v[14:15], v[14:15], v[92:93]\n"
"v_pk_add_f32 v[8:9], v[8:9], v[150:151]\n"
"v_pk_add_f32 v[10:11], v[10:11], v[86:87]\n"
"v_pk_add_f32 v[12:13], v[12:13], v[158:159]\n"
"v_pk_add_f32 v[14:15], v[14:15], v[94:95]\n"
"v_add_f32 v40, v8, v9\n"
"v_add_f32 v42, v10, v11\n"
"v_add_f32 v44, v12, v13\n"
"v_add_f32 v46, v14, v15\n"
"v_pk_mul_f32 v[128:129], v[128:129], v[32:33] op_sel_hi:[1,0]\n"
"v_permlane32_swap_b32 v40, v42\n"
"v_pk_mul_f32 v[130:131], v[130:131], v[32:33] op_sel_hi:[1,0]\n"
"v_permlane32_swap_b32 v44, v46\n"
"v_pk_mul_f32 v[132:133], v[132:133], v[32:33] op_sel_hi:[1,0]\n"
"v_add_f32 v40, v40, v42\n"
"v_pk_mul_f32 v[134:135], v[134:135], v[32:33] op_sel_hi:[1,0]\n"
"v_add_f32 v44, v44, v46\n"
"v_cvt_pk_bf16_f32 v128, v128, v129\n"
"s_nop 0\n"
"v_permlane16_swap_b32 v40, v44\n"
"v_cvt_pk_bf16_f32 v129, v130, v131\n"
"v_add_f32 v40, v40, v44\n"
"v_cvt_pk_bf16_f32 v130, v132, v133\n"
"v_rcp_f32 v40, v40\n"
"v_cvt_pk_bf16_f32 v131, v134, v135\n"
"v_mov_b32 v44, v40\n"
"v_pk_mul_f32 v[64:65], v[64:65], v[34:35] op_sel_hi:[1,0]\n"
"s_nop 0\n"
"v_permlane16_swap_b32 v40, v44\n"
"v_pk_mul_f32 v[66:67], v[66:67], v[34:35] op_sel_hi:[1,0]\n"
"v_mov_b32 v42, v40\n"
"v_pk_mul_f32 v[68:69], v[68:69], v[34:35] op_sel_hi:[1,0]\n"
"v_mov_b32 v46, v44\n"
"v_pk_mul_f32 v[70:71], v[70:71], v[34:35] op_sel_hi:[1,0]\n"
"v_permlane32_swap_b32 v40, v42\n"
"v_cvt_pk_bf16_f32 v64, v64, v65\n"
"v_permlane32_swap_b32 v44, v46\n"
"v_cvt_pk_bf16_f32 v65, v66, v67\n"
"v_cvt_pk_bf16_f32 v66, v68, v69\n"
"v_cvt_pk_bf16_f32 v67, v70, v71\n"
"v_pk_mul_f32 v[136:137], v[136:137], v[36:37] op_sel_hi:[1,0]\n"
"v_pk_mul_f32 v[138:139], v[138:139], v[36:37] op_sel_hi:[1,0]\n"
"v_pk_mul_f32 v[140:141], v[140:141], v[36:37] op_sel_hi:[1,0]\n"
"v_pk_mul_f32 v[142:143], v[142:143], v[36:37] op_sel_hi:[1,0]\n"
"v_cvt_pk_bf16_f32 v136, v136, v137\n"
"v_cvt_pk_bf16_f32 v137, v138, v139\n"
"v_cvt_pk_bf16_f32 v138, v140, v141\n"
"v_cvt_pk_bf16_f32 v139, v142, v143\n"
"v_pk_mul_f32 v[72:73], v[72:73], v[38:39] op_sel_hi:[1,0]\n"
"v_pk_mul_f32 v[74:75], v[74:75], v[38:39] op_sel_hi:[1,0]\n"
"v_pk_mul_f32 v[76:77], v[76:77], v[38:39] op_sel_hi:[1,0]\n"
"v_pk_mul_f32 v[78:79], v[78:79], v[38:39] op_sel_hi:[1,0]\n"
"v_cvt_pk_bf16_f32 v72, v72, v73\n"
"v_cvt_pk_bf16_f32 v73, v74, v75\n"
"v_cvt_pk_bf16_f32 v74, v76, v77\n"
"v_cvt_pk_bf16_f32 v75, v78, v79\n"
"v_pk_mul_f32 v[144:145], v[144:145], v[40:41] op_sel_hi:[1,0]\n"
"v_pk_mul_f32 v[146:147], v[146:147], v[40:41] op_sel_hi:[1,0]\n"
"v_pk_mul_f32 v[148:149], v[148:149], v[40:41] op_sel_hi:[1,0]\n"
"v_pk_mul_f32 v[150:151], v[150:151], v[40:41] op_sel_hi:[1,0]\n"
"v_cvt_pk_bf16_f32 v144, v144, v145\n"
"v_cvt_pk_bf16_f32 v145, v146, v147\n"
"v_cvt_pk_bf16_f32 v146, v148, v149\n"
"v_cvt_pk_bf16_f32 v147, v150, v151\n"
"v_pk_mul_f32 v[80:81], v[80:81], v[42:43] op_sel_hi:[1,0]\n"
"v_pk_mul_f32 v[82:83], v[82:83], v[42:43] op_sel_hi:[1,0]\n"
"v_pk_mul_f32 v[84:85], v[84:85], v[42:43] op_sel_hi:[1,0]\n"
"v_pk_mul_f32 v[86:87], v[86:87], v[42:43] op_sel_hi:[1,0]\n"
"v_cvt_pk_bf16_f32 v80, v80, v81\n"
"v_cvt_pk_bf16_f32 v81, v82, v83\n"
"v_cvt_pk_bf16_f32 v82, v84, v85\n"
"v_cvt_pk_bf16_f32 v83, v86, v87\n"
"v_pk_mul_f32 v[152:153], v[152:153], v[44:45] op_sel_hi:[1,0]\n"
"v_pk_mul_f32 v[154:155], v[154:155], v[44:45] op_sel_hi:[1,0]\n"
"v_pk_mul_f32 v[156:157], v[156:157], v[44:45] op_sel_hi:[1,0]\n"
"v_pk_mul_f32 v[158:159], v[158:159], v[44:45] op_sel_hi:[1,0]\n"
"v_cvt_pk_bf16_f32 v152, v152, v153\n"
"v_cvt_pk_bf16_f32 v153, v154, v155\n"
"v_cvt_pk_bf16_f32 v154, v156, v157\n"
"v_cvt_pk_bf16_f32 v155, v158, v159\n"
"v_pk_mul_f32 v[88:89], v[88:89], v[46:47] op_sel_hi:[1,0]\n"
"v_pk_mul_f32 v[90:91], v[90:91], v[46:47] op_sel_hi:[1,0]\n"
"v_pk_mul_f32 v[92:93], v[92:93], v[46:47] op_sel_hi:[1,0]\n"
"v_pk_mul_f32 v[94:95], v[94:95], v[46:47] op_sel_hi:[1,0]\n"
"v_cvt_pk_bf16_f32 v88, v88, v89\n"
"v_cvt_pk_bf16_f32 v89, v90, v91\n"
"v_cvt_pk_bf16_f32 v90, v92, v93\n"
"v_cvt_pk_bf16_f32 v91, v94, v95\n"
"ds_write_b128 v17, v[128:131] offset:0\n"
"ds_write_b128 v29, v[64:67] offset:0\n"
"ds_write_b128 v17, v[136:139] offset:2048\n"
"ds_write_b128 v29, v[72:75] offset:2048\n"
"ds_write_b128 v17, v[144:147] offset:16384\n"
"ds_write_b128 v29, v[80:83] offset:16384\n"
"ds_write_b128 v17, v[152:155] offset:18432\n"
"ds_write_b128 v29, v[88:91] offset:18432\n"
"v_mfma_f32_16x16x32_bf16 v[64:67], a[0:3], v[48:51], 0\n"
"v_mfma_f32_16x16x32_bf16 v[68:71], a[4:7], v[48:51], 0\n"
"v_mfma_f32_16x16x32_bf16 v[72:75], a[0:3], v[52:55], 0\n"
"v_mfma_f32_16x16x32_bf16 v[76:79], a[4:7], v[52:55], 0\n"
"v_mfma_f32_16x16x32_bf16 v[80:83], a[0:3], v[56:59], 0\n"
"v_mfma_f32_16x16x32_bf16 v[84:87], a[4:7], v[56:59], 0\n"
"v_mfma_f32_16x16x32_bf16 v[88:91], a[0:3], v[60:63], 0\n"
"v_mfma_f32_16x16x32_bf16 v[92:95], a[4:7], v[60:63], 0\n"
"ds_read_b64_tr_b16 a[16:17], v18 offset:0\n"
"ds_read_b64_tr_b16 a[18:19], v18 offset:2048\n"
"ds_read_b64_tr_b16 a[20:21], v18 offset:8\n"
"ds_read_b64_tr_b16 a[22:23], v18 offset:2056\n"
"ds_read_b64_tr_b16 a[24:25], v30 offset:0\n"
"ds_read_b64_tr_b16 a[26:27], v30 offset:2048\n"
"ds_read_b64_tr_b16 a[28:29], v30 offset:8\n"
"ds_read_b64_tr_b16 a[30:31], v30 offset:2056\n"
"ds_read_b64_tr_b16 a[32:33], v18 offset:16384\n"
"ds_read_b64_tr_b16 a[34:35], v18 offset:18432\n"
"ds_read_b64_tr_b16 a[36:37], v18 offset:16392\n"
"ds_read_b64_tr_b16 a[38:39], v18 offset:18440\n"
"ds_read_b64_tr_b16 a[40:41], v30 offset:16384\n"
"ds_read_b64_tr_b16 a[42:43], v30 offset:18432\n"
"ds_read_b64_tr_b16 a[44:45], v30 offset:16392\n"
"ds_read_b64_tr_b16 a[46:47], v30 offset:18440\n"
"s_mov_b32 s95, 32768\n"
"v_accvgpr_read_b32 v4, a248\n"
"v_accvgpr_read_b32 v5, a250\n"
"v_max3_f32 v32, v64, v65, v66\n"
"v_max3_f32 v34, -v64, -v65, -v66\n"
"v_max3_f32 v36, v72, v73, v74\n"
"v_max3_f32 v38, -v72, -v73, -v74\n"
"v_max3_f32 v32, v32, v67, v68\n"
"v_max3_f32 v34, v34, -v67, -v68\n"
"buffer_load_dwordx4 a[128:131], v22, s[44:47], 0 offen offset:0\n"
"v_max3_f32 v36, v36, v75, v76\n"
"v_max3_f32 v38, v38, -v75, -v76\n"
"v_max3_f32 v32, v32, v69, v70\n"
"v_max3_f32 v34, v34, -v69, -v70\n"
"v_max3_f32 v36, v36, v77, v78\n"
"v_max3_f32 v38, v38, -v77, -v78\n"
"v_max_f32 v32, v32, v71\n"
"v_max_f32_e64 v34, v34, -v71\n"
"buffer_load_dwordx4 a[132:135], v22, s[44:47], 0 offen offset:64\n"
"v_max_f32 v36, v36, v79\n"
"v_max_f32_e64 v38, v38, -v79\n"
"v_permlane32_swap_b32 v32, v34\n"
"v_accvgpr_read_b32 v6, a252\n"
"v_permlane32_swap_b32 v36, v38\n"
"v_accvgpr_read_b32 v7, a254\n"
"v_max_f32 v32, v32, v34\n"
"v_max3_f32 v40, v80, v81, v82\n"
"buffer_load_dwordx4 a[136:139], v22, s[44:47], s95 offen offset:0\n"
"v_max_f32 v36, v36, v38\n"
"v_max3_f32 v42, -v80, -v81, -v82\n"
"s_nop 0\n"
"v_permlane16_swap_b32 v32, v36\n"
"v_max3_f32 v44, v88, v89, v90\n"
"v_max_f32 v32, v32, v36\n"
"v_max3_f32 v46, -v88, -v89, -v90\n"
"v_mov_b32 v36, v32\n"
"v_max3_f32 v40, v40, v83, v84\n"
"buffer_load_dwordx4 a[140:143], v22, s[44:47], s95 offen offset:64\n"
"v_permlane16_swap_b32 v32, v36\n"
"v_max3_f32 v42, v42, -v83, -v84\n"
"v_mov_b32 v34, v32\n"
"v_max3_f32 v44, v44, v91, v92\n"
"v_mov_b32 v38, v36\n"
"v_max3_f32 v46, v46, -v91, -v92\n"
"v_permlane32_swap_b32 v32, v34\n"
"v_max3_f32 v40, v40, v85, v86\n"
"s_waitcnt lgkmcnt(0)\n"
"v_permlane32_swap_b32 v36, v38\n"
"v_max3_f32 v42, v42, -v85, -v86\n"
"v_mfma_f32_16x16x32_bf16 a[80:83], a[16:19], v[96:99], 0\n"
"v_mul_f32_e64 v32, -v4, v32\n"
"v_max3_f32 v44, v44, v93, v94\n"
"v_mul_f32_e64 v34, -v4, v34\n"
"v_max3_f32 v46, v46, -v93, -v94\n"
"v_mul_f32_e64 v36, -v5, v36\n"
"v_max_f32 v40, v40, v87\n"
"v_mfma_f32_16x16x32_bf16 a[84:87], a[16:19], v[112:115], 0\n"
"v_mul_f32_e64 v38, -v5, v38\n"
"v_max_f32_e64 v42, v42, -v87\n"
"v_max_f32 v44, v44, v95\n"
"v_max_f32_e64 v46, v46, -v95\n"
"v_pk_fma_f32 v[128:129], v[64:65], v[4:5], v[32:33] op_sel:[0,0,0] op_sel_hi:[1,0,0]\n"
"v_permlane32_swap_b32 v40, v42\n"
"v_pk_fma_f32 v[130:131], v[66:67], v[4:5], v[32:33] op_sel:[0,0,0] op_sel_hi:[1,0,0]\n"
"v_permlane32_swap_b32 v44, v46\n"
"v_pk_fma_f32 v[132:133], v[68:69], v[4:5], v[32:33] op_sel:[0,0,0] op_sel_hi:[1,0,0]\n"
"v_max_f32 v40, v40, v42\n"
"v_pk_fma_f32 v[134:135], v[70:71], v[4:5], v[32:33] op_sel:[0,0,0] op_sel_hi:[1,0,0]\n"
"v_max_f32 v44, v44, v46\n"
"v_pk_fma_f32 v[64:65], v[64:65], v[4:5], v[34:35] op_sel:[0,0,0] op_sel_hi:[1,0,0] neg_lo:[0,1,0] neg_hi:[0,1,0]\n"
"s_nop 0\n"
"v_permlane16_swap_b32 v40, v44\n"
"v_pk_fma_f32 v[66:67], v[66:67], v[4:5], v[34:35] op_sel:[0,0,0] op_sel_hi:[1,0,0] neg_lo:[0,1,0] neg_hi:[0,1,0]\n"
"v_max_f32 v40, v40, v44\n"
"v_pk_fma_f32 v[68:69], v[68:69], v[4:5], v[34:35] op_sel:[0,0,0] op_sel_hi:[1,0,0] neg_lo:[0,1,0] neg_hi:[0,1,0]\n"
"v_mov_b32 v44, v40\n"
"v_pk_fma_f32 v[70:71], v[70:71], v[4:5], v[34:35] op_sel:[0,0,0] op_sel_hi:[1,0,0] neg_lo:[0,1,0] neg_hi:[0,1,0]\n"
"s_nop 0\n"
"v_permlane16_swap_b32 v40, v44\n"
"v_pk_fma_f32 v[136:137], v[72:73], v[4:5], v[36:37] op_sel:[0,1,0] op_sel_hi:[1,1,0]\n"
"v_mov_b32 v42, v40\n"
"v_pk_fma_f32 v[138:139], v[74:75], v[4:5], v[36:37] op_sel:[0,1,0] op_sel_hi:[1,1,0]\n"
"v_mov_b32 v46, v44\n"
"v_pk_fma_f32 v[140:141], v[76:77], v[4:5], v[36:37] op_sel:[0,1,0] op_sel_hi:[1,1,0]\n"
"v_permlane32_swap_b32 v40, v42\n"
"v_pk_fma_f32 v[142:143], v[78:79], v[4:5], v[36:37] op_sel:[0,1,0] op_sel_hi:[1,1,0]\n"
"v_permlane32_swap_b32 v44, v46\n"
"v_pk_fma_f32 v[72:73], v[72:73], v[4:5], v[38:39] op_sel:[0,1,0] op_sel_hi:[1,1,0] neg_lo:[0,1,0] neg_hi:[0,1,0]\n"
"v_mul_f32_e64 v40, -v6, v40\n"
"v_pk_fma_f32 v[74:75], v[74:75], v[4:5], v[38:39] op_sel:[0,1,0] op_sel_hi:[1,1,0] neg_lo:[0,1,0] neg_hi:[0,1,0]\n"
"v_mul_f32_e64 v42, -v6, v42\n"
"v_pk_fma_f32 v[76:77], v[76:77], v[4:5], v[38:39] op_sel:[0,1,0] op_sel_hi:[1,1,0] neg_lo:[0,1,0] neg_hi:[0,1,0]\n"
"v_mul_f32_e64 v44, -v7, v44\n"
"v_pk_fma_f32 v[78:79], v[78:79], v[4:5], v[38:39] op_sel:[0,1,0] op_sel_hi:[1,1,0] neg_lo:[0,1,0] neg_hi:[0,1,0]\n"
"v_mul_f32_e64 v46, -v7, v46\n"
"v_exp_f32 v128, v128\n"
"v_exp_f32 v129, v129\n"
"v_mfma_f32_16x16x32_bf16 a[112:115], a[16:19], a[48:51], 0\n"
"v_exp_f32 v130, v130\n"
"v_exp_f32 v131, v131\n"
"v_exp_f32 v132, v132\n"
"v_exp_f32 v133, v133\n"
"v_exp_f32 v134, v134\n"
"v_exp_f32 v135, v135\n"
"v_mfma_f32_16x16x32_bf16 a[88:91], a[20:23], v[96:99], 0\n"
"v_exp_f32 v64, v64\n"
"v_exp_f32 v65, v65\n"
"v_exp_f32 v66, v66\n"
"v_exp_f32 v67, v67\n"
"v_exp_f32 v68, v68\n"
"v_exp_f32 v69, v69\n"
"v_exp_f32 v70, v70\n"
"v_mfma_f32_16x16x32_bf16 a[92:95], a[20:23], v[112:115], 0\n"
"v_exp_f32 v71, v71\n"
"v_exp_f32 v136, v136\n"
"v_exp_f32 v137, v137\n"
"v_exp_f32 v138, v138\n"
"v_exp_f32 v139, v139\n"
"v_exp_f32 v140, v140\n"
"v_mfma_f32_16x16x32_bf16 a[112:115], a[20:23], a[52:55], a[112:115]\n"
"v_exp_f32 v141, v141\n"
"v_exp_f32 v142, v142\n"
"v_exp_f32 v143, v143\n"
"v_exp_f32 v72, v72\n"
"v_exp_f32 v73, v73\n"
"v_exp_f32 v74, v74\n"
"v_mfma_f32_16x16x32_bf16 a[96:99], a[24:27], v[96:99], 0\n"
"v_exp_f32 v75, v75\n"
"v_exp_f32 v76, v76\n"
"v_exp_f32 v77, v77\n"
"v_exp_f32 v78, v78\n"
"v_exp_f32 v79, v79\n"
"v_pk_fma_f32 v[144:145], v[80:81], v[6:7], v[40:41] op_sel:[0,0,0] op_sel_hi:[1,0,0]\n"
"v_pk_fma_f32 v[146:147], v[82:83], v[6:7], v[40:41] op_sel:[0,0,0] op_sel_hi:[1,0,0]\n"
"v_pk_fma_f32 v[148:149], v[84:85], v[6:7], v[40:41] op_sel:[0,0,0] op_sel_hi:[1,0,0]\n"
"v_pk_fma_f32 v[150:151], v[86:87], v[6:7], v[40:41] op_sel:[0,0,0] op_sel_hi:[1,0,0]\n"
"v_pk_fma_f32 v[80:81], v[80:81], v[6:7], v[42:43] op_sel:[0,0,0] op_sel_hi:[1,0,0] neg_lo:[0,1,0] neg_hi:[0,1,0]\n"
"v_pk_fma_f32 v[82:83], v[82:83], v[6:7], v[42:43] op_sel:[0,0,0] op_sel_hi:[1,0,0] neg_lo:[0,1,0] neg_hi:[0,1,0]\n"
"v_pk_fma_f32 v[84:85], v[84:85], v[6:7], v[42:43] op_sel:[0,0,0] op_sel_hi:[1,0,0] neg_lo:[0,1,0] neg_hi:[0,1,0]\n"
"v_pk_fma_f32 v[86:87], v[86:87], v[6:7], v[42:43] op_sel:[0,0,0] op_sel_hi:[1,0,0] neg_lo:[0,1,0] neg_hi:[0,1,0]\n"
"v_pk_fma_f32 v[152:153], v[88:89], v[6:7], v[44:45] op_sel:[0,1,0] op_sel_hi:[1,1,0]\n"
"v_pk_fma_f32 v[154:155], v[90:91], v[6:7], v[44:45] op_sel:[0,1,0] op_sel_hi:[1,1,0]\n"
"v_pk_fma_f32 v[156:157], v[92:93], v[6:7], v[44:45] op_sel:[0,1,0] op_sel_hi:[1,1,0]\n"
"v_pk_fma_f32 v[158:159], v[94:95], v[6:7], v[44:45] op_sel:[0,1,0] op_sel_hi:[1,1,0]\n"
"v_pk_fma_f32 v[88:89], v[88:89], v[6:7], v[46:47] op_sel:[0,1,0] op_sel_hi:[1,1,0] neg_lo:[0,1,0] neg_hi:[0,1,0]\n"
"v_pk_fma_f32 v[90:91], v[90:91], v[6:7], v[46:47] op_sel:[0,1,0] op_sel_hi:[1,1,0] neg_lo:[0,1,0] neg_hi:[0,1,0]\n"
"v_pk_fma_f32 v[92:93], v[92:93], v[6:7], v[46:47] op_sel:[0,1,0] op_sel_hi:[1,1,0] neg_lo:[0,1,0] neg_hi:[0,1,0]\n"
"v_pk_fma_f32 v[94:95], v[94:95], v[6:7], v[46:47] op_sel:[0,1,0] op_sel_hi:[1,1,0] neg_lo:[0,1,0] neg_hi:[0,1,0]\n"
"v_pk_add_f32 v[8:9], v[128:129], v[130:131]\n"
"v_pk_add_f32 v[10:11], v[64:65], v[66:67]\n"
"v_mfma_f32_16x16x32_bf16 a[100:103], a[24:27], v[112:115], 0\n"
"v_pk_add_f32 v[12:13], v[136:137], v[138:139]\n"
"v_pk_add_f32 v[14:15], v[72:73], v[74:75]\n"
"v_pk_add_f32 v[8:9], v[8:9], v[132:133]\n"
"v_pk_add_f32 v[10:11], v[10:11], v[68:69]\n"
"v_pk_add_f32 v[12:13], v[12:13], v[140:141]\n"
"v_pk_add_f32 v[14:15], v[14:15], v[76:77]\n"
"v_pk_add_f32 v[8:9], v[8:9], v[134:135]\n"
"v_mfma_f32_16x16x32_bf16 a[112:115], a[24:27], a[56:59], a[112:115]\n"
"v_pk_add_f32 v[10:11], v[10:11], v[70:71]\n"
"v_pk_add_f32 v[12:13], v[12:13], v[142:143]\n"
"v_pk_add_f32 v[14:15], v[14:15], v[78:79]\n"
"v_add_f32 v32, v8, v9\n"
"v_add_f32 v34, v10, v11\n"
"v_add_f32 v36, v12, v13\n"
"v_mfma_f32_16x16x32_bf16 a[104:107], a[28:31], v[96:99], 0\n"
"v_add_f32 v38, v14, v15\n"
"v_permlane32_swap_b32 v32, v34\n"
"v_exp_f32 v144, v144\n"
"v_permlane32_swap_b32 v36, v38\n"
"v_exp_f32 v145, v145\n"
"v_add_f32 v32, v32, v34\n"
"v_mfma_f32_16x16x32_bf16 a[108:111], a[28:31], v[112:115], 0\n"
"v_exp_f32 v146, v146\n"
"v_add_f32 v36, v36, v38\n"
"v_exp_f32 v147, v147\n"
"s_nop 0\n"
"v_permlane16_swap_b32 v32, v36\n"
"v_exp_f32 v148, v148\n"
"v_add_f32 v32, v32, v36\n"
"v_mfma_f32_16x16x32_bf16 a[112:115], a[28:31], a[60:63], a[112:115]\n"
"v_exp_f32 v149, v149\n"
"v_rcp_f32 v32, v32\n"
"v_exp_f32 v150, v150\n"
"v_mov_b32 v36, v32\n"
"v_exp_f32 v151, v151\n"
"s_nop 0\n"
"v_permlane16_swap_b32 v32, v36\n"
"v_exp_f32 v80, v80\n"
"v_mfma_f32_16x16x32_bf16 a[80:83], a[32:35], v[100:103], a[80:83]\n"
"v_mov_b32 v34, v32\n"
"v_exp_f32 v81, v81\n"
"v_mov_b32 v38, v36\n"
"v_exp_f32 v82, v82\n"
"v_permlane32_swap_b32 v32, v34\n"
"v_exp_f32 v83, v83\n"
"v_mfma_f32_16x16x32_bf16 a[84:87], a[32:35], v[116:119], a[84:87]\n"
"v_permlane32_swap_b32 v36, v38\n"
"v_exp_f32 v84, v84\n"
"v_exp_f32 v85, v85\n"
"v_exp_f32 v86, v86\n"
"v_exp_f32 v87, v87\n"
"v_exp_f32 v152, v152\n"
"v_mfma_f32_16x16x32_bf16 a[112:115], a[32:35], a[48:51], a[112:115]\n"
"v_exp_f32 v153, v153\n"
"v_exp_f32 v154, v154\n"
"v_exp_f32 v155, v155\n"
"v_exp_f32 v156, v156\n"
"v_exp_f32 v157, v157\n"
"v_exp_f32 v158, v158\n"
"v_mfma_f32_16x16x32_bf16 a[88:91], a[36:39], v[100:103], a[88:91]\n"
"v_exp_f32 v159, v159\n"
"v_exp_f32 v88, v88\n"
"v_exp_f32 v89, v89\n"
"v_exp_f32 v90, v90\n"
"v_exp_f32 v91, v91\n"
"v_exp_f32 v92, v92\n"
"v_exp_f32 v93, v93\n"
"v_mfma_f32_16x16x32_bf16 a[92:95], a[36:39], v[116:119], a[92:95]\n"
"v_exp_f32 v94, v94\n"
"v_exp_f32 v95, v95\n"
"v_pk_add_f32 v[8:9], v[144:145], v[146:147]\n"
"v_pk_add_f32 v[10:11], v[80:81], v[82:83]\n"
"v_pk_add_f32 v[12:13], v[152:153], v[154:155]\n"
"v_pk_add_f32 v[14:15], v[88:89], v[90:91]\n"
"v_mfma_f32_16x16x32_bf16 a[112:115], a[36:39], a[52:55], a[112:115]\n"
"v_pk_add_f32 v[8:9], v[8:9], v[148:149]\n"
"v_pk_add_f32 v[10:11], v[10:11], v[84:85]\n"
"v_pk_add_f32 v[12:13], v[12:13], v[156:157]\n"
"v_pk_add_f32 v[14:15], v[14:15], v[92:93]\n"
"v_pk_add_f32 v[8:9], v[8:9], v[150:151]\n"
"v_pk_add_f32 v[10:11], v[10:11], v[86:87]\n"
"v_mfma_f32_16x16x32_bf16 a[96:99], a[40:43], v[100:103], a[96:99]\n"
"v_pk_add_f32 v[12:13], v[12:13], v[158:159]\n"
"v_pk_add_f32 v[14:15], v[14:15], v[94:95]\n"
"v_add_f32 v40, v8, v9\n"
"v_add_f32 v42, v10, v11\n"
"v_add_f32 v44, v12, v13\n"
"v_add_f32 v46, v14, v15\n"
"v_mfma_f32_16x16x32_bf16 a[100:103], a[40:43], v[116:119], a[100:103]\n"
"v_pk_mul_f32 v[128:129], v[128:129], v[32:33] op_sel_hi:[1,0]\n"
"v_permlane32_swap_b32 v40, v42\n"
"v_pk_mul_f32 v[130:131], v[130:131], v[32:33] op_sel_hi:[1,0]\n"
"v_permlane32_swap_b32 v44, v46\n"
"v_pk_mul_f32 v[132:133], v[132:133], v[32:33] op_sel_hi:[1,0]\n"
"v_add_f32 v40, v40, v42\n"
"v_pk_mul_f32 v[134:135], v[134:135], v[32:33] op_sel_hi:[1,0]\n"
"v_add_f32 v44, v44, v46\n"
"v_cvt_pk_bf16_f32 v128, v128, v129\n"
"s_nop 0\n"
"v_permlane16_swap_b32 v40, v44\n"
"v_cvt_pk_bf16_f32 v129, v130, v131\n"
"v_add_f32 v40, v40, v44\n"
"v_cvt_pk_bf16_f32 v130, v132, v133\n"
"v_rcp_f32 v40, v40\n"
"v_cvt_pk_bf16_f32 v131, v134, v135\n"
"v_mfma_f32_16x16x32_bf16 a[112:115], a[40:43], a[56:59], a[112:115]\n"
"v_mov_b32 v44, v40\n"
"v_pk_mul_f32 v[64:65], v[64:65], v[34:35] op_sel_hi:[1,0]\n"
"s_nop 0\n"
"v_permlane16_swap_b32 v40, v44\n"
"v_pk_mul_f32 v[66:67], v[66:67], v[34:35] op_sel_hi:[1,0]\n"
"v_mov_b32 v42, v40\n"
"v_pk_mul_f32 v[68:69], v[68:69], v[34:35] op_sel_hi:[1,0]\n"
"v_mov_b32 v46, v44\n"
"v_pk_mul_f32 v[70:71], v[70:71], v[34:35] op_sel_hi:[1,0]\n"
"v_permlane32_swap_b32 v40, v42\n"
"v_cvt_pk_bf16_f32 v64, v64, v65\n"
"v_permlane32_swap_b32 v44, v46\n"
"v_cvt_pk_bf16_f32 v65, v66, v67\n"
"v_cvt_pk_bf16_f32 v66, v68, v69\n"
"v_cvt_pk_bf16_f32 v67, v70, v71\n"
"v_mfma_f32_16x16x32_bf16 a[104:107], a[44:47], v[100:103], a[104:107]\n"
"v_pk_mul_f32 v[136:137], v[136:137], v[36:37] op_sel_hi:[1,0]\n"
"v_pk_mul_f32 v[138:139], v[138:139], v[36:37] op_sel_hi:[1,0]\n"
"v_pk_mul_f32 v[140:141], v[140:141], v[36:37] op_sel_hi:[1,0]\n"
"v_pk_mul_f32 v[142:143], v[142:143], v[36:37] op_sel_hi:[1,0]\n"
"v_cvt_pk_bf16_f32 v136, v136, v137\n"
"v_cvt_pk_bf16_f32 v137, v138, v139\n"
"v_cvt_pk_bf16_f32 v138, v140, v141\n"
"v_cvt_pk_bf16_f32 v139, v142, v143\n"
"v_pk_mul_f32 v[72:73], v[72:73], v[38:39] op_sel_hi:[1,0]\n"
"v_pk_mul_f32 v[74:75], v[74:75], v[38:39] op_sel_hi:[1,0]\n"
"v_pk_mul_f32 v[76:77], v[76:77], v[38:39] op_sel_hi:[1,0]\n"
"v_pk_mul_f32 v[78:79], v[78:79], v[38:39] op_sel_hi:[1,0]\n"
"v_cvt_pk_bf16_f32 v72, v72, v73\n"
"v_cvt_pk_bf16_f32 v73, v74, v75\n"
"v_cvt_pk_bf16_f32 v74, v76, v77\n"
"v_cvt_pk_bf16_f32 v75, v78, v79\n"
"v_mfma_f32_16x16x32_bf16 a[108:111], a[44:47], v[116:119], a[108:111]\n"
"v_pk_mul_f32 v[144:145], v[144:145], v[40:41] op_sel_hi:[1,0]\n"
"v_pk_mul_f32 v[146:147], v[146:147], v[40:41] op_sel_hi:[1,0]\n"
"v_pk_mul_f32 v[148:149], v[148:149], v[40:41] op_sel_hi:[1,0]\n"
"v_pk_mul_f32 v[150:151], v[150:151], v[40:41] op_sel_hi:[1,0]\n"
"v_cvt_pk_bf16_f32 v144, v144, v145\n"
"v_cvt_pk_bf16_f32 v145, v146, v147\n"
"v_cvt_pk_bf16_f32 v146, v148, v149\n"
"v_cvt_pk_bf16_f32 v147, v150, v151\n"
"v_pk_mul_f32 v[80:81], v[80:81], v[42:43] op_sel_hi:[1,0]\n"
"v_pk_mul_f32 v[82:83], v[82:83], v[42:43] op_sel_hi:[1,0]\n"
"v_pk_mul_f32 v[84:85], v[84:85], v[42:43] op_sel_hi:[1,0]\n"
"v_pk_mul_f32 v[86:87], v[86:87], v[42:43] op_sel_hi:[1,0]\n"
"v_cvt_pk_bf16_f32 v80, v80, v81\n"
"v_cvt_pk_bf16_f32 v81, v82, v83\n"
"v_cvt_pk_bf16_f32 v82, v84, v85\n"
"v_cvt_pk_bf16_f32 v83, v86, v87\n"
"v_mfma_f32_16x16x32_bf16 a[112:115], a[44:47], a[60:63], a[112:115]\n"
"v_pk_mul_f32 v[152:153], v[152:153], v[44:45] op_sel_hi:[1,0]\n"
"v_pk_mul_f32 v[154:155], v[154:155], v[44:45] op_sel_hi:[1,0]\n"
"v_pk_mul_f32 v[156:157], v[156:157], v[44:45] op_sel_hi:[1,0]\n"
"v_pk_mul_f32 v[158:159], v[158:159], v[44:45] op_sel_hi:[1,0]\n"
"v_cvt_pk_bf16_f32 v152, v152, v153\n"
"v_cvt_pk_bf16_f32 v153, v154, v155\n"
"v_cvt_pk_bf16_f32 v154, v156, v157\n"
"v_cvt_pk_bf16_f32 v155, v158, v159\n"
"v_pk_mul_f32 v[88:89], v[88:89], v[46:47] op_sel_hi:[1,0]\n"
"v_pk_mul_f32 v[90:91], v[90:91], v[46:47] op_sel_hi:[1,0]\n"
"v_pk_mul_f32 v[92:93], v[92:93], v[46:47] op_sel_hi:[1,0]\n"
"v_pk_mul_f32 v[94:95], v[94:95], v[46:47] op_sel_hi:[1,0]\n"
"v_cvt_pk_bf16_f32 v88, v88, v89\n"
"v_cvt_pk_bf16_f32 v89, v90, v91\n"
"v_cvt_pk_bf16_f32 v90, v92, v93\n"
"v_cvt_pk_bf16_f32 v91, v94, v95\n"
"ds_write_b128 v17, v[128:131] offset:0\n"
"ds_write_b128 v29, v[64:67] offset:0\n"
"ds_write_b128 v17, v[136:139] offset:2048\n"
"ds_write_b128 v29, v[72:75] offset:2048\n"
"ds_write_b128 v17, v[144:147] offset:16384\n"
"ds_write_b128 v29, v[80:83] offset:16384\n"
"ds_write_b128 v17, v[152:155] offset:18432\n"
"ds_write_b128 v29, v[88:91] offset:18432\n"
"ds_read_b64_tr_b16 a[16:17], v18 offset:0\n"
"ds_read_b64_tr_b16 a[18:19], v18 offset:2048\n"
"ds_read_b64_tr_b16 a[20:21], v18 offset:8\n"
"ds_read_b64_tr_b16 a[22:23], v18 offset:2056\n"
"ds_read_b64_tr_b16 a[24:25], v30 offset:0\n"
"ds_read_b64_tr_b16 a[26:27], v30 offset:2048\n"
"ds_read_b64_tr_b16 a[28:29], v30 offset:8\n"
"ds_read_b64_tr_b16 a[30:31], v30 offset:2056\n"
"ds_read_b64_tr_b16 a[32:33], v18 offset:16384\n"
"ds_read_b64_tr_b16 a[34:35], v18 offset:18432\n"
"ds_read_b64_tr_b16 a[36:37], v18 offset:16392\n"
"ds_read_b64_tr_b16 a[38:39], v18 offset:18440\n"
"ds_read_b64_tr_b16 a[40:41], v30 offset:16384\n"
"ds_read_b64_tr_b16 a[42:43], v30 offset:18432\n"
"ds_read_b64_tr_b16 a[44:45], v30 offset:16392\n"
"ds_read_b64_tr_b16 a[46:47], v30 offset:18440\n"
"v_xor_b32 v4, 64, v23\n"
"v_xor_b32 v5, 8, v24\n"
"v_xor_b32 v6, 64, v24\n"
"v_xor_b32 v7, 0x48, v24\n"
"v_lshrrev_b32 v8, 1, v0\n"
"v_and_b32 v9, 0x78, v8\n"
"v_and_b32 v10, 0xf8, v8\n"
"v_and_b32 v8, 0x200, v0\n"
"v_lshrrev_b32 v11, 4, v8\n"
"v_xor_b32 v11, v11, v23\n"
"v_lshl_add_u32 v11, v8, 2, v11\n"
"v_xor_b32 v12, 32, v11\n"
"v_xor_b32 v13, 64, v11\n"
"v_xor_b32 v1, 0x60, v11\n"
"v_mov_b32 v2, s90\n"
"v_mov_b32 v3, s91\n"
"v_and_b32 v44, 0xf0, v0\n"
"v_lshrrev_b32 v45, 8, v0\n"
"v_lshlrev_b32 v45, 4, v45\n"
"v_lshl_add_u32 v44, v44, 2, v45\n"
"s_add_u32 s95, s70, 4096\n"
"s_add_u32 s96, s70, 8192\n"
"v_accvgpr_read_b32 v46, a240\n"
"v_accvgpr_read_b32 v47, a241\n"
"v_pk_mul_f32 v[160:161], v[160:161], v[46:47] op_sel_hi:[1,0] neg_lo:[0,1] neg_hi:[0,1]\n"
"v_pk_mul_f32 v[162:163], v[162:163], v[46:47] op_sel_hi:[1,0] neg_lo:[0,1] neg_hi:[0,1]\n"
"v_pk_mul_f32 v[192:193], v[192:193], v[46:47] op_sel_hi:[1,0] neg_lo:[0,1] neg_hi:[0,1]\n"
"v_pk_mul_f32 v[194:195], v[194:195], v[46:47] op_sel_hi:[1,0] neg_lo:[0,1] neg_hi:[0,1]\n"
"v_pk_mul_f32 v[224:225], v[224:225], v[46:47] op_sel_hi:[1,0] neg_lo:[0,1] neg_hi:[0,1]\n"
"v_pk_mul_f32 v[226:227], v[226:227], v[46:47] op_sel_hi:[1,0] neg_lo:[0,1] neg_hi:[0,1]\n"
"v_exp_f32 v160, v160\n"
"v_exp_f32 v161, v161\n"
"v_exp_f32 v162, v162\n"
"v_exp_f32 v163, v163\n"
"v_exp_f32 v192, v192\n"
"v_exp_f32 v193, v193\n"
"v_exp_f32 v194, v194\n"
"v_exp_f32 v195, v195\n"
"v_exp_f32 v224, v224\n"
"v_exp_f32 v225, v225\n"
"s_waitcnt lgkmcnt(0)\n"
"v_exp_f32 v226, v226\n"
"v_exp_f32 v227, v227\n"
"v_mfma_f32_16x16x32_bf16 a[80:83], a[16:19], v[104:107], a[80:83]\n"
"v_accvgpr_read_b32 v32, a144\n"
"v_mfma_f32_16x16x32_bf16 a[84:87], a[16:19], v[120:123], a[84:87]\n"
"v_accvgpr_read_b32 v33, a145\n"
"v_mfma_f32_16x16x32_bf16 a[112:115], a[16:19], a[48:51], a[112:115]\n"
"v_accvgpr_read_b32 v34, a146\n"
"v_accvgpr_read_b32 v35, a147\n"
"v_mfma_f32_16x16x32_bf16 a[88:91], a[20:23], v[104:107], a[88:91]\n"
"v_accvgpr_read_b32 v36, a176\n"
"v_mfma_f32_16x16x32_bf16 a[92:95], a[20:23], v[120:123], a[92:95]\n"
"v_accvgpr_read_b32 v37, a177\n"
"v_mfma_f32_16x16x32_bf16 a[112:115], a[20:23], a[52:55], a[112:115]\n"
"v_accvgpr_read_b32 v38, a178\n"
"v_accvgpr_read_b32 v39, a179\n"
"v_mfma_f32_16x16x32_bf16 a[96:99], a[24:27], v[104:107], a[96:99]\n"
"v_accvgpr_read_b32 v40, a208\n"
"v_mfma_f32_16x16x32_bf16 a[100:103], a[24:27], v[120:123], a[100:103]\n"
"v_accvgpr_read_b32 v41, a209\n"
"v_mfma_f32_16x16x32_bf16 a[112:115], a[24:27], a[56:59], a[112:115]\n"
"v_accvgpr_read_b32 v42, a210\n"
"v_accvgpr_read_b32 v43, a211\n"
"v_mfma_f32_16x16x32_bf16 a[104:107], a[28:31], v[104:107], a[104:107]\n"
"v_pk_fma_f32 v[160:161], v[160:161], v[46:47], v[46:47] op_sel:[0,1,1] op_sel_hi:[1,1,1]\n"
"v_pk_fma_f32 v[162:163], v[162:163], v[46:47], v[46:47] op_sel:[0,1,1] op_sel_hi:[1,1,1]\n"
"v_pk_fma_f32 v[192:193], v[192:193], v[46:47], v[46:47] op_sel:[0,1,1] op_sel_hi:[1,1,1]\n"
"v_pk_fma_f32 v[194:195], v[194:195], v[46:47], v[46:47] op_sel:[0,1,1] op_sel_hi:[1,1,1]\n"
"v_pk_fma_f32 v[224:225], v[224:225], v[46:47], v[46:47] op_sel:[0,1,1] op_sel_hi:[1,1,1]\n"
"v_pk_fma_f32 v[226:227], v[226:227], v[46:47], v[46:47] op_sel:[0,1,1] op_sel_hi:[1,1,1]\n"
"v_rcp_f32 v160, v160\n"
"v_rcp_f32 v161, v161\n"
"v_mfma_f32_16x16x32_bf16 a[108:111], a[28:31], v[120:123], a[108:111]\n"
"v_rcp_f32 v162, v162\n"
"v_mfma_f32_16x16x32_bf16 a[112:115], a[28:31], a[60:63], a[112:115]\n"
"v_rcp_f32 v163, v163\n"
"v_rcp_f32 v192, v192\n"
"v_mfma_f32_16x16x32_bf16 a[80:83], a[32:35], v[108:111], a[80:83]\n"
"v_rcp_f32 v193, v193\n"
"v_mfma_f32_16x16x32_bf16 a[84:87], a[32:35], v[124:127], a[84:87]\n"
"v_rcp_f32 v194, v194\n"
"v_mfma_f32_16x16x32_bf16 a[112:115], a[32:35], a[48:51], a[112:115]\n"
"v_rcp_f32 v195, v195\n"
"v_rcp_f32 v224, v224\n"
"v_mfma_f32_16x16x32_bf16 a[88:91], a[36:39], v[108:111], a[88:91]\n"
"v_rcp_f32 v225, v225\n"
"v_mfma_f32_16x16x32_bf16 a[92:95], a[36:39], v[124:127], a[92:95]\n"
"v_rcp_f32 v226, v226\n"
"v_mfma_f32_16x16x32_bf16 a[112:115], a[36:39], a[52:55], a[112:115]\n"
"v_rcp_f32 v227, v227\n"
"v_pk_mul_f32 v[160:161], v[160:161], v[32:33]\n"
"v_mfma_f32_16x16x32_bf16 a[96:99], a[40:43], v[108:111], a[96:99]\n"
"v_pk_mul_f32 v[162:163], v[162:163], v[34:35]\n"
"v_mfma_f32_16x16x32_bf16 a[100:103], a[40:43], v[124:127], a[100:103]\n"
"v_pk_mul_f32 v[192:193], v[192:193], v[36:37]\n"
"v_mfma_f32_16x16x32_bf16 a[112:115], a[40:43], a[56:59], a[112:115]\n"
"v_pk_mul_f32 v[194:195], v[194:195], v[38:39]\n"
"v_pk_mul_f32 v[224:225], v[224:225], v[40:41]\n"
"v_mfma_f32_16x16x32_bf16 a[104:107], a[44:47], v[108:111], a[104:107]\n"
"v_pk_mul_f32 v[226:227], v[226:227], v[42:43]\n"
"v_mfma_f32_16x16x32_bf16 a[108:111], a[44:47], v[124:127], a[108:111]\n"
"v_cvt_pk_bf16_f32 v160, v160, v161\n"
"v_mfma_f32_16x16x32_bf16 a[112:115], a[44:47], a[60:63], a[112:115]\n"
"v_cvt_pk_bf16_f32 v161, v162, v163\n"
"v_cvt_pk_bf16_f32 v162, v192, v193\n"
"v_cvt_pk_bf16_f32 v163, v194, v195\n"
"v_cvt_pk_bf16_f32 v224, v224, v225\n"
"v_cvt_pk_bf16_f32 v225, v226, v227\n"
"v_accvgpr_read_b32 v48, a242\n"
"v_accvgpr_read_b32 v49, a243\n"
"v_pk_mul_f32 v[164:165], v[164:165], v[48:49] op_sel_hi:[1,0] neg_lo:[0,1] neg_hi:[0,1]\n"
"v_pk_mul_f32 v[166:167], v[166:167], v[48:49] op_sel_hi:[1,0] neg_lo:[0,1] neg_hi:[0,1]\n"
"v_pk_mul_f32 v[196:197], v[196:197], v[48:49] op_sel_hi:[1,0] neg_lo:[0,1] neg_hi:[0,1]\n"
"v_pk_mul_f32 v[198:199], v[198:199], v[48:49] op_sel_hi:[1,0] neg_lo:[0,1] neg_hi:[0,1]\n"
"v_pk_mul_f32 v[228:229], v[228:229], v[48:49] op_sel_hi:[1,0] neg_lo:[0,1] neg_hi:[0,1]\n"
"v_pk_mul_f32 v[230:231], v[230:231], v[48:49] op_sel_hi:[1,0] neg_lo:[0,1] neg_hi:[0,1]\n"
"v_exp_f32 v164, v164\n"
"buffer_store_dwordx4 a[80:83], v0, s[20:23], s70 offen offset:0 sc1\n"
"v_exp_f32 v165, v165\n"
"v_exp_f32 v166, v166\n"
"v_exp_f32 v167, v167\n"
"v_exp_f32 v196, v196\n"
"v_exp_f32 v197, v197\n"
"v_exp_f32 v198, v198\n"
"buffer_store_dwordx4 a[84:87], v0, s[20:23], s70 offen offset:1024 sc1\n"
"v_exp_f32 v199, v199\n"
"v_exp_f32 v228, v228\n"
"v_exp_f32 v229, v229\n"
"v_exp_f32 v230, v230\n"
"v_exp_f32 v231, v231\n"
"v_accvgpr_read_b32 v32, a148\n"
"buffer_store_dwordx4 a[88:91], v0, s[20:23], s70 offen offset:2048 sc1\n"
"v_accvgpr_read_b32 v33, a149\n"
"v_accvgpr_read_b32 v34, a150\n"
"v_accvgpr_read_b32 v35, a151\n"
"v_accvgpr_read_b32 v36, a180\n"
"v_accvgpr_read_b32 v37, a181\n"
"v_accvgpr_read_b32 v38, a182\n"
"buffer_store_dwordx4 a[92:95], v0, s[20:23], s70 offen offset:3072 sc1\n"
"v_accvgpr_read_b32 v39, a183\n"
"v_accvgpr_read_b32 v40, a212\n"
"v_accvgpr_read_b32 v41, a213\n"
"v_accvgpr_read_b32 v42, a214\n"
"v_accvgpr_read_b32 v43, a215\n"
"v_pk_fma_f32 v[164:165], v[164:165], v[48:49], v[48:49] op_sel:[0,1,1] op_sel_hi:[1,1,1]\n"
"buffer_store_dwordx4 a[96:99], v0, s[20:23], s95 offen offset:0 sc1\n"
"v_pk_fma_f32 v[166:167], v[166:167], v[48:49], v[48:49] op_sel:[0,1,1] op_sel_hi:[1,1,1]\n"
"v_pk_fma_f32 v[196:197], v[196:197], v[48:49], v[48:49] op_sel:[0,1,1] op_sel_hi:[1,1,1]\n"
"v_pk_fma_f32 v[198:199], v[198:199], v[48:49], v[48:49] op_sel:[0,1,1] op_sel_hi:[1,1,1]\n"
"v_pk_fma_f32 v[228:229], v[228:229], v[48:49], v[48:49] op_sel:[0,1,1] op_sel_hi:[1,1,1]\n"
"v_pk_fma_f32 v[230:231], v[230:231], v[48:49], v[48:49] op_sel:[0,1,1] op_sel_hi:[1,1,1]\n"
"v_rcp_f32 v164, v164\n"
"buffer_store_dwordx4 a[100:103], v0, s[20:23], s95 offen offset:1024 sc1\n"
"v_rcp_f32 v165, v165\n"
"v_rcp_f32 v166, v166\n"
"v_rcp_f32 v167, v167\n"
"v_rcp_f32 v196, v196\n"
"v_rcp_f32 v197, v197\n"
"v_rcp_f32 v198, v198\n"
"buffer_store_dwordx4 a[104:107], v0, s[20:23], s95 offen offset:2048 sc1\n"
"v_rcp_f32 v199, v199\n"
"v_rcp_f32 v228, v228\n"
"v_rcp_f32 v229, v229\n"
"v_rcp_f32 v230, v230\n"
"v_rcp_f32 v231, v231\n"
"v_pk_mul_f32 v[164:165], v[164:165], v[32:33]\n"
"buffer_store_dwordx4 a[108:111], v0, s[20:23], s95 offen offset:3072 sc1\n"
"v_pk_mul_f32 v[166:167], v[166:167], v[34:35]\n"
"v_pk_mul_f32 v[196:197], v[196:197], v[36:37]\n"
"v_pk_mul_f32 v[198:199], v[198:199], v[38:39]\n"
"v_pk_mul_f32 v[228:229], v[228:229], v[40:41]\n"
"v_pk_mul_f32 v[230:231], v[230:231], v[42:43]\n"
"v_cvt_pk_bf16_f32 v164, v164, v165\n"
"s_mov_b32 exec_lo, 0xf000f\n"
"s_mov_b32 exec_hi, 0xf000f\n"
"buffer_store_dwordx4 a[112:115], v44, s[20:23], s96 offen sc1\n"
"s_mov_b64 exec, -1\n"
"v_cvt_pk_bf16_f32 v165, v166, v167\n"
"v_cvt_pk_bf16_f32 v166, v196, v197\n"
"v_cvt_pk_bf16_f32 v167, v198, v199\n"
"v_cvt_pk_bf16_f32 v226, v228, v229\n"
"v_cvt_pk_bf16_f32 v227, v230, v231\n"
"s_nop 0\n"
"v_permlane16_swap_b32 v224, v226\n"
"v_permlane16_swap_b32 v225, v227\n"
"s_mov_b32 s94, 0\n"
"buffer_store_dwordx4 v[160:163], v19, s[20:23], s94 offen offset:0 sc1\n"
"v_accvgpr_read_b32 v46, a244\n"
"v_accvgpr_read_b32 v47, a245\n"
"v_pk_mul_f32 v[168:169], v[168:169], v[46:47] op_sel_hi:[1,0] neg_lo:[0,1] neg_hi:[0,1]\n"
"v_pk_mul_f32 v[170:171], v[170:171], v[46:47] op_sel_hi:[1,0] neg_lo:[0,1] neg_hi:[0,1]\n"
"v_pk_mul_f32 v[200:201], v[200:201], v[46:47] op_sel_hi:[1,0] neg_lo:[0,1] neg_hi:[0,1]\n"
"v_pk_mul_f32 v[202:203], v[202:203], v[46:47] op_sel_hi:[1,0] neg_lo:[0,1] neg_hi:[0,1]\n"
"v_pk_mul_f32 v[232:233], v[232:233], v[46:47] op_sel_hi:[1,0] neg_lo:[0,1] neg_hi:[0,1]\n"
"v_pk_mul_f32 v[234:235], v[234:235], v[46:47] op_sel_hi:[1,0] neg_lo:[0,1] neg_hi:[0,1]\n"
"v_exp_f32 v168, v168\n"
"v_exp_f32 v169, v169\n"
"v_exp_f32 v170, v170\n"
"v_exp_f32 v171, v171\n"
"v_exp_f32 v200, v200\n"
"v_exp_f32 v201, v201\n"
"v_exp_f32 v202, v202\n"
"v_exp_f32 v203, v203\n"
"v_exp_f32 v232, v232\n"
"v_exp_f32 v233, v233\n"
"v_exp_f32 v234, v234\n"
"v_exp_f32 v235, v235\n"
"s_mov_b32 s94, 0\n"
"buffer_store_dwordx4 v[164:167], v19, s[20:23], s94 offen offset:2048 sc1\n"
"v_accvgpr_read_b32 v32, a152\n"
"v_accvgpr_read_b32 v33, a153\n"
"v_accvgpr_read_b32 v34, a154\n"
"v_accvgpr_read_b32 v35, a155\n"
"v_accvgpr_read_b32 v36, a184\n"
"v_accvgpr_read_b32 v37, a185\n"
"v_accvgpr_read_b32 v38, a186\n"
"v_accvgpr_read_b32 v39, a187\n"
"v_accvgpr_read_b32 v40, a216\n"
"v_accvgpr_read_b32 v41, a217\n"
"v_accvgpr_read_b32 v42, a218\n"
"v_accvgpr_read_b32 v43, a219\n"
"v_pk_fma_f32 v[168:169], v[168:169], v[46:47], v[46:47] op_sel:[0,1,1] op_sel_hi:[1,1,1]\n"
"v_pk_fma_f32 v[170:171], v[170:171], v[46:47], v[46:47] op_sel:[0,1,1] op_sel_hi:[1,1,1]\n"
"v_pk_fma_f32 v[200:201], v[200:201], v[46:47], v[46:47] op_sel:[0,1,1] op_sel_hi:[1,1,1]\n"
"v_pk_fma_f32 v[202:203], v[202:203], v[46:47], v[46:47] op_sel:[0,1,1] op_sel_hi:[1,1,1]\n"
"v_pk_fma_f32 v[232:233], v[232:233], v[46:47], v[46:47] op_sel:[0,1,1] op_sel_hi:[1,1,1]\n"
"v_pk_fma_f32 v[234:235], v[234:235], v[46:47], v[46:47] op_sel:[0,1,1] op_sel_hi:[1,1,1]\n"
"v_rcp_f32 v168, v168\n"
"v_rcp_f32 v169, v169\n"
"v_rcp_f32 v170, v170\n"
"buffer_store_dwordx4 v[224:227], v20, s[20:23], s94 offen offset:0 sc1\n"
"v_rcp_f32 v171, v171\n"
"v_rcp_f32 v200, v200\n"
"v_rcp_f32 v201, v201\n"
"v_rcp_f32 v202, v202\n"
"v_rcp_f32 v203, v203\n"
"v_rcp_f32 v232, v232\n"
"v_rcp_f32 v233, v233\n"
"v_rcp_f32 v234, v234\n"
"v_rcp_f32 v235, v235\n"
"v_pk_mul_f32 v[168:169], v[168:169], v[32:33]\n"
"v_pk_mul_f32 v[170:171], v[170:171], v[34:35]\n"
"v_pk_mul_f32 v[200:201], v[200:201], v[36:37]\n"
"v_pk_mul_f32 v[202:203], v[202:203], v[38:39]\n"
"v_pk_mul_f32 v[232:233], v[232:233], v[40:41]\n"
"v_pk_mul_f32 v[234:235], v[234:235], v[42:43]\n"
"v_cvt_pk_bf16_f32 v168, v168, v169\n"
"v_cvt_pk_bf16_f32 v169, v170, v171\n"
"v_cvt_pk_bf16_f32 v170, v200, v201\n"
"v_cvt_pk_bf16_f32 v171, v202, v203\n"
"v_cvt_pk_bf16_f32 v232, v232, v233\n"
"v_cvt_pk_bf16_f32 v233, v234, v235\n"
"s_mov_b32 s94, 4096\n"
"buffer_store_dwordx4 v[168:171], v19, s[20:23], s94 offen offset:0 sc1\n"
"v_accvgpr_read_b32 v48, a246\n"
"v_accvgpr_read_b32 v49, a247\n"
"v_pk_mul_f32 v[172:173], v[172:173], v[48:49] op_sel_hi:[1,0] neg_lo:[0,1] neg_hi:[0,1]\n"
"v_pk_mul_f32 v[174:175], v[174:175], v[48:49] op_sel_hi:[1,0] neg_lo:[0,1] neg_hi:[0,1]\n"
"v_pk_mul_f32 v[204:205], v[204:205], v[48:49] op_sel_hi:[1,0] neg_lo:[0,1] neg_hi:[0,1]\n"
"v_pk_mul_f32 v[206:207], v[206:207], v[48:49] op_sel_hi:[1,0] neg_lo:[0,1] neg_hi:[0,1]\n"
"v_pk_mul_f32 v[236:237], v[236:237], v[48:49] op_sel_hi:[1,0] neg_lo:[0,1] neg_hi:[0,1]\n"
"v_pk_mul_f32 v[238:239], v[238:239], v[48:49] op_sel_hi:[1,0] neg_lo:[0,1] neg_hi:[0,1]\n"
"v_exp_f32 v172, v172\n"
"v_exp_f32 v173, v173\n"
"v_exp_f32 v174, v174\n"
"v_exp_f32 v175, v175\n"
"v_exp_f32 v204, v204\n"
"v_exp_f32 v205, v205\n"
"v_exp_f32 v206, v206\n"
"v_exp_f32 v207, v207\n"
"v_exp_f32 v236, v236\n"
"v_exp_f32 v237, v237\n"
"v_exp_f32 v238, v238\n"
"v_exp_f32 v239, v239\n"
"v_accvgpr_read_b32 v32, a156\n"
"v_accvgpr_read_b32 v33, a157\n"
"v_accvgpr_read_b32 v34, a158\n"
"v_accvgpr_read_b32 v35, a159\n"
"v_accvgpr_read_b32 v36, a188\n"
"v_accvgpr_read_b32 v37, a189\n"
"v_accvgpr_read_b32 v38, a190\n"
"v_accvgpr_read_b32 v39, a191\n"
"v_accvgpr_read_b32 v40, a220\n"
"v_accvgpr_read_b32 v41, a221\n"
"v_accvgpr_read_b32 v42, a222\n"
"v_accvgpr_read_b32 v43, a223\n"
"v_pk_fma_f32 v[172:173], v[172:173], v[48:49], v[48:49] op_sel:[0,1,1] op_sel_hi:[1,1,1]\n"
"v_pk_fma_f32 v[174:175], v[174:175], v[48:49], v[48:49] op_sel:[0,1,1] op_sel_hi:[1,1,1]\n"
"v_pk_fma_f32 v[204:205], v[204:205], v[48:49], v[48:49] op_sel:[0,1,1] op_sel_hi:[1,1,1]\n"
"v_pk_fma_f32 v[206:207], v[206:207], v[48:49], v[48:49] op_sel:[0,1,1] op_sel_hi:[1,1,1]\n"
"v_pk_fma_f32 v[236:237], v[236:237], v[48:49], v[48:49] op_sel:[0,1,1] op_sel_hi:[1,1,1]\n"
"v_pk_fma_f32 v[238:239], v[238:239], v[48:49], v[48:49] op_sel:[0,1,1] op_sel_hi:[1,1,1]\n"
"v_rcp_f32 v172, v172\n"
"v_rcp_f32 v173, v173\n"
"v_rcp_f32 v174, v174\n"
"v_rcp_f32 v175, v175\n"
"v_rcp_f32 v204, v204\n"
"v_rcp_f32 v205, v205\n"
"v_rcp_f32 v206, v206\n"
"v_rcp_f32 v207, v207\n"
"v_rcp_f32 v236, v236\n"
"v_rcp_f32 v237, v237\n"
"v_rcp_f32 v238, v238\n"
"v_rcp_f32 v239, v239\n"
"v_pk_mul_f32 v[172:173], v[172:173], v[32:33]\n"
"v_pk_mul_f32 v[174:175], v[174:175], v[34:35]\n"
"v_pk_mul_f32 v[204:205], v[204:205], v[36:37]\n"
"v_pk_mul_f32 v[206:207], v[206:207], v[38:39]\n"
"v_pk_mul_f32 v[236:237], v[236:237], v[40:41]\n"
"v_pk_mul_f32 v[238:239], v[238:239], v[42:43]\n"
"v_cvt_pk_bf16_f32 v172, v172, v173\n"
"v_cvt_pk_bf16_f32 v173, v174, v175\n"
"v_cvt_pk_bf16_f32 v174, v204, v205\n"
"v_cvt_pk_bf16_f32 v175, v206, v207\n"
"v_cvt_pk_bf16_f32 v234, v236, v237\n"
"v_cvt_pk_bf16_f32 v235, v238, v239\n"
"s_nop 0\n"
"v_permlane16_swap_b32 v232, v234\n"
"v_permlane16_swap_b32 v233, v235\n"
"s_waitcnt vmcnt(4)\n"
"buffer_load_dwordx2 v[14:15], v9, s[20:23], s69 offen sc1\n"
"s_mov_b64 exec, 1\n"
"buffer_store_dwordx2 v[2:3], off, s[20:23], s71 sc1\n"
"s_mov_b64 exec, -1\n"
"v_accvgpr_read_b32 v46, a248\n"
"v_accvgpr_read_b32 v47, a249\n"
"v_pk_mul_f32 v[176:177], v[176:177], v[46:47] op_sel_hi:[1,0] neg_lo:[0,1] neg_hi:[0,1]\n"
"v_pk_mul_f32 v[178:179], v[178:179], v[46:47] op_sel_hi:[1,0] neg_lo:[0,1] neg_hi:[0,1]\n"
"v_pk_mul_f32 v[208:209], v[208:209], v[46:47] op_sel_hi:[1,0] neg_lo:[0,1] neg_hi:[0,1]\n"
"v_pk_mul_f32 v[210:211], v[210:211], v[46:47] op_sel_hi:[1,0] neg_lo:[0,1] neg_hi:[0,1]\n"
"v_pk_mul_f32 v[240:241], v[240:241], v[46:47] op_sel_hi:[1,0] neg_lo:[0,1] neg_hi:[0,1]\n"
"v_pk_mul_f32 v[242:243], v[242:243], v[46:47] op_sel_hi:[1,0] neg_lo:[0,1] neg_hi:[0,1]\n"
"v_exp_f32 v176, v176\n"
"v_exp_f32 v177, v177\n"
"v_exp_f32 v178, v178\n"
"v_exp_f32 v179, v179\n"
"v_exp_f32 v208, v208\n"
"v_exp_f32 v209, v209\n"
"v_exp_f32 v210, v210\n"
"v_exp_f32 v211, v211\n"
"v_exp_f32 v240, v240\n"
"v_exp_f32 v241, v241\n"
"v_exp_f32 v242, v242\n"
"v_exp_f32 v243, v243\n"
"v_accvgpr_read_b32 v32, a160\n"
"v_accvgpr_read_b32 v33, a161\n"
"v_accvgpr_read_b32 v34, a162\n"
"v_accvgpr_read_b32 v35, a163\n"
"v_accvgpr_read_b32 v36, a192\n"
"v_accvgpr_read_b32 v37, a193\n"
"v_accvgpr_read_b32 v38, a194\n"
"v_accvgpr_read_b32 v39, a195\n"
"v_accvgpr_read_b32 v40, a224\n"
"v_accvgpr_read_b32 v41, a225\n"
"v_accvgpr_read_b32 v42, a226\n"
"v_accvgpr_read_b32 v43, a227\n"
"v_pk_fma_f32 v[176:177], v[176:177], v[46:47], v[46:47] op_sel:[0,1,1] op_sel_hi:[1,1,1]\n"
"v_pk_fma_f32 v[178:179], v[178:179], v[46:47], v[46:47] op_sel:[0,1,1] op_sel_hi:[1,1,1]\n"
"v_pk_fma_f32 v[208:209], v[208:209], v[46:47], v[46:47] op_sel:[0,1,1] op_sel_hi:[1,1,1]\n"
"v_pk_fma_f32 v[210:211], v[210:211], v[46:47], v[46:47] op_sel:[0,1,1] op_sel_hi:[1,1,1]\n"
"v_pk_fma_f32 v[240:241], v[240:241], v[46:47], v[46:47] op_sel:[0,1,1] op_sel_hi:[1,1,1]\n"
"v_pk_fma_f32 v[242:243], v[242:243], v[46:47], v[46:47] op_sel:[0,1,1] op_sel_hi:[1,1,1]\n"
"v_rcp_f32 v176, v176\n"
"v_rcp_f32 v177, v177\n"
"v_rcp_f32 v178, v178\n"
"v_rcp_f32 v179, v179\n"
"v_rcp_f32 v208, v208\n"
"v_rcp_f32 v209, v209\n"
"v_rcp_f32 v210, v210\n"
"v_rcp_f32 v211, v211\n"
"v_rcp_f32 v240, v240\n"
"v_rcp_f32 v241, v241\n"
"v_rcp_f32 v242, v242\n"
"v_rcp_f32 v243, v243\n"
"v_pk_mul_f32 v[176:177], v[176:177], v[32:33]\n"
"v_pk_mul_f32 v[178:179], v[178:179], v[34:35]\n"
"v_pk_mul_f32 v[208:209], v[208:209], v[36:37]\n"
"v_pk_mul_f32 v[210:211], v[210:211], v[38:39]\n"
"v_pk_mul_f32 v[240:241], v[240:241], v[40:41]\n"
"v_pk_mul_f32 v[242:243], v[242:243], v[42:43]\n"
"v_cvt_pk_bf16_f32 v176, v176, v177\n"
"v_cvt_pk_bf16_f32 v177, v178, v179\n"
"v_cvt_pk_bf16_f32 v178, v208, v209\n"
"v_cvt_pk_bf16_f32 v179, v210, v211\n"
"v_cvt_pk_bf16_f32 v240, v240, v241\n"
"v_cvt_pk_bf16_f32 v241, v242, v243\n"
"v_accvgpr_read_b32 v48, a250\n"
"v_accvgpr_read_b32 v49, a251\n"
"v_pk_mul_f32 v[180:181], v[180:181], v[48:49] op_sel_hi:[1,0] neg_lo:[0,1] neg_hi:[0,1]\n"
"v_pk_mul_f32 v[182:183], v[182:183], v[48:49] op_sel_hi:[1,0] neg_lo:[0,1] neg_hi:[0,1]\n"
"v_pk_mul_f32 v[212:213], v[212:213], v[48:49] op_sel_hi:[1,0] neg_lo:[0,1] neg_hi:[0,1]\n"
"v_pk_mul_f32 v[214:215], v[214:215], v[48:49] op_sel_hi:[1,0] neg_lo:[0,1] neg_hi:[0,1]\n"
"v_pk_mul_f32 v[244:245], v[244:245], v[48:49] op_sel_hi:[1,0] neg_lo:[0,1] neg_hi:[0,1]\n"
"v_pk_mul_f32 v[246:247], v[246:247], v[48:49] op_sel_hi:[1,0] neg_lo:[0,1] neg_hi:[0,1]\n"
"v_exp_f32 v180, v180\n"
"v_exp_f32 v181, v181\n"
"v_exp_f32 v182, v182\n"
"v_exp_f32 v183, v183\n"
"v_exp_f32 v212, v212\n"
"v_exp_f32 v213, v213\n"
"v_exp_f32 v214, v214\n"
"v_exp_f32 v215, v215\n"
"v_exp_f32 v244, v244\n"
"v_exp_f32 v245, v245\n"
"v_exp_f32 v246, v246\n"
"v_exp_f32 v247, v247\n"
"v_accvgpr_read_b32 v32, a164\n"
"v_accvgpr_read_b32 v33, a165\n"
"v_accvgpr_read_b32 v34, a166\n"
"v_accvgpr_read_b32 v35, a167\n"
"v_accvgpr_read_b32 v36, a196\n"
"v_accvgpr_read_b32 v37, a197\n"
"v_accvgpr_read_b32 v38, a198\n"
"v_accvgpr_read_b32 v39, a199\n"
"v_accvgpr_read_b32 v40, a228\n"
"v_accvgpr_read_b32 v41, a229\n"
"v_accvgpr_read_b32 v42, a230\n"
"v_accvgpr_read_b32 v43, a231\n"
"v_pk_fma_f32 v[180:181], v[180:181], v[48:49], v[48:49] op_sel:[0,1,1] op_sel_hi:[1,1,1]\n"
"v_pk_fma_f32 v[182:183], v[182:183], v[48:49], v[48:49] op_sel:[0,1,1] op_sel_hi:[1,1,1]\n"
"v_pk_fma_f32 v[212:213], v[212:213], v[48:49], v[48:49] op_sel:[0,1,1] op_sel_hi:[1,1,1]\n"
"v_pk_fma_f32 v[214:215], v[214:215], v[48:49], v[48:49] op_sel:[0,1,1] op_sel_hi:[1,1,1]\n"
"v_pk_fma_f32 v[244:245], v[244:245], v[48:49], v[48:49] op_sel:[0,1,1] op_sel_hi:[1,1,1]\n"
"v_pk_fma_f32 v[246:247], v[246:247], v[48:49], v[48:49] op_sel:[0,1,1] op_sel_hi:[1,1,1]\n"
"v_rcp_f32 v180, v180\n"
"v_rcp_f32 v181, v181\n"
"v_rcp_f32 v182, v182\n"
"v_rcp_f32 v183, v183\n"
"v_rcp_f32 v212, v212\n"
"v_rcp_f32 v213, v213\n"
"v_rcp_f32 v214, v214\n"
"v_rcp_f32 v215, v215\n"
"v_rcp_f32 v244, v244\n"
"v_rcp_f32 v245, v245\n"
"v_rcp_f32 v246, v246\n"
"v_rcp_f32 v247, v247\n"
"v_pk_mul_f32 v[180:181], v[180:181], v[32:33]\n"
"v_pk_mul_f32 v[182:183], v[182:183], v[34:35]\n"
"v_pk_mul_f32 v[212:213], v[212:213], v[36:37]\n"
"v_pk_mul_f32 v[214:215], v[214:215], v[38:39]\n"
"v_pk_mul_f32 v[244:245], v[244:245], v[40:41]\n"
"v_pk_mul_f32 v[246:247], v[246:247], v[42:43]\n"
"v_cvt_pk_bf16_f32 v180, v180, v181\n"
"v_cvt_pk_bf16_f32 v181, v182, v183\n"
"v_cvt_pk_bf16_f32 v182, v212, v213\n"
"v_cvt_pk_bf16_f32 v183, v214, v215\n"
"v_cvt_pk_bf16_f32 v242, v244, v245\n"
"v_cvt_pk_bf16_f32 v243, v246, v247\n"
"s_nop 0\n"
"v_permlane16_swap_b32 v240, v242\n"
"v_permlane16_swap_b32 v241, v243\n"
"s_mov_b32 s4, s20\n"
"s_mov_b32 s5, s21\n"
"s_mov_b32 s7, s23\n"
"s_waitcnt vmcnt(1)\n"
"v_cmp_ne_u64 vcc, s[90:91], v[14:15]\n"
"s_nop 0\n"
"s_mov_b64 s[18:19], vcc\n"
"s_cmp_eq_u64 vcc, 0\n"
"s_cselect_b32 s6, s22, 0\n"
"s_add_u32 m0, s76, 0\n"
"s_nop 0\n"
"buffer_load_dwordx4 v0, s[4:7], s72 offen offset:0 sc1 lds\n"
"v_accvgpr_read_b32 v46, a252\n"
"v_accvgpr_read_b32 v47, a253\n"
"v_pk_mul_f32 v[184:185], v[184:185], v[46:47] op_sel_hi:[1,0] neg_lo:[0,1] neg_hi:[0,1]\n"
"v_pk_mul_f32 v[186:187], v[186:187], v[46:47] op_sel_hi:[1,0] neg_lo:[0,1] neg_hi:[0,1]\n"
"buffer_load_dwordx4 v0, s[4:7], s72 offen offset:1024 sc1 lds\n"
"v_pk_mul_f32 v[216:217], v[216:217], v[46:47] op_sel_hi:[1,0] neg_lo:[0,1] neg_hi:[0,1]\n"
"v_pk_mul_f32 v[218:219], v[218:219], v[46:47] op_sel_hi:[1,0] neg_lo:[0,1] neg_hi:[0,1]\n"
"v_pk_mul_f32 v[248:249], v[248:249], v[46:47] op_sel_hi:[1,0] neg_lo:[0,1] neg_hi:[0,1]\n"
"v_pk_mul_f32 v[250:251], v[250:251], v[46:47] op_sel_hi:[1,0] neg_lo:[0,1] neg_hi:[0,1]\n"
"v_exp_f32 v184, v184\n"
"buffer_load_dwordx4 v0, s[4:7], s72 offen offset:2048 sc1 lds\n"
"v_exp_f32 v185, v185\n"
"v_exp_f32 v186, v186\n"
"v_exp_f32 v187, v187\n"
"v_exp_f32 v216, v216\n"
"v_exp_f32 v217, v217\n"
"buffer_load_dwordx4 v0, s[4:7], s72 offen offset:3072 sc1 lds\n"
"s_add_u32 s72, s72, 16384\n"
"v_exp_f32 v218, v218\n"
"v_exp_f32 v219, v219\n"
"v_exp_f32 v248, v248\n"
"v_exp_f32 v249, v249\n"
"v_exp_f32 v250, v250\n"
"s_add_u32 m0, s76, 16384\n"
"s_nop 0\n"
"buffer_load_dwordx4 v0, s[4:7], s72 offen offset:0 sc1 lds\n"
"v_exp_f32 v251, v251\n"
"v_accvgpr_read_b32 v32, a168\n"
"v_accvgpr_read_b32 v33, a169\n"
"v_accvgpr_read_b32 v34, a170\n"
"buffer_load_dwordx4 v0, s[4:7], s72 offen offset:1024 sc1 lds\n"
"v_accvgpr_read_b32 v35, a171\n"
"v_accvgpr_read_b32 v36, a200\n"
"v_accvgpr_read_b32 v37, a201\n"
"v_accvgpr_read_b32 v38, a202\n"
"v_accvgpr_read_b32 v39, a203\n"
"buffer_load_dwordx4 v0, s[4:7], s72 offen offset:2048 sc1 lds\n"
"v_accvgpr_read_b32 v40, a232\n"
"v_accvgpr_read_b32 v41, a233\n"
"v_accvgpr_read_b32 v42, a234\n"
"v_accvgpr_read_b32 v43, a235\n"
"v_pk_fma_f32 v[184:185], v[184:185], v[46:47], v[46:47] op_sel:[0,1,1] op_sel_hi:[1,1,1]\n"
"buffer_load_dwordx4 v0, s[4:7], s72 offen offset:3072 sc1 lds\n"
"s_add_u32 s72, s72, 65536\n"
"v_pk_fma_f32 v[186:187], v[186:187], v[46:47], v[46:47] op_sel:[0,1,1] op_sel_hi:[1,1,1]\n"
"v_pk_fma_f32 v[216:217], v[216:217], v[46:47], v[46:47] op_sel:[0,1,1] op_sel_hi:[1,1,1]\n"
"v_pk_fma_f32 v[218:219], v[218:219], v[46:47], v[46:47] op_sel:[0,1,1] op_sel_hi:[1,1,1]\n"
"v_pk_fma_f32 v[248:249], v[248:249], v[46:47], v[46:47] op_sel:[0,1,1] op_sel_hi:[1,1,1]\n"
"v_pk_fma_f32 v[250:251], v[250:251], v[46:47], v[46:47] op_sel:[0,1,1] op_sel_hi:[1,1,1]\n"
"s_mov_b32 s94, 4096\n"
"buffer_store_dwordx4 v[172:175], v19, s[20:23], s94 offen offset:2048 sc1\n"
"v_rcp_f32 v184, v184\n"
"v_rcp_f32 v185, v185\n"
"v_rcp_f32 v186, v186\n"
"v_rcp_f32 v187, v187\n"
"buffer_store_dwordx4 v[232:235], v20, s[20:23], s94 offen offset:0 sc1\n"
"v_rcp_f32 v216, v216\n"
"v_rcp_f32 v217, v217\n"
"v_rcp_f32 v218, v218\n"
"v_rcp_f32 v219, v219\n"
"v_rcp_f32 v248, v248\n"
"s_mov_b32 s94, 8192\n"
"buffer_store_dwordx4 v[176:179], v19, s[20:23], s94 offen offset:0 sc1\n"
"v_rcp_f32 v249, v249\n"
"v_rcp_f32 v250, v250\n"
"v_rcp_f32 v251, v251\n"
"v_pk_mul_f32 v[184:185], v[184:185], v[32:33]\n"
"v_pk_mul_f32 v[186:187], v[186:187], v[34:35]\n"
"s_mov_b32 s94, 8192\n"
"buffer_store_dwordx4 v[180:183], v19, s[20:23], s94 offen offset:2048 sc1\n"
"v_pk_mul_f32 v[216:217], v[216:217], v[36:37]\n"
"v_pk_mul_f32 v[218:219], v[218:219], v[38:39]\n"
"v_pk_mul_f32 v[248:249], v[248:249], v[40:41]\n"
"v_pk_mul_f32 v[250:251], v[250:251], v[42:43]\n"
"v_cvt_pk_bf16_f32 v184, v184, v185\n"
"buffer_store_dwordx4 v[240:243], v20, s[20:23], s94 offen offset:0 sc1\n"
"v_cvt_pk_bf16_f32 v185, v186, v187\n"
"v_cvt_pk_bf16_f32 v186, v216, v217\n"
"v_cvt_pk_bf16_f32 v187, v218, v219\n"
"v_cvt_pk_bf16_f32 v248, v248, v249\n"
"v_cvt_pk_bf16_f32 v249, v250, v251\n"
"s_add_u32 m0, s76, 32768\n"
"s_nop 0\n"
"buffer_load_dwordx4 v0, s[4:7], s72 offen offset:0 sc1 lds\n"
"v_accvgpr_read_b32 v48, a254\n"
"v_accvgpr_read_b32 v49, a255\n"
"v_pk_mul_f32 v[188:189], v[188:189], v[48:49] op_sel_hi:[1,0] neg_lo:[0,1] neg_hi:[0,1]\n"
"v_pk_mul_f32 v[190:191], v[190:191], v[48:49] op_sel_hi:[1,0] neg_lo:[0,1] neg_hi:[0,1]\n"
"v_pk_mul_f32 v[220:221], v[220:221], v[48:49] op_sel_hi:[1,0] neg_lo:[0,1] neg_hi:[0,1]\n"
"v_pk_mul_f32 v[222:223], v[222:223], v[48:49] op_sel_hi:[1,0] neg_lo:[0,1] neg_hi:[0,1]\n"
"v_pk_mul_f32 v[252:253], v[252:253], v[48:49] op_sel_hi:[1,0] neg_lo:[0,1] neg_hi:[0,1]\n"
"buffer_load_dwordx4 v0, s[4:7], s72 offen offset:1024 sc1 lds\n"
"v_pk_mul_f32 v[254:255], v[254:255], v[48:49] op_sel_hi:[1,0] neg_lo:[0,1] neg_hi:[0,1]\n"
"v_exp_f32 v188, v188\n"
"v_exp_f32 v189, v189\n"
"v_exp_f32 v190, v190\n"
"v_exp_f32 v191, v191\n"
"v_exp_f32 v220, v220\n"
"v_exp_f32 v221, v221\n"
"buffer_load_dwordx4 v0, s[4:7], s72 offen offset:2048 sc1 lds\n"
"v_exp_f32 v222, v222\n"
"v_exp_f32 v223, v223\n"
"v_exp_f32 v252, v252\n"
"v_exp_f32 v253, v253\n"
"v_exp_f32 v254, v254\n"
"v_exp_f32 v255, v255\n"
"v_accvgpr_read_b32 v32, a172\n"
"buffer_load_dwordx4 v0, s[4:7], s72 offen offset:3072 sc1 lds\n"
"s_add_u32 s72, s72, 16384\n"
"v_accvgpr_read_b32 v33, a173\n"
"v_accvgpr_read_b32 v34, a174\n"
"v_accvgpr_read_b32 v35, a175\n"
"v_accvgpr_read_b32 v36, a204\n"
"v_accvgpr_read_b32 v37, a205\n"
"v_accvgpr_read_b32 v38, a206\n"
"v_accvgpr_read_b32 v39, a207\n"
"s_add_u32 m0, s76, 49152\n"
"s_nop 0\n"
"buffer_load_dwordx4 v0, s[4:7], s72 offen offset:0 sc1 lds\n"
"v_accvgpr_read_b32 v40, a236\n"
"v_accvgpr_read_b32 v41, a237\n"
"v_accvgpr_read_b32 v42, a238\n"
"v_accvgpr_read_b32 v43, a239\n"
"v_pk_fma_f32 v[188:189], v[188:189], v[48:49], v[48:49] op_sel:[0,1,1] op_sel_hi:[1,1,1]\n"
"v_pk_fma_f32 v[190:191], v[190:191], v[48:49], v[48:49] op_sel:[0,1,1] op_sel_hi:[1,1,1]\n"
"v_pk_fma_f32 v[220:221], v[220:221], v[48:49], v[48:49] op_sel:[0,1,1] op_sel_hi:[1,1,1]\n"
"buffer_load_dwordx4 v0, s[4:7], s72 offen offset:1024 sc1 lds\n"
"v_pk_fma_f32 v[222:223], v[222:223], v[48:49], v[48:49] op_sel:[0,1,1] op_sel_hi:[1,1,1]\n"
"v_pk_fma_f32 v[252:253], v[252:253], v[48:49], v[48:49] op_sel:[0,1,1] op_sel_hi:[1,1,1]\n"
"v_pk_fma_f32 v[254:255], v[254:255], v[48:49], v[48:49] op_sel:[0,1,1] op_sel_hi:[1,1,1]\n"
"v_rcp_f32 v188, v188\n"
"v_rcp_f32 v189, v189\n"
"v_rcp_f32 v190, v190\n"
"v_rcp_f32 v191, v191\n"
"buffer_load_dwordx4 v0, s[4:7], s72 offen offset:2048 sc1 lds\n"
"v_rcp_f32 v220, v220\n"
"v_rcp_f32 v221, v221\n"
"v_rcp_f32 v222, v222\n"
"v_rcp_f32 v223, v223\n"
"v_rcp_f32 v252, v252\n"
"v_rcp_f32 v253, v253\n"
"v_rcp_f32 v254, v254\n"
"buffer_load_dwordx4 v0, s[4:7], s72 offen offset:3072 sc1 lds\n"
"s_add_u32 s72, s72, 16384\n"
"v_rcp_f32 v255, v255\n"
"v_pk_mul_f32 v[188:189], v[188:189], v[32:33]\n"
"v_pk_mul_f32 v[190:191], v[190:191], v[34:35]\n"
"v_pk_mul_f32 v[220:221], v[220:221], v[36:37]\n"
"v_pk_mul_f32 v[222:223], v[222:223], v[38:39]\n"
"v_pk_mul_f32 v[252:253], v[252:253], v[40:41]\n"
"v_pk_mul_f32 v[254:255], v[254:255], v[42:43]\n"
"s_mov_b32 s94, 12288\n"
"buffer_store_dwordx4 v[184:187], v19, s[20:23], s94 offen offset:0 sc1\n"
"v_cvt_pk_bf16_f32 v188, v188, v189\n"
"v_cvt_pk_bf16_f32 v189, v190, v191\n"
"v_cvt_pk_bf16_f32 v190, v220, v221\n"
"v_cvt_pk_bf16_f32 v191, v222, v223\n"
"v_cvt_pk_bf16_f32 v250, v252, v253\n"
"v_cvt_pk_bf16_f32 v251, v254, v255\n"
"s_nop 0\n"
"v_permlane16_swap_b32 v248, v250\n"
"v_permlane16_swap_b32 v249, v251\n"
"s_mov_b32 s94, 12288\n"
"buffer_store_dwordx4 v[188:191], v19, s[20:23], s94 offen offset:2048 sc1\n"
"buffer_store_dwordx4 v[248:251], v20, s[20:23], s94 offen offset:0 sc1\n"
"s_cmp_lg_u64 s[18:19], 0\n"
"s_cbranch_scc1 LXKV17_%=\n"
"LBKV18_%=:\n"
"s_waitcnt vmcnt(20)\n"
"s_barrier\n"
"ds_read_b128 a[0:3], v23 offset:0\n"
"ds_read_b64_tr_b16 a[32:33], v24 offset:0\n"
"ds_read_b64_tr_b16 a[34:35], v24 offset:1024\n"
"ds_read_b128 a[8:11], v23 offset:2048\n"
"ds_read_b64_tr_b16 a[40:41], v5 offset:0\n"
"ds_read_b64_tr_b16 a[42:43], v5 offset:1024\n"
"ds_read_b128 a[16:19], v23 offset:4096\n"
"ds_read_b64_tr_b16 a[48:49], v6 offset:0\n"
"ds_read_b64_tr_b16 a[50:51], v6 offset:1024\n"
"ds_read_b128 a[24:27], v23 offset:6144\n"
"ds_read_b64_tr_b16 a[56:57], v7 offset:0\n"
"ds_read_b64_tr_b16 a[58:59], v7 offset:1024\n"
"ds_read_b128 a[4:7], v4 offset:0\n"
"ds_read_b64_tr_b16 a[36:37], v24 offset:8192\n"
"ds_read_b64_tr_b16 a[38:39], v24 offset:9216\n"
"ds_read_b128 a[12:15], v4 offset:2048\n"
"ds_read_b64_tr_b16 a[44:45], v5 offset:8192\n"
"ds_read_b64_tr_b16 a[46:47], v5 offset:9216\n"
"ds_read_b128 a[20:23], v4 offset:4096\n"
"ds_read_b64_tr_b16 a[52:53], v6 offset:8192\n"
"ds_read_b64_tr_b16 a[54:55], v6 offset:9216\n"
"ds_read_b128 a[28:31], v4 offset:6144\n"
"ds_read_b64_tr_b16 a[60:61], v7 offset:8192\n"
"ds_read_b64_tr_b16 a[62:63], v7 offset:9216\n"
"s_waitcnt vmcnt(16) lgkmcnt(0)\n"
"s_barrier\n"
"v_mfma_f32_16x16x32_bf16 v[128:131], a[32:35], a[0:3], 0\n"
"ds_read_b128 a[64:67], v23 offset:16384\n"
"s_add_u32 m0, s76, 0\n"
"v_mfma_f32_16x16x32_bf16 v[132:135], a[40:43], a[0:3], 0\n"
"ds_read_b64_tr_b16 a[96:97], v24 offset:16384\n"
"buffer_load_dwordx4 v0, s[20:23], s72 offen offset:0 sc1 lds\n"
"v_mfma_f32_16x16x32_bf16 v[136:139], a[48:51], a[0:3], 0\n"
"ds_read_b64_tr_b16 a[98:99], v24 offset:17408\n"
"v_mfma_f32_16x16x32_bf16 v[140:143], a[56:59], a[0:3], 0\n"
"ds_read_b128 a[72:75], v23 offset:18432\n"
"v_mfma_f32_16x16x32_bf16 v[156:159], a[56:59], a[8:11], 0\n"
"ds_read_b64_tr_b16 a[104:105], v5 offset:16384\n"
"buffer_load_dwordx4 v0, s[20:23], s72 offen offset:1024 sc1 lds\n"
"v_mfma_f32_16x16x32_bf16 v[152:155], a[48:51], a[8:11], 0\n"
"ds_read_b64_tr_b16 a[106:107], v5 offset:17408\n"
"v_mfma_f32_16x16x32_bf16 v[148:151], a[40:43], a[8:11], 0\n"
"ds_read_b128 a[80:83], v23 offset:20480\n"
"v_mfma_f32_16x16x32_bf16 v[144:147], a[32:35], a[8:11], 0\n"
"ds_read_b64_tr_b16 a[112:113], v6 offset:16384\n"
"v_mfma_f32_16x16x32_bf16 v[160:163], a[32:35], a[16:19], 0\n"
"ds_read_b64_tr_b16 a[114:115], v6 offset:17408\n"
"buffer_load_dwordx4 v0, s[20:23], s72 offen offset:2048 sc1 lds\n"
"v_mfma_f32_16x16x32_bf16 v[164:167], a[40:43], a[16:19], 0\n"
"ds_read_b128 a[88:91], v23 offset:22528\n"
"v_mfma_f32_16x16x32_bf16 v[168:171], a[48:51], a[16:19], 0\n"
"ds_read_b64_tr_b16 a[120:121], v7 offset:16384\n"
"v_mfma_f32_16x16x32_bf16 v[172:175], a[56:59], a[16:19], 0\n"
"ds_read_b64_tr_b16 a[122:123], v7 offset:17408\n"
"v_mfma_f32_16x16x32_bf16 v[188:191], a[56:59], a[24:27], 0\n"
"buffer_load_dwordx4 v0, s[20:23], s72 offen offset:3072 sc1 lds\n"
"s_add_u32 s72, s72, 16384\n"
"v_mfma_f32_16x16x32_bf16 v[184:187], a[48:51], a[24:27], 0\n"
"ds_read_b128 a[68:71], v4 offset:16384\n"
"v_mfma_f32_16x16x32_bf16 v[180:183], a[40:43], a[24:27], 0\n"
"ds_read_b64_tr_b16 a[100:101], v24 offset:24576\n"
"v_mfma_f32_16x16x32_bf16 v[176:179], a[32:35], a[24:27], 0\n"
"ds_read_b64_tr_b16 a[102:103], v24 offset:25600\n"
"s_add_u32 m0, s77, 0\n"
"v_mfma_f32_16x16x32_bf16 v[128:131], a[36:39], a[4:7], v[128:131]\n"
"ds_read_b128 a[76:79], v4 offset:18432\n"
"buffer_load_dwordx4 v25, s[52:55], s74 offen offset:0 lds\n"
"v_mfma_f32_16x16x32_bf16 v[132:135], a[44:47], a[4:7], v[132:135]\n"
"ds_read_b64_tr_b16 a[108:109], v5 offset:24576\n"
"v_mfma_f32_16x16x32_bf16 v[136:139], a[52:55], a[4:7], v[136:139]\n"
"ds_read_b64_tr_b16 a[110:111], v5 offset:25600\n"
"v_mfma_f32_16x16x32_bf16 v[140:143], a[60:63], a[4:7], v[140:143]\n"
"ds_read_b128 a[84:87], v4 offset:20480\n"
"buffer_load_dwordx4 v26, s[52:55], s74 offen offset:1024 lds\n"
"v_mfma_f32_16x16x32_bf16 v[156:159], a[60:63], a[12:15], v[156:159]\n"
"ds_read_b64_tr_b16 a[116:117], v6 offset:24576\n"
"v_mfma_f32_16x16x32_bf16 v[152:155], a[52:55], a[12:15], v[152:155]\n"
"ds_read_b64_tr_b16 a[118:119], v6 offset:25600\n"
"v_mfma_f32_16x16x32_bf16 v[148:151], a[44:47], a[12:15], v[148:151]\n"
"ds_read_b128 a[92:95], v4 offset:22528\n"
"v_mfma_f32_16x16x32_bf16 v[144:147], a[36:39], a[12:15], v[144:147]\n"
"ds_read_b64_tr_b16 a[124:125], v7 offset:24576\n"
"buffer_load_dwordx4 v27, s[52:55], s74 offen offset:2048 lds\n"
"v_mfma_f32_16x16x32_bf16 v[160:163], a[36:39], a[20:23], v[160:163]\n"
"ds_read_b64_tr_b16 a[126:127], v7 offset:25600\n"
"v_mfma_f32_16x16x32_bf16 v[164:167], a[44:47], a[20:23], v[164:167]\n"
"v_mfma_f32_16x16x32_bf16 v[168:171], a[52:55], a[20:23], v[168:171]\n"
"v_mfma_f32_16x16x32_bf16 v[172:175], a[60:63], a[20:23], v[172:175]\n"
"buffer_load_dwordx4 v28, s[52:55], s74 offen offset:3072 lds\n"
"s_add_u32 s74, s74, 131072\n"
"v_mfma_f32_16x16x32_bf16 v[188:191], a[60:63], a[28:31], v[188:191]\n"
"v_mfma_f32_16x16x32_bf16 v[184:187], a[52:55], a[28:31], v[184:187]\n"
"v_mfma_f32_16x16x32_bf16 v[180:183], a[44:47], a[28:31], v[180:183]\n"
"v_mfma_f32_16x16x32_bf16 v[176:179], a[36:39], a[28:31], v[176:179]\n"
"s_waitcnt vmcnt(15) lgkmcnt(0)\n"
"s_barrier\n"
"v_mfma_f32_16x16x32_bf16 v[128:131], a[96:99], a[64:67], v[128:131]\n"
"ds_read_b128 a[0:3], v23 offset:32768\n"
"s_add_u32 m0, s76, 16384\n"
"v_mfma_f32_16x16x32_bf16 v[132:135], a[104:107], a[64:67], v[132:135]\n"
"ds_read_b64_tr_b16 a[32:33], v24 offset:32768\n"
"buffer_load_dwordx4 v0, s[20:23], s72 offen offset:0 sc1 lds\n"
"v_mfma_f32_16x16x32_bf16 v[136:139], a[112:115], a[64:67], v[136:139]\n"
"ds_read_b64_tr_b16 a[34:35], v24 offset:33792\n"
"v_mfma_f32_16x16x32_bf16 v[140:143], a[120:123], a[64:67], v[140:143]\n"
"ds_read_b128 a[8:11], v23 offset:34816\n"
"v_mfma_f32_16x16x32_bf16 v[156:159], a[120:123], a[72:75], v[156:159]\n"
"ds_read_b64_tr_b16 a[40:41], v5 offset:32768\n"
"buffer_load_dwordx4 v0, s[20:23], s72 offen offset:1024 sc1 lds\n"
"v_mfma_f32_16x16x32_bf16 v[152:155], a[112:115], a[72:75], v[152:155]\n"
"ds_read_b64_tr_b16 a[42:43], v5 offset:33792\n"
"v_mfma_f32_16x16x32_bf16 v[148:151], a[104:107], a[72:75], v[148:151]\n"
"ds_read_b128 a[16:19], v23 offset:36864\n"
"v_mfma_f32_16x16x32_bf16 v[144:147], a[96:99], a[72:75], v[144:147]\n"
"ds_read_b64_tr_b16 a[48:49], v6 offset:32768\n"
"v_mfma_f32_16x16x32_bf16 v[160:163], a[96:99], a[80:83], v[160:163]\n"
"ds_read_b64_tr_b16 a[50:51], v6 offset:33792\n"
"buffer_load_dwordx4 v0, s[20:23], s72 offen offset:2048 sc1 lds\n"
"v_mfma_f32_16x16x32_bf16 v[164:167], a[104:107], a[80:83], v[164:167]\n"
"ds_read_b128 a[24:27], v23 offset:38912\n"
"v_mfma_f32_16x16x32_bf16 v[168:171], a[112:115], a[80:83], v[168:171]\n"
"ds_read_b64_tr_b16 a[56:57], v7 offset:32768\n"
"v_mfma_f32_16x16x32_bf16 v[172:175], a[120:123], a[80:83], v[172:175]\n"
"ds_read_b64_tr_b16 a[58:59], v7 offset:33792\n"
"v_mfma_f32_16x16x32_bf16 v[188:191], a[120:123], a[88:91], v[188:191]\n"
"buffer_load_dwordx4 v0, s[20:23], s72 offen offset:3072 sc1 lds\n"
"s_add_u32 s72, s72, 65536\n"
"v_mfma_f32_16x16x32_bf16 v[184:187], a[112:115], a[88:91], v[184:187]\n"
"ds_read_b128 a[4:7], v4 offset:32768\n"
"v_mfma_f32_16x16x32_bf16 v[180:183], a[104:107], a[88:91], v[180:183]\n"
"ds_read_b64_tr_b16 a[36:37], v24 offset:40960\n"
"v_mfma_f32_16x16x32_bf16 v[176:179], a[96:99], a[88:91], v[176:179]\n"
"ds_read_b64_tr_b16 a[38:39], v24 offset:41984\n"
"s_add_u32 m0, s77, 16384\n"
"v_mfma_f32_16x16x32_bf16 v[128:131], a[100:103], a[68:71], v[128:131]\n"
"ds_read_b128 a[12:15], v4 offset:34816\n"
"buffer_load_dwordx4 v25, s[52:55], s74 offen offset:0 lds\n"
"v_mfma_f32_16x16x32_bf16 v[132:135], a[108:111], a[68:71], v[132:135]\n"
"ds_read_b64_tr_b16 a[44:45], v5 offset:40960\n"
"v_mfma_f32_16x16x32_bf16 v[136:139], a[116:119], a[68:71], v[136:139]\n"
"ds_read_b64_tr_b16 a[46:47], v5 offset:41984\n"
"v_mfma_f32_16x16x32_bf16 v[140:143], a[124:127], a[68:71], v[140:143]\n"
"ds_read_b128 a[20:23], v4 offset:36864\n"
"buffer_load_dwordx4 v26, s[52:55], s74 offen offset:1024 lds\n"
"v_mfma_f32_16x16x32_bf16 v[156:159], a[124:127], a[76:79], v[156:159]\n"
"ds_read_b64_tr_b16 a[52:53], v6 offset:40960\n"
"v_mfma_f32_16x16x32_bf16 v[152:155], a[116:119], a[76:79], v[152:155]\n"
"ds_read_b64_tr_b16 a[54:55], v6 offset:41984\n"
"v_mfma_f32_16x16x32_bf16 v[148:151], a[108:111], a[76:79], v[148:151]\n"
"ds_read_b128 a[28:31], v4 offset:38912\n"
"v_mfma_f32_16x16x32_bf16 v[144:147], a[100:103], a[76:79], v[144:147]\n"
"ds_read_b64_tr_b16 a[60:61], v7 offset:40960\n"
"buffer_load_dwordx4 v27, s[52:55], s74 offen offset:2048 lds\n"
"v_mfma_f32_16x16x32_bf16 v[160:163], a[100:103], a[84:87], v[160:163]\n"
"ds_read_b64_tr_b16 a[62:63], v7 offset:41984\n"
"v_mfma_f32_16x16x32_bf16 v[164:167], a[108:111], a[84:87], v[164:167]\n"
"v_mfma_f32_16x16x32_bf16 v[168:171], a[116:119], a[84:87], v[168:171]\n"
"v_mfma_f32_16x16x32_bf16 v[172:175], a[124:127], a[84:87], v[172:175]\n"
"buffer_load_dwordx4 v28, s[52:55], s74 offen offset:3072 lds\n"
"s_add_u32 s74, s74, 524288\n"
"v_mfma_f32_16x16x32_bf16 v[188:191], a[124:127], a[92:95], v[188:191]\n"
"v_mfma_f32_16x16x32_bf16 v[184:187], a[116:119], a[92:95], v[184:187]\n"
"v_mfma_f32_16x16x32_bf16 v[180:183], a[108:111], a[92:95], v[180:183]\n"
"v_mfma_f32_16x16x32_bf16 v[176:179], a[100:103], a[92:95], v[176:179]\n"
"s_waitcnt vmcnt(19) lgkmcnt(0)\n"
"s_barrier\n"
"s_waitcnt vmcnt(16)\n"
"v_mfma_f32_16x16x32_bf16 v[128:131], a[32:35], a[0:3], v[128:131]\n"
"ds_read_b128 a[64:67], v23 offset:49152\n"
"s_add_u32 m0, s76, 32768\n"
"s_mov_b64 exec, 1\n"
"buffer_store_dwordx2 v[2:3], off, s[20:23], s68 sc1\n"
"s_mov_b64 exec, -1\n"
"v_mfma_f32_16x16x32_bf16 v[132:135], a[40:43], a[0:3], v[132:135]\n"
"ds_read_b64_tr_b16 a[96:97], v24 offset:49152\n"
"buffer_load_dwordx4 v0, s[20:23], s72 offen offset:0 sc1 lds\n"
"v_mfma_f32_16x16x32_bf16 v[136:139], a[48:51], a[0:3], v[136:139]\n"
"ds_read_b64_tr_b16 a[98:99], v24 offset:50176\n"
"v_mfma_f32_16x16x32_bf16 v[140:143], a[56:59], a[0:3], v[140:143]\n"
"ds_read_b128 a[72:75], v23 offset:51200\n"
"v_mfma_f32_16x16x32_bf16 v[156:159], a[56:59], a[8:11], v[156:159]\n"
"ds_read_b64_tr_b16 a[104:105], v5 offset:49152\n"
"buffer_load_dwordx4 v0, s[20:23], s72 offen offset:1024 sc1 lds\n"
"v_mfma_f32_16x16x32_bf16 v[152:155], a[48:51], a[8:11], v[152:155]\n"
"ds_read_b64_tr_b16 a[106:107], v5 offset:50176\n"
"v_mfma_f32_16x16x32_bf16 v[148:151], a[40:43], a[8:11], v[148:151]\n"
"ds_read_b128 a[80:83], v23 offset:53248\n"
"v_mfma_f32_16x16x32_bf16 v[144:147], a[32:35], a[8:11], v[144:147]\n"
"ds_read_b64_tr_b16 a[112:113], v6 offset:49152\n"
"v_mfma_f32_16x16x32_bf16 v[160:163], a[32:35], a[16:19], v[160:163]\n"
"ds_read_b64_tr_b16 a[114:115], v6 offset:50176\n"
"buffer_load_dwordx4 v0, s[20:23], s72 offen offset:2048 sc1 lds\n"
"v_mfma_f32_16x16x32_bf16 v[164:167], a[40:43], a[16:19], v[164:167]\n"
"ds_read_b128 a[88:91], v23 offset:55296\n"
"v_mfma_f32_16x16x32_bf16 v[168:171], a[48:51], a[16:19], v[168:171]\n"
"ds_read_b64_tr_b16 a[120:121], v7 offset:49152\n"
"v_mfma_f32_16x16x32_bf16 v[172:175], a[56:59], a[16:19], v[172:175]\n"
"ds_read_b64_tr_b16 a[122:123], v7 offset:50176\n"
"v_mfma_f32_16x16x32_bf16 v[188:191], a[56:59], a[24:27], v[188:191]\n"
"buffer_load_dwordx4 v0, s[20:23], s72 offen offset:3072 sc1 lds\n"
"s_add_u32 s72, s72, 16384\n"
"v_mfma_f32_16x16x32_bf16 v[184:187], a[48:51], a[24:27], v[184:187]\n"
"ds_read_b128 a[68:71], v4 offset:49152\n"
"v_mfma_f32_16x16x32_bf16 v[180:183], a[40:43], a[24:27], v[180:183]\n"
"ds_read_b64_tr_b16 a[100:101], v24 offset:57344\n"
"v_mfma_f32_16x16x32_bf16 v[176:179], a[32:35], a[24:27], v[176:179]\n"
"ds_read_b64_tr_b16 a[102:103], v24 offset:58368\n"
"s_add_u32 m0, s77, 32768\n"
"v_mfma_f32_16x16x32_bf16 v[128:131], a[36:39], a[4:7], v[128:131]\n"
"ds_read_b128 a[76:79], v4 offset:51200\n"
"buffer_load_dwordx4 v25, s[52:55], s74 offen offset:0 lds\n"
"v_mfma_f32_16x16x32_bf16 v[132:135], a[44:47], a[4:7], v[132:135]\n"
"ds_read_b64_tr_b16 a[108:109], v5 offset:57344\n"
"v_mfma_f32_16x16x32_bf16 v[136:139], a[52:55], a[4:7], v[136:139]\n"
"ds_read_b64_tr_b16 a[110:111], v5 offset:58368\n"
"v_mfma_f32_16x16x32_bf16 v[140:143], a[60:63], a[4:7], v[140:143]\n"
"ds_read_b128 a[84:87], v4 offset:53248\n"
"buffer_load_dwordx4 v26, s[52:55], s74 offen offset:1024 lds\n"
"v_mfma_f32_16x16x32_bf16 v[156:159], a[60:63], a[12:15], v[156:159]\n"
"ds_read_b64_tr_b16 a[116:117], v6 offset:57344\n"
"v_mfma_f32_16x16x32_bf16 v[152:155], a[52:55], a[12:15], v[152:155]\n"
"ds_read_b64_tr_b16 a[118:119], v6 offset:58368\n"
"v_mfma_f32_16x16x32_bf16 v[148:151], a[44:47], a[12:15], v[148:151]\n"
"ds_read_b128 a[92:95], v4 offset:55296\n"
"v_mfma_f32_16x16x32_bf16 v[144:147], a[36:39], a[12:15], v[144:147]\n"
"ds_read_b64_tr_b16 a[124:125], v7 offset:57344\n"
"buffer_load_dwordx4 v27, s[52:55], s74 offen offset:2048 lds\n"
"v_mfma_f32_16x16x32_bf16 v[160:163], a[36:39], a[20:23], v[160:163]\n"
"ds_read_b64_tr_b16 a[126:127], v7 offset:58368\n"
"v_mfma_f32_16x16x32_bf16 v[164:167], a[44:47], a[20:23], v[164:167]\n"
"v_mfma_f32_16x16x32_bf16 v[168:171], a[52:55], a[20:23], v[168:171]\n"
"v_mfma_f32_16x16x32_bf16 v[172:175], a[60:63], a[20:23], v[172:175]\n"
"buffer_load_dwordx4 v28, s[52:55], s74 offen offset:3072 lds\n"
"s_add_u32 s74, s74, 131072\n"
"v_mfma_f32_16x16x32_bf16 v[188:191], a[60:63], a[28:31], v[188:191]\n"
"v_mfma_f32_16x16x32_bf16 v[184:187], a[52:55], a[28:31], v[184:187]\n"
"v_mfma_f32_16x16x32_bf16 v[180:183], a[44:47], a[28:31], v[180:183]\n"
"v_mfma_f32_16x16x32_bf16 v[176:179], a[36:39], a[28:31], v[176:179]\n"
"s_waitcnt vmcnt(17) lgkmcnt(0)\n"
"s_barrier\n"
"v_mfma_f32_16x16x32_bf16 v[128:131], a[96:99], a[64:67], v[128:131]\n"
"ds_read_b128 a[0:3], v23 offset:0\n"
"s_add_u32 m0, s76, 49152\n"
"v_mfma_f32_16x16x32_bf16 v[132:135], a[104:107], a[64:67], v[132:135]\n"
"ds_read_b64_tr_b16 a[32:33], v24 offset:0\n"
"buffer_load_dwordx4 v0, s[20:23], s72 offen offset:0 sc1 lds\n"
"v_mfma_f32_16x16x32_bf16 v[136:139], a[112:115], a[64:67], v[136:139]\n"
"ds_read_b64_tr_b16 a[34:35], v24 offset:1024\n"
"v_mfma_f32_16x16x32_bf16 v[140:143], a[120:123], a[64:67], v[140:143]\n"
"ds_read_b128 a[8:11], v23 offset:2048\n"
"v_mfma_f32_16x16x32_bf16 v[156:159], a[120:123], a[72:75], v[156:159]\n"
"ds_read_b64_tr_b16 a[40:41], v5 offset:0\n"
"buffer_load_dwordx4 v0, s[20:23], s72 offen offset:1024 sc1 lds\n"
"v_mfma_f32_16x16x32_bf16 v[152:155], a[112:115], a[72:75], v[152:155]\n"
"ds_read_b64_tr_b16 a[42:43], v5 offset:1024\n"
"v_mfma_f32_16x16x32_bf16 v[148:151], a[104:107], a[72:75], v[148:151]\n"
"ds_read_b128 a[16:19], v23 offset:4096\n"
"v_mfma_f32_16x16x32_bf16 v[144:147], a[96:99], a[72:75], v[144:147]\n"
"ds_read_b64_tr_b16 a[48:49], v6 offset:0\n"
"v_mfma_f32_16x16x32_bf16 v[160:163], a[96:99], a[80:83], v[160:163]\n"
"ds_read_b64_tr_b16 a[50:51], v6 offset:1024\n"
"buffer_load_dwordx4 v0, s[20:23], s72 offen offset:2048 sc1 lds\n"
"v_mfma_f32_16x16x32_bf16 v[164:167], a[104:107], a[80:83], v[164:167]\n"
"ds_read_b128 a[24:27], v23 offset:6144\n"
"v_mfma_f32_16x16x32_bf16 v[168:171], a[112:115], a[80:83], v[168:171]\n"
"ds_read_b64_tr_b16 a[56:57], v7 offset:0\n"
"v_mfma_f32_16x16x32_bf16 v[172:175], a[120:123], a[80:83], v[172:175]\n"
"ds_read_b64_tr_b16 a[58:59], v7 offset:1024\n"
"v_mfma_f32_16x16x32_bf16 v[188:191], a[120:123], a[88:91], v[188:191]\n"
"buffer_load_dwordx4 v0, s[20:23], s72 offen offset:3072 sc1 lds\n"
"s_add_u32 s72, s72, 16384\n"
"v_mfma_f32_16x16x32_bf16 v[184:187], a[112:115], a[88:91], v[184:187]\n"
"ds_read_b128 a[4:7], v4 offset:0\n"
"v_mfma_f32_16x16x32_bf16 v[180:183], a[104:107], a[88:91], v[180:183]\n"
"ds_read_b64_tr_b16 a[36:37], v24 offset:8192\n"
"v_mfma_f32_16x16x32_bf16 v[176:179], a[96:99], a[88:91], v[176:179]\n"
"ds_read_b64_tr_b16 a[38:39], v24 offset:9216\n"
"s_add_u32 m0, s77, 49152\n"
"v_mfma_f32_16x16x32_bf16 v[128:131], a[100:103], a[68:71], v[128:131]\n"
"ds_read_b128 a[12:15], v4 offset:2048\n"
"buffer_load_dwordx4 v25, s[52:55], s74 offen offset:0 lds\n"
"v_mfma_f32_16x16x32_bf16 v[132:135], a[108:111], a[68:71], v[132:135]\n"
"ds_read_b64_tr_b16 a[44:45], v5 offset:8192\n"
"v_mfma_f32_16x16x32_bf16 v[136:139], a[116:119], a[68:71], v[136:139]\n"
"ds_read_b64_tr_b16 a[46:47], v5 offset:9216\n"
"v_mfma_f32_16x16x32_bf16 v[140:143], a[124:127], a[68:71], v[140:143]\n"
"ds_read_b128 a[20:23], v4 offset:4096\n"
"buffer_load_dwordx4 v26, s[52:55], s74 offen offset:1024 lds\n"
"v_mfma_f32_16x16x32_bf16 v[156:159], a[124:127], a[76:79], v[156:159]\n"
"ds_read_b64_tr_b16 a[52:53], v6 offset:8192\n"
"v_mfma_f32_16x16x32_bf16 v[152:155], a[116:119], a[76:79], v[152:155]\n"
"ds_read_b64_tr_b16 a[54:55], v6 offset:9216\n"
"v_mfma_f32_16x16x32_bf16 v[148:151], a[108:111], a[76:79], v[148:151]\n"
"ds_read_b128 a[28:31], v4 offset:6144\n"
"v_mfma_f32_16x16x32_bf16 v[144:147], a[100:103], a[76:79], v[144:147]\n"
"ds_read_b64_tr_b16 a[60:61], v7 offset:8192\n"
"buffer_load_dwordx4 v27, s[52:55], s74 offen offset:2048 lds\n"
"v_mfma_f32_16x16x32_bf16 v[160:163], a[100:103], a[84:87], v[160:163]\n"
"ds_read_b64_tr_b16 a[62:63], v7 offset:9216\n"
"v_mfma_f32_16x16x32_bf16 v[164:167], a[108:111], a[84:87], v[164:167]\n"
"v_mfma_f32_16x16x32_bf16 v[168:171], a[116:119], a[84:87], v[168:171]\n"
"v_mfma_f32_16x16x32_bf16 v[172:175], a[124:127], a[84:87], v[172:175]\n"
"buffer_load_dwordx4 v28, s[52:55], s74 offen offset:3072 lds\n"
"s_add_u32 s74, s74, 131072\n"
"v_mfma_f32_16x16x32_bf16 v[188:191], a[124:127], a[92:95], v[188:191]\n"
"v_mfma_f32_16x16x32_bf16 v[184:187], a[116:119], a[92:95], v[184:187]\n"
"v_mfma_f32_16x16x32_bf16 v[180:183], a[108:111], a[92:95], v[180:183]\n"
"v_mfma_f32_16x16x32_bf16 v[176:179], a[100:103], a[92:95], v[176:179]\n"
"s_waitcnt vmcnt(17) lgkmcnt(0)\n"
"s_barrier\n"
"v_mfma_f32_16x16x32_bf16 v[128:131], a[32:35], a[0:3], v[128:131]\n"
"ds_read_b128 a[64:67], v23 offset:16384\n"
"s_add_u32 m0, s76, 0\n"
"v_mfma_f32_16x16x32_bf16 v[132:135], a[40:43], a[0:3], v[132:135]\n"
"ds_read_b64_tr_b16 a[96:97], v24 offset:16384\n"
"buffer_load_dwordx4 v0, s[20:23], s72 offen offset:0 sc1 lds\n"
"v_mfma_f32_16x16x32_bf16 v[136:139], a[48:51], a[0:3], v[136:139]\n"
"ds_read_b64_tr_b16 a[98:99], v24 offset:17408\n"
"v_mfma_f32_16x16x32_bf16 v[140:143], a[56:59], a[0:3], v[140:143]\n"
"ds_read_b128 a[72:75], v23 offset:18432\n"
"v_mfma_f32_16x16x32_bf16 v[156:159], a[56:59], a[8:11], v[156:159]\n"
"ds_read_b64_tr_b16 a[104:105], v5 offset:16384\n"
"buffer_load_dwordx4 v0, s[20:23], s72 offen offset:1024 sc1 lds\n"
"v_mfma_f32_16x16x32_bf16 v[152:155], a[48:51], a[8:11], v[152:155]\n"
"ds_read_b64_tr_b16 a[106:107], v5 offset:17408\n"
"v_mfma_f32_16x16x32_bf16 v[148:151], a[40:43], a[8:11], v[148:151]\n"
"ds_read_b128 a[80:83], v23 offset:20480\n"
"v_mfma_f32_16x16x32_bf16 v[144:147], a[32:35], a[8:11], v[144:147]\n"
"ds_read_b64_tr_b16 a[112:113], v6 offset:16384\n"
"v_mfma_f32_16x16x32_bf16 v[160:163], a[32:35], a[16:19], v[160:163]\n"
"ds_read_b64_tr_b16 a[114:115], v6 offset:17408\n"
"buffer_load_dwordx4 v0, s[20:23], s72 offen offset:2048 sc1 lds\n"
"v_mfma_f32_16x16x32_bf16 v[164:167], a[40:43], a[16:19], v[164:167]\n"
"ds_read_b128 a[88:91], v23 offset:22528\n"
"v_mfma_f32_16x16x32_bf16 v[168:171], a[48:51], a[16:19], v[168:171]\n"
"ds_read_b64_tr_b16 a[120:121], v7 offset:16384\n"
"v_mfma_f32_16x16x32_bf16 v[172:175], a[56:59], a[16:19], v[172:175]\n"
"ds_read_b64_tr_b16 a[122:123], v7 offset:17408\n"
"v_mfma_f32_16x16x32_bf16 v[188:191], a[56:59], a[24:27], v[188:191]\n"
"buffer_load_dwordx4 v0, s[20:23], s72 offen offset:3072 sc1 lds\n"
"s_add_u32 s72, s72, 16384\n"
"v_mfma_f32_16x16x32_bf16 v[184:187], a[48:51], a[24:27], v[184:187]\n"
"ds_read_b128 a[68:71], v4 offset:16384\n"
"v_mfma_f32_16x16x32_bf16 v[180:183], a[40:43], a[24:27], v[180:183]\n"
"ds_read_b64_tr_b16 a[100:101], v24 offset:24576\n"
"v_mfma_f32_16x16x32_bf16 v[176:179], a[32:35], a[24:27], v[176:179]\n"
"ds_read_b64_tr_b16 a[102:103], v24 offset:25600\n"
"s_add_u32 m0, s77, 0\n"
"v_mfma_f32_16x16x32_bf16 v[128:131], a[36:39], a[4:7], v[128:131]\n"
"ds_read_b128 a[76:79], v4 offset:18432\n"
"buffer_load_dwordx4 v25, s[52:55], s74 offen offset:0 lds\n"
"v_mfma_f32_16x16x32_bf16 v[132:135], a[44:47], a[4:7], v[132:135]\n"
"ds_read_b64_tr_b16 a[108:109], v5 offset:24576\n"
"v_mfma_f32_16x16x32_bf16 v[136:139], a[52:55], a[4:7], v[136:139]\n"
"ds_read_b64_tr_b16 a[110:111], v5 offset:25600\n"
"v_mfma_f32_16x16x32_bf16 v[140:143], a[60:63], a[4:7], v[140:143]\n"
"ds_read_b128 a[84:87], v4 offset:20480\n"
"buffer_load_dwordx4 v26, s[52:55], s74 offen offset:1024 lds\n"
"v_mfma_f32_16x16x32_bf16 v[156:159], a[60:63], a[12:15], v[156:159]\n"
"ds_read_b64_tr_b16 a[116:117], v6 offset:24576\n"
"v_mfma_f32_16x16x32_bf16 v[152:155], a[52:55], a[12:15], v[152:155]\n"
"ds_read_b64_tr_b16 a[118:119], v6 offset:25600\n"
"v_mfma_f32_16x16x32_bf16 v[148:151], a[44:47], a[12:15], v[148:151]\n"
"ds_read_b128 a[92:95], v4 offset:22528\n"
"v_mfma_f32_16x16x32_bf16 v[144:147], a[36:39], a[12:15], v[144:147]\n"
"ds_read_b64_tr_b16 a[124:125], v7 offset:24576\n"
"buffer_load_dwordx4 v27, s[52:55], s74 offen offset:2048 lds\n"
"v_mfma_f32_16x16x32_bf16 v[160:163], a[36:39], a[20:23], v[160:163]\n"
"ds_read_b64_tr_b16 a[126:127], v7 offset:25600\n"
"v_mfma_f32_16x16x32_bf16 v[164:167], a[44:47], a[20:23], v[164:167]\n"
"v_mfma_f32_16x16x32_bf16 v[168:171], a[52:55], a[20:23], v[168:171]\n"
"v_mfma_f32_16x16x32_bf16 v[172:175], a[60:63], a[20:23], v[172:175]\n"
"buffer_load_dwordx4 v28, s[52:55], s74 offen offset:3072 lds\n"
"s_add_u32 s74, s74, 131072\n"
"v_mfma_f32_16x16x32_bf16 v[188:191], a[60:63], a[28:31], v[188:191]\n"
"v_mfma_f32_16x16x32_bf16 v[184:187], a[52:55], a[28:31], v[184:187]\n"
"v_mfma_f32_16x16x32_bf16 v[180:183], a[44:47], a[28:31], v[180:183]\n"
"v_mfma_f32_16x16x32_bf16 v[176:179], a[36:39], a[28:31], v[176:179]\n"
"s_waitcnt vmcnt(16) lgkmcnt(0)\n"
"s_barrier\n"
"v_mfma_f32_16x16x32_bf16 v[128:131], a[96:99], a[64:67], v[128:131]\n"
"ds_read_b128 a[0:3], v23 offset:32768\n"
"s_add_u32 m0, s76, 16384\n"
"v_mfma_f32_16x16x32_bf16 v[132:135], a[104:107], a[64:67], v[132:135]\n"
"ds_read_b64_tr_b16 a[32:33], v24 offset:32768\n"
"buffer_load_dwordx4 v0, s[20:23], s72 offen offset:0 sc1 lds\n"
"v_mfma_f32_16x16x32_bf16 v[136:139], a[112:115], a[64:67], v[136:139]\n"
"ds_read_b64_tr_b16 a[34:35], v24 offset:33792\n"
"v_mfma_f32_16x16x32_bf16 v[140:143], a[120:123], a[64:67], v[140:143]\n"
"ds_read_b128 a[8:11], v23 offset:34816\n"
"v_mfma_f32_16x16x32_bf16 v[156:159], a[120:123], a[72:75], v[156:159]\n"
"ds_read_b64_tr_b16 a[40:41], v5 offset:32768\n"
"buffer_load_dwordx4 v0, s[20:23], s72 offen offset:1024 sc1 lds\n"
"v_mfma_f32_16x16x32_bf16 v[152:155], a[112:115], a[72:75], v[152:155]\n"
"ds_read_b64_tr_b16 a[42:43], v5 offset:33792\n"
"v_mfma_f32_16x16x32_bf16 v[148:151], a[104:107], a[72:75], v[148:151]\n"
"ds_read_b128 a[16:19], v23 offset:36864\n"
"v_mfma_f32_16x16x32_bf16 v[144:147], a[96:99], a[72:75], v[144:147]\n"
"ds_read_b64_tr_b16 a[48:49], v6 offset:32768\n"
"v_mfma_f32_16x16x32_bf16 v[160:163], a[96:99], a[80:83], v[160:163]\n"
"ds_read_b64_tr_b16 a[50:51], v6 offset:33792\n"
"buffer_load_dwordx4 v0, s[20:23], s72 offen offset:2048 sc1 lds\n"
"v_mfma_f32_16x16x32_bf16 v[164:167], a[104:107], a[80:83], v[164:167]\n"
"ds_read_b128 a[24:27], v23 offset:38912\n"
"v_mfma_f32_16x16x32_bf16 v[168:171], a[112:115], a[80:83], v[168:171]\n"
"ds_read_b64_tr_b16 a[56:57], v7 offset:32768\n"
"v_mfma_f32_16x16x32_bf16 v[172:175], a[120:123], a[80:83], v[172:175]\n"
"ds_read_b64_tr_b16 a[58:59], v7 offset:33792\n"
"v_mfma_f32_16x16x32_bf16 v[188:191], a[120:123], a[88:91], v[188:191]\n"
"buffer_load_dwordx4 v0, s[20:23], s72 offen offset:3072 sc1 lds\n"
"s_add_u32 s72, s72, 65536\n"
"v_mfma_f32_16x16x32_bf16 v[184:187], a[112:115], a[88:91], v[184:187]\n"
"ds_read_b128 a[4:7], v4 offset:32768\n"
"v_mfma_f32_16x16x32_bf16 v[180:183], a[104:107], a[88:91], v[180:183]\n"
"ds_read_b64_tr_b16 a[36:37], v24 offset:40960\n"
"v_mfma_f32_16x16x32_bf16 v[176:179], a[96:99], a[88:91], v[176:179]\n"
"ds_read_b64_tr_b16 a[38:39], v24 offset:41984\n"
"s_add_u32 m0, s77, 16384\n"
"v_mfma_f32_16x16x32_bf16 v[128:131], a[100:103], a[68:71], v[128:131]\n"
"ds_read_b128 a[12:15], v4 offset:34816\n"
"buffer_load_dwordx4 v25, s[52:55], s74 offen offset:0 lds\n"
"v_mfma_f32_16x16x32_bf16 v[132:135], a[108:111], a[68:71], v[132:135]\n"
"ds_read_b64_tr_b16 a[44:45], v5 offset:40960\n"
"v_mfma_f32_16x16x32_bf16 v[136:139], a[116:119], a[68:71], v[136:139]\n"
"ds_read_b64_tr_b16 a[46:47], v5 offset:41984\n"
"v_mfma_f32_16x16x32_bf16 v[140:143], a[124:127], a[68:71], v[140:143]\n"
"ds_read_b128 a[20:23], v4 offset:36864\n"
"buffer_load_dwordx4 v26, s[52:55], s74 offen offset:1024 lds\n"
"v_mfma_f32_16x16x32_bf16 v[156:159], a[124:127], a[76:79], v[156:159]\n"
"ds_read_b64_tr_b16 a[52:53], v6 offset:40960\n"
"v_mfma_f32_16x16x32_bf16 v[152:155], a[116:119], a[76:79], v[152:155]\n"
"ds_read_b64_tr_b16 a[54:55], v6 offset:41984\n"
"v_mfma_f32_16x16x32_bf16 v[148:151], a[108:111], a[76:79], v[148:151]\n"
"ds_read_b128 a[28:31], v4 offset:38912\n"
"v_mfma_f32_16x16x32_bf16 v[144:147], a[100:103], a[76:79], v[144:147]\n"
"ds_read_b64_tr_b16 a[60:61], v7 offset:40960\n"
"buffer_load_dwordx4 v27, s[52:55], s74 offen offset:2048 lds\n"
"v_mfma_f32_16x16x32_bf16 v[160:163], a[100:103], a[84:87], v[160:163]\n"
"ds_read_b64_tr_b16 a[62:63], v7 offset:41984\n"
"v_mfma_f32_16x16x32_bf16 v[164:167], a[108:111], a[84:87], v[164:167]\n"
"v_mfma_f32_16x16x32_bf16 v[168:171], a[116:119], a[84:87], v[168:171]\n"
"v_mfma_f32_16x16x32_bf16 v[172:175], a[124:127], a[84:87], v[172:175]\n"
"buffer_load_dwordx4 v28, s[52:55], s74 offen offset:3072 lds\n"
"s_add_u32 s74, s74, 524288\n"
"v_mfma_f32_16x16x32_bf16 v[188:191], a[124:127], a[92:95], v[188:191]\n"
"v_mfma_f32_16x16x32_bf16 v[184:187], a[116:119], a[92:95], v[184:187]\n"
"v_mfma_f32_16x16x32_bf16 v[180:183], a[108:111], a[92:95], v[180:183]\n"
"v_mfma_f32_16x16x32_bf16 v[176:179], a[100:103], a[92:95], v[176:179]\n"
"s_waitcnt vmcnt(16) lgkmcnt(0)\n"
"s_barrier\n"
"v_mfma_f32_16x16x32_bf16 v[128:131], a[32:35], a[0:3], v[128:131]\n"
"ds_read_b128 a[64:67], v23 offset:49152\n"
"s_add_u32 m0, s76, 32768\n"
"v_mfma_f32_16x16x32_bf16 v[132:135], a[40:43], a[0:3], v[132:135]\n"
"ds_read_b64_tr_b16 a[96:97], v24 offset:49152\n"
"buffer_load_dwordx4 v0, s[20:23], s72 offen offset:0 sc1 lds\n"
"v_mfma_f32_16x16x32_bf16 v[136:139], a[48:51], a[0:3], v[136:139]\n"
"ds_read_b64_tr_b16 a[98:99], v24 offset:50176\n"
"v_mfma_f32_16x16x32_bf16 v[140:143], a[56:59], a[0:3], v[140:143]\n"
"ds_read_b128 a[72:75], v23 offset:51200\n"
"v_mfma_f32_16x16x32_bf16 v[156:159], a[56:59], a[8:11], v[156:159]\n"
"ds_read_b64_tr_b16 a[104:105], v5 offset:49152\n"
"buffer_load_dwordx4 v0, s[20:23], s72 offen offset:1024 sc1 lds\n"
"v_mfma_f32_16x16x32_bf16 v[152:155], a[48:51], a[8:11], v[152:155]\n"
"ds_read_b64_tr_b16 a[106:107], v5 offset:50176\n"
"v_mfma_f32_16x16x32_bf16 v[148:151], a[40:43], a[8:11], v[148:151]\n"
"ds_read_b128 a[80:83], v23 offset:53248\n"
"v_mfma_f32_16x16x32_bf16 v[144:147], a[32:35], a[8:11], v[144:147]\n"
"ds_read_b64_tr_b16 a[112:113], v6 offset:49152\n"
"v_mfma_f32_16x16x32_bf16 v[160:163], a[32:35], a[16:19], v[160:163]\n"
"ds_read_b64_tr_b16 a[114:115], v6 offset:50176\n"
"buffer_load_dwordx4 v0, s[20:23], s72 offen offset:2048 sc1 lds\n"
"v_mfma_f32_16x16x32_bf16 v[164:167], a[40:43], a[16:19], v[164:167]\n"
"ds_read_b128 a[88:91], v23 offset:55296\n"
"v_mfma_f32_16x16x32_bf16 v[168:171], a[48:51], a[16:19], v[168:171]\n"
"ds_read_b64_tr_b16 a[120:121], v7 offset:49152\n"
"v_mfma_f32_16x16x32_bf16 v[172:175], a[56:59], a[16:19], v[172:175]\n"
"ds_read_b64_tr_b16 a[122:123], v7 offset:50176\n"
"v_mfma_f32_16x16x32_bf16 v[188:191], a[56:59], a[24:27], v[188:191]\n"
"buffer_load_dwordx4 v0, s[20:23], s72 offen offset:3072 sc1 lds\n"
"s_add_u32 s72, s72, 16384\n"
"v_mfma_f32_16x16x32_bf16 v[184:187], a[48:51], a[24:27], v[184:187]\n"
"ds_read_b128 a[68:71], v4 offset:49152\n"
"v_mfma_f32_16x16x32_bf16 v[180:183], a[40:43], a[24:27], v[180:183]\n"
"ds_read_b64_tr_b16 a[100:101], v24 offset:57344\n"
"v_mfma_f32_16x16x32_bf16 v[176:179], a[32:35], a[24:27], v[176:179]\n"
"ds_read_b64_tr_b16 a[102:103], v24 offset:58368\n"
"s_add_u32 m0, s77, 32768\n"
"v_mfma_f32_16x16x32_bf16 v[128:131], a[36:39], a[4:7], v[128:131]\n"
"ds_read_b128 a[76:79], v4 offset:51200\n"
"buffer_load_dwordx4 v25, s[52:55], s74 offen offset:0 lds\n"
"v_mfma_f32_16x16x32_bf16 v[132:135], a[44:47], a[4:7], v[132:135]\n"
"ds_read_b64_tr_b16 a[108:109], v5 offset:57344\n"
"v_mfma_f32_16x16x32_bf16 v[136:139], a[52:55], a[4:7], v[136:139]\n"
"ds_read_b64_tr_b16 a[110:111], v5 offset:58368\n"
"v_mfma_f32_16x16x32_bf16 v[140:143], a[60:63], a[4:7], v[140:143]\n"
"ds_read_b128 a[84:87], v4 offset:53248\n"
"buffer_load_dwordx4 v26, s[52:55], s74 offen offset:1024 lds\n"
"v_mfma_f32_16x16x32_bf16 v[156:159], a[60:63], a[12:15], v[156:159]\n"
"ds_read_b64_tr_b16 a[116:117], v6 offset:57344\n"
"v_mfma_f32_16x16x32_bf16 v[152:155], a[52:55], a[12:15], v[152:155]\n"
"ds_read_b64_tr_b16 a[118:119], v6 offset:58368\n"
"v_mfma_f32_16x16x32_bf16 v[148:151], a[44:47], a[12:15], v[148:151]\n"
"ds_read_b128 a[92:95], v4 offset:55296\n"
"v_mfma_f32_16x16x32_bf16 v[144:147], a[36:39], a[12:15], v[144:147]\n"
"ds_read_b64_tr_b16 a[124:125], v7 offset:57344\n"
"buffer_load_dwordx4 v27, s[52:55], s74 offen offset:2048 lds\n"
"v_mfma_f32_16x16x32_bf16 v[160:163], a[36:39], a[20:23], v[160:163]\n"
"ds_read_b64_tr_b16 a[126:127], v7 offset:58368\n"
"v_mfma_f32_16x16x32_bf16 v[164:167], a[44:47], a[20:23], v[164:167]\n"
"v_mfma_f32_16x16x32_bf16 v[168:171], a[52:55], a[20:23], v[168:171]\n"
"v_mfma_f32_16x16x32_bf16 v[172:175], a[60:63], a[20:23], v[172:175]\n"
"buffer_load_dwordx4 v28, s[52:55], s74 offen offset:3072 lds\n"
"s_add_u32 s74, s74, 131072\n"
"v_mfma_f32_16x16x32_bf16 v[188:191], a[60:63], a[28:31], v[188:191]\n"
"v_mfma_f32_16x16x32_bf16 v[184:187], a[52:55], a[28:31], v[184:187]\n"
"v_mfma_f32_16x16x32_bf16 v[180:183], a[44:47], a[28:31], v[180:183]\n"
"v_mfma_f32_16x16x32_bf16 v[176:179], a[36:39], a[28:31], v[176:179]\n"
"s_waitcnt vmcnt(16) lgkmcnt(0)\n"
"s_barrier\n"
"v_mfma_f32_16x16x32_bf16 v[128:131], a[96:99], a[64:67], v[128:131]\n"
"ds_read_b128 a[0:3], v23 offset:0\n"
"s_add_u32 m0, s76, 49152\n"
"v_mfma_f32_16x16x32_bf16 v[132:135], a[104:107], a[64:67], v[132:135]\n"
"ds_read_b64_tr_b16 a[32:33], v24 offset:0\n"
"buffer_load_dwordx4 v0, s[20:23], s72 offen offset:0 sc1 lds\n"
"v_mfma_f32_16x16x32_bf16 v[136:139], a[112:115], a[64:67], v[136:139]\n"
"ds_read_b64_tr_b16 a[34:35], v24 offset:1024\n"
"v_mfma_f32_16x16x32_bf16 v[140:143], a[120:123], a[64:67], v[140:143]\n"
"ds_read_b128 a[8:11], v23 offset:2048\n"
"v_mfma_f32_16x16x32_bf16 v[156:159], a[120:123], a[72:75], v[156:159]\n"
"ds_read_b64_tr_b16 a[40:41], v5 offset:0\n"
"buffer_load_dwordx4 v0, s[20:23], s72 offen offset:1024 sc1 lds\n"
"v_mfma_f32_16x16x32_bf16 v[152:155], a[112:115], a[72:75], v[152:155]\n"
"ds_read_b64_tr_b16 a[42:43], v5 offset:1024\n"
"v_mfma_f32_16x16x32_bf16 v[148:151], a[104:107], a[72:75], v[148:151]\n"
"ds_read_b128 a[16:19], v23 offset:4096\n"
"v_mfma_f32_16x16x32_bf16 v[144:147], a[96:99], a[72:75], v[144:147]\n"
"ds_read_b64_tr_b16 a[48:49], v6 offset:0\n"
"v_mfma_f32_16x16x32_bf16 v[160:163], a[96:99], a[80:83], v[160:163]\n"
"ds_read_b64_tr_b16 a[50:51], v6 offset:1024\n"
"buffer_load_dwordx4 v0, s[20:23], s72 offen offset:2048 sc1 lds\n"
"v_mfma_f32_16x16x32_bf16 v[164:167], a[104:107], a[80:83], v[164:167]\n"
"ds_read_b128 a[24:27], v23 offset:6144\n"
"v_mfma_f32_16x16x32_bf16 v[168:171], a[112:115], a[80:83], v[168:171]\n"
"ds_read_b64_tr_b16 a[56:57], v7 offset:0\n"
"v_mfma_f32_16x16x32_bf16 v[172:175], a[120:123], a[80:83], v[172:175]\n"
"ds_read_b64_tr_b16 a[58:59], v7 offset:1024\n"
"v_mfma_f32_16x16x32_bf16 v[188:191], a[120:123], a[88:91], v[188:191]\n"
"buffer_load_dwordx4 v0, s[20:23], s72 offen offset:3072 sc1 lds\n"
"s_add_u32 s72, s72, 16384\n"
"v_mfma_f32_16x16x32_bf16 v[184:187], a[112:115], a[88:91], v[184:187]\n"
"ds_read_b128 a[4:7], v4 offset:0\n"
"v_mfma_f32_16x16x32_bf16 v[180:183], a[104:107], a[88:91], v[180:183]\n"
"ds_read_b64_tr_b16 a[36:37], v24 offset:8192\n"
"v_mfma_f32_16x16x32_bf16 v[176:179], a[96:99], a[88:91], v[176:179]\n"
"ds_read_b64_tr_b16 a[38:39], v24 offset:9216\n"
"s_add_u32 m0, s77, 49152\n"
"v_mfma_f32_16x16x32_bf16 v[128:131], a[100:103], a[68:71], v[128:131]\n"
"ds_read_b128 a[12:15], v4 offset:2048\n"
"buffer_load_dwordx4 v25, s[52:55], s74 offen offset:0 lds\n"
"v_mfma_f32_16x16x32_bf16 v[132:135], a[108:111], a[68:71], v[132:135]\n"
"ds_read_b64_tr_b16 a[44:45], v5 offset:8192\n"
"v_mfma_f32_16x16x32_bf16 v[136:139], a[116:119], a[68:71], v[136:139]\n"
"ds_read_b64_tr_b16 a[46:47], v5 offset:9216\n"
"v_mfma_f32_16x16x32_bf16 v[140:143], a[124:127], a[68:71], v[140:143]\n"
"ds_read_b128 a[20:23], v4 offset:4096\n"
"buffer_load_dwordx4 v26, s[52:55], s74 offen offset:1024 lds\n"
"v_mfma_f32_16x16x32_bf16 v[156:159], a[124:127], a[76:79], v[156:159]\n"
"ds_read_b64_tr_b16 a[52:53], v6 offset:8192\n"
"v_mfma_f32_16x16x32_bf16 v[152:155], a[116:119], a[76:79], v[152:155]\n"
"ds_read_b64_tr_b16 a[54:55], v6 offset:9216\n"
"v_mfma_f32_16x16x32_bf16 v[148:151], a[108:111], a[76:79], v[148:151]\n"
"ds_read_b128 a[28:31], v4 offset:6144\n"
"v_mfma_f32_16x16x32_bf16 v[144:147], a[100:103], a[76:79], v[144:147]\n"
"ds_read_b64_tr_b16 a[60:61], v7 offset:8192\n"
"buffer_load_dwordx4 v27, s[52:55], s74 offen offset:2048 lds\n"
"v_mfma_f32_16x16x32_bf16 v[160:163], a[100:103], a[84:87], v[160:163]\n"
"ds_read_b64_tr_b16 a[62:63], v7 offset:9216\n"
"v_mfma_f32_16x16x32_bf16 v[164:167], a[108:111], a[84:87], v[164:167]\n"
"v_mfma_f32_16x16x32_bf16 v[168:171], a[116:119], a[84:87], v[168:171]\n"
"v_mfma_f32_16x16x32_bf16 v[172:175], a[124:127], a[84:87], v[172:175]\n"
"buffer_load_dwordx4 v28, s[52:55], s74 offen offset:3072 lds\n"
"s_add_u32 s74, s74, 131072\n"
"v_mfma_f32_16x16x32_bf16 v[188:191], a[124:127], a[92:95], v[188:191]\n"
"v_mfma_f32_16x16x32_bf16 v[184:187], a[116:119], a[92:95], v[184:187]\n"
"v_mfma_f32_16x16x32_bf16 v[180:183], a[108:111], a[92:95], v[180:183]\n"
"v_mfma_f32_16x16x32_bf16 v[176:179], a[100:103], a[92:95], v[176:179]\n"
"s_waitcnt vmcnt(16) lgkmcnt(0)\n"
"s_barrier\n"
"v_mfma_f32_16x16x32_bf16 v[128:131], a[32:35], a[0:3], v[128:131]\n"
"ds_read_b128 a[64:67], v23 offset:16384\n"
"s_add_u32 m0, s76, 0\n"
"v_mfma_f32_16x16x32_bf16 v[132:135], a[40:43], a[0:3], v[132:135]\n"
"ds_read_b64_tr_b16 a[96:97], v24 offset:16384\n"
"buffer_load_dwordx4 v0, s[20:23], s72 offen offset:0 sc1 lds\n"
"v_mfma_f32_16x16x32_bf16 v[136:139], a[48:51], a[0:3], v[136:139]\n"
"ds_read_b64_tr_b16 a[98:99], v24 offset:17408\n"
"v_mfma_f32_16x16x32_bf16 v[140:143], a[56:59], a[0:3], v[140:143]\n"
"ds_read_b128 a[72:75], v23 offset:18432\n"
"v_mfma_f32_16x16x32_bf16 v[156:159], a[56:59], a[8:11], v[156:159]\n"
"ds_read_b64_tr_b16 a[104:105], v5 offset:16384\n"
"buffer_load_dwordx4 v0, s[20:23], s72 offen offset:1024 sc1 lds\n"
"v_mfma_f32_16x16x32_bf16 v[152:155], a[48:51], a[8:11], v[152:155]\n"
"ds_read_b64_tr_b16 a[106:107], v5 offset:17408\n"
"v_mfma_f32_16x16x32_bf16 v[148:151], a[40:43], a[8:11], v[148:151]\n"
"ds_read_b128 a[80:83], v23 offset:20480\n"
"v_mfma_f32_16x16x32_bf16 v[144:147], a[32:35], a[8:11], v[144:147]\n"
"ds_read_b64_tr_b16 a[112:113], v6 offset:16384\n"
"v_mfma_f32_16x16x32_bf16 v[160:163], a[32:35], a[16:19], v[160:163]\n"
"ds_read_b64_tr_b16 a[114:115], v6 offset:17408\n"
"buffer_load_dwordx4 v0, s[20:23], s72 offen offset:2048 sc1 lds\n"
"v_mfma_f32_16x16x32_bf16 v[164:167], a[40:43], a[16:19], v[164:167]\n"
"ds_read_b128 a[88:91], v23 offset:22528\n"
"v_mfma_f32_16x16x32_bf16 v[168:171], a[48:51], a[16:19], v[168:171]\n"
"ds_read_b64_tr_b16 a[120:121], v7 offset:16384\n"
"v_mfma_f32_16x16x32_bf16 v[172:175], a[56:59], a[16:19], v[172:175]\n"
"ds_read_b64_tr_b16 a[122:123], v7 offset:17408\n"
"v_mfma_f32_16x16x32_bf16 v[188:191], a[56:59], a[24:27], v[188:191]\n"
"buffer_load_dwordx4 v0, s[20:23], s72 offen offset:3072 sc1 lds\n"
"s_add_u32 s72, s72, 16384\n"
"v_mfma_f32_16x16x32_bf16 v[184:187], a[48:51], a[24:27], v[184:187]\n"
"ds_read_b128 a[68:71], v4 offset:16384\n"
"v_mfma_f32_16x16x32_bf16 v[180:183], a[40:43], a[24:27], v[180:183]\n"
"ds_read_b64_tr_b16 a[100:101], v24 offset:24576\n"
"v_mfma_f32_16x16x32_bf16 v[176:179], a[32:35], a[24:27], v[176:179]\n"
"ds_read_b64_tr_b16 a[102:103], v24 offset:25600\n"
"s_add_u32 m0, s77, 0\n"
"v_mfma_f32_16x16x32_bf16 v[128:131], a[36:39], a[4:7], v[128:131]\n"
"ds_read_b128 a[76:79], v4 offset:18432\n"
"buffer_load_dwordx4 v25, s[52:55], s74 offen offset:0 lds\n"
"v_mfma_f32_16x16x32_bf16 v[132:135], a[44:47], a[4:7], v[132:135]\n"
"ds_read_b64_tr_b16 a[108:109], v5 offset:24576\n"
"v_mfma_f32_16x16x32_bf16 v[136:139], a[52:55], a[4:7], v[136:139]\n"
"ds_read_b64_tr_b16 a[110:111], v5 offset:25600\n"
"v_mfma_f32_16x16x32_bf16 v[140:143], a[60:63], a[4:7], v[140:143]\n"
"ds_read_b128 a[84:87], v4 offset:20480\n"
"buffer_load_dwordx4 v26, s[52:55], s74 offen offset:1024 lds\n"
"v_mfma_f32_16x16x32_bf16 v[156:159], a[60:63], a[12:15], v[156:159]\n"
"ds_read_b64_tr_b16 a[116:117], v6 offset:24576\n"
"v_mfma_f32_16x16x32_bf16 v[152:155], a[52:55], a[12:15], v[152:155]\n"
"ds_read_b64_tr_b16 a[118:119], v6 offset:25600\n"
"v_mfma_f32_16x16x32_bf16 v[148:151], a[44:47], a[12:15], v[148:151]\n"
"ds_read_b128 a[92:95], v4 offset:22528\n"
"v_mfma_f32_16x16x32_bf16 v[144:147], a[36:39], a[12:15], v[144:147]\n"
"ds_read_b64_tr_b16 a[124:125], v7 offset:24576\n"
"buffer_load_dwordx4 v27, s[52:55], s74 offen offset:2048 lds\n"
"v_mfma_f32_16x16x32_bf16 v[160:163], a[36:39], a[20:23], v[160:163]\n"
"ds_read_b64_tr_b16 a[126:127], v7 offset:25600\n"
"v_mfma_f32_16x16x32_bf16 v[164:167], a[44:47], a[20:23], v[164:167]\n"
"v_mfma_f32_16x16x32_bf16 v[168:171], a[52:55], a[20:23], v[168:171]\n"
"v_mfma_f32_16x16x32_bf16 v[172:175], a[60:63], a[20:23], v[172:175]\n"
"buffer_load_dwordx4 v28, s[52:55], s74 offen offset:3072 lds\n"
"s_add_u32 s74, s74, 131072\n"
"v_mfma_f32_16x16x32_bf16 v[188:191], a[60:63], a[28:31], v[188:191]\n"
"v_mfma_f32_16x16x32_bf16 v[184:187], a[52:55], a[28:31], v[184:187]\n"
"v_mfma_f32_16x16x32_bf16 v[180:183], a[44:47], a[28:31], v[180:183]\n"
"v_mfma_f32_16x16x32_bf16 v[176:179], a[36:39], a[28:31], v[176:179]\n"
"s_waitcnt vmcnt(16) lgkmcnt(0)\n"
"s_barrier\n"
"v_mfma_f32_16x16x32_bf16 v[128:131], a[96:99], a[64:67], v[128:131]\n"
"ds_read_b128 a[0:3], v23 offset:32768\n"
"s_add_u32 m0, s76, 16384\n"
"buffer_load_dwordx2 v[250:251], v9, s[20:23], s75 offen sc1\n"
"v_mfma_f32_16x16x32_bf16 v[132:135], a[104:107], a[64:67], v[132:135]\n"
"ds_read_b64_tr_b16 a[32:33], v24 offset:32768\n"
"buffer_load_dwordx4 v0, s[20:23], s72 offen offset:0 sc1 lds\n"
"v_mfma_f32_16x16x32_bf16 v[136:139], a[112:115], a[64:67], v[136:139]\n"
"ds_read_b64_tr_b16 a[34:35], v24 offset:33792\n"
"v_mfma_f32_16x16x32_bf16 v[140:143], a[120:123], a[64:67], v[140:143]\n"
"ds_read_b128 a[8:11], v23 offset:34816\n"
"v_mfma_f32_16x16x32_bf16 v[156:159], a[120:123], a[72:75], v[156:159]\n"
"ds_read_b64_tr_b16 a[40:41], v5 offset:32768\n"
"buffer_load_dwordx4 v0, s[20:23], s72 offen offset:1024 sc1 lds\n"
"v_mfma_f32_16x16x32_bf16 v[152:155], a[112:115], a[72:75], v[152:155]\n"
"ds_read_b64_tr_b16 a[42:43], v5 offset:33792\n"
"v_mfma_f32_16x16x32_bf16 v[148:151], a[104:107], a[72:75], v[148:151]\n"
"ds_read_b128 a[16:19], v23 offset:36864\n"
"v_mfma_f32_16x16x32_bf16 v[144:147], a[96:99], a[72:75], v[144:147]\n"
"ds_read_b64_tr_b16 a[48:49], v6 offset:32768\n"
"v_mfma_f32_16x16x32_bf16 v[160:163], a[96:99], a[80:83], v[160:163]\n"
"ds_read_b64_tr_b16 a[50:51], v6 offset:33792\n"
"buffer_load_dwordx4 v0, s[20:23], s72 offen offset:2048 sc1 lds\n"
"v_mfma_f32_16x16x32_bf16 v[164:167], a[104:107], a[80:83], v[164:167]\n"
"ds_read_b128 a[24:27], v23 offset:38912\n"
"v_mfma_f32_16x16x32_bf16 v[168:171], a[112:115], a[80:83], v[168:171]\n"
"ds_read_b64_tr_b16 a[56:57], v7 offset:32768\n"
"v_mfma_f32_16x16x32_bf16 v[172:175], a[120:123], a[80:83], v[172:175]\n"
"ds_read_b64_tr_b16 a[58:59], v7 offset:33792\n"
"v_mfma_f32_16x16x32_bf16 v[188:191], a[120:123], a[88:91], v[188:191]\n"
"buffer_load_dwordx4 v0, s[20:23], s72 offen offset:3072 sc1 lds\n"
"s_add_u32 s72, s72, 65536\n"
"v_mfma_f32_16x16x32_bf16 v[184:187], a[112:115], a[88:91], v[184:187]\n"
"ds_read_b128 a[4:7], v4 offset:32768\n"
"v_mfma_f32_16x16x32_bf16 v[180:183], a[104:107], a[88:91], v[180:183]\n"
"ds_read_b64_tr_b16 a[36:37], v24 offset:40960\n"
"v_mfma_f32_16x16x32_bf16 v[176:179], a[96:99], a[88:91], v[176:179]\n"
"ds_read_b64_tr_b16 a[38:39], v24 offset:41984\n"
"s_add_u32 m0, s77, 16384\n"
"v_mfma_f32_16x16x32_bf16 v[128:131], a[100:103], a[68:71], v[128:131]\n"
"ds_read_b128 a[12:15], v4 offset:34816\n"
"buffer_load_dwordx4 v25, s[52:55], s74 offen offset:0 lds\n"
"v_mfma_f32_16x16x32_bf16 v[132:135], a[108:111], a[68:71], v[132:135]\n"
"ds_read_b64_tr_b16 a[44:45], v5 offset:40960\n"
"v_mfma_f32_16x16x32_bf16 v[136:139], a[116:119], a[68:71], v[136:139]\n"
"ds_read_b64_tr_b16 a[46:47], v5 offset:41984\n"
"v_mfma_f32_16x16x32_bf16 v[140:143], a[124:127], a[68:71], v[140:143]\n"
"ds_read_b128 a[20:23], v4 offset:36864\n"
"buffer_load_dwordx4 v26, s[52:55], s74 offen offset:1024 lds\n"
"v_mfma_f32_16x16x32_bf16 v[156:159], a[124:127], a[76:79], v[156:159]\n"
"ds_read_b64_tr_b16 a[52:53], v6 offset:40960\n"
"v_mfma_f32_16x16x32_bf16 v[152:155], a[116:119], a[76:79], v[152:155]\n"
"ds_read_b64_tr_b16 a[54:55], v6 offset:41984\n"
"v_mfma_f32_16x16x32_bf16 v[148:151], a[108:111], a[76:79], v[148:151]\n"
"ds_read_b128 a[28:31], v4 offset:38912\n"
"v_mfma_f32_16x16x32_bf16 v[144:147], a[100:103], a[76:79], v[144:147]\n"
"ds_read_b64_tr_b16 a[60:61], v7 offset:40960\n"
"buffer_load_dwordx4 v27, s[52:55], s74 offen offset:2048 lds\n"
"v_mfma_f32_16x16x32_bf16 v[160:163], a[100:103], a[84:87], v[160:163]\n"
"ds_read_b64_tr_b16 a[62:63], v7 offset:41984\n"
"v_mfma_f32_16x16x32_bf16 v[164:167], a[108:111], a[84:87], v[164:167]\n"
"v_mfma_f32_16x16x32_bf16 v[168:171], a[116:119], a[84:87], v[168:171]\n"
"v_mfma_f32_16x16x32_bf16 v[172:175], a[124:127], a[84:87], v[172:175]\n"
"buffer_load_dwordx4 v28, s[52:55], s74 offen offset:3072 lds\n"
"s_add_u32 s74, s74, 524288\n"
"v_mfma_f32_16x16x32_bf16 v[188:191], a[124:127], a[92:95], v[188:191]\n"
"v_mfma_f32_16x16x32_bf16 v[184:187], a[116:119], a[92:95], v[184:187]\n"
"v_mfma_f32_16x16x32_bf16 v[180:183], a[108:111], a[92:95], v[180:183]\n"
"v_mfma_f32_16x16x32_bf16 v[176:179], a[100:103], a[92:95], v[176:179]\n"
"s_waitcnt vmcnt(17) lgkmcnt(0)\n"
"s_barrier\n"
"v_mfma_f32_16x16x32_bf16 v[128:131], a[32:35], a[0:3], v[128:131]\n"
"ds_read_b128 a[64:67], v23 offset:49152\n"
"s_add_u32 m0, s76, 32768\n"
"v_mfma_f32_16x16x32_bf16 v[132:135], a[40:43], a[0:3], v[132:135]\n"
"ds_read_b64_tr_b16 a[96:97], v24 offset:49152\n"
"buffer_load_dwordx4 v0, s[20:23], s72 offen offset:0 sc1 lds\n"
"v_mfma_f32_16x16x32_bf16 v[136:139], a[48:51], a[0:3], v[136:139]\n"
"ds_read_b64_tr_b16 a[98:99], v24 offset:50176\n"
"v_mfma_f32_16x16x32_bf16 v[140:143], a[56:59], a[0:3], v[140:143]\n"
"ds_read_b128 a[72:75], v23 offset:51200\n"
"v_mfma_f32_16x16x32_bf16 v[156:159], a[56:59], a[8:11], v[156:159]\n"
"ds_read_b64_tr_b16 a[104:105], v5 offset:49152\n"
"buffer_load_dwordx4 v0, s[20:23], s72 offen offset:1024 sc1 lds\n"
"v_mfma_f32_16x16x32_bf16 v[152:155], a[48:51], a[8:11], v[152:155]\n"
"ds_read_b64_tr_b16 a[106:107], v5 offset:50176\n"
"v_mfma_f32_16x16x32_bf16 v[148:151], a[40:43], a[8:11], v[148:151]\n"
"ds_read_b128 a[80:83], v23 offset:53248\n"
"v_mfma_f32_16x16x32_bf16 v[144:147], a[32:35], a[8:11], v[144:147]\n"
"ds_read_b64_tr_b16 a[112:113], v6 offset:49152\n"
"v_mfma_f32_16x16x32_bf16 v[160:163], a[32:35], a[16:19], v[160:163]\n"
"ds_read_b64_tr_b16 a[114:115], v6 offset:50176\n"
"buffer_load_dwordx4 v0, s[20:23], s72 offen offset:2048 sc1 lds\n"
"v_mfma_f32_16x16x32_bf16 v[164:167], a[40:43], a[16:19], v[164:167]\n"
"ds_read_b128 a[88:91], v23 offset:55296\n"
"v_mfma_f32_16x16x32_bf16 v[168:171], a[48:51], a[16:19], v[168:171]\n"
"ds_read_b64_tr_b16 a[120:121], v7 offset:49152\n"
"v_mfma_f32_16x16x32_bf16 v[172:175], a[56:59], a[16:19], v[172:175]\n"
"ds_read_b64_tr_b16 a[122:123], v7 offset:50176\n"
"v_mfma_f32_16x16x32_bf16 v[188:191], a[56:59], a[24:27], v[188:191]\n"
"buffer_load_dwordx4 v0, s[20:23], s72 offen offset:3072 sc1 lds\n"
"s_add_u32 s72, s72, 16384\n"
"v_mfma_f32_16x16x32_bf16 v[184:187], a[48:51], a[24:27], v[184:187]\n"
"ds_read_b128 a[68:71], v4 offset:49152\n"
"v_mfma_f32_16x16x32_bf16 v[180:183], a[40:43], a[24:27], v[180:183]\n"
"ds_read_b64_tr_b16 a[100:101], v24 offset:57344\n"
"v_mfma_f32_16x16x32_bf16 v[176:179], a[32:35], a[24:27], v[176:179]\n"
"ds_read_b64_tr_b16 a[102:103], v24 offset:58368\n"
"s_add_u32 m0, s77, 32768\n"
"v_mfma_f32_16x16x32_bf16 v[128:131], a[36:39], a[4:7], v[128:131]\n"
"ds_read_b128 a[76:79], v4 offset:51200\n"
"buffer_load_dwordx4 v25, s[52:55], s74 offen offset:0 lds\n"
"v_mfma_f32_16x16x32_bf16 v[132:135], a[44:47], a[4:7], v[132:135]\n"
"ds_read_b64_tr_b16 a[108:109], v5 offset:57344\n"
"v_mfma_f32_16x16x32_bf16 v[136:139], a[52:55], a[4:7], v[136:139]\n"
"ds_read_b64_tr_b16 a[110:111], v5 offset:58368\n"
"v_mfma_f32_16x16x32_bf16 v[140:143], a[60:63], a[4:7], v[140:143]\n"
"ds_read_b128 a[84:87], v4 offset:53248\n"
"buffer_load_dwordx4 v26, s[52:55], s74 offen offset:1024 lds\n"
"v_mfma_f32_16x16x32_bf16 v[156:159], a[60:63], a[12:15], v[156:159]\n"
"ds_read_b64_tr_b16 a[116:117], v6 offset:57344\n"
"v_mfma_f32_16x16x32_bf16 v[152:155], a[52:55], a[12:15], v[152:155]\n"
"ds_read_b64_tr_b16 a[118:119], v6 offset:58368\n"
"v_mfma_f32_16x16x32_bf16 v[148:151], a[44:47], a[12:15], v[148:151]\n"
"ds_read_b128 a[92:95], v4 offset:55296\n"
"v_mfma_f32_16x16x32_bf16 v[144:147], a[36:39], a[12:15], v[144:147]\n"
"ds_read_b64_tr_b16 a[124:125], v7 offset:57344\n"
"buffer_load_dwordx4 v27, s[52:55], s74 offen offset:2048 lds\n"
"v_mfma_f32_16x16x32_bf16 v[160:163], a[36:39], a[20:23], v[160:163]\n"
"ds_read_b64_tr_b16 a[126:127], v7 offset:58368\n"
"v_mfma_f32_16x16x32_bf16 v[164:167], a[44:47], a[20:23], v[164:167]\n"
"v_mfma_f32_16x16x32_bf16 v[168:171], a[52:55], a[20:23], v[168:171]\n"
"v_mfma_f32_16x16x32_bf16 v[172:175], a[60:63], a[20:23], v[172:175]\n"
"buffer_load_dwordx4 v28, s[52:55], s74 offen offset:3072 lds\n"
"s_add_u32 s74, s74, 131072\n"
"v_mfma_f32_16x16x32_bf16 v[188:191], a[60:63], a[28:31], v[188:191]\n"
"v_mfma_f32_16x16x32_bf16 v[184:187], a[52:55], a[28:31], v[184:187]\n"
"v_mfma_f32_16x16x32_bf16 v[180:183], a[44:47], a[28:31], v[180:183]\n"
"v_mfma_f32_16x16x32_bf16 v[176:179], a[36:39], a[28:31], v[176:179]\n"
"s_waitcnt vmcnt(17) lgkmcnt(0)\n"
"s_barrier\n"
"v_mfma_f32_16x16x32_bf16 v[128:131], a[96:99], a[64:67], v[128:131]\n"
"ds_read_b128 a[0:3], v23 offset:0\n"
"s_add_u32 m0, s76, 49152\n"
"v_mfma_f32_16x16x32_bf16 v[132:135], a[104:107], a[64:67], v[132:135]\n"
"ds_read_b64_tr_b16 a[32:33], v24 offset:0\n"
"buffer_load_dwordx4 v0, s[20:23], s72 offen offset:0 sc1 lds\n"
"v_mfma_f32_16x16x32_bf16 v[136:139], a[112:115], a[64:67], v[136:139]\n"
"ds_read_b64_tr_b16 a[34:35], v24 offset:1024\n"
"v_mfma_f32_16x16x32_bf16 v[140:143], a[120:123], a[64:67], v[140:143]\n"
"ds_read_b128 a[8:11], v23 offset:2048\n"
"v_mfma_f32_16x16x32_bf16 v[156:159], a[120:123], a[72:75], v[156:159]\n"
"ds_read_b64_tr_b16 a[40:41], v5 offset:0\n"
"buffer_load_dwordx4 v0, s[20:23], s72 offen offset:1024 sc1 lds\n"
"v_mfma_f32_16x16x32_bf16 v[152:155], a[112:115], a[72:75], v[152:155]\n"
"ds_read_b64_tr_b16 a[42:43], v5 offset:1024\n"
"v_mfma_f32_16x16x32_bf16 v[148:151], a[104:107], a[72:75], v[148:151]\n"
"ds_read_b128 a[16:19], v23 offset:4096\n"
"v_mfma_f32_16x16x32_bf16 v[144:147], a[96:99], a[72:75], v[144:147]\n"
"ds_read_b64_tr_b16 a[48:49], v6 offset:0\n"
"v_mfma_f32_16x16x32_bf16 v[160:163], a[96:99], a[80:83], v[160:163]\n"
"ds_read_b64_tr_b16 a[50:51], v6 offset:1024\n"
"buffer_load_dwordx4 v0, s[20:23], s72 offen offset:2048 sc1 lds\n"
"v_mfma_f32_16x16x32_bf16 v[164:167], a[104:107], a[80:83], v[164:167]\n"
"ds_read_b128 a[24:27], v23 offset:6144\n"
"v_mfma_f32_16x16x32_bf16 v[168:171], a[112:115], a[80:83], v[168:171]\n"
"ds_read_b64_tr_b16 a[56:57], v7 offset:0\n"
"v_mfma_f32_16x16x32_bf16 v[172:175], a[120:123], a[80:83], v[172:175]\n"
"ds_read_b64_tr_b16 a[58:59], v7 offset:1024\n"
"v_mfma_f32_16x16x32_bf16 v[188:191], a[120:123], a[88:91], v[188:191]\n"
"buffer_load_dwordx4 v0, s[20:23], s72 offen offset:3072 sc1 lds\n"
"s_sub_u32 s72, s72, 409600\n"
"v_mfma_f32_16x16x32_bf16 v[184:187], a[112:115], a[88:91], v[184:187]\n"
"ds_read_b128 a[4:7], v4 offset:0\n"
"v_mfma_f32_16x16x32_bf16 v[180:183], a[104:107], a[88:91], v[180:183]\n"
"ds_read_b64_tr_b16 a[36:37], v24 offset:8192\n"
"v_mfma_f32_16x16x32_bf16 v[176:179], a[96:99], a[88:91], v[176:179]\n"
"ds_read_b64_tr_b16 a[38:39], v24 offset:9216\n"
"s_add_u32 m0, s77, 49152\n"
"v_mfma_f32_16x16x32_bf16 v[128:131], a[100:103], a[68:71], v[128:131]\n"
"ds_read_b128 a[12:15], v4 offset:2048\n"
"buffer_load_dwordx4 v25, s[52:55], s74 offen offset:0 lds\n"
"v_mfma_f32_16x16x32_bf16 v[132:135], a[108:111], a[68:71], v[132:135]\n"
"ds_read_b64_tr_b16 a[44:45], v5 offset:8192\n"
"v_mfma_f32_16x16x32_bf16 v[136:139], a[116:119], a[68:71], v[136:139]\n"
"ds_read_b64_tr_b16 a[46:47], v5 offset:9216\n"
"v_mfma_f32_16x16x32_bf16 v[140:143], a[124:127], a[68:71], v[140:143]\n"
"ds_read_b128 a[20:23], v4 offset:4096\n"
"buffer_load_dwordx4 v26, s[52:55], s74 offen offset:1024 lds\n"
"v_mfma_f32_16x16x32_bf16 v[156:159], a[124:127], a[76:79], v[156:159]\n"
"ds_read_b64_tr_b16 a[52:53], v6 offset:8192\n"
"v_mfma_f32_16x16x32_bf16 v[152:155], a[116:119], a[76:79], v[152:155]\n"
"ds_read_b64_tr_b16 a[54:55], v6 offset:9216\n"
"v_mfma_f32_16x16x32_bf16 v[148:151], a[108:111], a[76:79], v[148:151]\n"
"ds_read_b128 a[28:31], v4 offset:6144\n"
"v_mfma_f32_16x16x32_bf16 v[144:147], a[100:103], a[76:79], v[144:147]\n"
"ds_read_b64_tr_b16 a[60:61], v7 offset:8192\n"
"buffer_load_dwordx4 v27, s[52:55], s74 offen offset:2048 lds\n"
"v_mfma_f32_16x16x32_bf16 v[160:163], a[100:103], a[84:87], v[160:163]\n"
"ds_read_b64_tr_b16 a[62:63], v7 offset:9216\n"
"v_mfma_f32_16x16x32_bf16 v[164:167], a[108:111], a[84:87], v[164:167]\n"
"v_mfma_f32_16x16x32_bf16 v[168:171], a[116:119], a[84:87], v[168:171]\n"
"v_mfma_f32_16x16x32_bf16 v[172:175], a[124:127], a[84:87], v[172:175]\n"
"buffer_load_dwordx4 v28, s[52:55], s74 offen offset:3072 lds\n"
"s_sub_u32 s74, s74, 3276800\n"
"v_mfma_f32_16x16x32_bf16 v[188:191], a[124:127], a[92:95], v[188:191]\n"
"v_mfma_f32_16x16x32_bf16 v[184:187], a[116:119], a[92:95], v[184:187]\n"
"v_mfma_f32_16x16x32_bf16 v[180:183], a[108:111], a[92:95], v[180:183]\n"
"v_mfma_f32_16x16x32_bf16 v[176:179], a[100:103], a[92:95], v[176:179]\n"
"s_waitcnt vmcnt(16) lgkmcnt(0)\n"
"s_barrier\n"
"s_waitcnt vmcnt(24)\n"
"v_cmp_ne_u64 vcc, s[90:91], v[250:251]\n"
"s_cbranch_vccnz SPKV11_%=\n"
"BKKV12_%=:\n"
"v_mfma_f32_16x16x32_bf16 v[128:131], a[32:35], a[0:3], v[128:131]\n"
"ds_read_b128 a[64:67], v23 offset:16384\n"
"s_add_u32 m0, s76, 0\n"
"v_mfma_f32_16x16x32_bf16 v[132:135], a[40:43], a[0:3], v[132:135]\n"
"ds_read_b64_tr_b16 a[96:97], v24 offset:16384\n"
"buffer_load_dwordx4 v0, s[20:23], s72 offen offset:0 sc1 lds\n"
"v_mfma_f32_16x16x32_bf16 v[136:139], a[48:51], a[0:3], v[136:139]\n"
"ds_read_b64_tr_b16 a[98:99], v24 offset:17408\n"
"v_mfma_f32_16x16x32_bf16 v[140:143], a[56:59], a[0:3], v[140:143]\n"
"ds_read_b128 a[72:75], v23 offset:18432\n"
"v_mfma_f32_16x16x32_bf16 v[156:159], a[56:59], a[8:11], v[156:159]\n"
"ds_read_b64_tr_b16 a[104:105], v5 offset:16384\n"
"buffer_load_dwordx4 v0, s[20:23], s72 offen offset:1024 sc1 lds\n"
"v_mfma_f32_16x16x32_bf16 v[152:155], a[48:51], a[8:11], v[152:155]\n"
"ds_read_b64_tr_b16 a[106:107], v5 offset:17408\n"
"v_mfma_f32_16x16x32_bf16 v[148:151], a[40:43], a[8:11], v[148:151]\n"
"ds_read_b128 a[80:83], v23 offset:20480\n"
"v_mfma_f32_16x16x32_bf16 v[144:147], a[32:35], a[8:11], v[144:147]\n"
"ds_read_b64_tr_b16 a[112:113], v6 offset:16384\n"
"v_mfma_f32_16x16x32_bf16 v[160:163], a[32:35], a[16:19], v[160:163]\n"
"ds_read_b64_tr_b16 a[114:115], v6 offset:17408\n"
"buffer_load_dwordx4 v0, s[20:23], s72 offen offset:2048 sc1 lds\n"
"v_mfma_f32_16x16x32_bf16 v[164:167], a[40:43], a[16:19], v[164:167]\n"
"ds_read_b128 a[88:91], v23 offset:22528\n"
"v_mfma_f32_16x16x32_bf16 v[168:171], a[48:51], a[16:19], v[168:171]\n"
"ds_read_b64_tr_b16 a[120:121], v7 offset:16384\n"
"v_mfma_f32_16x16x32_bf16 v[172:175], a[56:59], a[16:19], v[172:175]\n"
"ds_read_b64_tr_b16 a[122:123], v7 offset:17408\n"
"v_mfma_f32_16x16x32_bf16 v[188:191], a[56:59], a[24:27], v[188:191]\n"
"buffer_load_dwordx4 v0, s[20:23], s72 offen offset:3072 sc1 lds\n"
"s_add_u32 s72, s72, 16384\n"
"v_mfma_f32_16x16x32_bf16 v[184:187], a[48:51], a[24:27], v[184:187]\n"
"ds_read_b128 a[68:71], v4 offset:16384\n"
"v_mfma_f32_16x16x32_bf16 v[180:183], a[40:43], a[24:27], v[180:183]\n"
"ds_read_b64_tr_b16 a[100:101], v24 offset:24576\n"
"v_mfma_f32_16x16x32_bf16 v[176:179], a[32:35], a[24:27], v[176:179]\n"
"ds_read_b64_tr_b16 a[102:103], v24 offset:25600\n"
"s_add_u32 m0, s77, 0\n"
"v_mfma_f32_16x16x32_bf16 v[128:131], a[36:39], a[4:7], v[128:131]\n"
"ds_read_b128 a[76:79], v4 offset:18432\n"
"buffer_load_dwordx4 v25, s[52:55], s74 offen offset:0 lds\n"
"v_mfma_f32_16x16x32_bf16 v[132:135], a[44:47], a[4:7], v[132:135]\n"
"ds_read_b64_tr_b16 a[108:109], v5 offset:24576\n"
"v_mfma_f32_16x16x32_bf16 v[136:139], a[52:55], a[4:7], v[136:139]\n"
"ds_read_b64_tr_b16 a[110:111], v5 offset:25600\n"
"v_mfma_f32_16x16x32_bf16 v[140:143], a[60:63], a[4:7], v[140:143]\n"
"ds_read_b128 a[84:87], v4 offset:20480\n"
"buffer_load_dwordx4 v26, s[52:55], s74 offen offset:1024 lds\n"
"v_mfma_f32_16x16x32_bf16 v[156:159], a[60:63], a[12:15], v[156:159]\n"
"ds_read_b64_tr_b16 a[116:117], v6 offset:24576\n"
"v_mfma_f32_16x16x32_bf16 v[152:155], a[52:55], a[12:15], v[152:155]\n"
"ds_read_b64_tr_b16 a[118:119], v6 offset:25600\n"
"v_mfma_f32_16x16x32_bf16 v[148:151], a[44:47], a[12:15], v[148:151]\n"
"ds_read_b128 a[92:95], v4 offset:22528\n"
"v_mfma_f32_16x16x32_bf16 v[144:147], a[36:39], a[12:15], v[144:147]\n"
"ds_read_b64_tr_b16 a[124:125], v7 offset:24576\n"
"buffer_load_dwordx4 v27, s[52:55], s74 offen offset:2048 lds\n"
"v_mfma_f32_16x16x32_bf16 v[160:163], a[36:39], a[20:23], v[160:163]\n"
"ds_read_b64_tr_b16 a[126:127], v7 offset:25600\n"
"v_mfma_f32_16x16x32_bf16 v[164:167], a[44:47], a[20:23], v[164:167]\n"
"v_mfma_f32_16x16x32_bf16 v[168:171], a[52:55], a[20:23], v[168:171]\n"
"v_mfma_f32_16x16x32_bf16 v[172:175], a[60:63], a[20:23], v[172:175]\n"
"buffer_load_dwordx4 v28, s[52:55], s74 offen offset:3072 lds\n"
"s_add_u32 s74, s74, 131072\n"
"v_mfma_f32_16x16x32_bf16 v[188:191], a[60:63], a[28:31], v[188:191]\n"
"v_mfma_f32_16x16x32_bf16 v[184:187], a[52:55], a[28:31], v[184:187]\n"
"v_mfma_f32_16x16x32_bf16 v[180:183], a[44:47], a[28:31], v[180:183]\n"
"v_mfma_f32_16x16x32_bf16 v[176:179], a[36:39], a[28:31], v[176:179]\n"
"s_waitcnt vmcnt(16) lgkmcnt(0)\n"
"s_barrier\n"
"v_mfma_f32_16x16x32_bf16 v[128:131], a[96:99], a[64:67], v[128:131]\n"
"ds_read_b128 a[0:3], v23 offset:32768\n"
"s_add_u32 m0, s76, 16384\n"
"v_mfma_f32_16x16x32_bf16 v[132:135], a[104:107], a[64:67], v[132:135]\n"
"ds_read_b64_tr_b16 a[32:33], v24 offset:32768\n"
"buffer_load_dwordx4 v0, s[20:23], s72 offen offset:0 sc1 lds\n"
"v_mfma_f32_16x16x32_bf16 v[136:139], a[112:115], a[64:67], v[136:139]\n"
"ds_read_b64_tr_b16 a[34:35], v24 offset:33792\n"
"v_mfma_f32_16x16x32_bf16 v[140:143], a[120:123], a[64:67], v[140:143]\n"
"ds_read_b128 a[8:11], v23 offset:34816\n"
"v_mfma_f32_16x16x32_bf16 v[156:159], a[120:123], a[72:75], v[156:159]\n"
"ds_read_b64_tr_b16 a[40:41], v5 offset:32768\n"
"buffer_load_dwordx4 v0, s[20:23], s72 offen offset:1024 sc1 lds\n"
"v_mfma_f32_16x16x32_bf16 v[152:155], a[112:115], a[72:75], v[152:155]\n"
"ds_read_b64_tr_b16 a[42:43], v5 offset:33792\n"
"v_mfma_f32_16x16x32_bf16 v[148:151], a[104:107], a[72:75], v[148:151]\n"
"ds_read_b128 a[16:19], v23 offset:36864\n"
"v_mfma_f32_16x16x32_bf16 v[144:147], a[96:99], a[72:75], v[144:147]\n"
"ds_read_b64_tr_b16 a[48:49], v6 offset:32768\n"
"v_mfma_f32_16x16x32_bf16 v[160:163], a[96:99], a[80:83], v[160:163]\n"
"ds_read_b64_tr_b16 a[50:51], v6 offset:33792\n"
"buffer_load_dwordx4 v0, s[20:23], s72 offen offset:2048 sc1 lds\n"
"v_mfma_f32_16x16x32_bf16 v[164:167], a[104:107], a[80:83], v[164:167]\n"
"ds_read_b128 a[24:27], v23 offset:38912\n"
"v_mfma_f32_16x16x32_bf16 v[168:171], a[112:115], a[80:83], v[168:171]\n"
"ds_read_b64_tr_b16 a[56:57], v7 offset:32768\n"
"v_mfma_f32_16x16x32_bf16 v[172:175], a[120:123], a[80:83], v[172:175]\n"
"ds_read_b64_tr_b16 a[58:59], v7 offset:33792\n"
"v_mfma_f32_16x16x32_bf16 v[188:191], a[120:123], a[88:91], v[188:191]\n"
"buffer_load_dwordx4 v0, s[20:23], s72 offen offset:3072 sc1 lds\n"
"s_add_u32 s72, s72, 16384\n"
"v_mfma_f32_16x16x32_bf16 v[184:187], a[112:115], a[88:91], v[184:187]\n"
"ds_read_b128 a[4:7], v4 offset:32768\n"
"v_mfma_f32_16x16x32_bf16 v[180:183], a[104:107], a[88:91], v[180:183]\n"
"ds_read_b64_tr_b16 a[36:37], v24 offset:40960\n"
"v_mfma_f32_16x16x32_bf16 v[176:179], a[96:99], a[88:91], v[176:179]\n"
"ds_read_b64_tr_b16 a[38:39], v24 offset:41984\n"
"s_add_u32 m0, s77, 16384\n"
"v_mfma_f32_16x16x32_bf16 v[128:131], a[100:103], a[68:71], v[128:131]\n"
"ds_read_b128 a[12:15], v4 offset:34816\n"
"buffer_load_dwordx4 v25, s[52:55], s74 offen offset:0 lds\n"
"v_mfma_f32_16x16x32_bf16 v[132:135], a[108:111], a[68:71], v[132:135]\n"
"ds_read_b64_tr_b16 a[44:45], v5 offset:40960\n"
"v_mfma_f32_16x16x32_bf16 v[136:139], a[116:119], a[68:71], v[136:139]\n"
"ds_read_b64_tr_b16 a[46:47], v5 offset:41984\n"
"v_mfma_f32_16x16x32_bf16 v[140:143], a[124:127], a[68:71], v[140:143]\n"
"ds_read_b128 a[20:23], v4 offset:36864\n"
"buffer_load_dwordx4 v26, s[52:55], s74 offen offset:1024 lds\n"
"v_mfma_f32_16x16x32_bf16 v[156:159], a[124:127], a[76:79], v[156:159]\n"
"ds_read_b64_tr_b16 a[52:53], v6 offset:40960\n"
"v_mfma_f32_16x16x32_bf16 v[152:155], a[116:119], a[76:79], v[152:155]\n"
"ds_read_b64_tr_b16 a[54:55], v6 offset:41984\n"
"v_mfma_f32_16x16x32_bf16 v[148:151], a[108:111], a[76:79], v[148:151]\n"
"ds_read_b128 a[28:31], v4 offset:38912\n"
"v_mfma_f32_16x16x32_bf16 v[144:147], a[100:103], a[76:79], v[144:147]\n"
"ds_read_b64_tr_b16 a[60:61], v7 offset:40960\n"
"buffer_load_dwordx4 v27, s[52:55], s74 offen offset:2048 lds\n"
"v_mfma_f32_16x16x32_bf16 v[160:163], a[100:103], a[84:87], v[160:163]\n"
"ds_read_b64_tr_b16 a[62:63], v7 offset:41984\n"
"v_mfma_f32_16x16x32_bf16 v[164:167], a[108:111], a[84:87], v[164:167]\n"
"v_mfma_f32_16x16x32_bf16 v[168:171], a[116:119], a[84:87], v[168:171]\n"
"v_mfma_f32_16x16x32_bf16 v[172:175], a[124:127], a[84:87], v[172:175]\n"
"buffer_load_dwordx4 v28, s[52:55], s74 offen offset:3072 lds\n"
"s_add_u32 s74, s74, 131072\n"
"v_mfma_f32_16x16x32_bf16 v[188:191], a[124:127], a[92:95], v[188:191]\n"
"v_mfma_f32_16x16x32_bf16 v[184:187], a[116:119], a[92:95], v[184:187]\n"
"v_mfma_f32_16x16x32_bf16 v[180:183], a[108:111], a[92:95], v[180:183]\n"
"v_mfma_f32_16x16x32_bf16 v[176:179], a[100:103], a[92:95], v[176:179]\n"
"s_waitcnt vmcnt(16) lgkmcnt(0)\n"
"s_barrier\n"
"v_mfma_f32_16x16x32_bf16 v[128:131], a[32:35], a[0:3], v[128:131]\n"
"ds_read_b128 a[64:67], v23 offset:49152\n"
"s_add_u32 m0, s76, 32768\n"
"v_mfma_f32_16x16x32_bf16 v[132:135], a[40:43], a[0:3], v[132:135]\n"
"ds_read_b64_tr_b16 a[96:97], v24 offset:49152\n"
"buffer_load_dwordx4 v0, s[20:23], s72 offen offset:0 sc1 lds\n"
"v_mfma_f32_16x16x32_bf16 v[136:139], a[48:51], a[0:3], v[136:139]\n"
"ds_read_b64_tr_b16 a[98:99], v24 offset:50176\n"
"v_mfma_f32_16x16x32_bf16 v[140:143], a[56:59], a[0:3], v[140:143]\n"
"ds_read_b128 a[72:75], v23 offset:51200\n"
"v_mfma_f32_16x16x32_bf16 v[156:159], a[56:59], a[8:11], v[156:159]\n"
"ds_read_b64_tr_b16 a[104:105], v5 offset:49152\n"
"buffer_load_dwordx4 v0, s[20:23], s72 offen offset:1024 sc1 lds\n"
"v_mfma_f32_16x16x32_bf16 v[152:155], a[48:51], a[8:11], v[152:155]\n"
"ds_read_b64_tr_b16 a[106:107], v5 offset:50176\n"
"v_mfma_f32_16x16x32_bf16 v[148:151], a[40:43], a[8:11], v[148:151]\n"
"ds_read_b128 a[80:83], v23 offset:53248\n"
"v_mfma_f32_16x16x32_bf16 v[144:147], a[32:35], a[8:11], v[144:147]\n"
"ds_read_b64_tr_b16 a[112:113], v6 offset:49152\n"
"v_mfma_f32_16x16x32_bf16 v[160:163], a[32:35], a[16:19], v[160:163]\n"
"ds_read_b64_tr_b16 a[114:115], v6 offset:50176\n"
"buffer_load_dwordx4 v0, s[20:23], s72 offen offset:2048 sc1 lds\n"
"v_mfma_f32_16x16x32_bf16 v[164:167], a[40:43], a[16:19], v[164:167]\n"
"ds_read_b128 a[88:91], v23 offset:55296\n"
"v_mfma_f32_16x16x32_bf16 v[168:171], a[48:51], a[16:19], v[168:171]\n"
"ds_read_b64_tr_b16 a[120:121], v7 offset:49152\n"
"v_mfma_f32_16x16x32_bf16 v[172:175], a[56:59], a[16:19], v[172:175]\n"
"ds_read_b64_tr_b16 a[122:123], v7 offset:50176\n"
"v_mfma_f32_16x16x32_bf16 v[188:191], a[56:59], a[24:27], v[188:191]\n"
"buffer_load_dwordx4 v0, s[20:23], s72 offen offset:3072 sc1 lds\n"
"s_add_u32 s72, s72, 81920\n"
"v_mfma_f32_16x16x32_bf16 v[184:187], a[48:51], a[24:27], v[184:187]\n"
"ds_read_b128 a[68:71], v4 offset:49152\n"
"v_mfma_f32_16x16x32_bf16 v[180:183], a[40:43], a[24:27], v[180:183]\n"
"ds_read_b64_tr_b16 a[100:101], v24 offset:57344\n"
"v_mfma_f32_16x16x32_bf16 v[176:179], a[32:35], a[24:27], v[176:179]\n"
"ds_read_b64_tr_b16 a[102:103], v24 offset:58368\n"
"s_add_u32 m0, s77, 32768\n"
"v_mfma_f32_16x16x32_bf16 v[128:131], a[36:39], a[4:7], v[128:131]\n"
"ds_read_b128 a[76:79], v4 offset:51200\n"
"buffer_load_dwordx4 v25, s[52:55], s74 offen offset:0 lds\n"
"v_mfma_f32_16x16x32_bf16 v[132:135], a[44:47], a[4:7], v[132:135]\n"
"ds_read_b64_tr_b16 a[108:109], v5 offset:57344\n"
"v_mfma_f32_16x16x32_bf16 v[136:139], a[52:55], a[4:7], v[136:139]\n"
"ds_read_b64_tr_b16 a[110:111], v5 offset:58368\n"
"v_mfma_f32_16x16x32_bf16 v[140:143], a[60:63], a[4:7], v[140:143]\n"
"ds_read_b128 a[84:87], v4 offset:53248\n"
"buffer_load_dwordx4 v26, s[52:55], s74 offen offset:1024 lds\n"
"v_mfma_f32_16x16x32_bf16 v[156:159], a[60:63], a[12:15], v[156:159]\n"
"ds_read_b64_tr_b16 a[116:117], v6 offset:57344\n"
"v_mfma_f32_16x16x32_bf16 v[152:155], a[52:55], a[12:15], v[152:155]\n"
"ds_read_b64_tr_b16 a[118:119], v6 offset:58368\n"
"v_mfma_f32_16x16x32_bf16 v[148:151], a[44:47], a[12:15], v[148:151]\n"
"ds_read_b128 a[92:95], v4 offset:55296\n"
"v_mfma_f32_16x16x32_bf16 v[144:147], a[36:39], a[12:15], v[144:147]\n"
"ds_read_b64_tr_b16 a[124:125], v7 offset:57344\n"
"buffer_load_dwordx4 v27, s[52:55], s74 offen offset:2048 lds\n"
"v_mfma_f32_16x16x32_bf16 v[160:163], a[36:39], a[20:23], v[160:163]\n"
"ds_read_b64_tr_b16 a[126:127], v7 offset:58368\n"
"v_mfma_f32_16x16x32_bf16 v[164:167], a[44:47], a[20:23], v[164:167]\n"
"v_mfma_f32_16x16x32_bf16 v[168:171], a[52:55], a[20:23], v[168:171]\n"
"v_mfma_f32_16x16x32_bf16 v[172:175], a[60:63], a[20:23], v[172:175]\n"
"buffer_load_dwordx4 v28, s[52:55], s74 offen offset:3072 lds\n"
"s_add_u32 s74, s74, 655360\n"
"v_mfma_f32_16x16x32_bf16 v[188:191], a[60:63], a[28:31], v[188:191]\n"
"v_mfma_f32_16x16x32_bf16 v[184:187], a[52:55], a[28:31], v[184:187]\n"
"v_mfma_f32_16x16x32_bf16 v[180:183], a[44:47], a[28:31], v[180:183]\n"
"v_mfma_f32_16x16x32_bf16 v[176:179], a[36:39], a[28:31], v[176:179]\n"
"s_waitcnt vmcnt(16) lgkmcnt(0)\n"
"s_barrier\n"
"v_mfma_f32_16x16x32_bf16 v[128:131], a[96:99], a[64:67], v[128:131]\n"
"ds_read_b128 a[0:3], v23 offset:0\n"
"s_add_u32 m0, s76, 49152\n"
"v_mfma_f32_16x16x32_bf16 v[132:135], a[104:107], a[64:67], v[132:135]\n"
"ds_read_b64_tr_b16 a[32:33], v24 offset:0\n"
"buffer_load_dwordx4 v0, s[20:23], s72 offen offset:0 sc1 lds\n"
"v_mfma_f32_16x16x32_bf16 v[136:139], a[112:115], a[64:67], v[136:139]\n"
"ds_read_b64_tr_b16 a[34:35], v24 offset:1024\n"
"v_mfma_f32_16x16x32_bf16 v[140:143], a[120:123], a[64:67], v[140:143]\n"
"ds_read_b128 a[8:11], v23 offset:2048\n"
"v_mfma_f32_16x16x32_bf16 v[156:159], a[120:123], a[72:75], v[156:159]\n"
"ds_read_b64_tr_b16 a[40:41], v5 offset:0\n"
"buffer_load_dwordx4 v0, s[20:23], s72 offen offset:1024 sc1 lds\n"
"v_mfma_f32_16x16x32_bf16 v[152:155], a[112:115], a[72:75], v[152:155]\n"
"ds_read_b64_tr_b16 a[42:43], v5 offset:1024\n"
"v_mfma_f32_16x16x32_bf16 v[148:151], a[104:107], a[72:75], v[148:151]\n"
"ds_read_b128 a[16:19], v23 offset:4096\n"
"v_mfma_f32_16x16x32_bf16 v[144:147], a[96:99], a[72:75], v[144:147]\n"
"ds_read_b64_tr_b16 a[48:49], v6 offset:0\n"
"v_mfma_f32_16x16x32_bf16 v[160:163], a[96:99], a[80:83], v[160:163]\n"
"ds_read_b64_tr_b16 a[50:51], v6 offset:1024\n"
"buffer_load_dwordx4 v0, s[20:23], s72 offen offset:2048 sc1 lds\n"
"v_mfma_f32_16x16x32_bf16 v[164:167], a[104:107], a[80:83], v[164:167]\n"
"ds_read_b128 a[24:27], v23 offset:6144\n"
"v_mfma_f32_16x16x32_bf16 v[168:171], a[112:115], a[80:83], v[168:171]\n"
"ds_read_b64_tr_b16 a[56:57], v7 offset:0\n"
"v_mfma_f32_16x16x32_bf16 v[172:175], a[120:123], a[80:83], v[172:175]\n"
"ds_read_b64_tr_b16 a[58:59], v7 offset:1024\n"
"v_mfma_f32_16x16x32_bf16 v[188:191], a[120:123], a[88:91], v[188:191]\n"
"buffer_load_dwordx4 v0, s[20:23], s72 offen offset:3072 sc1 lds\n"
"s_add_u32 s72, s72, 16384\n"
"v_mfma_f32_16x16x32_bf16 v[184:187], a[112:115], a[88:91], v[184:187]\n"
"ds_read_b128 a[4:7], v13 offset:0\n"
"v_mfma_f32_16x16x32_bf16 v[180:183], a[104:107], a[88:91], v[180:183]\n"
"ds_read_b64_tr_b16 a[36:37], v24 offset:8192\n"
"v_mfma_f32_16x16x32_bf16 v[176:179], a[96:99], a[88:91], v[176:179]\n"
"ds_read_b64_tr_b16 a[38:39], v24 offset:9216\n"
"s_add_u32 m0, s77, 49152\n"
"v_mfma_f32_16x16x32_bf16 v[128:131], a[100:103], a[68:71], v[128:131]\n"
"ds_read_b128 a[12:15], v1 offset:0\n"
"buffer_load_dwordx4 v25, s[52:55], s74 offen offset:0 lds\n"
"v_mfma_f32_16x16x32_bf16 v[132:135], a[108:111], a[68:71], v[132:135]\n"
"ds_read_b64_tr_b16 a[44:45], v5 offset:8192\n"
"v_mfma_f32_16x16x32_bf16 v[136:139], a[116:119], a[68:71], v[136:139]\n"
"ds_read_b64_tr_b16 a[46:47], v5 offset:9216\n"
"v_mfma_f32_16x16x32_bf16 v[140:143], a[124:127], a[68:71], v[140:143]\n"
"ds_read_b128 a[20:23], v13 offset:4096\n"
"buffer_load_dwordx4 v26, s[52:55], s74 offen offset:1024 lds\n"
"v_mfma_f32_16x16x32_bf16 v[156:159], a[124:127], a[76:79], v[156:159]\n"
"ds_read_b64_tr_b16 a[52:53], v6 offset:8192\n"
"v_mfma_f32_16x16x32_bf16 v[152:155], a[116:119], a[76:79], v[152:155]\n"
"ds_read_b64_tr_b16 a[54:55], v6 offset:9216\n"
"v_mfma_f32_16x16x32_bf16 v[148:151], a[108:111], a[76:79], v[148:151]\n"
"ds_read_b128 a[28:31], v1 offset:4096\n"
"v_mfma_f32_16x16x32_bf16 v[144:147], a[100:103], a[76:79], v[144:147]\n"
"ds_read_b64_tr_b16 a[60:61], v7 offset:8192\n"
"buffer_load_dwordx4 v27, s[52:55], s74 offen offset:2048 lds\n"
"v_mfma_f32_16x16x32_bf16 v[160:163], a[100:103], a[84:87], v[160:163]\n"
"ds_read_b64_tr_b16 a[62:63], v7 offset:9216\n"
"v_mfma_f32_16x16x32_bf16 v[164:167], a[108:111], a[84:87], v[164:167]\n"
"v_mfma_f32_16x16x32_bf16 v[168:171], a[116:119], a[84:87], v[168:171]\n"
"v_mfma_f32_16x16x32_bf16 v[172:175], a[124:127], a[84:87], v[172:175]\n"
"buffer_load_dwordx4 v28, s[52:55], s74 offen offset:3072 lds\n"
"s_add_u32 s74, s74, 131072\n"
"v_mfma_f32_16x16x32_bf16 v[188:191], a[124:127], a[92:95], v[188:191]\n"
"v_mfma_f32_16x16x32_bf16 v[184:187], a[116:119], a[92:95], v[184:187]\n"
"v_mfma_f32_16x16x32_bf16 v[180:183], a[108:111], a[92:95], v[180:183]\n"
"v_mfma_f32_16x16x32_bf16 v[176:179], a[100:103], a[92:95], v[176:179]\n"
"s_waitcnt vmcnt(16) lgkmcnt(0)\n"
"s_barrier\n"
"v_mfma_f32_16x16x32_bf16 v[128:131], a[32:35], a[0:3], v[128:131]\n"
"ds_read_b128 a[64:67], v23 offset:16384\n"
"s_add_u32 m0, s76, 0\n"
"v_mfma_f32_16x16x32_bf16 v[132:135], a[40:43], a[0:3], v[132:135]\n"
"ds_read_b64_tr_b16 a[96:97], v24 offset:16384\n"
"buffer_load_dwordx4 v0, s[20:23], s72 offen offset:0 sc1 lds\n"
"v_mfma_f32_16x16x32_bf16 v[136:139], a[48:51], a[0:3], v[136:139]\n"
"ds_read_b64_tr_b16 a[98:99], v24 offset:17408\n"
"v_mfma_f32_16x16x32_bf16 v[140:143], a[56:59], a[0:3], v[140:143]\n"
"ds_read_b128 a[72:75], v23 offset:18432\n"
"v_mfma_f32_16x16x32_bf16 v[156:159], a[56:59], a[8:11], v[156:159]\n"
"ds_read_b64_tr_b16 a[104:105], v5 offset:16384\n"
"buffer_load_dwordx4 v0, s[20:23], s72 offen offset:1024 sc1 lds\n"
"v_mfma_f32_16x16x32_bf16 v[152:155], a[48:51], a[8:11], v[152:155]\n"
"ds_read_b64_tr_b16 a[106:107], v5 offset:17408\n"
"v_mfma_f32_16x16x32_bf16 v[148:151], a[40:43], a[8:11], v[148:151]\n"
"ds_read_b128 a[80:83], v23 offset:20480\n"
"v_mfma_f32_16x16x32_bf16 v[144:147], a[32:35], a[8:11], v[144:147]\n"
"ds_read_b64_tr_b16 a[112:113], v6 offset:16384\n"
"v_mfma_f32_16x16x32_bf16 v[160:163], a[32:35], a[16:19], v[160:163]\n"
"ds_read_b64_tr_b16 a[114:115], v6 offset:17408\n"
"buffer_load_dwordx4 v0, s[20:23], s72 offen offset:2048 sc1 lds\n"
"v_mfma_f32_16x16x32_bf16 v[164:167], a[40:43], a[16:19], v[164:167]\n"
"ds_read_b128 a[88:91], v23 offset:22528\n"
"v_mfma_f32_16x16x32_bf16 v[168:171], a[48:51], a[16:19], v[168:171]\n"
"ds_read_b64_tr_b16 a[120:121], v7 offset:16384\n"
"v_mfma_f32_16x16x32_bf16 v[172:175], a[56:59], a[16:19], v[172:175]\n"
"ds_read_b64_tr_b16 a[122:123], v7 offset:17408\n"
"v_mfma_f32_16x16x32_bf16 v[188:191], a[56:59], a[24:27], v[188:191]\n"
"buffer_load_dwordx4 v0, s[20:23], s72 offen offset:3072 sc1 lds\n"
"s_add_u32 s72, s72, 16384\n"
"v_mfma_f32_16x16x32_bf16 v[184:187], a[48:51], a[24:27], v[184:187]\n"
"ds_read_b128 a[68:71], v4 offset:16384\n"
"v_mfma_f32_16x16x32_bf16 v[180:183], a[40:43], a[24:27], v[180:183]\n"
"ds_read_b64_tr_b16 a[100:101], v24 offset:24576\n"
"v_mfma_f32_16x16x32_bf16 v[176:179], a[32:35], a[24:27], v[176:179]\n"
"ds_read_b64_tr_b16 a[102:103], v24 offset:25600\n"
"s_add_u32 m0, s77, 0\n"
"v_mfma_f32_16x16x32_bf16 v[128:131], a[36:39], a[4:7], v[128:131]\n"
"ds_read_b128 a[76:79], v4 offset:18432\n"
"buffer_load_dwordx4 v25, s[52:55], s74 offen offset:0 lds\n"
"v_mfma_f32_16x16x32_bf16 v[132:135], a[44:47], a[4:7], v[132:135]\n"
"ds_read_b64_tr_b16 a[108:109], v5 offset:24576\n"
"v_mfma_f32_16x16x32_bf16 v[136:139], a[52:55], a[4:7], v[136:139]\n"
"ds_read_b64_tr_b16 a[110:111], v5 offset:25600\n"
"v_mfma_f32_16x16x32_bf16 v[140:143], a[60:63], a[4:7], v[140:143]\n"
"ds_read_b128 a[84:87], v4 offset:20480\n"
"buffer_load_dwordx4 v26, s[52:55], s74 offen offset:1024 lds\n"
"v_mfma_f32_16x16x32_bf16 v[156:159], a[60:63], a[12:15], v[156:159]\n"
"ds_read_b64_tr_b16 a[116:117], v6 offset:24576\n"
"v_mfma_f32_16x16x32_bf16 v[152:155], a[52:55], a[12:15], v[152:155]\n"
"ds_read_b64_tr_b16 a[118:119], v6 offset:25600\n"
"v_mfma_f32_16x16x32_bf16 v[148:151], a[44:47], a[12:15], v[148:151]\n"
"ds_read_b128 a[92:95], v4 offset:22528\n"
"v_mfma_f32_16x16x32_bf16 v[144:147], a[36:39], a[12:15], v[144:147]\n"
"ds_read_b64_tr_b16 a[124:125], v7 offset:24576\n"
"buffer_load_dwordx4 v27, s[52:55], s74 offen offset:2048 lds\n"
"v_mfma_f32_16x16x32_bf16 v[160:163], a[36:39], a[20:23], v[160:163]\n"
"ds_read_b64_tr_b16 a[126:127], v7 offset:25600\n"
"v_mfma_f32_16x16x32_bf16 v[164:167], a[44:47], a[20:23], v[164:167]\n"
"v_mfma_f32_16x16x32_bf16 v[168:171], a[52:55], a[20:23], v[168:171]\n"
"v_mfma_f32_16x16x32_bf16 v[172:175], a[60:63], a[20:23], v[172:175]\n"
"buffer_load_dwordx4 v28, s[52:55], s74 offen offset:3072 lds\n"
"s_add_u32 s74, s74, 131072\n"
"v_mfma_f32_16x16x32_bf16 v[188:191], a[60:63], a[28:31], v[188:191]\n"
"v_mfma_f32_16x16x32_bf16 v[184:187], a[52:55], a[28:31], v[184:187]\n"
"v_mfma_f32_16x16x32_bf16 v[180:183], a[44:47], a[28:31], v[180:183]\n"
"v_mfma_f32_16x16x32_bf16 v[176:179], a[36:39], a[28:31], v[176:179]\n"
"s_waitcnt vmcnt(16) lgkmcnt(0)\n"
"s_barrier\n"
"v_mfma_f32_16x16x32_bf16 v[128:131], a[96:99], a[64:67], v[128:131]\n"
"ds_read_b128 a[0:3], v11 offset:32768\n"
"s_add_u32 m0, s76, 16384\n"
"v_mfma_f32_16x16x32_bf16 v[132:135], a[104:107], a[64:67], v[132:135]\n"
"ds_read_b64_tr_b16 a[32:33], v24 offset:32768\n"
"buffer_load_dwordx4 v0, s[20:23], s72 offen offset:0 sc1 lds\n"
"v_mfma_f32_16x16x32_bf16 v[136:139], a[112:115], a[64:67], v[136:139]\n"
"ds_read_b64_tr_b16 a[34:35], v24 offset:33792\n"
"v_mfma_f32_16x16x32_bf16 v[140:143], a[120:123], a[64:67], v[140:143]\n"
"ds_read_b128 a[8:11], v12 offset:32768\n"
"v_mfma_f32_16x16x32_bf16 v[156:159], a[120:123], a[72:75], v[156:159]\n"
"ds_read_b64_tr_b16 a[40:41], v5 offset:32768\n"
"buffer_load_dwordx4 v0, s[20:23], s72 offen offset:1024 sc1 lds\n"
"v_mfma_f32_16x16x32_bf16 v[152:155], a[112:115], a[72:75], v[152:155]\n"
"ds_read_b64_tr_b16 a[42:43], v5 offset:33792\n"
"v_mfma_f32_16x16x32_bf16 v[148:151], a[104:107], a[72:75], v[148:151]\n"
"ds_read_b128 a[16:19], v11 offset:36864\n"
"v_mfma_f32_16x16x32_bf16 v[144:147], a[96:99], a[72:75], v[144:147]\n"
"ds_read_b64_tr_b16 a[48:49], v6 offset:32768\n"
"v_mfma_f32_16x16x32_bf16 v[160:163], a[96:99], a[80:83], v[160:163]\n"
"ds_read_b64_tr_b16 a[50:51], v6 offset:33792\n"
"buffer_load_dwordx4 v0, s[20:23], s72 offen offset:2048 sc1 lds\n"
"v_mfma_f32_16x16x32_bf16 v[164:167], a[104:107], a[80:83], v[164:167]\n"
"ds_read_b128 a[24:27], v12 offset:36864\n"
"v_mfma_f32_16x16x32_bf16 v[168:171], a[112:115], a[80:83], v[168:171]\n"
"ds_read_b64_tr_b16 a[56:57], v7 offset:32768\n"
"v_mfma_f32_16x16x32_bf16 v[172:175], a[120:123], a[80:83], v[172:175]\n"
"ds_read_b64_tr_b16 a[58:59], v7 offset:33792\n"
"v_mfma_f32_16x16x32_bf16 v[188:191], a[120:123], a[88:91], v[188:191]\n"
"buffer_load_dwordx4 v0, s[20:23], s72 offen offset:3072 sc1 lds\n"
"s_add_u32 s72, s72, 81920\n"
"v_mfma_f32_16x16x32_bf16 v[184:187], a[112:115], a[88:91], v[184:187]\n"
"ds_read_b128 a[4:7], v4 offset:32768\n"
"v_mfma_f32_16x16x32_bf16 v[180:183], a[104:107], a[88:91], v[180:183]\n"
"ds_read_b64_tr_b16 a[36:37], v24 offset:40960\n"
"v_mfma_f32_16x16x32_bf16 v[176:179], a[96:99], a[88:91], v[176:179]\n"
"ds_read_b64_tr_b16 a[38:39], v24 offset:41984\n"
"s_add_u32 m0, s77, 16384\n"
"v_mfma_f32_16x16x32_bf16 v[128:131], a[100:103], a[68:71], v[128:131]\n"
"ds_read_b128 a[12:15], v4 offset:34816\n"
"buffer_load_dwordx4 v25, s[52:55], s74 offen offset:0 lds\n"
"v_mfma_f32_16x16x32_bf16 v[132:135], a[108:111], a[68:71], v[132:135]\n"
"ds_read_b64_tr_b16 a[44:45], v5 offset:40960\n"
"v_mfma_f32_16x16x32_bf16 v[136:139], a[116:119], a[68:71], v[136:139]\n"
"ds_read_b64_tr_b16 a[46:47], v5 offset:41984\n"
"v_mfma_f32_16x16x32_bf16 v[140:143], a[124:127], a[68:71], v[140:143]\n"
"ds_read_b128 a[20:23], v4 offset:36864\n"
"buffer_load_dwordx4 v26, s[52:55], s74 offen offset:1024 lds\n"
"v_mfma_f32_16x16x32_bf16 v[156:159], a[124:127], a[76:79], v[156:159]\n"
"ds_read_b64_tr_b16 a[52:53], v6 offset:40960\n"
"v_mfma_f32_16x16x32_bf16 v[152:155], a[116:119], a[76:79], v[152:155]\n"
"ds_read_b64_tr_b16 a[54:55], v6 offset:41984\n"
"v_mfma_f32_16x16x32_bf16 v[148:151], a[108:111], a[76:79], v[148:151]\n"
"ds_read_b128 a[28:31], v4 offset:38912\n"
"v_mfma_f32_16x16x32_bf16 v[144:147], a[100:103], a[76:79], v[144:147]\n"
"ds_read_b64_tr_b16 a[60:61], v7 offset:40960\n"
"buffer_load_dwordx4 v27, s[52:55], s74 offen offset:2048 lds\n"
"v_mfma_f32_16x16x32_bf16 v[160:163], a[100:103], a[84:87], v[160:163]\n"
"ds_read_b64_tr_b16 a[62:63], v7 offset:41984\n"
"v_mfma_f32_16x16x32_bf16 v[164:167], a[108:111], a[84:87], v[164:167]\n"
"v_mfma_f32_16x16x32_bf16 v[168:171], a[116:119], a[84:87], v[168:171]\n"
"v_mfma_f32_16x16x32_bf16 v[172:175], a[124:127], a[84:87], v[172:175]\n"
"buffer_load_dwordx4 v28, s[52:55], s74 offen offset:3072 lds\n"
"s_add_u32 s74, s74, 655360\n"
"v_mfma_f32_16x16x32_bf16 v[188:191], a[124:127], a[92:95], v[188:191]\n"
"v_mfma_f32_16x16x32_bf16 v[184:187], a[116:119], a[92:95], v[184:187]\n"
"v_mfma_f32_16x16x32_bf16 v[180:183], a[108:111], a[92:95], v[180:183]\n"
"v_mfma_f32_16x16x32_bf16 v[176:179], a[100:103], a[92:95], v[176:179]\n"
"s_waitcnt vmcnt(16) lgkmcnt(0)\n"
"s_barrier\n"
"v_mfma_f32_16x16x32_bf16 v[128:131], a[32:35], a[0:3], v[128:131]\n"
"ds_read_b128 a[64:67], v23 offset:49152\n"
"s_add_u32 m0, s76, 32768\n"
"v_mfma_f32_16x16x32_bf16 v[132:135], a[40:43], a[0:3], v[132:135]\n"
"ds_read_b64_tr_b16 a[96:97], v24 offset:49152\n"
"buffer_load_dwordx4 v0, s[20:23], s72 offen offset:0 sc1 lds\n"
"v_mfma_f32_16x16x32_bf16 v[136:139], a[48:51], a[0:3], v[136:139]\n"
"ds_read_b64_tr_b16 a[98:99], v24 offset:50176\n"
"v_mfma_f32_16x16x32_bf16 v[140:143], a[56:59], a[0:3], v[140:143]\n"
"ds_read_b128 a[72:75], v23 offset:51200\n"
"v_mfma_f32_16x16x32_bf16 v[156:159], a[56:59], a[8:11], v[156:159]\n"
"ds_read_b64_tr_b16 a[104:105], v5 offset:49152\n"
"buffer_load_dwordx4 v0, s[20:23], s72 offen offset:1024 sc1 lds\n"
"v_mfma_f32_16x16x32_bf16 v[152:155], a[48:51], a[8:11], v[152:155]\n"
"ds_read_b64_tr_b16 a[106:107], v5 offset:50176\n"
"v_mfma_f32_16x16x32_bf16 v[148:151], a[40:43], a[8:11], v[148:151]\n"
"ds_read_b128 a[80:83], v23 offset:53248\n"
"v_mfma_f32_16x16x32_bf16 v[144:147], a[32:35], a[8:11], v[144:147]\n"
"ds_read_b64_tr_b16 a[112:113], v6 offset:49152\n"
"v_mfma_f32_16x16x32_bf16 v[160:163], a[32:35], a[16:19], v[160:163]\n"
"ds_read_b64_tr_b16 a[114:115], v6 offset:50176\n"
"buffer_load_dwordx4 v0, s[20:23], s72 offen offset:2048 sc1 lds\n"
"v_mfma_f32_16x16x32_bf16 v[164:167], a[40:43], a[16:19], v[164:167]\n"
"ds_read_b128 a[88:91], v23 offset:55296\n"
"v_mfma_f32_16x16x32_bf16 v[168:171], a[48:51], a[16:19], v[168:171]\n"
"ds_read_b64_tr_b16 a[120:121], v7 offset:49152\n"
"v_mfma_f32_16x16x32_bf16 v[172:175], a[56:59], a[16:19], v[172:175]\n"
"ds_read_b64_tr_b16 a[122:123], v7 offset:50176\n"
"v_mfma_f32_16x16x32_bf16 v[188:191], a[56:59], a[24:27], v[188:191]\n"
"buffer_load_dwordx4 v0, s[20:23], s72 offen offset:3072 sc1 lds\n"
"s_add_u32 s72, s72, 16384\n"
"v_mfma_f32_16x16x32_bf16 v[184:187], a[48:51], a[24:27], v[184:187]\n"
"ds_read_b128 a[68:71], v13 offset:49152\n"
"v_mfma_f32_16x16x32_bf16 v[180:183], a[40:43], a[24:27], v[180:183]\n"
"ds_read_b64_tr_b16 a[100:101], v24 offset:57344\n"
"v_mfma_f32_16x16x32_bf16 v[176:179], a[32:35], a[24:27], v[176:179]\n"
"ds_read_b64_tr_b16 a[102:103], v24 offset:58368\n"
"s_add_u32 m0, s77, 32768\n"
"v_mfma_f32_16x16x32_bf16 v[128:131], a[36:39], a[4:7], v[128:131]\n"
"ds_read_b128 a[76:79], v1 offset:49152\n"
"buffer_load_dwordx4 v25, s[52:55], s74 offen offset:0 lds\n"
"v_mfma_f32_16x16x32_bf16 v[132:135], a[44:47], a[4:7], v[132:135]\n"
"ds_read_b64_tr_b16 a[108:109], v5 offset:57344\n"
"v_mfma_f32_16x16x32_bf16 v[136:139], a[52:55], a[4:7], v[136:139]\n"
"ds_read_b64_tr_b16 a[110:111], v5 offset:58368\n"
"v_mfma_f32_16x16x32_bf16 v[140:143], a[60:63], a[4:7], v[140:143]\n"
"ds_read_b128 a[84:87], v13 offset:53248\n"
"buffer_load_dwordx4 v26, s[52:55], s74 offen offset:1024 lds\n"
"v_mfma_f32_16x16x32_bf16 v[156:159], a[60:63], a[12:15], v[156:159]\n"
"ds_read_b64_tr_b16 a[116:117], v6 offset:57344\n"
"v_mfma_f32_16x16x32_bf16 v[152:155], a[52:55], a[12:15], v[152:155]\n"
"ds_read_b64_tr_b16 a[118:119], v6 offset:58368\n"
"v_mfma_f32_16x16x32_bf16 v[148:151], a[44:47], a[12:15], v[148:151]\n"
"ds_read_b128 a[92:95], v1 offset:53248\n"
"v_mfma_f32_16x16x32_bf16 v[144:147], a[36:39], a[12:15], v[144:147]\n"
"ds_read_b64_tr_b16 a[124:125], v7 offset:57344\n"
"buffer_load_dwordx4 v27, s[52:55], s74 offen offset:2048 lds\n"
"v_mfma_f32_16x16x32_bf16 v[160:163], a[36:39], a[20:23], v[160:163]\n"
"ds_read_b64_tr_b16 a[126:127], v7 offset:58368\n"
"v_mfma_f32_16x16x32_bf16 v[164:167], a[44:47], a[20:23], v[164:167]\n"
"v_mfma_f32_16x16x32_bf16 v[168:171], a[52:55], a[20:23], v[168:171]\n"
"v_mfma_f32_16x16x32_bf16 v[172:175], a[60:63], a[20:23], v[172:175]\n"
"buffer_load_dwordx4 v28, s[52:55], s74 offen offset:3072 lds\n"
"s_add_u32 s74, s74, 131072\n"
"v_mfma_f32_16x16x32_bf16 v[188:191], a[60:63], a[28:31], v[188:191]\n"
"v_mfma_f32_16x16x32_bf16 v[184:187], a[52:55], a[28:31], v[184:187]\n"
"v_mfma_f32_16x16x32_bf16 v[180:183], a[44:47], a[28:31], v[180:183]\n"
"v_mfma_f32_16x16x32_bf16 v[176:179], a[36:39], a[28:31], v[176:179]\n"
"s_waitcnt vmcnt(16) lgkmcnt(0)\n"
"s_barrier\n"
"v_mfma_f32_16x16x32_bf16 v[128:131], a[96:99], a[64:67], v[128:131]\n"
"ds_read_b128 a[0:3], v23 offset:0\n"
"s_add_u32 m0, s76, 49152\n"
"v_mfma_f32_16x16x32_bf16 v[132:135], a[104:107], a[64:67], v[132:135]\n"
"ds_read_b64_tr_b16 a[32:33], v24 offset:0\n"
"buffer_load_dwordx4 v0, s[20:23], s72 offen offset:0 sc1 lds\n"
"v_mfma_f32_16x16x32_bf16 v[136:139], a[112:115], a[64:67], v[136:139]\n"
"ds_read_b64_tr_b16 a[34:35], v24 offset:1024\n"
"v_mfma_f32_16x16x32_bf16 v[140:143], a[120:123], a[64:67], v[140:143]\n"
"ds_read_b128 a[8:11], v23 offset:2048\n"
"v_mfma_f32_16x16x32_bf16 v[156:159], a[120:123], a[72:75], v[156:159]\n"
"ds_read_b64_tr_b16 a[40:41], v5 offset:0\n"
"buffer_load_dwordx4 v0, s[20:23], s72 offen offset:1024 sc1 lds\n"
"v_mfma_f32_16x16x32_bf16 v[152:155], a[112:115], a[72:75], v[152:155]\n"
"ds_read_b64_tr_b16 a[42:43], v5 offset:1024\n"
"v_mfma_f32_16x16x32_bf16 v[148:151], a[104:107], a[72:75], v[148:151]\n"
"ds_read_b128 a[16:19], v23 offset:4096\n"
"v_mfma_f32_16x16x32_bf16 v[144:147], a[96:99], a[72:75], v[144:147]\n"
"ds_read_b64_tr_b16 a[48:49], v6 offset:0\n"
"v_mfma_f32_16x16x32_bf16 v[160:163], a[96:99], a[80:83], v[160:163]\n"
"ds_read_b64_tr_b16 a[50:51], v6 offset:1024\n"
"buffer_load_dwordx4 v0, s[20:23], s72 offen offset:2048 sc1 lds\n"
"v_mfma_f32_16x16x32_bf16 v[164:167], a[104:107], a[80:83], v[164:167]\n"
"ds_read_b128 a[24:27], v23 offset:6144\n"
"v_mfma_f32_16x16x32_bf16 v[168:171], a[112:115], a[80:83], v[168:171]\n"
"ds_read_b64_tr_b16 a[56:57], v7 offset:0\n"
"v_mfma_f32_16x16x32_bf16 v[172:175], a[120:123], a[80:83], v[172:175]\n"
"ds_read_b64_tr_b16 a[58:59], v7 offset:1024\n"
"v_mfma_f32_16x16x32_bf16 v[188:191], a[120:123], a[88:91], v[188:191]\n"
"buffer_load_dwordx4 v0, s[20:23], s72 offen offset:3072 sc1 lds\n"
"s_add_u32 s72, s72, 16384\n"
"v_mfma_f32_16x16x32_bf16 v[184:187], a[112:115], a[88:91], v[184:187]\n"
"ds_read_b128 a[4:7], v4 offset:0\n"
"v_mfma_f32_16x16x32_bf16 v[180:183], a[104:107], a[88:91], v[180:183]\n"
"ds_read_b64_tr_b16 a[36:37], v24 offset:8192\n"
"v_mfma_f32_16x16x32_bf16 v[176:179], a[96:99], a[88:91], v[176:179]\n"
"ds_read_b64_tr_b16 a[38:39], v24 offset:9216\n"
"s_add_u32 m0, s77, 49152\n"
"v_mfma_f32_16x16x32_bf16 v[128:131], a[100:103], a[68:71], v[128:131]\n"
"ds_read_b128 a[12:15], v4 offset:2048\n"
"buffer_load_dwordx4 v25, s[52:55], s74 offen offset:0 lds\n"
"v_mfma_f32_16x16x32_bf16 v[132:135], a[108:111], a[68:71], v[132:135]\n"
"ds_read_b64_tr_b16 a[44:45], v5 offset:8192\n"
"v_mfma_f32_16x16x32_bf16 v[136:139], a[116:119], a[68:71], v[136:139]\n"
"ds_read_b64_tr_b16 a[46:47], v5 offset:9216\n"
"v_mfma_f32_16x16x32_bf16 v[140:143], a[124:127], a[68:71], v[140:143]\n"
"ds_read_b128 a[20:23], v4 offset:4096\n"
"buffer_load_dwordx4 v26, s[52:55], s74 offen offset:1024 lds\n"
"v_mfma_f32_16x16x32_bf16 v[156:159], a[124:127], a[76:79], v[156:159]\n"
"ds_read_b64_tr_b16 a[52:53], v6 offset:8192\n"
"v_mfma_f32_16x16x32_bf16 v[152:155], a[116:119], a[76:79], v[152:155]\n"
"ds_read_b64_tr_b16 a[54:55], v6 offset:9216\n"
"v_mfma_f32_16x16x32_bf16 v[148:151], a[108:111], a[76:79], v[148:151]\n"
"ds_read_b128 a[28:31], v4 offset:6144\n"
"v_mfma_f32_16x16x32_bf16 v[144:147], a[100:103], a[76:79], v[144:147]\n"
"ds_read_b64_tr_b16 a[60:61], v7 offset:8192\n"
"buffer_load_dwordx4 v27, s[52:55], s74 offen offset:2048 lds\n"
"v_mfma_f32_16x16x32_bf16 v[160:163], a[100:103], a[84:87], v[160:163]\n"
"ds_read_b64_tr_b16 a[62:63], v7 offset:9216\n"
"v_mfma_f32_16x16x32_bf16 v[164:167], a[108:111], a[84:87], v[164:167]\n"
"v_mfma_f32_16x16x32_bf16 v[168:171], a[116:119], a[84:87], v[168:171]\n"
"v_mfma_f32_16x16x32_bf16 v[172:175], a[124:127], a[84:87], v[172:175]\n"
"buffer_load_dwordx4 v28, s[52:55], s74 offen offset:3072 lds\n"
"s_add_u32 s74, s74, 131072\n"
"v_mfma_f32_16x16x32_bf16 v[188:191], a[124:127], a[92:95], v[188:191]\n"
"v_mfma_f32_16x16x32_bf16 v[184:187], a[116:119], a[92:95], v[184:187]\n"
"v_mfma_f32_16x16x32_bf16 v[180:183], a[108:111], a[92:95], v[180:183]\n"
"v_mfma_f32_16x16x32_bf16 v[176:179], a[100:103], a[92:95], v[176:179]\n"
"s_waitcnt vmcnt(16) lgkmcnt(0)\n"
"s_barrier\n"
"v_mfma_f32_16x16x32_bf16 v[128:131], a[32:35], a[0:3], v[128:131]\n"
"ds_read_b128 a[64:67], v11 offset:16384\n"
"s_add_u32 m0, s76, 0\n"
"v_lshrrev_b32 v16, 7, v0\n"
"v_mfma_f32_16x16x32_bf16 v[132:135], a[40:43], a[0:3], v[132:135]\n"
"ds_read_b64_tr_b16 a[96:97], v24 offset:16384\n"
"buffer_load_dwordx4 v0, s[20:23], s72 offen offset:0 sc1 lds\n"
"v_mfma_f32_16x16x32_bf16 v[136:139], a[48:51], a[0:3], v[136:139]\n"
"ds_read_b64_tr_b16 a[98:99], v24 offset:17408\n"
"v_and_b32 v17, 0x70, v0\n"
"v_mfma_f32_16x16x32_bf16 v[140:143], a[56:59], a[0:3], v[140:143]\n"
"ds_read_b128 a[72:75], v12 offset:16384\n"
"v_mfma_f32_16x16x32_bf16 v[156:159], a[56:59], a[8:11], v[156:159]\n"
"ds_read_b64_tr_b16 a[104:105], v5 offset:16384\n"
"buffer_load_dwordx4 v0, s[20:23], s72 offen offset:1024 sc1 lds\n"
"v_mfma_f32_16x16x32_bf16 v[152:155], a[48:51], a[8:11], v[152:155]\n"
"ds_read_b64_tr_b16 a[106:107], v5 offset:17408\n"
"v_lshlrev_b32 v17, 3, v17\n"
"v_mfma_f32_16x16x32_bf16 v[148:151], a[40:43], a[8:11], v[148:151]\n"
"ds_read_b128 a[80:83], v11 offset:20480\n"
"v_mfma_f32_16x16x32_bf16 v[144:147], a[32:35], a[8:11], v[144:147]\n"
"ds_read_b64_tr_b16 a[112:113], v6 offset:16384\n"
"v_mfma_f32_16x16x32_bf16 v[160:163], a[32:35], a[16:19], v[160:163]\n"
"ds_read_b64_tr_b16 a[114:115], v6 offset:17408\n"
"buffer_load_dwordx4 v0, s[20:23], s72 offen offset:2048 sc1 lds\n"
"v_lshl_or_b32 v16, v16, 11, v17\n"
"v_mfma_f32_16x16x32_bf16 v[164:167], a[40:43], a[16:19], v[164:167]\n"
"ds_read_b128 a[88:91], v12 offset:20480\n"
"v_mfma_f32_16x16x32_bf16 v[168:171], a[48:51], a[16:19], v[168:171]\n"
"ds_read_b64_tr_b16 a[120:121], v7 offset:16384\n"
"v_mfma_f32_16x16x32_bf16 v[172:175], a[56:59], a[16:19], v[172:175]\n"
"ds_read_b64_tr_b16 a[122:123], v7 offset:17408\n"
"s_bfe_u32 s0, s85, 0x50012\n"
"v_mfma_f32_16x16x32_bf16 v[188:191], a[56:59], a[24:27], v[188:191]\n"
"buffer_load_dwordx4 v0, s[20:23], s72 offen offset:3072 sc1 lds\n"
"s_add_u32 s72, s72, 81920\n"
"v_mfma_f32_16x16x32_bf16 v[184:187], a[48:51], a[24:27], v[184:187]\n"
"ds_read_b128 a[68:71], v4 offset:16384\n"
"v_mfma_f32_16x16x32_bf16 v[180:183], a[40:43], a[24:27], v[180:183]\n"
"ds_read_b64_tr_b16 a[100:101], v24 offset:24576\n"
"s_bfe_u32 s1, s85, 0x2000d\n"
"v_mfma_f32_16x16x32_bf16 v[176:179], a[32:35], a[24:27], v[176:179]\n"
"ds_read_b64_tr_b16 a[102:103], v24 offset:25600\n"
"s_add_u32 m0, s77, 0\n"
"v_mfma_f32_16x16x32_bf16 v[128:131], a[36:39], a[4:7], v[128:131]\n"
"ds_read_b128 a[76:79], v4 offset:18432\n"
"buffer_load_dwordx4 v25, s[52:55], s74 offen offset:0 lds\n"
"v_mfma_f32_16x16x32_bf16 v[132:135], a[44:47], a[4:7], v[132:135]\n"
"ds_read_b64_tr_b16 a[108:109], v5 offset:24576\n"
"s_lshl2_add_u32 s0, s0, s1\n"
"v_mfma_f32_16x16x32_bf16 v[136:139], a[52:55], a[4:7], v[136:139]\n"
"ds_read_b64_tr_b16 a[110:111], v5 offset:25600\n"
"v_mfma_f32_16x16x32_bf16 v[140:143], a[60:63], a[4:7], v[140:143]\n"
"ds_read_b128 a[84:87], v4 offset:20480\n"
"buffer_load_dwordx4 v26, s[52:55], s74 offen offset:1024 lds\n"
"v_mfma_f32_16x16x32_bf16 v[156:159], a[60:63], a[12:15], v[156:159]\n"
"ds_read_b64_tr_b16 a[116:117], v6 offset:24576\n"
"s_bfe_u32 s1, s85, 65552\n"
"v_mfma_f32_16x16x32_bf16 v[152:155], a[52:55], a[12:15], v[152:155]\n"
"ds_read_b64_tr_b16 a[118:119], v6 offset:25600\n"
"v_mfma_f32_16x16x32_bf16 v[148:151], a[44:47], a[12:15], v[148:151]\n"
"ds_read_b128 a[92:95], v4 offset:22528\n"
"v_mfma_f32_16x16x32_bf16 v[144:147], a[36:39], a[12:15], v[144:147]\n"
"ds_read_b64_tr_b16 a[124:125], v7 offset:24576\n"
"buffer_load_dwordx4 v27, s[52:55], s74 offen offset:2048 lds\n"
"s_lshl4_add_u32 s0, s0, s1\n"
"v_mfma_f32_16x16x32_bf16 v[160:163], a[36:39], a[20:23], v[160:163]\n"
"ds_read_b64_tr_b16 a[126:127], v7 offset:25600\n"
"v_mfma_f32_16x16x32_bf16 v[164:167], a[44:47], a[20:23], v[164:167]\n"
"v_mfma_f32_16x16x32_bf16 v[168:171], a[52:55], a[20:23], v[168:171]\n"
"s_add_u32 s0, s0, 4\n"
"v_mfma_f32_16x16x32_bf16 v[172:175], a[60:63], a[20:23], v[172:175]\n"
"buffer_load_dwordx4 v28, s[52:55], s74 offen offset:3072 lds\n"
"s_add_u32 s74, s74, 655360\n"
"v_mfma_f32_16x16x32_bf16 v[188:191], a[60:63], a[28:31], v[188:191]\n"
"v_mfma_f32_16x16x32_bf16 v[184:187], a[52:55], a[28:31], v[184:187]\n"
"s_lshl_b32 s0, s0, 10\n"
"v_mfma_f32_16x16x32_bf16 v[180:183], a[44:47], a[28:31], v[180:183]\n"
"v_mfma_f32_16x16x32_bf16 v[176:179], a[36:39], a[28:31], v[176:179]\n"
"s_waitcnt vmcnt(16) lgkmcnt(0)\n"
"s_barrier\n"
"v_mfma_f32_16x16x32_bf16 v[128:131], a[96:99], a[64:67], v[128:131]\n"
"ds_read_b128 a[0:3], v23 offset:32768\n"
"s_add_u32 m0, s76, 16384\n"
"v_mfma_f32_16x16x32_bf16 v[132:135], a[104:107], a[64:67], v[132:135]\n"
"ds_read_b64_tr_b16 a[32:33], v24 offset:32768\n"
"buffer_load_dwordx4 v0, s[20:23], s72 offen offset:0 sc1 lds\n"
"v_mfma_f32_16x16x32_bf16 v[136:139], a[112:115], a[64:67], v[136:139]\n"
"ds_read_b64_tr_b16 a[34:35], v24 offset:33792\n"
"v_mfma_f32_16x16x32_bf16 v[140:143], a[120:123], a[64:67], v[140:143]\n"
"ds_read_b128 a[8:11], v23 offset:34816\n"
"v_mfma_f32_16x16x32_bf16 v[156:159], a[120:123], a[72:75], v[156:159]\n"
"ds_read_b64_tr_b16 a[40:41], v5 offset:32768\n"
"buffer_load_dwordx4 v0, s[20:23], s72 offen offset:1024 sc1 lds\n"
"v_mfma_f32_16x16x32_bf16 v[152:155], a[112:115], a[72:75], v[152:155]\n"
"ds_read_b64_tr_b16 a[42:43], v5 offset:33792\n"
"v_mfma_f32_16x16x32_bf16 v[148:151], a[104:107], a[72:75], v[148:151]\n"
"ds_read_b128 a[16:19], v23 offset:36864\n"
"v_mfma_f32_16x16x32_bf16 v[144:147], a[96:99], a[72:75], v[144:147]\n"
"ds_read_b64_tr_b16 a[48:49], v6 offset:32768\n"
"v_mfma_f32_16x16x32_bf16 v[160:163], a[96:99], a[80:83], v[160:163]\n"
"ds_read_b64_tr_b16 a[50:51], v6 offset:33792\n"
"buffer_load_dwordx4 v0, s[20:23], s72 offen offset:2048 sc1 lds\n"
"v_mfma_f32_16x16x32_bf16 v[164:167], a[104:107], a[80:83], v[164:167]\n"
"ds_read_b128 a[24:27], v23 offset:38912\n"
"v_mfma_f32_16x16x32_bf16 v[168:171], a[112:115], a[80:83], v[168:171]\n"
"ds_read_b64_tr_b16 a[56:57], v7 offset:32768\n"
"v_mfma_f32_16x16x32_bf16 v[172:175], a[120:123], a[80:83], v[172:175]\n"
"ds_read_b64_tr_b16 a[58:59], v7 offset:33792\n"
"v_mfma_f32_16x16x32_bf16 v[188:191], a[120:123], a[88:91], v[188:191]\n"
"buffer_load_dwordx4 v0, s[20:23], s72 offen offset:3072 sc1 lds\n"
"s_add_u32 s72, s72, 16384\n"
"v_mfma_f32_16x16x32_bf16 v[184:187], a[112:115], a[88:91], v[184:187]\n"
"ds_read_b128 a[4:7], v13 offset:32768\n"
"v_mfma_f32_16x16x32_bf16 v[180:183], a[104:107], a[88:91], v[180:183]\n"
"ds_read_b64_tr_b16 a[36:37], v24 offset:40960\n"
"v_mfma_f32_16x16x32_bf16 v[176:179], a[96:99], a[88:91], v[176:179]\n"
"ds_read_b64_tr_b16 a[38:39], v24 offset:41984\n"
"s_add_u32 m0, s77, 16384\n"
"v_mfma_f32_16x16x32_bf16 v[128:131], a[100:103], a[68:71], v[128:131]\n"
"ds_read_b128 a[12:15], v1 offset:32768\n"
"buffer_load_dwordx4 v25, s[52:55], s74 offen offset:0 lds\n"
"v_mfma_f32_16x16x32_bf16 v[132:135], a[108:111], a[68:71], v[132:135]\n"
"ds_read_b64_tr_b16 a[44:45], v5 offset:40960\n"
"v_mfma_f32_16x16x32_bf16 v[136:139], a[116:119], a[68:71], v[136:139]\n"
"ds_read_b64_tr_b16 a[46:47], v5 offset:41984\n"
"v_mfma_f32_16x16x32_bf16 v[140:143], a[124:127], a[68:71], v[140:143]\n"
"ds_read_b128 a[20:23], v13 offset:36864\n"
"buffer_load_dwordx4 v26, s[52:55], s74 offen offset:1024 lds\n"
"v_mfma_f32_16x16x32_bf16 v[156:159], a[124:127], a[76:79], v[156:159]\n"
"ds_read_b64_tr_b16 a[52:53], v6 offset:40960\n"
"v_mfma_f32_16x16x32_bf16 v[152:155], a[116:119], a[76:79], v[152:155]\n"
"ds_read_b64_tr_b16 a[54:55], v6 offset:41984\n"
"v_mfma_f32_16x16x32_bf16 v[148:151], a[108:111], a[76:79], v[148:151]\n"
"ds_read_b128 a[28:31], v1 offset:36864\n"
"v_mfma_f32_16x16x32_bf16 v[144:147], a[100:103], a[76:79], v[144:147]\n"
"ds_read_b64_tr_b16 a[60:61], v7 offset:40960\n"
"buffer_load_dwordx4 v27, s[52:55], s74 offen offset:2048 lds\n"
"v_mfma_f32_16x16x32_bf16 v[160:163], a[100:103], a[84:87], v[160:163]\n"
"ds_read_b64_tr_b16 a[62:63], v7 offset:41984\n"
"v_mfma_f32_16x16x32_bf16 v[164:167], a[108:111], a[84:87], v[164:167]\n"
"v_mfma_f32_16x16x32_bf16 v[168:171], a[116:119], a[84:87], v[168:171]\n"
"v_mfma_f32_16x16x32_bf16 v[172:175], a[124:127], a[84:87], v[172:175]\n"
"buffer_load_dwordx4 v28, s[52:55], s74 offen offset:3072 lds\n"
"s_add_u32 s74, s74, 131072\n"
"v_mfma_f32_16x16x32_bf16 v[188:191], a[124:127], a[92:95], v[188:191]\n"
"v_mfma_f32_16x16x32_bf16 v[184:187], a[116:119], a[92:95], v[184:187]\n"
"v_mfma_f32_16x16x32_bf16 v[180:183], a[108:111], a[92:95], v[180:183]\n"
"v_mfma_f32_16x16x32_bf16 v[176:179], a[100:103], a[92:95], v[176:179]\n"
"s_waitcnt vmcnt(16) lgkmcnt(0)\n"
"s_barrier\n"
"v_mfma_f32_16x16x32_bf16 v[128:131], a[32:35], a[0:3], v[128:131]\n"
"ds_read_b128 a[64:67], v23 offset:49152\n"
"s_add_u32 m0, s76, 32768\n"
"v_mfma_f32_16x16x32_bf16 v[132:135], a[40:43], a[0:3], v[132:135]\n"
"ds_read_b64_tr_b16 a[96:97], v24 offset:49152\n"
"buffer_load_dwordx4 v0, s[20:23], s72 offen offset:0 sc1 lds\n"
"v_mfma_f32_16x16x32_bf16 v[136:139], a[48:51], a[0:3], v[136:139]\n"
"ds_read_b64_tr_b16 a[98:99], v24 offset:50176\n"
"v_mfma_f32_16x16x32_bf16 v[140:143], a[56:59], a[0:3], v[140:143]\n"
"ds_read_b128 a[72:75], v23 offset:51200\n"
"v_mfma_f32_16x16x32_bf16 v[156:159], a[56:59], a[8:11], v[156:159]\n"
"ds_read_b64_tr_b16 a[104:105], v5 offset:49152\n"
"buffer_load_dwordx4 v0, s[20:23], s72 offen offset:1024 sc1 lds\n"
"v_mfma_f32_16x16x32_bf16 v[152:155], a[48:51], a[8:11], v[152:155]\n"
"ds_read_b64_tr_b16 a[106:107], v5 offset:50176\n"
"v_mfma_f32_16x16x32_bf16 v[148:151], a[40:43], a[8:11], v[148:151]\n"
"ds_read_b128 a[80:83], v23 offset:53248\n"
"v_mfma_f32_16x16x32_bf16 v[144:147], a[32:35], a[8:11], v[144:147]\n"
"ds_read_b64_tr_b16 a[112:113], v6 offset:49152\n"
"v_mfma_f32_16x16x32_bf16 v[160:163], a[32:35], a[16:19], v[160:163]\n"
"ds_read_b64_tr_b16 a[114:115], v6 offset:50176\n"
"buffer_load_dwordx4 v0, s[20:23], s72 offen offset:2048 sc1 lds\n"
"v_mfma_f32_16x16x32_bf16 v[164:167], a[40:43], a[16:19], v[164:167]\n"
"ds_read_b128 a[88:91], v23 offset:55296\n"
"v_mfma_f32_16x16x32_bf16 v[168:171], a[48:51], a[16:19], v[168:171]\n"
"ds_read_b64_tr_b16 a[120:121], v7 offset:49152\n"
"v_mfma_f32_16x16x32_bf16 v[172:175], a[56:59], a[16:19], v[172:175]\n"
"ds_read_b64_tr_b16 a[122:123], v7 offset:50176\n"
"v_mfma_f32_16x16x32_bf16 v[188:191], a[56:59], a[24:27], v[188:191]\n"
"buffer_load_dwordx4 v0, s[20:23], s72 offen offset:3072 sc1 lds\n"
"s_add_u32 s72, s72, 16384\n"
"v_mfma_f32_16x16x32_bf16 v[184:187], a[48:51], a[24:27], v[184:187]\n"
"ds_read_b128 a[68:71], v4 offset:49152\n"
"v_mfma_f32_16x16x32_bf16 v[180:183], a[40:43], a[24:27], v[180:183]\n"
"ds_read_b64_tr_b16 a[100:101], v24 offset:57344\n"
"v_mfma_f32_16x16x32_bf16 v[176:179], a[32:35], a[24:27], v[176:179]\n"
"ds_read_b64_tr_b16 a[102:103], v24 offset:58368\n"
"s_add_u32 m0, s77, 32768\n"
"v_mfma_f32_16x16x32_bf16 v[128:131], a[36:39], a[4:7], v[128:131]\n"
"ds_read_b128 a[76:79], v4 offset:51200\n"
"buffer_load_dwordx4 v25, s[52:55], s74 offen offset:0 lds\n"
"v_mfma_f32_16x16x32_bf16 v[132:135], a[44:47], a[4:7], v[132:135]\n"
"ds_read_b64_tr_b16 a[108:109], v5 offset:57344\n"
"v_mfma_f32_16x16x32_bf16 v[136:139], a[52:55], a[4:7], v[136:139]\n"
"ds_read_b64_tr_b16 a[110:111], v5 offset:58368\n"
"v_mfma_f32_16x16x32_bf16 v[140:143], a[60:63], a[4:7], v[140:143]\n"
"ds_read_b128 a[84:87], v4 offset:53248\n"
"buffer_load_dwordx4 v26, s[52:55], s74 offen offset:1024 lds\n"
"v_mfma_f32_16x16x32_bf16 v[156:159], a[60:63], a[12:15], v[156:159]\n"
"ds_read_b64_tr_b16 a[116:117], v6 offset:57344\n"
"v_mfma_f32_16x16x32_bf16 v[152:155], a[52:55], a[12:15], v[152:155]\n"
"ds_read_b64_tr_b16 a[118:119], v6 offset:58368\n"
"v_mfma_f32_16x16x32_bf16 v[148:151], a[44:47], a[12:15], v[148:151]\n"
"ds_read_b128 a[92:95], v4 offset:55296\n"
"v_mfma_f32_16x16x32_bf16 v[144:147], a[36:39], a[12:15], v[144:147]\n"
"ds_read_b64_tr_b16 a[124:125], v7 offset:57344\n"
"buffer_load_dwordx4 v27, s[52:55], s74 offen offset:2048 lds\n"
"v_mfma_f32_16x16x32_bf16 v[160:163], a[36:39], a[20:23], v[160:163]\n"
"ds_read_b64_tr_b16 a[126:127], v7 offset:58368\n"
"v_mfma_f32_16x16x32_bf16 v[164:167], a[44:47], a[20:23], v[164:167]\n"
"v_mfma_f32_16x16x32_bf16 v[168:171], a[52:55], a[20:23], v[168:171]\n"
"v_mfma_f32_16x16x32_bf16 v[172:175], a[60:63], a[20:23], v[172:175]\n"
"buffer_load_dwordx4 v28, s[52:55], s74 offen offset:3072 lds\n"
"s_add_u32 s74, s74, 131072\n"
"v_mfma_f32_16x16x32_bf16 v[188:191], a[60:63], a[28:31], v[188:191]\n"
"v_mfma_f32_16x16x32_bf16 v[184:187], a[52:55], a[28:31], v[184:187]\n"
"v_mfma_f32_16x16x32_bf16 v[180:183], a[44:47], a[28:31], v[180:183]\n"
"v_mfma_f32_16x16x32_bf16 v[176:179], a[36:39], a[28:31], v[176:179]\n"
"s_waitcnt vmcnt(16) lgkmcnt(0)\n"
"s_barrier\n"
"v_mfma_f32_16x16x32_bf16 v[128:131], a[96:99], a[64:67], v[128:131]\n"
"ds_read_b128 a[0:3], v11 offset:0\n"
"s_add_u32 m0, s76, 49152\n"
"v_lshlrev_b32 v17, 2, v0\n"
"v_mfma_f32_16x16x32_bf16 v[132:135], a[104:107], a[64:67], v[132:135]\n"
"ds_read_b64_tr_b16 a[32:33], v24 offset:0\n"
"buffer_load_dwordx4 v0, s[20:23], s72 offen offset:0 sc1 lds\n"
"v_mfma_f32_16x16x32_bf16 v[136:139], a[112:115], a[64:67], v[136:139]\n"
"ds_read_b64_tr_b16 a[34:35], v24 offset:1024\n"
"v_mfma_f32_16x16x32_bf16 v[140:143], a[120:123], a[64:67], v[140:143]\n"
"ds_read_b128 a[8:11], v12 offset:0\n"
"v_mfma_f32_16x16x32_bf16 v[156:159], a[120:123], a[72:75], v[156:159]\n"
"ds_read_b64_tr_b16 a[40:41], v5 offset:0\n"
"buffer_load_dwordx4 v0, s[20:23], s72 offen offset:1024 sc1 lds\n"
"v_mfma_f32_16x16x32_bf16 v[152:155], a[112:115], a[72:75], v[152:155]\n"
"ds_read_b64_tr_b16 a[42:43], v5 offset:1024\n"
"v_mfma_f32_16x16x32_bf16 v[148:151], a[104:107], a[72:75], v[148:151]\n"
"ds_read_b128 a[16:19], v11 offset:4096\n"
"v_mfma_f32_16x16x32_bf16 v[144:147], a[96:99], a[72:75], v[144:147]\n"
"ds_read_b64_tr_b16 a[48:49], v6 offset:0\n"
"v_mfma_f32_16x16x32_bf16 v[160:163], a[96:99], a[80:83], v[160:163]\n"
"ds_read_b64_tr_b16 a[50:51], v6 offset:1024\n"
"buffer_load_dwordx4 v0, s[20:23], s72 offen offset:2048 sc1 lds\n"
"v_mfma_f32_16x16x32_bf16 v[164:167], a[104:107], a[80:83], v[164:167]\n"
"ds_read_b128 a[24:27], v12 offset:4096\n"
"v_mfma_f32_16x16x32_bf16 v[168:171], a[112:115], a[80:83], v[168:171]\n"
"ds_read_b64_tr_b16 a[56:57], v7 offset:0\n"
"v_mfma_f32_16x16x32_bf16 v[172:175], a[120:123], a[80:83], v[172:175]\n"
"ds_read_b64_tr_b16 a[58:59], v7 offset:1024\n"
"v_mfma_f32_16x16x32_bf16 v[188:191], a[120:123], a[88:91], v[188:191]\n"
"buffer_load_dwordx4 v0, s[20:23], s72 offen offset:3072 sc1 lds\n"
"s_add_u32 s72, s72, 16384\n"
"v_mfma_f32_16x16x32_bf16 v[184:187], a[112:115], a[88:91], v[184:187]\n"
"ds_read_b128 a[4:7], v4 offset:0\n"
"v_mfma_f32_16x16x32_bf16 v[180:183], a[104:107], a[88:91], v[180:183]\n"
"ds_read_b64_tr_b16 a[36:37], v24 offset:8192\n"
"v_mfma_f32_16x16x32_bf16 v[176:179], a[96:99], a[88:91], v[176:179]\n"
"ds_read_b64_tr_b16 a[38:39], v24 offset:9216\n"
"s_add_u32 m0, s77, 49152\n"
"v_mfma_f32_16x16x32_bf16 v[128:131], a[100:103], a[68:71], v[128:131]\n"
"ds_read_b128 a[12:15], v4 offset:2048\n"
"buffer_load_dwordx4 v25, s[52:55], s74 offen offset:0 lds\n"
"v_mfma_f32_16x16x32_bf16 v[132:135], a[108:111], a[68:71], v[132:135]\n"
"ds_read_b64_tr_b16 a[44:45], v5 offset:8192\n"
"v_mfma_f32_16x16x32_bf16 v[136:139], a[116:119], a[68:71], v[136:139]\n"
"ds_read_b64_tr_b16 a[46:47], v5 offset:9216\n"
"v_mfma_f32_16x16x32_bf16 v[140:143], a[124:127], a[68:71], v[140:143]\n"
"ds_read_b128 a[20:23], v4 offset:4096\n"
"buffer_load_dwordx4 v26, s[52:55], s74 offen offset:1024 lds\n"
"v_mfma_f32_16x16x32_bf16 v[156:159], a[124:127], a[76:79], v[156:159]\n"
"ds_read_b64_tr_b16 a[52:53], v6 offset:8192\n"
"v_mfma_f32_16x16x32_bf16 v[152:155], a[116:119], a[76:79], v[152:155]\n"
"ds_read_b64_tr_b16 a[54:55], v6 offset:9216\n"
"v_mfma_f32_16x16x32_bf16 v[148:151], a[108:111], a[76:79], v[148:151]\n"
"ds_read_b128 a[28:31], v4 offset:6144\n"
"v_mfma_f32_16x16x32_bf16 v[144:147], a[100:103], a[76:79], v[144:147]\n"
"ds_read_b64_tr_b16 a[60:61], v7 offset:8192\n"
"buffer_load_dwordx4 v27, s[52:55], s74 offen offset:2048 lds\n"
"v_mfma_f32_16x16x32_bf16 v[160:163], a[100:103], a[84:87], v[160:163]\n"
"ds_read_b64_tr_b16 a[62:63], v7 offset:9216\n"
"v_mfma_f32_16x16x32_bf16 v[164:167], a[108:111], a[84:87], v[164:167]\n"
"v_mfma_f32_16x16x32_bf16 v[168:171], a[116:119], a[84:87], v[168:171]\n"
"v_mfma_f32_16x16x32_bf16 v[172:175], a[124:127], a[84:87], v[172:175]\n"
"buffer_load_dwordx4 v28, s[52:55], s74 offen offset:3072 lds\n"
"s_add_u32 s74, s74, 131072\n"
"v_mfma_f32_16x16x32_bf16 v[188:191], a[124:127], a[92:95], v[188:191]\n"
"v_mfma_f32_16x16x32_bf16 v[184:187], a[116:119], a[92:95], v[184:187]\n"
"v_mfma_f32_16x16x32_bf16 v[180:183], a[108:111], a[92:95], v[180:183]\n"
"v_mfma_f32_16x16x32_bf16 v[176:179], a[100:103], a[92:95], v[176:179]\n"
"s_waitcnt vmcnt(16) lgkmcnt(0)\n"
"s_barrier\n"
"s_mov_b32 s72, s73\n"
"s_mov_b32 s74, 0\n"
"s_mov_b32 s52, s56\n"
"s_mov_b32 s53, s57\n"
"s_mov_b32 s54, s58\n"
"s_and_b32 s76, s77, 0x2000\n"
"s_and_b32 s95, s77, 0x1000\n"
"s_lshr_b32 s95, s95, 1\n"
"s_or_b32 s76, s76, s95\n"
"v_mfma_f32_16x16x32_bf16 v[128:131], a[32:35], a[0:3], v[128:131]\n"
"ds_read_b128 a[64:67], v23 offset:16384\n"
"s_add_u32 m0, s77, 0\n"
"v_mfma_f32_16x16x32_bf16 v[132:135], a[40:43], a[0:3], v[132:135]\n"
"ds_read_b64_tr_b16 a[96:97], v24 offset:16384\n"
"buffer_load_dwordx4 v25, s[52:55], s74 offen offset:0 lds\n"
"v_mfma_f32_16x16x32_bf16 v[136:139], a[48:51], a[0:3], v[136:139]\n"
"ds_read_b64_tr_b16 a[98:99], v24 offset:17408\n"
"v_mfma_f32_16x16x32_bf16 v[140:143], a[56:59], a[0:3], v[140:143]\n"
"ds_read_b128 a[72:75], v23 offset:18432\n"
"v_mfma_f32_16x16x32_bf16 v[156:159], a[56:59], a[8:11], v[156:159]\n"
"ds_read_b64_tr_b16 a[104:105], v5 offset:16384\n"
"v_mfma_f32_16x16x32_bf16 v[152:155], a[48:51], a[8:11], v[152:155]\n"
"ds_read_b64_tr_b16 a[106:107], v5 offset:17408\n"
"v_mfma_f32_16x16x32_bf16 v[148:151], a[40:43], a[8:11], v[148:151]\n"
"ds_read_b128 a[80:83], v23 offset:20480\n"
"v_mfma_f32_16x16x32_bf16 v[144:147], a[32:35], a[8:11], v[144:147]\n"
"ds_read_b64_tr_b16 a[112:113], v6 offset:16384\n"
"v_mfma_f32_16x16x32_bf16 v[160:163], a[32:35], a[16:19], v[160:163]\n"
"ds_read_b64_tr_b16 a[114:115], v6 offset:17408\n"
"buffer_load_dwordx4 v26, s[52:55], s74 offen offset:1024 lds\n"
"v_mfma_f32_16x16x32_bf16 v[164:167], a[40:43], a[16:19], v[164:167]\n"
"ds_read_b128 a[88:91], v23 offset:22528\n"
"v_mfma_f32_16x16x32_bf16 v[168:171], a[48:51], a[16:19], v[168:171]\n"
"ds_read_b64_tr_b16 a[120:121], v7 offset:16384\n"
"v_mfma_f32_16x16x32_bf16 v[172:175], a[56:59], a[16:19], v[172:175]\n"
"ds_read_b64_tr_b16 a[122:123], v7 offset:17408\n"
"v_mfma_f32_16x16x32_bf16 v[188:191], a[56:59], a[24:27], v[188:191]\n"
"v_mfma_f32_16x16x32_bf16 v[184:187], a[48:51], a[24:27], v[184:187]\n"
"ds_read_b128 a[68:71], v13 offset:16384\n"
"v_mfma_f32_16x16x32_bf16 v[180:183], a[40:43], a[24:27], v[180:183]\n"
"ds_read_b64_tr_b16 a[100:101], v24 offset:24576\n"
"v_mfma_f32_16x16x32_bf16 v[176:179], a[32:35], a[24:27], v[176:179]\n"
"ds_read_b64_tr_b16 a[102:103], v24 offset:25600\n"
"v_mfma_f32_16x16x32_bf16 v[128:131], a[36:39], a[4:7], v[128:131]\n"
"ds_read_b128 a[76:79], v1 offset:16384\n"
"buffer_load_dwordx4 v27, s[52:55], s74 offen offset:2048 lds\n"
"v_mfma_f32_16x16x32_bf16 v[132:135], a[44:47], a[4:7], v[132:135]\n"
"ds_read_b64_tr_b16 a[108:109], v5 offset:24576\n"
"v_mfma_f32_16x16x32_bf16 v[136:139], a[52:55], a[4:7], v[136:139]\n"
"ds_read_b64_tr_b16 a[110:111], v5 offset:25600\n"
"v_mfma_f32_16x16x32_bf16 v[140:143], a[60:63], a[4:7], v[140:143]\n"
"ds_read_b128 a[84:87], v13 offset:20480\n"
"v_mfma_f32_16x16x32_bf16 v[156:159], a[60:63], a[12:15], v[156:159]\n"
"ds_read_b64_tr_b16 a[116:117], v6 offset:24576\n"
"v_mfma_f32_16x16x32_bf16 v[152:155], a[52:55], a[12:15], v[152:155]\n"
"ds_read_b64_tr_b16 a[118:119], v6 offset:25600\n"
"v_mfma_f32_16x16x32_bf16 v[148:151], a[44:47], a[12:15], v[148:151]\n"
"ds_read_b128 a[92:95], v1 offset:20480\n"
"v_mfma_f32_16x16x32_bf16 v[144:147], a[36:39], a[12:15], v[144:147]\n"
"ds_read_b64_tr_b16 a[124:125], v7 offset:24576\n"
"buffer_load_dwordx4 v28, s[52:55], s74 offen offset:3072 lds\n"
"s_add_u32 s74, s74, 131072\n"
"v_mfma_f32_16x16x32_bf16 v[160:163], a[36:39], a[20:23], v[160:163]\n"
"ds_read_b64_tr_b16 a[126:127], v7 offset:25600\n"
"v_mfma_f32_16x16x32_bf16 v[164:167], a[44:47], a[20:23], v[164:167]\n"
"v_mfma_f32_16x16x32_bf16 v[168:171], a[52:55], a[20:23], v[168:171]\n"
"v_mfma_f32_16x16x32_bf16 v[172:175], a[60:63], a[20:23], v[172:175]\n"
"v_mfma_f32_16x16x32_bf16 v[188:191], a[60:63], a[28:31], v[188:191]\n"
"v_mfma_f32_16x16x32_bf16 v[184:187], a[52:55], a[28:31], v[184:187]\n"
"v_mfma_f32_16x16x32_bf16 v[180:183], a[44:47], a[28:31], v[180:183]\n"
"v_mfma_f32_16x16x32_bf16 v[176:179], a[36:39], a[28:31], v[176:179]\n"
"s_waitcnt vmcnt(12) lgkmcnt(0)\n"
"s_barrier\n"
"v_mfma_f32_16x16x32_bf16 v[128:131], a[96:99], a[64:67], v[128:131]\n"
"ds_read_b128 a[0:3], v23 offset:32768\n"
"s_add_u32 m0, s77, 16384\n"
"v_mfma_f32_16x16x32_bf16 v[132:135], a[104:107], a[64:67], v[132:135]\n"
"ds_read_b64_tr_b16 a[32:33], v24 offset:32768\n"
"buffer_load_dwordx4 v25, s[52:55], s74 offen offset:0 lds\n"
"v_mfma_f32_16x16x32_bf16 v[136:139], a[112:115], a[64:67], v[136:139]\n"
"ds_read_b64_tr_b16 a[34:35], v24 offset:33792\n"
"v_mfma_f32_16x16x32_bf16 v[140:143], a[120:123], a[64:67], v[140:143]\n"
"ds_read_b128 a[8:11], v23 offset:34816\n"
"v_mfma_f32_16x16x32_bf16 v[156:159], a[120:123], a[72:75], v[156:159]\n"
"ds_read_b64_tr_b16 a[40:41], v5 offset:32768\n"
"v_mfma_f32_16x16x32_bf16 v[152:155], a[112:115], a[72:75], v[152:155]\n"
"ds_read_b64_tr_b16 a[42:43], v5 offset:33792\n"
"v_mfma_f32_16x16x32_bf16 v[148:151], a[104:107], a[72:75], v[148:151]\n"
"ds_read_b128 a[16:19], v23 offset:36864\n"
"v_mfma_f32_16x16x32_bf16 v[144:147], a[96:99], a[72:75], v[144:147]\n"
"ds_read_b64_tr_b16 a[48:49], v6 offset:32768\n"
"v_mfma_f32_16x16x32_bf16 v[160:163], a[96:99], a[80:83], v[160:163]\n"
"ds_read_b64_tr_b16 a[50:51], v6 offset:33792\n"
"buffer_load_dwordx4 v26, s[52:55], s74 offen offset:1024 lds\n"
"v_mfma_f32_16x16x32_bf16 v[164:167], a[104:107], a[80:83], v[164:167]\n"
"ds_read_b128 a[24:27], v23 offset:38912\n"
"v_mfma_f32_16x16x32_bf16 v[168:171], a[112:115], a[80:83], v[168:171]\n"
"ds_read_b64_tr_b16 a[56:57], v7 offset:32768\n"
"v_mfma_f32_16x16x32_bf16 v[172:175], a[120:123], a[80:83], v[172:175]\n"
"ds_read_b64_tr_b16 a[58:59], v7 offset:33792\n"
"v_mfma_f32_16x16x32_bf16 v[188:191], a[120:123], a[88:91], v[188:191]\n"
"v_mfma_f32_16x16x32_bf16 v[184:187], a[112:115], a[88:91], v[184:187]\n"
"ds_read_b128 a[4:7], v4 offset:32768\n"
"v_mfma_f32_16x16x32_bf16 v[180:183], a[104:107], a[88:91], v[180:183]\n"
"ds_read_b64_tr_b16 a[36:37], v24 offset:40960\n"
"v_mfma_f32_16x16x32_bf16 v[176:179], a[96:99], a[88:91], v[176:179]\n"
"ds_read_b64_tr_b16 a[38:39], v24 offset:41984\n"
"v_mfma_f32_16x16x32_bf16 v[128:131], a[100:103], a[68:71], v[128:131]\n"
"ds_read_b128 a[12:15], v4 offset:34816\n"
"buffer_load_dwordx4 v27, s[52:55], s74 offen offset:2048 lds\n"
"v_mfma_f32_16x16x32_bf16 v[132:135], a[108:111], a[68:71], v[132:135]\n"
"ds_read_b64_tr_b16 a[44:45], v5 offset:40960\n"
"v_mfma_f32_16x16x32_bf16 v[136:139], a[116:119], a[68:71], v[136:139]\n"
"ds_read_b64_tr_b16 a[46:47], v5 offset:41984\n"
"v_mfma_f32_16x16x32_bf16 v[140:143], a[124:127], a[68:71], v[140:143]\n"
"ds_read_b128 a[20:23], v4 offset:36864\n"
"v_mfma_f32_16x16x32_bf16 v[156:159], a[124:127], a[76:79], v[156:159]\n"
"ds_read_b64_tr_b16 a[52:53], v6 offset:40960\n"
"v_mfma_f32_16x16x32_bf16 v[152:155], a[116:119], a[76:79], v[152:155]\n"
"ds_read_b64_tr_b16 a[54:55], v6 offset:41984\n"
"v_mfma_f32_16x16x32_bf16 v[148:151], a[108:111], a[76:79], v[148:151]\n"
"ds_read_b128 a[28:31], v4 offset:38912\n"
"v_mfma_f32_16x16x32_bf16 v[144:147], a[100:103], a[76:79], v[144:147]\n"
"ds_read_b64_tr_b16 a[60:61], v7 offset:40960\n"
"buffer_load_dwordx4 v28, s[52:55], s74 offen offset:3072 lds\n"
"s_add_u32 s74, s74, 131072\n"
"v_mfma_f32_16x16x32_bf16 v[160:163], a[100:103], a[84:87], v[160:163]\n"
"ds_read_b64_tr_b16 a[62:63], v7 offset:41984\n"
"v_mfma_f32_16x16x32_bf16 v[164:167], a[108:111], a[84:87], v[164:167]\n"
"v_mfma_f32_16x16x32_bf16 v[168:171], a[116:119], a[84:87], v[168:171]\n"
"v_mfma_f32_16x16x32_bf16 v[172:175], a[124:127], a[84:87], v[172:175]\n"
"v_mfma_f32_16x16x32_bf16 v[188:191], a[124:127], a[92:95], v[188:191]\n"
"v_mfma_f32_16x16x32_bf16 v[184:187], a[116:119], a[92:95], v[184:187]\n"
"v_mfma_f32_16x16x32_bf16 v[180:183], a[108:111], a[92:95], v[180:183]\n"
"v_mfma_f32_16x16x32_bf16 v[176:179], a[100:103], a[92:95], v[176:179]\n"
"s_waitcnt vmcnt(8) lgkmcnt(0)\n"
"s_barrier\n"
"v_mfma_f32_16x16x32_bf16 v[128:131], a[32:35], a[0:3], v[128:131]\n"
"ds_read_b128 a[64:67], v11 offset:49152\n"
"s_add_u32 m0, s77, 32768\n"
"buffer_load_dwordx2 v[250:251], v10, s[20:23], s84 offen sc1\n"
"v_mfma_f32_16x16x32_bf16 v[132:135], a[40:43], a[0:3], v[132:135]\n"
"ds_read_b64_tr_b16 a[96:97], v24 offset:49152\n"
"buffer_load_dwordx4 v25, s[52:55], s74 offen offset:0 lds\n"
"v_mfma_f32_16x16x32_bf16 v[136:139], a[48:51], a[0:3], v[136:139]\n"
"ds_read_b64_tr_b16 a[98:99], v24 offset:50176\n"
"v_mfma_f32_16x16x32_bf16 v[140:143], a[56:59], a[0:3], v[140:143]\n"
"ds_read_b128 a[72:75], v12 offset:49152\n"
"v_mfma_f32_16x16x32_bf16 v[156:159], a[56:59], a[8:11], v[156:159]\n"
"ds_read_b64_tr_b16 a[104:105], v5 offset:49152\n"
"v_mfma_f32_16x16x32_bf16 v[152:155], a[48:51], a[8:11], v[152:155]\n"
"ds_read_b64_tr_b16 a[106:107], v5 offset:50176\n"
"v_mfma_f32_16x16x32_bf16 v[148:151], a[40:43], a[8:11], v[148:151]\n"
"ds_read_b128 a[80:83], v11 offset:53248\n"
"v_mfma_f32_16x16x32_bf16 v[144:147], a[32:35], a[8:11], v[144:147]\n"
"ds_read_b64_tr_b16 a[112:113], v6 offset:49152\n"
"v_mfma_f32_16x16x32_bf16 v[160:163], a[32:35], a[16:19], v[160:163]\n"
"ds_read_b64_tr_b16 a[114:115], v6 offset:50176\n"
"buffer_load_dwordx4 v26, s[52:55], s74 offen offset:1024 lds\n"
"v_mfma_f32_16x16x32_bf16 v[164:167], a[40:43], a[16:19], v[164:167]\n"
"ds_read_b128 a[88:91], v12 offset:53248\n"
"v_mfma_f32_16x16x32_bf16 v[168:171], a[48:51], a[16:19], v[168:171]\n"
"ds_read_b64_tr_b16 a[120:121], v7 offset:49152\n"
"v_mfma_f32_16x16x32_bf16 v[172:175], a[56:59], a[16:19], v[172:175]\n"
"ds_read_b64_tr_b16 a[122:123], v7 offset:50176\n"
"v_mfma_f32_16x16x32_bf16 v[188:191], a[56:59], a[24:27], v[188:191]\n"
"v_mfma_f32_16x16x32_bf16 v[184:187], a[48:51], a[24:27], v[184:187]\n"
"ds_read_b128 a[68:71], v4 offset:49152\n"
"v_mfma_f32_16x16x32_bf16 v[180:183], a[40:43], a[24:27], v[180:183]\n"
"ds_read_b64_tr_b16 a[100:101], v24 offset:57344\n"
"v_mfma_f32_16x16x32_bf16 v[176:179], a[32:35], a[24:27], v[176:179]\n"
"ds_read_b64_tr_b16 a[102:103], v24 offset:58368\n"
"v_mfma_f32_16x16x32_bf16 v[128:131], a[36:39], a[4:7], v[128:131]\n"
"ds_read_b128 a[76:79], v4 offset:51200\n"
"buffer_load_dwordx4 v27, s[52:55], s74 offen offset:2048 lds\n"
"v_mfma_f32_16x16x32_bf16 v[132:135], a[44:47], a[4:7], v[132:135]\n"
"ds_read_b64_tr_b16 a[108:109], v5 offset:57344\n"
"v_mfma_f32_16x16x32_bf16 v[136:139], a[52:55], a[4:7], v[136:139]\n"
"ds_read_b64_tr_b16 a[110:111], v5 offset:58368\n"
"v_mfma_f32_16x16x32_bf16 v[140:143], a[60:63], a[4:7], v[140:143]\n"
"ds_read_b128 a[84:87], v4 offset:53248\n"
"v_mfma_f32_16x16x32_bf16 v[156:159], a[60:63], a[12:15], v[156:159]\n"
"ds_read_b64_tr_b16 a[116:117], v6 offset:57344\n"
"v_mfma_f32_16x16x32_bf16 v[152:155], a[52:55], a[12:15], v[152:155]\n"
"ds_read_b64_tr_b16 a[118:119], v6 offset:58368\n"
"v_mfma_f32_16x16x32_bf16 v[148:151], a[44:47], a[12:15], v[148:151]\n"
"ds_read_b128 a[92:95], v4 offset:55296\n"
"v_mfma_f32_16x16x32_bf16 v[144:147], a[36:39], a[12:15], v[144:147]\n"
"ds_read_b64_tr_b16 a[124:125], v7 offset:57344\n"
"buffer_load_dwordx4 v28, s[52:55], s74 offen offset:3072 lds\n"
"s_add_u32 s74, s74, 131072\n"
"v_mfma_f32_16x16x32_bf16 v[160:163], a[36:39], a[20:23], v[160:163]\n"
"ds_read_b64_tr_b16 a[126:127], v7 offset:58368\n"
"v_mfma_f32_16x16x32_bf16 v[164:167], a[44:47], a[20:23], v[164:167]\n"
"v_mfma_f32_16x16x32_bf16 v[168:171], a[52:55], a[20:23], v[168:171]\n"
"v_mfma_f32_16x16x32_bf16 v[172:175], a[60:63], a[20:23], v[172:175]\n"
"v_mfma_f32_16x16x32_bf16 v[188:191], a[60:63], a[28:31], v[188:191]\n"
"v_mfma_f32_16x16x32_bf16 v[184:187], a[52:55], a[28:31], v[184:187]\n"
"v_mfma_f32_16x16x32_bf16 v[180:183], a[44:47], a[28:31], v[180:183]\n"
"v_mfma_f32_16x16x32_bf16 v[176:179], a[36:39], a[28:31], v[176:179]\n"
"s_waitcnt vmcnt(9) lgkmcnt(0)\n"
"s_barrier\n"
"v_mfma_f32_16x16x32_bf16 v[128:131], a[96:99], a[64:67], v[128:131]\n"
"ds_read_b64_tr_b16 a[32:33], v24 offset:0\n"
"s_add_u32 m0, s77, 49152\n"
"v_mfma_f32_16x16x32_bf16 v[132:135], a[104:107], a[64:67], v[132:135]\n"
"ds_read_b64_tr_b16 a[34:35], v24 offset:1024\n"
"buffer_load_dwordx4 v25, s[52:55], s74 offen offset:0 lds\n"
"v_mfma_f32_16x16x32_bf16 v[136:139], a[112:115], a[64:67], v[136:139]\n"
"v_mfma_f32_16x16x32_bf16 v[140:143], a[120:123], a[64:67], v[140:143]\n"
"ds_read_b64_tr_b16 a[40:41], v5 offset:0\n"
"v_mfma_f32_16x16x32_bf16 v[156:159], a[120:123], a[72:75], v[156:159]\n"
"ds_read_b64_tr_b16 a[42:43], v5 offset:1024\n"
"v_mfma_f32_16x16x32_bf16 v[152:155], a[112:115], a[72:75], v[152:155]\n"
"v_mfma_f32_16x16x32_bf16 v[148:151], a[104:107], a[72:75], v[148:151]\n"
"ds_read_b64_tr_b16 a[48:49], v6 offset:0\n"
"v_mfma_f32_16x16x32_bf16 v[144:147], a[96:99], a[72:75], v[144:147]\n"
"v_mfma_f32_16x16x32_bf16 v[160:163], a[96:99], a[80:83], v[160:163]\n"
"ds_read_b64_tr_b16 a[50:51], v6 offset:1024\n"
"buffer_load_dwordx4 v26, s[52:55], s74 offen offset:1024 lds\n"
"v_mfma_f32_16x16x32_bf16 v[164:167], a[104:107], a[80:83], v[164:167]\n"
"ds_read_b64_tr_b16 a[56:57], v7 offset:0\n"
"v_mfma_f32_16x16x32_bf16 v[168:171], a[112:115], a[80:83], v[168:171]\n"
"v_mfma_f32_16x16x32_bf16 v[172:175], a[120:123], a[80:83], v[172:175]\n"
"ds_read_b64_tr_b16 a[58:59], v7 offset:1024\n"
"v_mfma_f32_16x16x32_bf16 v[188:191], a[120:123], a[88:91], v[188:191]\n"
"v_mfma_f32_16x16x32_bf16 v[184:187], a[112:115], a[88:91], v[184:187]\n"
"ds_read_b64_tr_b16 a[36:37], v24 offset:8192\n"
"v_mfma_f32_16x16x32_bf16 v[180:183], a[104:107], a[88:91], v[180:183]\n"
"ds_read_b64_tr_b16 a[38:39], v24 offset:9216\n"
"v_mfma_f32_16x16x32_bf16 v[176:179], a[96:99], a[88:91], v[176:179]\n"
"v_mfma_f32_16x16x32_bf16 v[128:131], a[100:103], a[68:71], v[128:131]\n"
"ds_read_b64_tr_b16 a[44:45], v5 offset:8192\n"
"buffer_load_dwordx4 v27, s[52:55], s74 offen offset:2048 lds\n"
"v_mfma_f32_16x16x32_bf16 v[132:135], a[108:111], a[68:71], v[132:135]\n"
"ds_read_b64_tr_b16 a[46:47], v5 offset:9216\n"
"v_mfma_f32_16x16x32_bf16 v[136:139], a[116:119], a[68:71], v[136:139]\n"
"v_mfma_f32_16x16x32_bf16 v[140:143], a[124:127], a[68:71], v[140:143]\n"
"ds_read_b64_tr_b16 a[52:53], v6 offset:8192\n"
"v_mfma_f32_16x16x32_bf16 v[156:159], a[124:127], a[76:79], v[156:159]\n"
"v_mfma_f32_16x16x32_bf16 v[152:155], a[116:119], a[76:79], v[152:155]\n"
"ds_read_b64_tr_b16 a[54:55], v6 offset:9216\n"
"v_mfma_f32_16x16x32_bf16 v[148:151], a[108:111], a[76:79], v[148:151]\n"
"ds_read_b64_tr_b16 a[60:61], v7 offset:8192\n"
"v_mfma_f32_16x16x32_bf16 v[144:147], a[100:103], a[76:79], v[144:147]\n"
"buffer_load_dwordx4 v28, s[52:55], s74 offen offset:3072 lds\n"
"s_add_u32 s74, s74, 131072\n"
"v_mfma_f32_16x16x32_bf16 v[160:163], a[100:103], a[84:87], v[160:163]\n"
"ds_read_b64_tr_b16 a[62:63], v7 offset:9216\n"
"v_mfma_f32_16x16x32_bf16 v[164:167], a[108:111], a[84:87], v[164:167]\n"
"v_mfma_f32_16x16x32_bf16 v[168:171], a[116:119], a[84:87], v[168:171]\n"
"v_mfma_f32_16x16x32_bf16 v[172:175], a[124:127], a[84:87], v[172:175]\n"
"v_mfma_f32_16x16x32_bf16 v[188:191], a[124:127], a[92:95], v[188:191]\n"
"v_mfma_f32_16x16x32_bf16 v[184:187], a[116:119], a[92:95], v[184:187]\n"
"v_mfma_f32_16x16x32_bf16 v[180:183], a[108:111], a[92:95], v[180:183]\n"
"v_mfma_f32_16x16x32_bf16 v[176:179], a[100:103], a[92:95], v[176:179]\n"
"s_waitcnt vmcnt(8)\n"
"v_cmp_ne_u64 vcc, s[90:91], v[250:251]\n"
"s_cbranch_vccnz SMKV21_%=\n"
"s_add_u32 m0, s76, 0\n"
"s_nop 0\n"
"buffer_load_dwordx4 v0, s[20:23], s72 offen offset:0 sc1 lds\n"
"buffer_load_dwordx4 v0, s[20:23], s72 offen offset:1024 sc1 lds\n"
"s_add_u32 s72, s72, 16384\n"
"s_add_u32 m0, s76, 16384\n"
"s_nop 0\n"
"buffer_load_dwordx4 v0, s[20:23], s72 offen offset:0 sc1 lds\n"
"buffer_load_dwordx4 v0, s[20:23], s72 offen offset:1024 sc1 lds\n"
"s_add_u32 s72, s72, 16384\n"
"s_add_u32 m0, s76, 32768\n"
"s_nop 0\n"
"buffer_load_dwordx4 v0, s[20:23], s72 offen offset:0 sc1 lds\n"
"buffer_load_dwordx4 v0, s[20:23], s72 offen offset:1024 sc1 lds\n"
"s_add_u32 s72, s72, 16384\n"
"s_add_u32 m0, s76, 49152\n"
"s_nop 0\n"
"buffer_load_dwordx4 v0, s[20:23], s72 offen offset:0 sc1 lds\n"
"buffer_load_dwordx4 v0, s[20:23], s72 offen offset:1024 sc1 lds\n"
"s_add_u32 s72, s72, 16384\n"
"v_cvt_pk_bf16_f32 v160, v160, v161\n"
"v_cvt_pk_bf16_f32 v161, v162, v163\n"
"v_cvt_pk_bf16_f32 v162, v164, v165\n"
"v_cvt_pk_bf16_f32 v163, v166, v167\n"
"buffer_store_dwordx4 v[160:163], v0, s[20:23], s85 offen offset:0 sc1\n"
"v_cvt_pk_bf16_f32 v164, v168, v169\n"
"v_cvt_pk_bf16_f32 v165, v170, v171\n"
"v_cvt_pk_bf16_f32 v166, v172, v173\n"
"v_cvt_pk_bf16_f32 v167, v174, v175\n"
"buffer_store_dwordx4 v[164:167], v0, s[20:23], s85 offen offset:1024 sc1\n"
"v_cvt_pk_bf16_f32 v168, v176, v177\n"
"v_cvt_pk_bf16_f32 v169, v178, v179\n"
"v_cvt_pk_bf16_f32 v170, v180, v181\n"
"v_cvt_pk_bf16_f32 v171, v182, v183\n"
"buffer_store_dwordx4 v[168:171], v0, s[20:23], s85 offen offset:2048 sc1\n"
"v_cvt_pk_bf16_f32 v172, v184, v185\n"
"v_cvt_pk_bf16_f32 v173, v186, v187\n"
"v_cvt_pk_bf16_f32 v174, v188, v189\n"
"v_cvt_pk_bf16_f32 v175, v190, v191\n"
"buffer_store_dwordx4 v[172:175], v0, s[20:23], s85 offen offset:3072 sc1\n"
"SBKV22_%=:\n"
"s_waitcnt vmcnt(10)\n"
"s_barrier\n"
"ds_read_b128 a[0:3], v23 offset:0\n"
"ds_read_b128 a[8:11], v23 offset:2048\n"
"ds_read_b128 a[4:7], v4 offset:0\n"
"ds_read_b128 a[12:15], v4 offset:2048\n"
"s_waitcnt vmcnt(8) lgkmcnt(0)\n"
"s_barrier\n"
"v_mfma_f32_16x16x32_bf16 v[192:195], a[32:35], a[0:3], 0\n"
"ds_read_b128 a[64:67], v23 offset:16384\n"
"ds_read_b64_tr_b16 a[96:97], v24 offset:16384\n"
"s_add_u32 m0, s76, 0\n"
"v_mfma_f32_16x16x32_bf16 v[196:199], a[40:43], a[0:3], 0\n"
"ds_read_b64_tr_b16 a[98:99], v24 offset:17408\n"
"ds_read_b64_tr_b16 a[104:105], v5 offset:16384\n"
"buffer_load_dwordx4 v0, s[20:23], s72 offen offset:0 sc1 lds\n"
"v_mfma_f32_16x16x32_bf16 v[200:203], a[48:51], a[0:3], 0\n"
"ds_read_b64_tr_b16 a[106:107], v5 offset:17408\n"
"v_mfma_f32_16x16x32_bf16 v[204:207], a[56:59], a[0:3], 0\n"
"ds_read_b128 a[72:75], v23 offset:18432\n"
"ds_read_b64_tr_b16 a[112:113], v6 offset:16384\n"
"buffer_load_dwordx4 v0, s[20:23], s72 offen offset:1024 sc1 lds\n"
"s_add_u32 s72, s72, 16384\n"
"v_mfma_f32_16x16x32_bf16 v[220:223], a[56:59], a[8:11], 0\n"
"ds_read_b64_tr_b16 a[114:115], v6 offset:17408\n"
"s_add_u32 m0, s77, 0\n"
"v_mfma_f32_16x16x32_bf16 v[216:219], a[48:51], a[8:11], 0\n"
"ds_read_b64_tr_b16 a[120:121], v7 offset:16384\n"
"ds_read_b64_tr_b16 a[122:123], v7 offset:17408\n"
"buffer_load_dwordx4 v25, s[52:55], s74 offen offset:0 lds\n"
"v_mfma_f32_16x16x32_bf16 v[212:215], a[40:43], a[8:11], 0\n"
"ds_read_b128 a[68:71], v4 offset:16384\n"
"v_mfma_f32_16x16x32_bf16 v[208:211], a[32:35], a[8:11], 0\n"
"ds_read_b64_tr_b16 a[100:101], v24 offset:24576\n"
"ds_read_b64_tr_b16 a[102:103], v24 offset:25600\n"
"v_mfma_f32_16x16x32_bf16 v[192:195], a[36:39], a[4:7], v[192:195]\n"
"ds_read_b64_tr_b16 a[108:109], v5 offset:24576\n"
"buffer_load_dwordx4 v26, s[52:55], s74 offen offset:1024 lds\n"
"v_mfma_f32_16x16x32_bf16 v[196:199], a[44:47], a[4:7], v[196:199]\n"
"ds_read_b64_tr_b16 a[110:111], v5 offset:25600\n"
"ds_read_b128 a[76:79], v4 offset:18432\n"
"v_mfma_f32_16x16x32_bf16 v[200:203], a[52:55], a[4:7], v[200:203]\n"
"ds_read_b64_tr_b16 a[116:117], v6 offset:24576\n"
"buffer_load_dwordx4 v27, s[52:55], s74 offen offset:2048 lds\n"
"v_mfma_f32_16x16x32_bf16 v[204:207], a[60:63], a[4:7], v[204:207]\n"
"ds_read_b64_tr_b16 a[118:119], v6 offset:25600\n"
"ds_read_b64_tr_b16 a[124:125], v7 offset:24576\n"
"v_mfma_f32_16x16x32_bf16 v[220:223], a[60:63], a[12:15], v[220:223]\n"
"ds_read_b64_tr_b16 a[126:127], v7 offset:25600\n"
"buffer_load_dwordx4 v28, s[52:55], s74 offen offset:3072 lds\n"
"s_add_u32 s74, s74, 131072\n"
"v_mfma_f32_16x16x32_bf16 v[216:219], a[52:55], a[12:15], v[216:219]\n"
"v_mfma_f32_16x16x32_bf16 v[212:215], a[44:47], a[12:15], v[212:215]\n"
"v_mfma_f32_16x16x32_bf16 v[208:211], a[36:39], a[12:15], v[208:211]\n"
"s_waitcnt vmcnt(12) lgkmcnt(0)\n"
"s_barrier\n"
"v_mfma_f32_16x16x32_bf16 v[192:195], a[96:99], a[64:67], v[192:195]\n"
"ds_read_b128 a[0:3], v23 offset:32768\n"
"ds_read_b64_tr_b16 a[32:33], v24 offset:32768\n"
"s_add_u32 m0, s76, 16384\n"
"v_mfma_f32_16x16x32_bf16 v[196:199], a[104:107], a[64:67], v[196:199]\n"
"ds_read_b64_tr_b16 a[34:35], v24 offset:33792\n"
"ds_read_b64_tr_b16 a[40:41], v5 offset:32768\n"
"buffer_load_dwordx4 v0, s[20:23], s72 offen offset:0 sc1 lds\n"
"v_mfma_f32_16x16x32_bf16 v[200:203], a[112:115], a[64:67], v[200:203]\n"
"ds_read_b64_tr_b16 a[42:43], v5 offset:33792\n"
"v_mfma_f32_16x16x32_bf16 v[204:207], a[120:123], a[64:67], v[204:207]\n"
"ds_read_b128 a[8:11], v23 offset:34816\n"
"ds_read_b64_tr_b16 a[48:49], v6 offset:32768\n"
"buffer_load_dwordx4 v0, s[20:23], s72 offen offset:1024 sc1 lds\n"
"s_add_u32 s72, s72, 16384\n"
"v_mfma_f32_16x16x32_bf16 v[220:223], a[120:123], a[72:75], v[220:223]\n"
"ds_read_b64_tr_b16 a[50:51], v6 offset:33792\n"
"s_add_u32 m0, s77, 16384\n"
"v_mfma_f32_16x16x32_bf16 v[216:219], a[112:115], a[72:75], v[216:219]\n"
"ds_read_b64_tr_b16 a[56:57], v7 offset:32768\n"
"ds_read_b64_tr_b16 a[58:59], v7 offset:33792\n"
"buffer_load_dwordx4 v25, s[52:55], s74 offen offset:0 lds\n"
"v_mfma_f32_16x16x32_bf16 v[212:215], a[104:107], a[72:75], v[212:215]\n"
"ds_read_b128 a[4:7], v4 offset:32768\n"
"v_mfma_f32_16x16x32_bf16 v[208:211], a[96:99], a[72:75], v[208:211]\n"
"ds_read_b64_tr_b16 a[36:37], v24 offset:40960\n"
"ds_read_b64_tr_b16 a[38:39], v24 offset:41984\n"
"v_mfma_f32_16x16x32_bf16 v[192:195], a[100:103], a[68:71], v[192:195]\n"
"ds_read_b64_tr_b16 a[44:45], v5 offset:40960\n"
"buffer_load_dwordx4 v26, s[52:55], s74 offen offset:1024 lds\n"
"v_mfma_f32_16x16x32_bf16 v[196:199], a[108:111], a[68:71], v[196:199]\n"
"ds_read_b64_tr_b16 a[46:47], v5 offset:41984\n"
"ds_read_b128 a[12:15], v4 offset:34816\n"
"v_mfma_f32_16x16x32_bf16 v[200:203], a[116:119], a[68:71], v[200:203]\n"
"ds_read_b64_tr_b16 a[52:53], v6 offset:40960\n"
"buffer_load_dwordx4 v27, s[52:55], s74 offen offset:2048 lds\n"
"v_mfma_f32_16x16x32_bf16 v[204:207], a[124:127], a[68:71], v[204:207]\n"
"ds_read_b64_tr_b16 a[54:55], v6 offset:41984\n"
"ds_read_b64_tr_b16 a[60:61], v7 offset:40960\n"
"v_mfma_f32_16x16x32_bf16 v[220:223], a[124:127], a[76:79], v[220:223]\n"
"ds_read_b64_tr_b16 a[62:63], v7 offset:41984\n"
"buffer_load_dwordx4 v28, s[52:55], s74 offen offset:3072 lds\n"
"s_add_u32 s74, s74, 131072\n"
"v_mfma_f32_16x16x32_bf16 v[216:219], a[116:119], a[76:79], v[216:219]\n"
"v_mfma_f32_16x16x32_bf16 v[212:215], a[108:111], a[76:79], v[212:215]\n"
"v_mfma_f32_16x16x32_bf16 v[208:211], a[100:103], a[76:79], v[208:211]\n"
"s_waitcnt vmcnt(16) lgkmcnt(0)\n"
"s_barrier\n"
"v_mfma_f32_16x16x32_bf16 v[192:195], a[32:35], a[0:3], v[192:195]\n"
"ds_read_b128 a[64:67], v23 offset:49152\n"
"ds_read_b64_tr_b16 a[96:97], v24 offset:49152\n"
"s_add_u32 m0, s76, 32768\n"
"v_mfma_f32_16x16x32_bf16 v[196:199], a[40:43], a[0:3], v[196:199]\n"
"ds_read_b64_tr_b16 a[98:99], v24 offset:50176\n"
"ds_read_b64_tr_b16 a[104:105], v5 offset:49152\n"
"buffer_load_dwordx4 v0, s[20:23], s72 offen offset:0 sc1 lds\n"
"v_mfma_f32_16x16x32_bf16 v[200:203], a[48:51], a[0:3], v[200:203]\n"
"ds_read_b64_tr_b16 a[106:107], v5 offset:50176\n"
"v_mfma_f32_16x16x32_bf16 v[204:207], a[56:59], a[0:3], v[204:207]\n"
"ds_read_b128 a[72:75], v23 offset:51200\n"
"ds_read_b64_tr_b16 a[112:113], v6 offset:49152\n"
"buffer_load_dwordx4 v0, s[20:23], s72 offen offset:1024 sc1 lds\n"
"s_add_u32 s72, s72, 16384\n"
"v_mfma_f32_16x16x32_bf16 v[220:223], a[56:59], a[8:11], v[220:223]\n"
"ds_read_b64_tr_b16 a[114:115], v6 offset:50176\n"
"s_add_u32 m0, s77, 32768\n"
"v_mfma_f32_16x16x32_bf16 v[216:219], a[48:51], a[8:11], v[216:219]\n"
"ds_read_b64_tr_b16 a[120:121], v7 offset:49152\n"
"ds_read_b64_tr_b16 a[122:123], v7 offset:50176\n"
"buffer_load_dwordx4 v25, s[52:55], s74 offen offset:0 lds\n"
"v_mfma_f32_16x16x32_bf16 v[212:215], a[40:43], a[8:11], v[212:215]\n"
"ds_read_b128 a[68:71], v4 offset:49152\n"
"v_mfma_f32_16x16x32_bf16 v[208:211], a[32:35], a[8:11], v[208:211]\n"
"ds_read_b64_tr_b16 a[100:101], v24 offset:57344\n"
"ds_read_b64_tr_b16 a[102:103], v24 offset:58368\n"
"v_mfma_f32_16x16x32_bf16 v[192:195], a[36:39], a[4:7], v[192:195]\n"
"ds_read_b64_tr_b16 a[108:109], v5 offset:57344\n"
"buffer_load_dwordx4 v26, s[52:55], s74 offen offset:1024 lds\n"
"v_mfma_f32_16x16x32_bf16 v[196:199], a[44:47], a[4:7], v[196:199]\n"
"ds_read_b64_tr_b16 a[110:111], v5 offset:58368\n"
"ds_read_b128 a[76:79], v4 offset:51200\n"
"v_mfma_f32_16x16x32_bf16 v[200:203], a[52:55], a[4:7], v[200:203]\n"
"ds_read_b64_tr_b16 a[116:117], v6 offset:57344\n"
"buffer_load_dwordx4 v27, s[52:55], s74 offen offset:2048 lds\n"
"v_mfma_f32_16x16x32_bf16 v[204:207], a[60:63], a[4:7], v[204:207]\n"
"ds_read_b64_tr_b16 a[118:119], v6 offset:58368\n"
"ds_read_b64_tr_b16 a[124:125], v7 offset:57344\n"
"v_mfma_f32_16x16x32_bf16 v[220:223], a[60:63], a[12:15], v[220:223]\n"
"ds_read_b64_tr_b16 a[126:127], v7 offset:58368\n"
"buffer_load_dwordx4 v28, s[52:55], s74 offen offset:3072 lds\n"
"s_add_u32 s74, s74, 131072\n"
"v_mfma_f32_16x16x32_bf16 v[216:219], a[52:55], a[12:15], v[216:219]\n"
"v_mfma_f32_16x16x32_bf16 v[212:215], a[44:47], a[12:15], v[212:215]\n"
"v_mfma_f32_16x16x32_bf16 v[208:211], a[36:39], a[12:15], v[208:211]\n"
"s_waitcnt vmcnt(12) lgkmcnt(0)\n"
"s_barrier\n"
"s_waitcnt vmcnt(18)\n"
"v_mfma_f32_16x16x32_bf16 v[192:195], a[96:99], a[64:67], v[192:195]\n"
"ds_read_b128 a[0:3], v23 offset:0\n"
"ds_read_b64_tr_b16 a[32:33], v24 offset:0\n"
"s_add_u32 m0, s76, 49152\n"
"s_mov_b64 exec, 1\n"
"buffer_store_dwordx2 v[2:3], off, s[20:23], s87 sc1\n"
"s_mov_b64 exec, -1\n"
"v_mfma_f32_16x16x32_bf16 v[196:199], a[104:107], a[64:67], v[196:199]\n"
"ds_read_b64_tr_b16 a[34:35], v24 offset:1024\n"
"ds_read_b64_tr_b16 a[40:41], v5 offset:0\n"
"buffer_load_dwordx4 v0, s[20:23], s72 offen offset:0 sc1 lds\n"
"v_mfma_f32_16x16x32_bf16 v[200:203], a[112:115], a[64:67], v[200:203]\n"
"ds_read_b64_tr_b16 a[42:43], v5 offset:1024\n"
"v_mfma_f32_16x16x32_bf16 v[204:207], a[120:123], a[64:67], v[204:207]\n"
"ds_read_b128 a[8:11], v23 offset:2048\n"
"ds_read_b64_tr_b16 a[48:49], v6 offset:0\n"
"buffer_load_dwordx4 v0, s[20:23], s72 offen offset:1024 sc1 lds\n"
"s_add_u32 s72, s72, 16384\n"
"v_mfma_f32_16x16x32_bf16 v[220:223], a[120:123], a[72:75], v[220:223]\n"
"ds_read_b64_tr_b16 a[50:51], v6 offset:1024\n"
"s_add_u32 m0, s77, 49152\n"
"v_mfma_f32_16x16x32_bf16 v[216:219], a[112:115], a[72:75], v[216:219]\n"
"ds_read_b64_tr_b16 a[56:57], v7 offset:0\n"
"ds_read_b64_tr_b16 a[58:59], v7 offset:1024\n"
"buffer_load_dwordx4 v25, s[52:55], s74 offen offset:0 lds\n"
"v_mfma_f32_16x16x32_bf16 v[212:215], a[104:107], a[72:75], v[212:215]\n"
"ds_read_b128 a[4:7], v4 offset:0\n"
"v_mfma_f32_16x16x32_bf16 v[208:211], a[96:99], a[72:75], v[208:211]\n"
"ds_read_b64_tr_b16 a[36:37], v24 offset:8192\n"
"ds_read_b64_tr_b16 a[38:39], v24 offset:9216\n"
"v_mfma_f32_16x16x32_bf16 v[192:195], a[100:103], a[68:71], v[192:195]\n"
"ds_read_b64_tr_b16 a[44:45], v5 offset:8192\n"
"buffer_load_dwordx4 v26, s[52:55], s74 offen offset:1024 lds\n"
"v_mfma_f32_16x16x32_bf16 v[196:199], a[108:111], a[68:71], v[196:199]\n"
"ds_read_b64_tr_b16 a[46:47], v5 offset:9216\n"
"ds_read_b128 a[12:15], v4 offset:2048\n"
"v_mfma_f32_16x16x32_bf16 v[200:203], a[116:119], a[68:71], v[200:203]\n"
"ds_read_b64_tr_b16 a[52:53], v6 offset:8192\n"
"buffer_load_dwordx4 v27, s[52:55], s74 offen offset:2048 lds\n"
"v_mfma_f32_16x16x32_bf16 v[204:207], a[124:127], a[68:71], v[204:207]\n"
"ds_read_b64_tr_b16 a[54:55], v6 offset:9216\n"
"ds_read_b64_tr_b16 a[60:61], v7 offset:8192\n"
"v_mfma_f32_16x16x32_bf16 v[220:223], a[124:127], a[76:79], v[220:223]\n"
"ds_read_b64_tr_b16 a[62:63], v7 offset:9216\n"
"buffer_load_dwordx4 v28, s[52:55], s74 offen offset:3072 lds\n"
"s_add_u32 s74, s74, 131072\n"
"v_mfma_f32_16x16x32_bf16 v[216:219], a[116:119], a[76:79], v[216:219]\n"
"v_mfma_f32_16x16x32_bf16 v[212:215], a[108:111], a[76:79], v[212:215]\n"
"v_mfma_f32_16x16x32_bf16 v[208:211], a[100:103], a[76:79], v[208:211]\n"
"s_waitcnt vmcnt(13) lgkmcnt(0)\n"
"s_barrier\n"
"v_mfma_f32_16x16x32_bf16 v[192:195], a[32:35], a[0:3], v[192:195]\n"
"ds_read_b128 a[64:67], v23 offset:16384\n"
"ds_read_b64_tr_b16 a[96:97], v24 offset:16384\n"
"s_add_u32 m0, s76, 0\n"
"v_lshrrev_b32 v246, 8, v0\n"
"v_mfma_f32_16x16x32_bf16 v[196:199], a[40:43], a[0:3], v[196:199]\n"
"ds_read_b64_tr_b16 a[98:99], v24 offset:17408\n"
"ds_read_b64_tr_b16 a[104:105], v5 offset:16384\n"
"buffer_load_dwordx4 v0, s[20:23], s72 offen offset:0 sc1 lds\n"
"v_bfe_u32 v247, v0, 6, 2\n"
"v_mfma_f32_16x16x32_bf16 v[200:203], a[48:51], a[0:3], v[200:203]\n"
"ds_read_b64_tr_b16 a[106:107], v5 offset:17408\n"
"v_cmp_eq_u32 vcc, v246, v247\n"
"v_mfma_f32_16x16x32_bf16 v[204:207], a[56:59], a[0:3], v[204:207]\n"
"ds_read_b128 a[72:75], v23 offset:18432\n"
"ds_read_b64_tr_b16 a[112:113], v6 offset:16384\n"
"buffer_load_dwordx4 v0, s[20:23], s72 offen offset:1024 sc1 lds\n"
"s_add_u32 s72, s72, 16384\n"
"v_and_b32 v246, 16, v0\n"
"v_mfma_f32_16x16x32_bf16 v[220:223], a[56:59], a[8:11], v[220:223]\n"
"ds_read_b64_tr_b16 a[114:115], v6 offset:17408\n"
"s_add_u32 m0, s77, 0\n"
"v_mov_b32 v247, 0x3f80\n"
"v_mfma_f32_16x16x32_bf16 v[216:219], a[48:51], a[8:11], v[216:219]\n"
"ds_read_b64_tr_b16 a[120:121], v7 offset:16384\n"
"ds_read_b64_tr_b16 a[122:123], v7 offset:17408\n"
"buffer_load_dwordx4 v25, s[52:55], s74 offen offset:0 lds\n"
"v_lshlrev_b32 v246, v246, v247\n"
"v_mfma_f32_16x16x32_bf16 v[212:215], a[40:43], a[8:11], v[212:215]\n"
"ds_read_b128 a[68:71], v4 offset:16384\n"
"v_cndmask_b32 v246, 0, v246, vcc\n"
"v_mfma_f32_16x16x32_bf16 v[208:211], a[32:35], a[8:11], v[208:211]\n"
"ds_read_b64_tr_b16 a[100:101], v24 offset:24576\n"
"ds_read_b64_tr_b16 a[102:103], v24 offset:25600\n"
"v_and_b32 v247, 32, v0\n"
"v_mfma_f32_16x16x32_bf16 v[192:195], a[36:39], a[4:7], v[192:195]\n"
"ds_read_b64_tr_b16 a[108:109], v5 offset:24576\n"
"buffer_load_dwordx4 v26, s[52:55], s74 offen offset:1024 lds\n"
"v_cmp_eq_u32 vcc, 0, v247\n"
"v_mfma_f32_16x16x32_bf16 v[196:199], a[44:47], a[4:7], v[196:199]\n"
"ds_read_b64_tr_b16 a[110:111], v5 offset:25600\n"
"ds_read_b128 a[76:79], v4 offset:18432\n"
"v_mov_b32 v240, 0\n"
"v_mfma_f32_16x16x32_bf16 v[200:203], a[52:55], a[4:7], v[200:203]\n"
"ds_read_b64_tr_b16 a[116:117], v6 offset:24576\n"
"buffer_load_dwordx4 v27, s[52:55], s74 offen offset:2048 lds\n"
"v_cndmask_b32 v242, 0, v246, vcc\n"
"v_mfma_f32_16x16x32_bf16 v[204:207], a[60:63], a[4:7], v[204:207]\n"
"ds_read_b64_tr_b16 a[118:119], v6 offset:25600\n"
"ds_read_b64_tr_b16 a[124:125], v7 offset:24576\n"
"v_mov_b32 v241, 0\n"
"v_mfma_f32_16x16x32_bf16 v[220:223], a[60:63], a[12:15], v[220:223]\n"
"ds_read_b64_tr_b16 a[126:127], v7 offset:25600\n"
"buffer_load_dwordx4 v28, s[52:55], s74 offen offset:3072 lds\n"
"s_add_u32 s74, s74, 131072\n"
"v_xor_b32 v243, v246, v242\n"
"v_mfma_f32_16x16x32_bf16 v[216:219], a[52:55], a[12:15], v[216:219]\n"
"v_mov_b32 v244, 0\n"
"v_mfma_f32_16x16x32_bf16 v[212:215], a[44:47], a[12:15], v[212:215]\n"
"v_mov_b32 v245, 0\n"
"v_mfma_f32_16x16x32_bf16 v[208:211], a[36:39], a[12:15], v[208:211]\n"
"s_waitcnt vmcnt(13) lgkmcnt(0)\n"
"s_barrier\n"
"v_mfma_f32_16x16x32_bf16 v[192:195], a[96:99], a[64:67], v[192:195]\n"
"ds_read_b128 a[0:3], v23 offset:32768\n"
"ds_read_b64_tr_b16 a[32:33], v24 offset:32768\n"
"s_add_u32 m0, s76, 16384\n"
"v_mov_b32 v8, 0\n"
"buffer_load_dwordx2 v[250:251], v8, s[20:23], s88 offen sc1\n"
"v_mfma_f32_16x16x32_bf16 v[196:199], a[104:107], a[64:67], v[196:199]\n"
"ds_read_b64_tr_b16 a[34:35], v24 offset:33792\n"
"ds_read_b64_tr_b16 a[40:41], v5 offset:32768\n"
"buffer_load_dwordx4 v0, s[20:23], s72 offen offset:0 sc1 lds\n"
"v_accvgpr_read_b32 v32, a128\n"
"v_mfma_f32_16x16x32_bf16 v[200:203], a[112:115], a[64:67], v[200:203]\n"
"ds_read_b64_tr_b16 a[42:43], v5 offset:33792\n"
"v_accvgpr_read_b32 v33, a129\n"
"v_mfma_f32_16x16x32_bf16 v[204:207], a[120:123], a[64:67], v[204:207]\n"
"ds_read_b128 a[8:11], v23 offset:34816\n"
"ds_read_b64_tr_b16 a[48:49], v6 offset:32768\n"
"buffer_load_dwordx4 v0, s[20:23], s72 offen offset:1024 sc1 lds\n"
"s_add_u32 s72, s72, 16384\n"
"v_accvgpr_read_b32 v34, a130\n"
"v_mfma_f32_16x16x32_bf16 v[220:223], a[120:123], a[72:75], v[220:223]\n"
"ds_read_b64_tr_b16 a[50:51], v6 offset:33792\n"
"s_add_u32 m0, s77, 16384\n"
"v_accvgpr_read_b32 v35, a131\n"
"v_mfma_f32_16x16x32_bf16 v[216:219], a[112:115], a[72:75], v[216:219]\n"
"ds_read_b64_tr_b16 a[56:57], v7 offset:32768\n"
"ds_read_b64_tr_b16 a[58:59], v7 offset:33792\n"
"buffer_load_dwordx4 v25, s[52:55], s74 offen offset:0 lds\n"
"v_accvgpr_read_b32 v36, a132\n"
"v_mfma_f32_16x16x32_bf16 v[212:215], a[104:107], a[72:75], v[212:215]\n"
"ds_read_b128 a[4:7], v4 offset:32768\n"
"v_accvgpr_read_b32 v37, a133\n"
"v_mfma_f32_16x16x32_bf16 v[208:211], a[96:99], a[72:75], v[208:211]\n"
"ds_read_b64_tr_b16 a[36:37], v24 offset:40960\n"
"ds_read_b64_tr_b16 a[38:39], v24 offset:41984\n"
"v_accvgpr_read_b32 v38, a134\n"
"v_mfma_f32_16x16x32_bf16 v[192:195], a[100:103], a[68:71], v[192:195]\n"
"ds_read_b64_tr_b16 a[44:45], v5 offset:40960\n"
"buffer_load_dwordx4 v26, s[52:55], s74 offen offset:1024 lds\n"
"v_accvgpr_read_b32 v39, a135\n"
"v_accvgpr_read_b32 v40, a136\n"
"v_mfma_f32_16x16x32_bf16 v[196:199], a[108:111], a[68:71], v[196:199]\n"
"ds_read_b64_tr_b16 a[46:47], v5 offset:41984\n"
"ds_read_b128 a[12:15], v4 offset:34816\n"
"v_accvgpr_read_b32 v41, a137\n"
"v_mfma_f32_16x16x32_bf16 v[200:203], a[116:119], a[68:71], v[200:203]\n"
"ds_read_b64_tr_b16 a[52:53], v6 offset:40960\n"
"buffer_load_dwordx4 v27, s[52:55], s74 offen offset:2048 lds\n"
"v_accvgpr_read_b32 v42, a138\n"
"v_mfma_f32_16x16x32_bf16 v[204:207], a[124:127], a[68:71], v[204:207]\n"
"ds_read_b64_tr_b16 a[54:55], v6 offset:41984\n"
"ds_read_b64_tr_b16 a[60:61], v7 offset:40960\n"
"v_accvgpr_read_b32 v43, a139\n"
"v_mfma_f32_16x16x32_bf16 v[220:223], a[124:127], a[76:79], v[220:223]\n"
"ds_read_b64_tr_b16 a[62:63], v7 offset:41984\n"
"buffer_load_dwordx4 v28, s[52:55], s74 offen offset:3072 lds\n"
"s_add_u32 s74, s74, 131072\n"
"v_accvgpr_read_b32 v44, a140\n"
"v_mfma_f32_16x16x32_bf16 v[216:219], a[116:119], a[76:79], v[216:219]\n"
"v_accvgpr_read_b32 v45, a141\n"
"v_mfma_f32_16x16x32_bf16 v[212:215], a[108:111], a[76:79], v[212:215]\n"
"v_accvgpr_read_b32 v46, a142\n"
"v_mfma_f32_16x16x32_bf16 v[208:211], a[100:103], a[76:79], v[208:211]\n"
"v_accvgpr_read_b32 v47, a143\n"
"s_waitcnt vmcnt(13) lgkmcnt(0)\n"
"s_barrier\n"
"v_mfma_f32_16x16x32_bf16 v[192:195], a[32:35], a[0:3], v[192:195]\n"
"ds_read_b128 a[64:67], v23 offset:49152\n"
"ds_read_b64_tr_b16 a[96:97], v24 offset:49152\n"
"s_add_u32 m0, s76, 32768\n"
"s_mov_b32 exec_lo, 0xffff0000\n"
"s_mov_b32 exec_hi, 0xffff0000\n"
"v_swap_b32 v32, v34\n"
"v_swap_b32 v33, v35\n"
"v_swap_b32 v36, v38\n"
"v_swap_b32 v37, v39\n"
"v_swap_b32 v40, v42\n"
"v_swap_b32 v41, v43\n"
"v_swap_b32 v44, v46\n"
"v_swap_b32 v45, v47\n"
"s_mov_b64 exec, -1\n"
"v_mfma_f32_16x16x32_bf16 v[196:199], a[40:43], a[0:3], v[196:199]\n"
"ds_read_b64_tr_b16 a[98:99], v24 offset:50176\n"
"ds_read_b64_tr_b16 a[104:105], v5 offset:49152\n"
"buffer_load_dwordx4 v0, s[20:23], s72 offen offset:0 sc1 lds\n"
"v_lshlrev_b32 v64, 16, v32\n"
"v_mfma_f32_16x16x32_bf16 v[200:203], a[48:51], a[0:3], v[200:203]\n"
"ds_read_b64_tr_b16 a[106:107], v5 offset:50176\n"
"v_mfma_f32_16x16x32_bf16 v[204:207], a[56:59], a[0:3], v[204:207]\n"
"ds_read_b128 a[72:75], v23 offset:51200\n"
"ds_read_b64_tr_b16 a[112:113], v6 offset:49152\n"
"buffer_load_dwordx4 v0, s[20:23], s72 offen offset:1024 sc1 lds\n"
"s_add_u32 s72, s72, 16384\n"
"v_and_b32 v65, 0xffff0000, v32\n"
"v_mfma_f32_16x16x32_bf16 v[220:223], a[56:59], a[8:11], v[220:223]\n"
"ds_read_b64_tr_b16 a[114:115], v6 offset:50176\n"
"s_add_u32 m0, s77, 32768\n"
"v_mfma_f32_16x16x32_bf16 v[216:219], a[48:51], a[8:11], v[216:219]\n"
"ds_read_b64_tr_b16 a[120:121], v7 offset:49152\n"
"ds_read_b64_tr_b16 a[122:123], v7 offset:50176\n"
"buffer_load_dwordx4 v25, s[52:55], s74 offen offset:0 lds\n"
"v_lshlrev_b32 v66, 16, v33\n"
"v_mfma_f32_16x16x32_bf16 v[212:215], a[40:43], a[8:11], v[212:215]\n"
"ds_read_b128 a[68:71], v4 offset:49152\n"
"v_mfma_f32_16x16x32_bf16 v[208:211], a[32:35], a[8:11], v[208:211]\n"
"ds_read_b64_tr_b16 a[100:101], v24 offset:57344\n"
"ds_read_b64_tr_b16 a[102:103], v24 offset:58368\n"
"v_and_b32 v67, 0xffff0000, v33\n"
"v_mfma_f32_16x16x32_bf16 v[192:195], a[36:39], a[4:7], v[192:195]\n"
"ds_read_b64_tr_b16 a[108:109], v5 offset:57344\n"
"buffer_load_dwordx4 v26, s[52:55], s74 offen offset:1024 lds\n"
"v_lshlrev_b32 v68, 16, v34\n"
"v_mfma_f32_16x16x32_bf16 v[196:199], a[44:47], a[4:7], v[196:199]\n"
"ds_read_b64_tr_b16 a[110:111], v5 offset:58368\n"
"ds_read_b128 a[76:79], v4 offset:51200\n"
"v_mfma_f32_16x16x32_bf16 v[200:203], a[52:55], a[4:7], v[200:203]\n"
"ds_read_b64_tr_b16 a[116:117], v6 offset:57344\n"
"buffer_load_dwordx4 v27, s[52:55], s74 offen offset:2048 lds\n"
"v_and_b32 v69, 0xffff0000, v34\n"
"v_mfma_f32_16x16x32_bf16 v[204:207], a[60:63], a[4:7], v[204:207]\n"
"ds_read_b64_tr_b16 a[118:119], v6 offset:58368\n"
"ds_read_b64_tr_b16 a[124:125], v7 offset:57344\n"
"v_mfma_f32_16x16x32_bf16 v[220:223], a[60:63], a[12:15], v[220:223]\n"
"ds_read_b64_tr_b16 a[126:127], v7 offset:58368\n"
"buffer_load_dwordx4 v28, s[52:55], s74 offen offset:3072 lds\n"
"s_add_u32 s74, s74, 131072\n"
"v_lshlrev_b32 v70, 16, v35\n"
"v_mfma_f32_16x16x32_bf16 v[216:219], a[52:55], a[12:15], v[216:219]\n"
"v_mfma_f32_16x16x32_bf16 v[212:215], a[44:47], a[12:15], v[212:215]\n"
"v_and_b32 v71, 0xffff0000, v35\n"
"v_mfma_f32_16x16x32_bf16 v[208:211], a[36:39], a[12:15], v[208:211]\n"
"s_waitcnt vmcnt(13) lgkmcnt(0)\n"
"s_barrier\n"
"s_waitcnt vmcnt(12)\n"
"v_cmp_ne_u64 vcc, s[90:91], v[250:251]\n"
"s_cbranch_vccnz SPKV14_%=\n"
"BKKV15_%=:\n"
"v_mfma_f32_16x16x32_bf16 v[192:195], a[96:99], a[64:67], v[192:195]\n"
"ds_read_b128 a[0:3], v23 offset:0\n"
"ds_read_b64_tr_b16 a[32:33], v24 offset:0\n"
"s_add_u32 m0, s76, 49152\n"
"buffer_load_dwordx4 v[224:227], v0, s[20:23], s86 offen offset:0 sc1\n"
"v_mfma_f32_16x16x32_bf16 v[196:199], a[104:107], a[64:67], v[196:199]\n"
"ds_read_b64_tr_b16 a[34:35], v24 offset:1024\n"
"ds_read_b64_tr_b16 a[40:41], v5 offset:0\n"
"buffer_load_dwordx4 v0, s[20:23], s72 offen offset:0 sc1 lds\n"
"v_mfma_f32_16x16x32_bf16 v[200:203], a[112:115], a[64:67], v[200:203]\n"
"ds_read_b64_tr_b16 a[42:43], v5 offset:1024\n"
"v_mfma_f32_16x16x32_bf16 v[204:207], a[120:123], a[64:67], v[204:207]\n"
"ds_read_b128 a[8:11], v23 offset:2048\n"
"ds_read_b64_tr_b16 a[48:49], v6 offset:0\n"
"buffer_load_dwordx4 v0, s[20:23], s72 offen offset:1024 sc1 lds\n"
"s_add_u32 s72, s72, 16384\n"
"v_mfma_f32_16x16x32_bf16 v[220:223], a[120:123], a[72:75], v[220:223]\n"
"ds_read_b64_tr_b16 a[50:51], v6 offset:1024\n"
"s_add_u32 m0, s77, 49152\n"
"buffer_load_dwordx4 v[228:231], v0, s[20:23], s86 offen offset:1024 sc1\n"
"v_mfma_f32_16x16x32_bf16 v[216:219], a[112:115], a[72:75], v[216:219]\n"
"ds_read_b64_tr_b16 a[56:57], v7 offset:0\n"
"ds_read_b64_tr_b16 a[58:59], v7 offset:1024\n"
"buffer_load_dwordx4 v25, s[52:55], s74 offen offset:0 lds\n"
"v_mfma_f32_16x16x32_bf16 v[212:215], a[104:107], a[72:75], v[212:215]\n"
"ds_read_b128 a[4:7], v4 offset:0\n"
"v_mfma_f32_16x16x32_bf16 v[208:211], a[96:99], a[72:75], v[208:211]\n"
"ds_read_b64_tr_b16 a[36:37], v24 offset:8192\n"
"ds_read_b64_tr_b16 a[38:39], v24 offset:9216\n"
"v_mfma_f32_16x16x32_bf16 v[192:195], a[100:103], a[68:71], v[192:195]\n"
"ds_read_b64_tr_b16 a[44:45], v5 offset:8192\n"
"buffer_load_dwordx4 v26, s[52:55], s74 offen offset:1024 lds\n"
"buffer_load_dwordx4 v[232:235], v0, s[20:23], s86 offen offset:2048 sc1\n"
"v_mfma_f32_16x16x32_bf16 v[196:199], a[108:111], a[68:71], v[196:199]\n"
"ds_read_b64_tr_b16 a[46:47], v5 offset:9216\n"
"ds_read_b128 a[12:15], v4 offset:2048\n"
"v_mfma_f32_16x16x32_bf16 v[200:203], a[116:119], a[68:71], v[200:203]\n"
"ds_read_b64_tr_b16 a[52:53], v6 offset:8192\n"
"buffer_load_dwordx4 v27, s[52:55], s74 offen offset:2048 lds\n"
"v_mfma_f32_16x16x32_bf16 v[204:207], a[124:127], a[68:71], v[204:207]\n"
"ds_read_b64_tr_b16 a[54:55], v6 offset:9216\n"
"ds_read_b64_tr_b16 a[60:61], v7 offset:8192\n"
"v_mfma_f32_16x16x32_bf16 v[220:223], a[124:127], a[76:79], v[220:223]\n"
"ds_read_b64_tr_b16 a[62:63], v7 offset:9216\n"
"buffer_load_dwordx4 v28, s[52:55], s74 offen offset:3072 lds\n"
"s_add_u32 s74, s74, 131072\n"
"buffer_load_dwordx4 v[236:239], v0, s[20:23], s86 offen offset:3072 sc1\n"
"v_mfma_f32_16x16x32_bf16 v[216:219], a[116:119], a[76:79], v[216:219]\n"
"v_mfma_f32_16x16x32_bf16 v[212:215], a[108:111], a[76:79], v[212:215]\n"
"v_mfma_f32_16x16x32_bf16 v[208:211], a[100:103], a[76:79], v[208:211]\n"
"s_waitcnt vmcnt(16) lgkmcnt(0)\n"
"s_barrier\n"
"v_mfma_f32_16x16x32_bf16 v[192:195], a[32:35], a[0:3], v[192:195]\n"
"ds_read_b128 a[64:67], v23 offset:16384\n"
"ds_read_b64_tr_b16 a[96:97], v24 offset:16384\n"
"s_add_u32 m0, s76, 0\n"
"v_lshlrev_b32 v72, 16, v36\n"
"v_and_b32 v73, 0xffff0000, v36\n"
"v_mfma_f32_16x16x32_bf16 v[196:199], a[40:43], a[0:3], v[196:199]\n"
"ds_read_b64_tr_b16 a[98:99], v24 offset:17408\n"
"ds_read_b64_tr_b16 a[104:105], v5 offset:16384\n"
"buffer_load_dwordx4 v0, s[20:23], s72 offen offset:0 sc1 lds\n"
"v_lshlrev_b32 v74, 16, v37\n"
"v_mfma_f32_16x16x32_bf16 v[200:203], a[48:51], a[0:3], v[200:203]\n"
"ds_read_b64_tr_b16 a[106:107], v5 offset:17408\n"
"v_and_b32 v75, 0xffff0000, v37\n"
"v_lshlrev_b32 v76, 16, v38\n"
"v_mfma_f32_16x16x32_bf16 v[204:207], a[56:59], a[0:3], v[204:207]\n"
"ds_read_b128 a[72:75], v23 offset:18432\n"
"ds_read_b64_tr_b16 a[112:113], v6 offset:16384\n"
"buffer_load_dwordx4 v0, s[20:23], s72 offen offset:1024 sc1 lds\n"
"s_add_u32 s72, s72, 16384\n"
"v_and_b32 v77, 0xffff0000, v38\n"
"v_mfma_f32_16x16x32_bf16 v[220:223], a[56:59], a[8:11], v[220:223]\n"
"ds_read_b64_tr_b16 a[114:115], v6 offset:17408\n"
"s_add_u32 m0, s77, 0\n"
"v_lshlrev_b32 v78, 16, v39\n"
"v_and_b32 v79, 0xffff0000, v39\n"
"v_mfma_f32_16x16x32_bf16 v[216:219], a[48:51], a[8:11], v[216:219]\n"
"ds_read_b64_tr_b16 a[120:121], v7 offset:16384\n"
"ds_read_b64_tr_b16 a[122:123], v7 offset:17408\n"
"buffer_load_dwordx4 v25, s[52:55], s74 offen offset:0 lds\n"
"v_lshlrev_b32 v80, 16, v40\n"
"v_mfma_f32_16x16x32_bf16 v[212:215], a[40:43], a[8:11], v[212:215]\n"
"ds_read_b128 a[68:71], v4 offset:16384\n"
"v_and_b32 v81, 0xffff0000, v40\n"
"v_lshlrev_b32 v82, 16, v41\n"
"v_mfma_f32_16x16x32_bf16 v[208:211], a[32:35], a[8:11], v[208:211]\n"
"ds_read_b64_tr_b16 a[100:101], v24 offset:24576\n"
"ds_read_b64_tr_b16 a[102:103], v24 offset:25600\n"
"v_and_b32 v83, 0xffff0000, v41\n"
"v_mfma_f32_16x16x32_bf16 v[192:195], a[36:39], a[4:7], v[192:195]\n"
"ds_read_b64_tr_b16 a[108:109], v5 offset:24576\n"
"buffer_load_dwordx4 v26, s[52:55], s74 offen offset:1024 lds\n"
"v_lshlrev_b32 v84, 16, v42\n"
"v_and_b32 v85, 0xffff0000, v42\n"
"v_mfma_f32_16x16x32_bf16 v[196:199], a[44:47], a[4:7], v[196:199]\n"
"ds_read_b64_tr_b16 a[110:111], v5 offset:25600\n"
"ds_read_b128 a[76:79], v4 offset:18432\n"
"v_lshlrev_b32 v86, 16, v43\n"
"v_mfma_f32_16x16x32_bf16 v[200:203], a[52:55], a[4:7], v[200:203]\n"
"ds_read_b64_tr_b16 a[116:117], v6 offset:24576\n"
"buffer_load_dwordx4 v27, s[52:55], s74 offen offset:2048 lds\n"
"v_and_b32 v87, 0xffff0000, v43\n"
"v_lshlrev_b32 v88, 16, v44\n"
"v_mfma_f32_16x16x32_bf16 v[204:207], a[60:63], a[4:7], v[204:207]\n"
"ds_read_b64_tr_b16 a[118:119], v6 offset:25600\n"
"ds_read_b64_tr_b16 a[124:125], v7 offset:24576\n"
"v_and_b32 v89, 0xffff0000, v44\n"
"v_mfma_f32_16x16x32_bf16 v[220:223], a[60:63], a[12:15], v[220:223]\n"
"ds_read_b64_tr_b16 a[126:127], v7 offset:25600\n"
"buffer_load_dwordx4 v28, s[52:55], s74 offen offset:3072 lds\n"
"s_add_u32 s74, s74, 131072\n"
"v_lshlrev_b32 v90, 16, v45\n"
"v_and_b32 v91, 0xffff0000, v45\n"
"v_mfma_f32_16x16x32_bf16 v[216:219], a[52:55], a[12:15], v[216:219]\n"
"v_lshlrev_b32 v92, 16, v46\n"
"v_mfma_f32_16x16x32_bf16 v[212:215], a[44:47], a[12:15], v[212:215]\n"
"v_and_b32 v93, 0xffff0000, v46\n"
"v_lshlrev_b32 v94, 16, v47\n"
"v_mfma_f32_16x16x32_bf16 v[208:211], a[36:39], a[12:15], v[208:211]\n"
"v_and_b32 v95, 0xffff0000, v47\n"
"s_waitcnt vmcnt(16) lgkmcnt(0)\n"
"s_barrier\n"
"v_mfma_f32_16x16x32_bf16 v[192:195], a[96:99], a[64:67], v[192:195]\n"
"ds_read_b128 a[0:3], v23 offset:32768\n"
"ds_read_b64_tr_b16 a[32:33], v24 offset:32768\n"
"s_add_u32 m0, s76, 16384\n"
"v_mfma_f32_16x16x32_bf16 v[196:199], a[104:107], a[64:67], v[196:199]\n"
"ds_read_b64_tr_b16 a[34:35], v24 offset:33792\n"
"ds_read_b64_tr_b16 a[40:41], v5 offset:32768\n"
"buffer_load_dwordx4 v0, s[20:23], s72 offen offset:0 sc1 lds\n"
"v_mfma_f32_16x16x32_bf16 v[200:203], a[112:115], a[64:67], v[200:203]\n"
"ds_read_b64_tr_b16 a[42:43], v5 offset:33792\n"
"v_mfma_f32_16x16x32_bf16 v[204:207], a[120:123], a[64:67], v[204:207]\n"
"ds_read_b128 a[8:11], v23 offset:34816\n"
"ds_read_b64_tr_b16 a[48:49], v6 offset:32768\n"
"buffer_load_dwordx4 v0, s[20:23], s72 offen offset:1024 sc1 lds\n"
"s_add_u32 s72, s72, 16384\n"
"v_mfma_f32_16x16x32_bf16 v[220:223], a[120:123], a[72:75], v[220:223]\n"
"ds_read_b64_tr_b16 a[50:51], v6 offset:33792\n"
"s_add_u32 m0, s77, 16384\n"
"v_mfma_f32_16x16x32_bf16 v[216:219], a[112:115], a[72:75], v[216:219]\n"
"ds_read_b64_tr_b16 a[56:57], v7 offset:32768\n"
"ds_read_b64_tr_b16 a[58:59], v7 offset:33792\n"
"buffer_load_dwordx4 v25, s[52:55], s74 offen offset:0 lds\n"
"v_mfma_f32_16x16x32_bf16 v[212:215], a[104:107], a[72:75], v[212:215]\n"
"ds_read_b128 a[4:7], v4 offset:32768\n"
"v_mfma_f32_16x16x32_bf16 v[208:211], a[96:99], a[72:75], v[208:211]\n"
"ds_read_b64_tr_b16 a[36:37], v24 offset:40960\n"
"ds_read_b64_tr_b16 a[38:39], v24 offset:41984\n"
"v_mfma_f32_16x16x32_bf16 v[192:195], a[100:103], a[68:71], v[192:195]\n"
"ds_read_b64_tr_b16 a[44:45], v5 offset:40960\n"
"buffer_load_dwordx4 v26, s[52:55], s74 offen offset:1024 lds\n"
"v_mfma_f32_16x16x32_bf16 v[196:199], a[108:111], a[68:71], v[196:199]\n"
"ds_read_b64_tr_b16 a[46:47], v5 offset:41984\n"
"ds_read_b128 a[12:15], v4 offset:34816\n"
"v_mfma_f32_16x16x32_bf16 v[200:203], a[116:119], a[68:71], v[200:203]\n"
"ds_read_b64_tr_b16 a[52:53], v6 offset:40960\n"
"buffer_load_dwordx4 v27, s[52:55], s74 offen offset:2048 lds\n"
"v_mfma_f32_16x16x32_bf16 v[204:207], a[124:127], a[68:71], v[204:207]\n"
"ds_read_b64_tr_b16 a[54:55], v6 offset:41984\n"
"ds_read_b64_tr_b16 a[60:61], v7 offset:40960\n"
"v_mfma_f32_16x16x32_bf16 v[220:223], a[124:127], a[76:79], v[220:223]\n"
"ds_read_b64_tr_b16 a[62:63], v7 offset:41984\n"
"buffer_load_dwordx4 v28, s[52:55], s74 offen offset:3072 lds\n"
"s_add_u32 s74, s74, 131072\n"
"v_mfma_f32_16x16x32_bf16 v[216:219], a[116:119], a[76:79], v[216:219]\n"
"v_mfma_f32_16x16x32_bf16 v[212:215], a[108:111], a[76:79], v[212:215]\n"
"v_mfma_f32_16x16x32_bf16 v[208:211], a[100:103], a[76:79], v[208:211]\n"
"s_waitcnt vmcnt(13) lgkmcnt(0)\n"
"s_barrier\n"
"s_waitcnt vmcnt(12)\n"
"v_mfma_f32_16x16x32_bf16 v[192:195], a[32:35], a[0:3], v[192:195]\n"
"ds_read_b128 a[64:67], v23 offset:49152\n"
"ds_read_b64_tr_b16 a[96:97], v24 offset:49152\n"
"s_add_u32 m0, s76, 32768\n"
"v_mfma_f32_16x16x32_bf16 v[128:131], v[242:245], v[224:227], v[128:131]\n"
"v_mfma_f32_16x16x32_bf16 v[132:135], v[240:243], v[224:227], v[132:135]\n"
"v_mfma_f32_16x16x32_bf16 v[196:199], a[40:43], a[0:3], v[196:199]\n"
"ds_read_b64_tr_b16 a[98:99], v24 offset:50176\n"
"ds_read_b64_tr_b16 a[104:105], v5 offset:49152\n"
"buffer_load_dwordx4 v0, s[20:23], s72 offen offset:0 sc1 lds\n"
"v_mfma_f32_16x16x32_bf16 v[136:139], v[242:245], v[228:231], v[136:139]\n"
"v_mfma_f32_16x16x32_bf16 v[200:203], a[48:51], a[0:3], v[200:203]\n"
"ds_read_b64_tr_b16 a[106:107], v5 offset:50176\n"
"v_mfma_f32_16x16x32_bf16 v[140:143], v[240:243], v[228:231], v[140:143]\n"
"v_mfma_f32_16x16x32_bf16 v[144:147], v[242:245], v[232:235], v[144:147]\n"
"v_mfma_f32_16x16x32_bf16 v[204:207], a[56:59], a[0:3], v[204:207]\n"
"ds_read_b128 a[72:75], v23 offset:51200\n"
"ds_read_b64_tr_b16 a[112:113], v6 offset:49152\n"
"buffer_load_dwordx4 v0, s[20:23], s72 offen offset:1024 sc1 lds\n"
"s_add_u32 s72, s72, 16384\n"
"v_mfma_f32_16x16x32_bf16 v[148:151], v[240:243], v[232:235], v[148:151]\n"
"v_mfma_f32_16x16x32_bf16 v[220:223], a[56:59], a[8:11], v[220:223]\n"
"ds_read_b64_tr_b16 a[114:115], v6 offset:50176\n"
"s_add_u32 m0, s77, 32768\n"
"v_mfma_f32_16x16x32_bf16 v[152:155], v[242:245], v[236:239], v[152:155]\n"
"v_mfma_f32_16x16x32_bf16 v[156:159], v[240:243], v[236:239], v[156:159]\n"
"v_mfma_f32_16x16x32_bf16 v[216:219], a[48:51], a[8:11], v[216:219]\n"
"ds_read_b64_tr_b16 a[120:121], v7 offset:49152\n"
"ds_read_b64_tr_b16 a[122:123], v7 offset:50176\n"
"buffer_load_dwordx4 v25, s[52:55], s74 offen offset:0 lds\n"
"v_pk_mul_f32 v[96:97], v[128:129], s[92:93] neg_lo:[0,1] neg_hi:[0,1]\n"
"v_mfma_f32_16x16x32_bf16 v[212:215], a[40:43], a[8:11], v[212:215]\n"
"ds_read_b128 a[68:71], v4 offset:49152\n"
"v_pk_mul_f32 v[98:99], v[130:131], s[92:93] neg_lo:[0,1] neg_hi:[0,1]\n"
"v_pk_mul_f32 v[100:101], v[132:133], s[92:93] neg_lo:[0,1] neg_hi:[0,1]\n"
"v_mfma_f32_16x16x32_bf16 v[208:211], a[32:35], a[8:11], v[208:211]\n"
"ds_read_b64_tr_b16 a[100:101], v24 offset:57344\n"
"ds_read_b64_tr_b16 a[102:103], v24 offset:58368\n"
"v_pk_mul_f32 v[102:103], v[134:135], s[92:93] neg_lo:[0,1] neg_hi:[0,1]\n"
"v_mfma_f32_16x16x32_bf16 v[192:195], a[36:39], a[4:7], v[192:195]\n"
"ds_read_b64_tr_b16 a[108:109], v5 offset:57344\n"
"buffer_load_dwordx4 v26, s[52:55], s74 offen offset:1024 lds\n"
"v_exp_f32 v96, v96\n"
"v_exp_f32 v97, v97\n"
"v_mfma_f32_16x16x32_bf16 v[196:199], a[44:47], a[4:7], v[196:199]\n"
"ds_read_b64_tr_b16 a[110:111], v5 offset:58368\n"
"ds_read_b128 a[76:79], v4 offset:51200\n"
"v_exp_f32 v98, v98\n"
"v_mfma_f32_16x16x32_bf16 v[200:203], a[52:55], a[4:7], v[200:203]\n"
"ds_read_b64_tr_b16 a[116:117], v6 offset:57344\n"
"buffer_load_dwordx4 v27, s[52:55], s74 offen offset:2048 lds\n"
"v_exp_f32 v99, v99\n"
"v_exp_f32 v100, v100\n"
"v_mfma_f32_16x16x32_bf16 v[204:207], a[60:63], a[4:7], v[204:207]\n"
"ds_read_b64_tr_b16 a[118:119], v6 offset:58368\n"
"ds_read_b64_tr_b16 a[124:125], v7 offset:57344\n"
"v_exp_f32 v101, v101\n"
"v_mfma_f32_16x16x32_bf16 v[220:223], a[60:63], a[12:15], v[220:223]\n"
"ds_read_b64_tr_b16 a[126:127], v7 offset:58368\n"
"buffer_load_dwordx4 v28, s[52:55], s74 offen offset:3072 lds\n"
"s_add_u32 s74, s74, 131072\n"
"v_exp_f32 v102, v102\n"
"v_exp_f32 v103, v103\n"
"v_mfma_f32_16x16x32_bf16 v[216:219], a[52:55], a[12:15], v[216:219]\n"
"v_pk_add_f32 v[96:97], v[96:97], 1.0 op_sel_hi:[1,0]\n"
"s_nop 1\n"
"v_mfma_f32_16x16x32_bf16 v[212:215], a[44:47], a[12:15], v[212:215]\n"
"v_pk_add_f32 v[98:99], v[98:99], 1.0 op_sel_hi:[1,0]\n"
"v_pk_add_f32 v[100:101], v[100:101], 1.0 op_sel_hi:[1,0]\n"
"s_nop 1\n"
"v_mfma_f32_16x16x32_bf16 v[208:211], a[36:39], a[12:15], v[208:211]\n"
"v_pk_add_f32 v[102:103], v[102:103], 1.0 op_sel_hi:[1,0]\n"
"s_waitcnt vmcnt(12) lgkmcnt(0)\n"
"s_barrier\n"
"v_mfma_f32_16x16x32_bf16 v[192:195], a[96:99], a[64:67], v[192:195]\n"
"ds_read_b128 a[0:3], v23 offset:0\n"
"ds_read_b64_tr_b16 a[32:33], v24 offset:0\n"
"s_add_u32 m0, s76, 49152\n"
"v_rcp_f32 v96, v96\n"
"v_rcp_f32 v97, v97\n"
"v_mfma_f32_16x16x32_bf16 v[196:199], a[104:107], a[64:67], v[196:199]\n"
"ds_read_b64_tr_b16 a[34:35], v24 offset:1024\n"
"ds_read_b64_tr_b16 a[40:41], v5 offset:0\n"
"buffer_load_dwordx4 v0, s[20:23], s72 offen offset:0 sc1 lds\n"
"v_rcp_f32 v98, v98\n"
"v_mfma_f32_16x16x32_bf16 v[200:203], a[112:115], a[64:67], v[200:203]\n"
"ds_read_b64_tr_b16 a[42:43], v5 offset:1024\n"
"v_rcp_f32 v99, v99\n"
"v_rcp_f32 v100, v100\n"
"v_mfma_f32_16x16x32_bf16 v[204:207], a[120:123], a[64:67], v[204:207]\n"
"ds_read_b128 a[8:11], v23 offset:2048\n"
"ds_read_b64_tr_b16 a[48:49], v6 offset:0\n"
"buffer_load_dwordx4 v0, s[20:23], s72 offen offset:1024 sc1 lds\n"
"s_add_u32 s72, s72, 16384\n"
"v_rcp_f32 v101, v101\n"
"v_mfma_f32_16x16x32_bf16 v[220:223], a[120:123], a[72:75], v[220:223]\n"
"ds_read_b64_tr_b16 a[50:51], v6 offset:1024\n"
"s_add_u32 m0, s77, 49152\n"
"v_rcp_f32 v102, v102\n"
"v_rcp_f32 v103, v103\n"
"v_mfma_f32_16x16x32_bf16 v[216:219], a[112:115], a[72:75], v[216:219]\n"
"ds_read_b64_tr_b16 a[56:57], v7 offset:0\n"
"ds_read_b64_tr_b16 a[58:59], v7 offset:1024\n"
"buffer_load_dwordx4 v25, s[52:55], s74 offen offset:0 lds\n"
"v_pk_mul_f32 v[128:129], v[128:129], v[96:97]\n"
"v_mfma_f32_16x16x32_bf16 v[212:215], a[104:107], a[72:75], v[212:215]\n"
"ds_read_b128 a[4:7], v4 offset:0\n"
"v_pk_mul_f32 v[130:131], v[130:131], v[98:99]\n"
"v_pk_mul_f32 v[132:133], v[132:133], v[100:101]\n"
"v_mfma_f32_16x16x32_bf16 v[208:211], a[96:99], a[72:75], v[208:211]\n"
"ds_read_b64_tr_b16 a[36:37], v24 offset:8192\n"
"ds_read_b64_tr_b16 a[38:39], v24 offset:9216\n"
"v_pk_mul_f32 v[134:135], v[134:135], v[102:103]\n"
"v_mfma_f32_16x16x32_bf16 v[192:195], a[100:103], a[68:71], v[192:195]\n"
"ds_read_b64_tr_b16 a[44:45], v5 offset:8192\n"
"buffer_load_dwordx4 v26, s[52:55], s74 offen offset:1024 lds\n"
"v_pk_mul_f32 v[104:105], v[136:137], s[92:93] neg_lo:[0,1] neg_hi:[0,1]\n"
"v_pk_mul_f32 v[106:107], v[138:139], s[92:93] neg_lo:[0,1] neg_hi:[0,1]\n"
"v_mfma_f32_16x16x32_bf16 v[196:199], a[108:111], a[68:71], v[196:199]\n"
"ds_read_b64_tr_b16 a[46:47], v5 offset:9216\n"
"ds_read_b128 a[12:15], v4 offset:2048\n"
"v_pk_mul_f32 v[108:109], v[140:141], s[92:93] neg_lo:[0,1] neg_hi:[0,1]\n"
"v_mfma_f32_16x16x32_bf16 v[200:203], a[116:119], a[68:71], v[200:203]\n"
"ds_read_b64_tr_b16 a[52:53], v6 offset:8192\n"
"buffer_load_dwordx4 v27, s[52:55], s74 offen offset:2048 lds\n"
"v_pk_mul_f32 v[110:111], v[142:143], s[92:93] neg_lo:[0,1] neg_hi:[0,1]\n"
"v_exp_f32 v104, v104\n"
"v_mfma_f32_16x16x32_bf16 v[204:207], a[124:127], a[68:71], v[204:207]\n"
"ds_read_b64_tr_b16 a[54:55], v6 offset:9216\n"
"ds_read_b64_tr_b16 a[60:61], v7 offset:8192\n"
"v_exp_f32 v105, v105\n"
"v_mfma_f32_16x16x32_bf16 v[220:223], a[124:127], a[76:79], v[220:223]\n"
"ds_read_b64_tr_b16 a[62:63], v7 offset:9216\n"
"buffer_load_dwordx4 v28, s[52:55], s74 offen offset:3072 lds\n"
"s_add_u32 s74, s74, 131072\n"
"v_exp_f32 v106, v106\n"
"v_exp_f32 v107, v107\n"
"v_mfma_f32_16x16x32_bf16 v[216:219], a[116:119], a[76:79], v[216:219]\n"
"v_exp_f32 v108, v108\n"
"v_mfma_f32_16x16x32_bf16 v[212:215], a[108:111], a[76:79], v[212:215]\n"
"v_exp_f32 v109, v109\n"
"v_exp_f32 v110, v110\n"
"v_mfma_f32_16x16x32_bf16 v[208:211], a[100:103], a[76:79], v[208:211]\n"
"v_exp_f32 v111, v111\n"
"s_waitcnt vmcnt(12) lgkmcnt(0)\n"
"s_barrier\n"
"v_mfma_f32_16x16x32_bf16 v[192:195], a[32:35], a[0:3], v[192:195]\n"
"ds_read_b128 a[64:67], v23 offset:16384\n"
"ds_read_b64_tr_b16 a[96:97], v24 offset:16384\n"
"v_pk_add_f32 v[104:105], v[104:105], 1.0 op_sel_hi:[1,0]\n"
"v_pk_add_f32 v[106:107], v[106:107], 1.0 op_sel_hi:[1,0]\n"
"s_nop 1\n"
"v_mfma_f32_16x16x32_bf16 v[196:199], a[40:43], a[0:3], v[196:199]\n"
"ds_read_b64_tr_b16 a[98:99], v24 offset:17408\n"
"ds_read_b64_tr_b16 a[104:105], v5 offset:16384\n"
"v_pk_add_f32 v[108:109], v[108:109], 1.0 op_sel_hi:[1,0]\n"
"s_nop 1\n"
"v_mfma_f32_16x16x32_bf16 v[200:203], a[48:51], a[0:3], v[200:203]\n"
"ds_read_b64_tr_b16 a[106:107], v5 offset:17408\n"
"v_pk_add_f32 v[110:111], v[110:111], 1.0 op_sel_hi:[1,0]\n"
"v_rcp_f32 v104, v104\n"
"s_nop 0\n"
"v_mfma_f32_16x16x32_bf16 v[204:207], a[56:59], a[0:3], v[204:207]\n"
"ds_read_b128 a[72:75], v23 offset:18432\n"
"ds_read_b64_tr_b16 a[112:113], v6 offset:16384\n"
"v_rcp_f32 v105, v105\n"
"v_mfma_f32_16x16x32_bf16 v[220:223], a[56:59], a[8:11], v[220:223]\n"
"ds_read_b64_tr_b16 a[114:115], v6 offset:17408\n"
"v_rcp_f32 v106, v106\n"
"v_rcp_f32 v107, v107\n"
"v_mfma_f32_16x16x32_bf16 v[216:219], a[48:51], a[8:11], v[216:219]\n"
"ds_read_b64_tr_b16 a[120:121], v7 offset:16384\n"
"ds_read_b64_tr_b16 a[122:123], v7 offset:17408\n"
"v_rcp_f32 v108, v108\n"
"v_mfma_f32_16x16x32_bf16 v[212:215], a[40:43], a[8:11], v[212:215]\n"
"ds_read_b128 a[68:71], v4 offset:16384\n"
"v_rcp_f32 v109, v109\n"
"v_rcp_f32 v110, v110\n"
"v_mfma_f32_16x16x32_bf16 v[208:211], a[32:35], a[8:11], v[208:211]\n"
"ds_read_b64_tr_b16 a[100:101], v24 offset:24576\n"
"ds_read_b64_tr_b16 a[102:103], v24 offset:25600\n"
"v_rcp_f32 v111, v111\n"
"v_mfma_f32_16x16x32_bf16 v[192:195], a[36:39], a[4:7], v[192:195]\n"
"ds_read_b64_tr_b16 a[108:109], v5 offset:24576\n"
"v_pk_mul_f32 v[136:137], v[136:137], v[104:105]\n"
"v_pk_mul_f32 v[138:139], v[138:139], v[106:107]\n"
"v_mfma_f32_16x16x32_bf16 v[196:199], a[44:47], a[4:7], v[196:199]\n"
"ds_read_b64_tr_b16 a[110:111], v5 offset:25600\n"
"ds_read_b128 a[76:79], v4 offset:18432\n"
"v_pk_mul_f32 v[140:141], v[140:141], v[108:109]\n"
"v_mfma_f32_16x16x32_bf16 v[200:203], a[52:55], a[4:7], v[200:203]\n"
"ds_read_b64_tr_b16 a[116:117], v6 offset:24576\n"
"v_pk_mul_f32 v[142:143], v[142:143], v[110:111]\n"
"v_pk_mul_f32 v[112:113], v[144:145], s[92:93] neg_lo:[0,1] neg_hi:[0,1]\n"
"v_mfma_f32_16x16x32_bf16 v[204:207], a[60:63], a[4:7], v[204:207]\n"
"ds_read_b64_tr_b16 a[118:119], v6 offset:25600\n"
"ds_read_b64_tr_b16 a[124:125], v7 offset:24576\n"
"v_pk_mul_f32 v[114:115], v[146:147], s[92:93] neg_lo:[0,1] neg_hi:[0,1]\n"
"v_mfma_f32_16x16x32_bf16 v[220:223], a[60:63], a[12:15], v[220:223]\n"
"ds_read_b64_tr_b16 a[126:127], v7 offset:25600\n"
"v_pk_mul_f32 v[116:117], v[148:149], s[92:93] neg_lo:[0,1] neg_hi:[0,1]\n"
"v_pk_mul_f32 v[118:119], v[150:151], s[92:93] neg_lo:[0,1] neg_hi:[0,1]\n"
"v_mfma_f32_16x16x32_bf16 v[216:219], a[52:55], a[12:15], v[216:219]\n"
"v_exp_f32 v112, v112\n"
"v_mfma_f32_16x16x32_bf16 v[212:215], a[44:47], a[12:15], v[212:215]\n"
"v_exp_f32 v113, v113\n"
"v_exp_f32 v114, v114\n"
"v_mfma_f32_16x16x32_bf16 v[208:211], a[36:39], a[12:15], v[208:211]\n"
"v_exp_f32 v115, v115\n"
"s_waitcnt vmcnt(6) lgkmcnt(0)\n"
"s_barrier\n"
"v_mfma_f32_16x16x32_bf16 v[192:195], a[96:99], a[64:67], v[192:195]\n"
"ds_read_b128 a[0:3], v23 offset:32768\n"
"ds_read_b64_tr_b16 a[32:33], v24 offset:32768\n"
"v_exp_f32 v116, v116\n"
"v_exp_f32 v117, v117\n"
"v_mfma_f32_16x16x32_bf16 v[196:199], a[104:107], a[64:67], v[196:199]\n"
"ds_read_b64_tr_b16 a[34:35], v24 offset:33792\n"
"ds_read_b64_tr_b16 a[40:41], v5 offset:32768\n"
"v_exp_f32 v118, v118\n"
"v_mfma_f32_16x16x32_bf16 v[200:203], a[112:115], a[64:67], v[200:203]\n"
"ds_read_b64_tr_b16 a[42:43], v5 offset:33792\n"
"v_exp_f32 v119, v119\n"
"v_pk_add_f32 v[112:113], v[112:113], 1.0 op_sel_hi:[1,0]\n"
"s_nop 1\n"
"v_mfma_f32_16x16x32_bf16 v[204:207], a[120:123], a[64:67], v[204:207]\n"
"ds_read_b128 a[8:11], v23 offset:34816\n"
"ds_read_b64_tr_b16 a[48:49], v6 offset:32768\n"
"v_pk_add_f32 v[114:115], v[114:115], 1.0 op_sel_hi:[1,0]\n"
"s_nop 1\n"
"v_mfma_f32_16x16x32_bf16 v[220:223], a[120:123], a[72:75], v[220:223]\n"
"ds_read_b64_tr_b16 a[50:51], v6 offset:33792\n"
"v_pk_add_f32 v[116:117], v[116:117], 1.0 op_sel_hi:[1,0]\n"
"v_pk_add_f32 v[118:119], v[118:119], 1.0 op_sel_hi:[1,0]\n"
"s_nop 1\n"
"v_mfma_f32_16x16x32_bf16 v[216:219], a[112:115], a[72:75], v[216:219]\n"
"ds_read_b64_tr_b16 a[56:57], v7 offset:32768\n"
"ds_read_b64_tr_b16 a[58:59], v7 offset:33792\n"
"v_rcp_f32 v112, v112\n"
"v_mfma_f32_16x16x32_bf16 v[212:215], a[104:107], a[72:75], v[212:215]\n"
"ds_read_b128 a[4:7], v4 offset:32768\n"
"v_rcp_f32 v113, v113\n"
"v_rcp_f32 v114, v114\n"
"v_mfma_f32_16x16x32_bf16 v[208:211], a[96:99], a[72:75], v[208:211]\n"
"ds_read_b64_tr_b16 a[36:37], v24 offset:40960\n"
"ds_read_b64_tr_b16 a[38:39], v24 offset:41984\n"
"v_rcp_f32 v115, v115\n"
"v_mfma_f32_16x16x32_bf16 v[192:195], a[100:103], a[68:71], v[192:195]\n"
"ds_read_b64_tr_b16 a[44:45], v5 offset:40960\n"
"v_rcp_f32 v116, v116\n"
"v_rcp_f32 v117, v117\n"
"v_mfma_f32_16x16x32_bf16 v[196:199], a[108:111], a[68:71], v[196:199]\n"
"ds_read_b64_tr_b16 a[46:47], v5 offset:41984\n"
"ds_read_b128 a[12:15], v4 offset:34816\n"
"v_rcp_f32 v118, v118\n"
"v_mfma_f32_16x16x32_bf16 v[200:203], a[116:119], a[68:71], v[200:203]\n"
"ds_read_b64_tr_b16 a[52:53], v6 offset:40960\n"
"v_rcp_f32 v119, v119\n"
"v_pk_mul_f32 v[144:145], v[144:145], v[112:113]\n"
"v_mfma_f32_16x16x32_bf16 v[204:207], a[124:127], a[68:71], v[204:207]\n"
"ds_read_b64_tr_b16 a[54:55], v6 offset:41984\n"
"ds_read_b64_tr_b16 a[60:61], v7 offset:40960\n"
"v_pk_mul_f32 v[146:147], v[146:147], v[114:115]\n"
"v_mfma_f32_16x16x32_bf16 v[220:223], a[124:127], a[76:79], v[220:223]\n"
"ds_read_b64_tr_b16 a[62:63], v7 offset:41984\n"
"v_pk_mul_f32 v[148:149], v[148:149], v[116:117]\n"
"v_pk_mul_f32 v[150:151], v[150:151], v[118:119]\n"
"v_mfma_f32_16x16x32_bf16 v[216:219], a[116:119], a[76:79], v[216:219]\n"
"v_pk_mul_f32 v[120:121], v[152:153], s[92:93] neg_lo:[0,1] neg_hi:[0,1]\n"
"v_mfma_f32_16x16x32_bf16 v[212:215], a[108:111], a[76:79], v[212:215]\n"
"v_pk_mul_f32 v[122:123], v[154:155], s[92:93] neg_lo:[0,1] neg_hi:[0,1]\n"
"v_pk_mul_f32 v[124:125], v[156:157], s[92:93] neg_lo:[0,1] neg_hi:[0,1]\n"
"v_mfma_f32_16x16x32_bf16 v[208:211], a[100:103], a[76:79], v[208:211]\n"
"v_pk_mul_f32 v[126:127], v[158:159], s[92:93] neg_lo:[0,1] neg_hi:[0,1]\n"
"s_waitcnt vmcnt(0) lgkmcnt(0)\n"
"s_barrier\n"
"v_mfma_f32_16x16x32_bf16 v[192:195], a[32:35], a[0:3], v[192:195]\n"
"ds_read_b128 a[64:67], v23 offset:49152\n"
"ds_read_b64_tr_b16 a[96:97], v24 offset:49152\n"
"v_exp_f32 v120, v120\n"
"v_exp_f32 v121, v121\n"
"v_mfma_f32_16x16x32_bf16 v[196:199], a[40:43], a[0:3], v[196:199]\n"
"ds_read_b64_tr_b16 a[98:99], v24 offset:50176\n"
"ds_read_b64_tr_b16 a[104:105], v5 offset:49152\n"
"v_exp_f32 v122, v122\n"
"v_mfma_f32_16x16x32_bf16 v[200:203], a[48:51], a[0:3], v[200:203]\n"
"ds_read_b64_tr_b16 a[106:107], v5 offset:50176\n"
"v_exp_f32 v123, v123\n"
"v_exp_f32 v124, v124\n"
"v_mfma_f32_16x16x32_bf16 v[204:207], a[56:59], a[0:3], v[204:207]\n"
"ds_read_b128 a[72:75], v23 offset:51200\n"
"ds_read_b64_tr_b16 a[112:113], v6 offset:49152\n"
"v_exp_f32 v125, v125\n"
"v_mfma_f32_16x16x32_bf16 v[220:223], a[56:59], a[8:11], v[220:223]\n"
"ds_read_b64_tr_b16 a[114:115], v6 offset:50176\n"
"v_exp_f32 v126, v126\n"
"v_exp_f32 v127, v127\n"
"v_mfma_f32_16x16x32_bf16 v[216:219], a[48:51], a[8:11], v[216:219]\n"
"ds_read_b64_tr_b16 a[120:121], v7 offset:49152\n"
"ds_read_b64_tr_b16 a[122:123], v7 offset:50176\n"
"v_pk_add_f32 v[120:121], v[120:121], 1.0 op_sel_hi:[1,0]\n"
"s_nop 1\n"
"v_mfma_f32_16x16x32_bf16 v[212:215], a[40:43], a[8:11], v[212:215]\n"
"ds_read_b128 a[68:71], v4 offset:49152\n"
"v_pk_add_f32 v[122:123], v[122:123], 1.0 op_sel_hi:[1,0]\n"
"v_pk_add_f32 v[124:125], v[124:125], 1.0 op_sel_hi:[1,0]\n"
"s_nop 1\n"
"v_mfma_f32_16x16x32_bf16 v[208:211], a[32:35], a[8:11], v[208:211]\n"
"ds_read_b64_tr_b16 a[100:101], v24 offset:57344\n"
"ds_read_b64_tr_b16 a[102:103], v24 offset:58368\n"
"v_pk_add_f32 v[126:127], v[126:127], 1.0 op_sel_hi:[1,0]\n"
"s_nop 1\n"
"v_mfma_f32_16x16x32_bf16 v[192:195], a[36:39], a[4:7], v[192:195]\n"
"ds_read_b64_tr_b16 a[108:109], v5 offset:57344\n"
"v_rcp_f32 v120, v120\n"
"v_rcp_f32 v121, v121\n"
"v_mfma_f32_16x16x32_bf16 v[196:199], a[44:47], a[4:7], v[196:199]\n"
"ds_read_b64_tr_b16 a[110:111], v5 offset:58368\n"
"ds_read_b128 a[76:79], v4 offset:51200\n"
"v_rcp_f32 v122, v122\n"
"v_mfma_f32_16x16x32_bf16 v[200:203], a[52:55], a[4:7], v[200:203]\n"
"ds_read_b64_tr_b16 a[116:117], v6 offset:57344\n"
"v_rcp_f32 v123, v123\n"
"v_rcp_f32 v124, v124\n"
"v_mfma_f32_16x16x32_bf16 v[204:207], a[60:63], a[4:7], v[204:207]\n"
"ds_read_b64_tr_b16 a[118:119], v6 offset:58368\n"
"ds_read_b64_tr_b16 a[124:125], v7 offset:57344\n"
"v_rcp_f32 v125, v125\n"
"v_mfma_f32_16x16x32_bf16 v[220:223], a[60:63], a[12:15], v[220:223]\n"
"ds_read_b64_tr_b16 a[126:127], v7 offset:58368\n"
"v_rcp_f32 v126, v126\n"
"v_rcp_f32 v127, v127\n"
"v_mfma_f32_16x16x32_bf16 v[216:219], a[52:55], a[12:15], v[216:219]\n"
"v_pk_mul_f32 v[152:153], v[152:153], v[120:121]\n"
"v_mfma_f32_16x16x32_bf16 v[212:215], a[44:47], a[12:15], v[212:215]\n"
"v_pk_mul_f32 v[154:155], v[154:155], v[122:123]\n"
"v_pk_mul_f32 v[156:157], v[156:157], v[124:125]\n"
"v_mfma_f32_16x16x32_bf16 v[208:211], a[36:39], a[12:15], v[208:211]\n"
"v_pk_mul_f32 v[158:159], v[158:159], v[126:127]\n"
"s_waitcnt lgkmcnt(0)\n"
"v_mfma_f32_16x16x32_bf16 v[192:195], a[96:99], a[64:67], v[192:195]\n"
"v_mfma_f32_16x16x32_bf16 v[196:199], a[104:107], a[64:67], v[196:199]\n"
"v_mfma_f32_16x16x32_bf16 v[200:203], a[112:115], a[64:67], v[200:203]\n"
"v_mfma_f32_16x16x32_bf16 v[204:207], a[120:123], a[64:67], v[204:207]\n"
"v_mfma_f32_16x16x32_bf16 v[204:207], a[124:127], a[68:71], v[204:207]\n"
"v_mfma_f32_16x16x32_bf16 v[200:203], a[116:119], a[68:71], v[200:203]\n"
"v_mfma_f32_16x16x32_bf16 v[196:199], a[108:111], a[68:71], v[196:199]\n"
"v_mfma_f32_16x16x32_bf16 v[192:195], a[100:103], a[68:71], v[192:195]\n"
"v_mfma_f32_16x16x32_bf16 v[208:211], a[96:99], a[72:75], v[208:211]\n"
"v_mfma_f32_16x16x32_bf16 v[212:215], a[104:107], a[72:75], v[212:215]\n"
"v_mfma_f32_16x16x32_bf16 v[216:219], a[112:115], a[72:75], v[216:219]\n"
"s_nop 1\n"
"v_pk_fma_f32 v[78:79], v[206:207], v[142:143], v[78:79]\n"
"v_pk_fma_f32 v[76:77], v[204:205], v[140:141], v[76:77]\n"
"v_pk_fma_f32 v[74:75], v[202:203], v[138:139], v[74:75]\n"
"v_mfma_f32_16x16x32_bf16 v[220:223], a[120:123], a[72:75], v[220:223]\n"
"v_pk_fma_f32 v[72:73], v[200:201], v[136:137], v[72:73]\n"
"v_pk_fma_f32 v[70:71], v[198:199], v[134:135], v[70:71]\n"
"v_pk_fma_f32 v[68:69], v[196:197], v[132:133], v[68:69]\n"
"v_mfma_f32_16x16x32_bf16 v[220:223], a[124:127], a[76:79], v[220:223]\n"
"v_pk_fma_f32 v[66:67], v[194:195], v[130:131], v[66:67]\n"
"v_pk_fma_f32 v[64:65], v[192:193], v[128:129], v[64:65]\n"
"v_mfma_f32_16x16x32_bf16 v[216:219], a[116:119], a[76:79], v[216:219]\n"
"v_cvt_pk_bf16_f32 v32, v64, v65\n"
"v_cvt_pk_bf16_f32 v33, v66, v67\n"
"v_cvt_pk_bf16_f32 v34, v68, v69\n"
"v_mfma_f32_16x16x32_bf16 v[212:215], a[108:111], a[76:79], v[212:215]\n"
"v_cvt_pk_bf16_f32 v35, v70, v71\n"
"v_cvt_pk_bf16_f32 v36, v72, v73\n"
"v_cvt_pk_bf16_f32 v37, v74, v75\n"
"v_mfma_f32_16x16x32_bf16 v[208:211], a[100:103], a[76:79], v[208:211]\n"
"v_cvt_pk_bf16_f32 v38, v76, v77\n"
"v_cvt_pk_bf16_f32 v39, v78, v79\n"
"s_mov_b32 s95, 32768\n"
"s_mov_b32 exec_lo, 0xffff0000\n"
"s_mov_b32 exec_hi, 0xffff0000\n"
"v_swap_b32 v32, v34\n"
"v_swap_b32 v33, v35\n"
"v_swap_b32 v36, v38\n"
"v_swap_b32 v37, v39\n"
"s_mov_b64 exec, -1\n"
"buffer_store_dwordx4 v[32:35], v22, s[48:51], 0 offen offset:0 sc1\n"
"buffer_store_dwordx4 v[36:39], v22, s[48:51], 0 offen offset:64 sc1\n"
"v_pk_fma_f32 v[94:95], v[222:223], v[158:159], v[94:95]\n"
"v_pk_fma_f32 v[92:93], v[220:221], v[156:157], v[92:93]\n"
"v_pk_fma_f32 v[90:91], v[218:219], v[154:155], v[90:91]\n"
"v_pk_fma_f32 v[88:89], v[216:217], v[152:153], v[88:89]\n"
"v_pk_fma_f32 v[86:87], v[214:215], v[150:151], v[86:87]\n"
"v_pk_fma_f32 v[84:85], v[212:213], v[148:149], v[84:85]\n"
"v_pk_fma_f32 v[82:83], v[210:211], v[146:147], v[82:83]\n"
"v_pk_fma_f32 v[80:81], v[208:209], v[144:145], v[80:81]\n"
"v_cvt_pk_bf16_f32 v40, v80, v81\n"
"v_cvt_pk_bf16_f32 v41, v82, v83\n"
"v_cvt_pk_bf16_f32 v42, v84, v85\n"
"v_cvt_pk_bf16_f32 v43, v86, v87\n"
"v_cvt_pk_bf16_f32 v44, v88, v89\n"
"v_cvt_pk_bf16_f32 v45, v90, v91\n"
"v_cvt_pk_bf16_f32 v46, v92, v93\n"
"v_cvt_pk_bf16_f32 v47, v94, v95\n"
"s_mov_b32 exec_lo, 0xffff0000\n"
"s_mov_b32 exec_hi, 0xffff0000\n"
"v_swap_b32 v40, v42\n"
"v_swap_b32 v41, v43\n"
"v_swap_b32 v44, v46\n"
"v_swap_b32 v45, v47\n"
"s_mov_b64 exec, -1\n"
"buffer_store_dwordx4 v[40:43], v22, s[48:51], s95 offen offset:0 sc1\n"
"buffer_store_dwordx4 v[44:47], v22, s[48:51], s95 offen offset:64 sc1\n"
"s_cmp_eq_u32 s89, 0\n"
"s_cbranch_scc0 EPKV26_%=\n"
"s_mov_b32 s94, 0\n"
"s_mov_b64 exec, 1\n"
"buffer_store_dwordx2 v[2:3], off, s[20:23], s94 sc1\n"
"s_mov_b64 exec, -1\n"
"EPKV26_%=:\n"
"s_branch END_%=\n"
"SDKV2_%=:\n"
"s_cmp_eq_u32 s41, s1\n"
"s_cbranch_scc0 SDKV4_%=\n"
"s_mov_b64 exec, 3\n"
"buffer_store_dwordx4 a[246:249], v0, s[20:23], s4 offen sc1\n"
"buffer_store_dwordx2 a[246:247], off, s[20:23], s64 sc1\n"
"s_mov_b64 exec, -1\n"
"global_load_dword a240, v0, s[18:19]\n"
"s_branch G1KV1_%=\n"
"SDKV4_%=:\n"
"s_cmp_eq_u32 s14, 0\n"
"s_cbranch_scc0 SDKV3_%=\n"
"s_waitcnt vmcnt(18) lgkmcnt(0)\n"
"s_barrier\n"
"v_mfma_f32_16x16x32_bf16 v[32:35], a[32:35], a[0:3], v[32:35]\n"
"ds_read_b128 a[72:75], v7 offset:0\n"
"v_and_b32 v23, 0xf0, v0\n"
"s_add_u32 m0, s40, 0\n"
"v_mfma_f32_16x16x32_bf16 v[64:67], a[36:39], a[0:3], v[64:67]\n"
"v_mov_b32 v25, 0x3f803f80\n"
"buffer_load_dwordx4 v1, s[24:27], s37 offen offset:0 lds\n"
"v_mfma_f32_16x16x32_bf16 v[96:99], a[0:3], a[40:43], v[96:99]\n"
"ds_read_b64_tr_b16 a[104:105], v8 offset:20480\n"
"s_cmp_le_u32 s41, 3\n"
"s_cselect_b32 s54, 7344128, 0\n"
"s_cselect_b32 s58, 2101248, 0\n"
"v_cmp_eq_u32 vcc, 0, v23\n"
"v_mfma_f32_16x16x32_bf16 v[128:131], a[0:3], a[44:47], v[128:131]\n"
"buffer_load_dword a240, v29, s[52:55], 0 offen\n"
"v_cndmask_b32 v24, 0, v25, vcc\n"
"v_mfma_f32_16x16x32_bf16 v[160:163], a[48:51], a[0:3], v[160:163]\n"
"ds_read_b64_tr_b16 a[106:107], v8 offset:20992\n"
"v_mov_b32 v25, v24\n"
"v_mfma_f32_16x16x32_bf16 v[192:195], a[52:55], a[0:3], v[192:195]\n"
"v_add_u32 v29, 2432, v29\n"
"v_mov_b32 v26, v24\n"
"v_mfma_f32_16x16x32_bf16 v[224:227], a[56:59], a[0:3], v[224:227]\n"
"v_mov_b32 v27, v24\n"
"v_mfma_f32_16x16x32_bf16 a[144:147], a[60:63], a[0:3], a[144:147]\n"
"ds_read_b128 a[76:79], v7 offset:2048\n"
"v_mfma_f32_16x16x32_bf16 a[176:179], a[64:67], a[0:3], a[176:179]\n"
"v_mfma_f32_16x16x32_bf16 a[208:211], a[68:71], a[0:3], a[208:211]\n"
"ds_read_b64_tr_b16 a[108:109], v9 offset:20480\n"
"v_mfma_f32_16x16x32_bf16 a[212:215], a[68:71], a[4:7], a[212:215]\n"
"v_mfma_f32_16x16x32_bf16 a[252:255], a[32:35], v[24:27], 0\n"
"v_mfma_f32_16x16x32_bf16 a[180:183], a[64:67], a[4:7], a[180:183]\n"
"ds_read_b64_tr_b16 a[110:111], v9 offset:20992\n"
"v_mfma_f32_16x16x32_bf16 a[148:151], a[60:63], a[4:7], a[148:151]\n"
"v_mfma_f32_16x16x32_bf16 v[228:231], a[56:59], a[4:7], v[228:231]\n"
"v_mfma_f32_16x16x32_bf16 v[196:199], a[52:55], a[4:7], v[196:199]\n"
"ds_read_b64_tr_b16 a[112:113], v8 offset:21504\n"
"v_mfma_f32_16x16x32_bf16 v[164:167], a[48:51], a[4:7], v[164:167]\n"
"v_mfma_f32_16x16x32_bf16 v[132:135], a[4:7], a[44:47], v[132:135]\n"
"ds_read_b128 a[80:83], v7 offset:4096\n"
"buffer_load_dwordx4 v2, s[24:27], s37 offen offset:1024 lds\n"
"s_add_u32 s37, s37, 196608\n"
"v_mfma_f32_16x16x32_bf16 v[100:103], a[4:7], a[40:43], v[100:103]\n"
"v_mfma_f32_16x16x32_bf16 v[68:71], a[36:39], a[4:7], v[68:71]\n"
"ds_read_b64_tr_b16 a[114:115], v8 offset:22016\n"
"v_mfma_f32_16x16x32_bf16 v[36:39], a[32:35], a[4:7], v[36:39]\n"
"v_mov_b32_dpp v24, v24 row_shr:1 row_mask:0xf bank_mask:0xf bound_ctrl:1\n"
"v_mfma_f32_16x16x32_bf16 v[40:43], a[32:35], a[8:11], v[40:43]\n"
"v_mov_b32_dpp v25, v25 row_shr:1 row_mask:0xf bank_mask:0xf bound_ctrl:1\n"
"v_mfma_f32_16x16x32_bf16 v[72:75], a[36:39], a[8:11], v[72:75]\n"
"ds_read_b64_tr_b16 a[116:117], v9 offset:21504\n"
"v_mov_b32_dpp v26, v26 row_shr:1 row_mask:0xf bank_mask:0xf bound_ctrl:1\n"
"v_mfma_f32_16x16x32_bf16 v[104:107], a[8:11], a[40:43], v[104:107]\n"
"v_mov_b32_dpp v27, v27 row_shr:1 row_mask:0xf bank_mask:0xf bound_ctrl:1\n"
"v_mfma_f32_16x16x32_bf16 v[136:139], a[8:11], a[44:47], v[136:139]\n"
"ds_read_b128 a[84:87], v7 offset:6144\n"
"v_mfma_f32_16x16x32_bf16 v[168:171], a[48:51], a[8:11], v[168:171]\n"
"v_mfma_f32_16x16x32_bf16 a[252:255], a[36:39], v[24:27], a[252:255]\n"
"v_mfma_f32_16x16x32_bf16 v[200:203], a[52:55], a[8:11], v[200:203]\n"
"ds_read_b64_tr_b16 a[118:119], v9 offset:22016\n"
"v_mfma_f32_16x16x32_bf16 v[232:235], a[56:59], a[8:11], v[232:235]\n"
"v_mfma_f32_16x16x32_bf16 a[152:155], a[60:63], a[8:11], a[152:155]\n"
"v_mfma_f32_16x16x32_bf16 a[184:187], a[64:67], a[8:11], a[184:187]\n"
"ds_read_b64_tr_b16 a[120:121], v10 offset:20480\n"
"v_mfma_f32_16x16x32_bf16 a[216:219], a[68:71], a[8:11], a[216:219]\n"
"v_mfma_f32_16x16x32_bf16 a[220:223], a[68:71], a[12:15], a[220:223]\n"
"ds_read_b64_tr_b16 a[122:123], v10 offset:21248\n"
"v_mfma_f32_16x16x32_bf16 a[188:191], a[64:67], a[12:15], a[188:191]\n"
"buffer_load_dwordx4 v3, s[28:31], s38 offen offset:2048 lds\n"
"v_mfma_f32_16x16x32_bf16 a[156:159], a[60:63], a[12:15], a[156:159]\n"
"v_mfma_f32_16x16x32_bf16 v[236:239], a[56:59], a[12:15], v[236:239]\n"
"ds_read_b128 a[88:91], v7 offset:8192\n"
"v_mov_b32_dpp v24, v24 row_shr:1 row_mask:0xf bank_mask:0xf bound_ctrl:1\n"
"v_mfma_f32_16x16x32_bf16 v[204:207], a[52:55], a[12:15], v[204:207]\n"
"v_mov_b32_dpp v25, v25 row_shr:1 row_mask:0xf bank_mask:0xf bound_ctrl:1\n"
"v_mfma_f32_16x16x32_bf16 v[172:175], a[48:51], a[12:15], v[172:175]\n"
"ds_read_b64_tr_b16 a[124:125], v11 offset:20480\n"
"v_mov_b32_dpp v26, v26 row_shr:1 row_mask:0xf bank_mask:0xf bound_ctrl:1\n"
"v_mfma_f32_16x16x32_bf16 v[140:143], a[12:15], a[44:47], v[140:143]\n"
"v_mov_b32_dpp v27, v27 row_shr:1 row_mask:0xf bank_mask:0xf bound_ctrl:1\n"
"v_mfma_f32_16x16x32_bf16 v[108:111], a[12:15], a[40:43], v[108:111]\n"
"ds_read_b64_tr_b16 a[126:127], v11 offset:21248\n"
"v_mfma_f32_16x16x32_bf16 v[76:79], a[36:39], a[12:15], v[76:79]\n"
"v_mfma_f32_16x16x32_bf16 a[252:255], a[40:43], v[24:27], a[252:255]\n"
"v_mfma_f32_16x16x32_bf16 v[44:47], a[32:35], a[12:15], v[44:47]\n"
"v_mfma_f32_16x16x32_bf16 v[48:51], a[32:35], a[16:19], v[48:51]\n"
"ds_read_b128 a[92:95], v7 offset:10240\n"
"buffer_load_dword a240, v30, s[56:59], 0 offen\n"
"v_mfma_f32_16x16x32_bf16 v[80:83], a[36:39], a[16:19], v[80:83]\n"
"v_mfma_f32_16x16x32_bf16 v[112:115], a[16:19], a[40:43], v[112:115]\n"
"ds_read_b64_tr_b16 a[128:129], v12 offset:20480\n"
"v_add_u32 v30, 704, v30\n"
"v_mfma_f32_16x16x32_bf16 v[144:147], a[16:19], a[44:47], v[144:147]\n"
"v_mfma_f32_16x16x32_bf16 v[176:179], a[48:51], a[16:19], v[176:179]\n"
"ds_read_b64_tr_b16 a[130:131], v12 offset:21248\n"
"v_mfma_f32_16x16x32_bf16 v[208:211], a[52:55], a[16:19], v[208:211]\n"
"v_mfma_f32_16x16x32_bf16 v[240:243], a[56:59], a[16:19], v[240:243]\n"
"v_mfma_f32_16x16x32_bf16 a[160:163], a[60:63], a[16:19], a[160:163]\n"
"ds_read_b64_tr_b16 a[132:133], v13 offset:20480\n"
"v_mov_b32_dpp v24, v24 row_shr:1 row_mask:0xf bank_mask:0xf bound_ctrl:1\n"
"buffer_load_dwordx4 v4, s[28:31], s38 offen offset:3072 lds\n"
"v_mfma_f32_16x16x32_bf16 a[192:195], a[64:67], a[16:19], a[192:195]\n"
"v_mov_b32_dpp v25, v25 row_shr:1 row_mask:0xf bank_mask:0xf bound_ctrl:1\n"
"v_mfma_f32_16x16x32_bf16 a[224:227], a[68:71], a[16:19], a[224:227]\n"
"ds_read_b128 a[96:99], v7 offset:12288\n"
"v_mov_b32_dpp v26, v26 row_shr:1 row_mask:0xf bank_mask:0xf bound_ctrl:1\n"
"v_mfma_f32_16x16x32_bf16 a[228:231], a[68:71], a[20:23], a[228:231]\n"
"v_mov_b32_dpp v27, v27 row_shr:1 row_mask:0xf bank_mask:0xf bound_ctrl:1\n"
"v_mfma_f32_16x16x32_bf16 a[196:199], a[64:67], a[20:23], a[196:199]\n"
"ds_read_b64_tr_b16 a[134:135], v13 offset:21248\n"
"v_mfma_f32_16x16x32_bf16 a[164:167], a[60:63], a[20:23], a[164:167]\n"
"v_mfma_f32_16x16x32_bf16 a[252:255], a[44:47], v[24:27], a[252:255]\n"
"v_mfma_f32_16x16x32_bf16 v[244:247], a[56:59], a[20:23], v[244:247]\n"
"v_mfma_f32_16x16x32_bf16 v[212:215], a[52:55], a[20:23], v[212:215]\n"
"ds_read_b64_tr_b16 a[136:137], v14 offset:20480\n"
"v_mfma_f32_16x16x32_bf16 v[180:183], a[48:51], a[20:23], v[180:183]\n"
"v_mfma_f32_16x16x32_bf16 v[148:151], a[20:23], a[44:47], v[148:151]\n"
"ds_read_b128 a[100:103], v7 offset:14336\n"
"v_mfma_f32_16x16x32_bf16 v[116:119], a[20:23], a[40:43], v[116:119]\n"
"v_mfma_f32_16x16x32_bf16 v[84:87], a[36:39], a[20:23], v[84:87]\n"
"ds_read_b64_tr_b16 a[138:139], v14 offset:21248\n"
"v_mfma_f32_16x16x32_bf16 v[52:55], a[32:35], a[20:23], v[52:55]\n"
"v_mfma_f32_16x16x32_bf16 v[56:59], a[32:35], a[24:27], v[56:59]\n"
"v_mfma_f32_16x16x32_bf16 v[88:91], a[36:39], a[24:27], v[88:91]\n"
"ds_read_b64_tr_b16 a[140:141], v15 offset:20480\n"
"v_mov_b32_dpp v24, v24 row_shr:1 row_mask:0xf bank_mask:0xf bound_ctrl:1\n"
"s_add_u32 m0, s40, 4096\n"
"v_mfma_f32_16x16x32_bf16 v[120:123], a[24:27], a[40:43], v[120:123]\n"
"v_mov_b32_dpp v25, v25 row_shr:1 row_mask:0xf bank_mask:0xf bound_ctrl:1\n"
"buffer_load_dwordx4 v5, s[28:31], s38 offen offset:0 lds\n"
"s_add_u32 s38, s38, 229376\n"
"v_mfma_f32_16x16x32_bf16 v[152:155], a[24:27], a[44:47], v[152:155]\n"
"ds_read_b64_tr_b16 a[142:143], v15 offset:21248\n"
"v_mov_b32_dpp v26, v26 row_shr:1 row_mask:0xf bank_mask:0xf bound_ctrl:1\n"
"v_mfma_f32_16x16x32_bf16 v[184:187], a[48:51], a[24:27], v[184:187]\n"
"v_mov_b32_dpp v27, v27 row_shr:1 row_mask:0xf bank_mask:0xf bound_ctrl:1\n"
"v_mfma_f32_16x16x32_bf16 v[216:219], a[52:55], a[24:27], v[216:219]\n"
"v_mfma_f32_16x16x32_bf16 v[248:251], a[56:59], a[24:27], v[248:251]\n"
"v_mfma_f32_16x16x32_bf16 a[252:255], a[48:51], v[24:27], a[252:255]\n"
"v_mfma_f32_16x16x32_bf16 a[168:171], a[60:63], a[24:27], a[168:171]\n"
"v_mfma_f32_16x16x32_bf16 a[200:203], a[64:67], a[24:27], a[200:203]\n"
"v_mfma_f32_16x16x32_bf16 a[232:235], a[68:71], a[24:27], a[232:235]\n"
"v_mfma_f32_16x16x32_bf16 a[236:239], a[68:71], a[28:31], a[236:239]\n"
"v_mfma_f32_16x16x32_bf16 a[204:207], a[64:67], a[28:31], a[204:207]\n"
"v_mfma_f32_16x16x32_bf16 a[172:175], a[60:63], a[28:31], a[172:175]\n"
"v_mfma_f32_16x16x32_bf16 v[252:255], a[56:59], a[28:31], v[252:255]\n"
"v_mfma_f32_16x16x32_bf16 v[220:223], a[52:55], a[28:31], v[220:223]\n"
"v_mfma_f32_16x16x32_bf16 v[188:191], a[48:51], a[28:31], v[188:191]\n"
"v_mfma_f32_16x16x32_bf16 v[156:159], a[28:31], a[44:47], v[156:159]\n"
"v_mfma_f32_16x16x32_bf16 v[124:127], a[28:31], a[40:43], v[124:127]\n"
"v_mfma_f32_16x16x32_bf16 v[92:95], a[36:39], a[28:31], v[92:95]\n"
"v_mfma_f32_16x16x32_bf16 v[60:63], a[32:35], a[28:31], v[60:63]\n"
"s_waitcnt vmcnt(12) lgkmcnt(0)\n"
"s_barrier\n"
"v_mfma_f32_16x16x32_bf16 v[32:35], a[104:107], a[72:75], v[32:35]\n"
"ds_read_b128 a[0:3], v6 offset:32768\n"
"s_add_u32 m0, s39, 0\n"
"v_mfma_f32_16x16x32_bf16 v[64:67], a[108:111], a[72:75], v[64:67]\n"
"buffer_load_dwordx4 v20, s[44:47], s36 offen offset:0 lds\n"
"s_add_u32 s36, s36, 15360\n"
"v_mfma_f32_16x16x32_bf16 v[96:99], a[72:75], a[112:115], v[96:99]\n"
"ds_read_b64_tr_b16 a[32:33], v8 offset:40960\n"
"v_mfma_f32_16x16x32_bf16 v[128:131], a[72:75], a[116:119], v[128:131]\n"
"v_mfma_f32_16x16x32_bf16 v[160:163], a[120:123], a[72:75], v[160:163]\n"
"ds_read_b64_tr_b16 a[34:35], v8 offset:41472\n"
"v_mfma_f32_16x16x32_bf16 v[192:195], a[124:127], a[72:75], v[192:195]\n"
"v_mfma_f32_16x16x32_bf16 v[224:227], a[128:131], a[72:75], v[224:227]\n"
"buffer_load_dwordx4 v21, s[44:47], s36 offen offset:1024 lds\n"
"s_add_u32 s36, s36, 15360\n"
"v_mfma_f32_16x16x32_bf16 a[144:147], a[132:135], a[72:75], a[144:147]\n"
"ds_read_b128 a[4:7], v6 offset:34816\n"
"v_mfma_f32_16x16x32_bf16 a[176:179], a[136:139], a[72:75], a[176:179]\n"
"v_mfma_f32_16x16x32_bf16 a[208:211], a[140:143], a[72:75], a[208:211]\n"
"ds_read_b64_tr_b16 a[36:37], v9 offset:40960\n"
"v_mfma_f32_16x16x32_bf16 a[212:215], a[140:143], a[76:79], a[212:215]\n"
"v_mfma_f32_16x16x32_bf16 a[252:255], a[120:123], v[24:27], a[252:255]\n"
"v_mfma_f32_16x16x32_bf16 a[180:183], a[136:139], a[76:79], a[180:183]\n"
"ds_read_b64_tr_b16 a[38:39], v9 offset:41472\n"
"v_mfma_f32_16x16x32_bf16 a[148:151], a[132:135], a[76:79], a[148:151]\n"
"buffer_load_dwordx4 v20, s[44:47], s36 offen offset:2048 lds\n"
"s_add_u32 s36, s36, 15360\n"
"v_mfma_f32_16x16x32_bf16 v[228:231], a[128:131], a[76:79], v[228:231]\n"
"v_mfma_f32_16x16x32_bf16 v[196:199], a[124:127], a[76:79], v[196:199]\n"
"ds_read_b64_tr_b16 a[40:41], v8 offset:41984\n"
"v_mfma_f32_16x16x32_bf16 v[164:167], a[120:123], a[76:79], v[164:167]\n"
"v_mfma_f32_16x16x32_bf16 v[132:135], a[76:79], a[116:119], v[132:135]\n"
"ds_read_b128 a[8:11], v6 offset:36864\n"
"v_mfma_f32_16x16x32_bf16 v[100:103], a[76:79], a[112:115], v[100:103]\n"
"v_mfma_f32_16x16x32_bf16 v[68:71], a[108:111], a[76:79], v[68:71]\n"
"ds_read_b64_tr_b16 a[42:43], v8 offset:42496\n"
"buffer_load_dwordx4 v21, s[44:47], s36 offen offset:3072 lds\n"
"s_add_u32 s36, s36, 19456\n"
"v_mfma_f32_16x16x32_bf16 v[36:39], a[104:107], a[76:79], v[36:39]\n"
"v_mov_b32_dpp v24, v24 row_shl:1 row_mask:0xf bank_mask:0xf bound_ctrl:1\n"
"v_mfma_f32_16x16x32_bf16 v[40:43], a[104:107], a[80:83], v[40:43]\n"
"v_mov_b32_dpp v25, v25 row_shl:1 row_mask:0xf bank_mask:0xf bound_ctrl:1\n"
"v_mfma_f32_16x16x32_bf16 v[72:75], a[108:111], a[80:83], v[72:75]\n"
"ds_read_b64_tr_b16 a[44:45], v9 offset:41984\n"
"v_mov_b32_dpp v26, v26 row_shl:1 row_mask:0xf bank_mask:0xf bound_ctrl:1\n"
"v_mfma_f32_16x16x32_bf16 v[104:107], a[80:83], a[112:115], v[104:107]\n"
"v_mov_b32_dpp v27, v27 row_shl:1 row_mask:0xf bank_mask:0xf bound_ctrl:1\n"
"v_mfma_f32_16x16x32_bf16 v[136:139], a[80:83], a[116:119], v[136:139]\n"
"ds_read_b128 a[12:15], v6 offset:38912\n"
"s_add_u32 m0, s39, 4096\n"
"v_mfma_f32_16x16x32_bf16 v[168:171], a[120:123], a[80:83], v[168:171]\n"
"v_mfma_f32_16x16x32_bf16 a[252:255], a[116:119], v[24:27], a[252:255]\n"
"buffer_load_dwordx4 v20, s[44:47], s36 offen offset:0 lds\n"
"s_add_u32 s36, s36, 15360\n"
"v_mfma_f32_16x16x32_bf16 v[200:203], a[124:127], a[80:83], v[200:203]\n"
"ds_read_b64_tr_b16 a[46:47], v9 offset:42496\n"
"v_mfma_f32_16x16x32_bf16 v[232:235], a[128:131], a[80:83], v[232:235]\n"
"v_mfma_f32_16x16x32_bf16 a[152:155], a[132:135], a[80:83], a[152:155]\n"
"v_mfma_f32_16x16x32_bf16 a[184:187], a[136:139], a[80:83], a[184:187]\n"
"ds_read_b64_tr_b16 a[48:49], v10 offset:40960\n"
"v_mfma_f32_16x16x32_bf16 a[216:219], a[140:143], a[80:83], a[216:219]\n"
"v_mfma_f32_16x16x32_bf16 a[220:223], a[140:143], a[84:87], a[220:223]\n"
"ds_read_b64_tr_b16 a[50:51], v10 offset:41728\n"
"buffer_load_dwordx4 v21, s[44:47], s36 offen offset:1024 lds\n"
"s_add_u32 s36, s36, 15360\n"
"v_mfma_f32_16x16x32_bf16 a[188:191], a[136:139], a[84:87], a[188:191]\n"
"v_mfma_f32_16x16x32_bf16 a[156:159], a[132:135], a[84:87], a[156:159]\n"
"v_mfma_f32_16x16x32_bf16 v[236:239], a[128:131], a[84:87], v[236:239]\n"
"ds_read_b128 a[16:19], v6 offset:40960\n"
"v_mov_b32_dpp v24, v24 row_shl:1 row_mask:0xf bank_mask:0xf bound_ctrl:1\n"
"v_mfma_f32_16x16x32_bf16 v[204:207], a[124:127], a[84:87], v[204:207]\n"
"v_mov_b32_dpp v25, v25 row_shl:1 row_mask:0xf bank_mask:0xf bound_ctrl:1\n"
"v_mfma_f32_16x16x32_bf16 v[172:175], a[120:123], a[84:87], v[172:175]\n"
"ds_read_b64_tr_b16 a[52:53], v11 offset:40960\n"
"v_mov_b32_dpp v26, v26 row_shl:1 row_mask:0xf bank_mask:0xf bound_ctrl:1\n"
"v_mfma_f32_16x16x32_bf16 v[140:143], a[84:87], a[116:119], v[140:143]\n"
"v_mov_b32_dpp v27, v27 row_shl:1 row_mask:0xf bank_mask:0xf bound_ctrl:1\n"
"buffer_load_dwordx4 v20, s[44:47], s36 offen offset:2048 lds\n"
"s_add_u32 s36, s36, 15360\n"
"v_mfma_f32_16x16x32_bf16 v[108:111], a[84:87], a[112:115], v[108:111]\n"
"ds_read_b64_tr_b16 a[54:55], v11 offset:41728\n"
"v_mfma_f32_16x16x32_bf16 v[76:79], a[108:111], a[84:87], v[76:79]\n"
"v_mfma_f32_16x16x32_bf16 a[252:255], a[112:115], v[24:27], a[252:255]\n"
"v_mfma_f32_16x16x32_bf16 v[44:47], a[104:107], a[84:87], v[44:47]\n"
"v_mfma_f32_16x16x32_bf16 v[48:51], a[104:107], a[88:91], v[48:51]\n"
"ds_read_b128 a[20:23], v6 offset:43008\n"
"v_mfma_f32_16x16x32_bf16 v[80:83], a[108:111], a[88:91], v[80:83]\n"
"v_mfma_f32_16x16x32_bf16 v[112:115], a[88:91], a[112:115], v[112:115]\n"
"ds_read_b64_tr_b16 a[56:57], v12 offset:40960\n"
"buffer_load_dwordx4 v21, s[44:47], s36 offen offset:3072 lds\n"
"s_sub_u32 s36, s36, 111488\n"
"v_mfma_f32_16x16x32_bf16 v[144:147], a[88:91], a[116:119], v[144:147]\n"
"v_mfma_f32_16x16x32_bf16 v[176:179], a[120:123], a[88:91], v[176:179]\n"
"ds_read_b64_tr_b16 a[58:59], v12 offset:41728\n"
"v_mfma_f32_16x16x32_bf16 v[208:211], a[124:127], a[88:91], v[208:211]\n"
"v_mfma_f32_16x16x32_bf16 v[240:243], a[128:131], a[88:91], v[240:243]\n"
"v_mfma_f32_16x16x32_bf16 a[160:163], a[132:135], a[88:91], a[160:163]\n"
"ds_read_b64_tr_b16 a[60:61], v13 offset:40960\n"
"v_mov_b32_dpp v24, v24 row_shl:1 row_mask:0xf bank_mask:0xf bound_ctrl:1\n"
"s_add_u32 m0, s40, 20480\n"
"v_mfma_f32_16x16x32_bf16 a[192:195], a[136:139], a[88:91], a[192:195]\n"
"v_mov_b32_dpp v25, v25 row_shl:1 row_mask:0xf bank_mask:0xf bound_ctrl:1\n"
"buffer_load_dwordx4 v1, s[24:27], s37 offen offset:0 lds\n"
"v_mfma_f32_16x16x32_bf16 a[224:227], a[140:143], a[88:91], a[224:227]\n"
"ds_read_b128 a[24:27], v6 offset:45056\n"
"v_mov_b32_dpp v26, v26 row_shl:1 row_mask:0xf bank_mask:0xf bound_ctrl:1\n"
"v_mfma_f32_16x16x32_bf16 a[228:231], a[140:143], a[92:95], a[228:231]\n"
"v_mov_b32_dpp v27, v27 row_shl:1 row_mask:0xf bank_mask:0xf bound_ctrl:1\n"
"v_mfma_f32_16x16x32_bf16 a[196:199], a[136:139], a[92:95], a[196:199]\n"
"ds_read_b64_tr_b16 a[62:63], v13 offset:41728\n"
"v_mfma_f32_16x16x32_bf16 a[164:167], a[132:135], a[92:95], a[164:167]\n"
"v_mfma_f32_16x16x32_bf16 a[252:255], a[108:111], v[24:27], a[252:255]\n"
"v_mfma_f32_16x16x32_bf16 v[244:247], a[128:131], a[92:95], v[244:247]\n"
"v_mfma_f32_16x16x32_bf16 v[212:215], a[124:127], a[92:95], v[212:215]\n"
"ds_read_b64_tr_b16 a[64:65], v14 offset:40960\n"
"buffer_load_dwordx4 v2, s[24:27], s37 offen offset:1024 lds\n"
"s_add_u32 s37, s37, 196608\n"
"v_mfma_f32_16x16x32_bf16 v[180:183], a[120:123], a[92:95], v[180:183]\n"
"v_mfma_f32_16x16x32_bf16 v[148:151], a[92:95], a[116:119], v[148:151]\n"
"ds_read_b128 a[28:31], v6 offset:47104\n"
"v_mfma_f32_16x16x32_bf16 v[116:119], a[92:95], a[112:115], v[116:119]\n"
"v_mfma_f32_16x16x32_bf16 v[84:87], a[108:111], a[92:95], v[84:87]\n"
"ds_read_b64_tr_b16 a[66:67], v14 offset:41728\n"
"v_mfma_f32_16x16x32_bf16 v[52:55], a[104:107], a[92:95], v[52:55]\n"
"v_mfma_f32_16x16x32_bf16 v[56:59], a[104:107], a[96:99], v[56:59]\n"
"buffer_load_dwordx4 v3, s[28:31], s38 offen offset:2048 lds\n"
"v_mfma_f32_16x16x32_bf16 v[88:91], a[108:111], a[96:99], v[88:91]\n"
"ds_read_b64_tr_b16 a[68:69], v15 offset:40960\n"
"v_mov_b32_dpp v24, v24 row_shl:1 row_mask:0xf bank_mask:0xf bound_ctrl:1\n"
"v_mfma_f32_16x16x32_bf16 v[120:123], a[96:99], a[112:115], v[120:123]\n"
"v_mov_b32_dpp v25, v25 row_shl:1 row_mask:0xf bank_mask:0xf bound_ctrl:1\n"
"v_mfma_f32_16x16x32_bf16 v[152:155], a[96:99], a[116:119], v[152:155]\n"
"ds_read_b64_tr_b16 a[70:71], v15 offset:41728\n"
"v_mov_b32_dpp v26, v26 row_shl:1 row_mask:0xf bank_mask:0xf bound_ctrl:1\n"
"v_mfma_f32_16x16x32_bf16 v[184:187], a[120:123], a[96:99], v[184:187]\n"
"v_mov_b32_dpp v27, v27 row_shl:1 row_mask:0xf bank_mask:0xf bound_ctrl:1\n"
"v_mfma_f32_16x16x32_bf16 v[216:219], a[124:127], a[96:99], v[216:219]\n"
"v_mfma_f32_16x16x32_bf16 v[248:251], a[128:131], a[96:99], v[248:251]\n"
"v_mfma_f32_16x16x32_bf16 a[252:255], a[104:107], v[24:27], a[252:255]\n"
"buffer_load_dwordx4 v4, s[28:31], s38 offen offset:3072 lds\n"
"v_mfma_f32_16x16x32_bf16 a[168:171], a[132:135], a[96:99], a[168:171]\n"
"v_mfma_f32_16x16x32_bf16 a[200:203], a[136:139], a[96:99], a[200:203]\n"
"v_and_b32 v23, 0xf0, v0\n"
"v_mfma_f32_16x16x32_bf16 a[232:235], a[140:143], a[96:99], a[232:235]\n"
"v_lshrrev_b32 v22, 8, v0\n"
"v_mfma_f32_16x16x32_bf16 a[236:239], a[140:143], a[100:103], a[236:239]\n"
"v_add_lshl_u32 v22, v22, v23, 6\n"
"v_mfma_f32_16x16x32_bf16 a[204:207], a[136:139], a[100:103], a[204:207]\n"
"v_cmp_lt_u32 vcc, 0x4f, v23\n"
"s_add_u32 m0, s40, 24576\n"
"v_mfma_f32_16x16x32_bf16 a[172:175], a[132:135], a[100:103], a[172:175]\n"
"v_bfrev_b32 v23, 1\n"
"buffer_load_dwordx4 v5, s[28:31], s38 offen offset:0 lds\n"
"s_add_u32 s38, s38, 229376\n"
"v_mfma_f32_16x16x32_bf16 v[252:255], a[128:131], a[100:103], v[252:255]\n"
"s_nop 0\n"
"v_mfma_f32_16x16x32_bf16 v[220:223], a[124:127], a[100:103], v[220:223]\n"
"v_cndmask_b32 v22, v22, v23, vcc\n"
"v_mfma_f32_16x16x32_bf16 v[188:191], a[120:123], a[100:103], v[188:191]\n"
"v_mfma_f32_16x16x32_bf16 v[156:159], a[100:103], a[116:119], v[156:159]\n"
"v_mfma_f32_16x16x32_bf16 v[124:127], a[100:103], a[112:115], v[124:127]\n"
"v_mfma_f32_16x16x32_bf16 v[92:95], a[108:111], a[100:103], v[92:95]\n"
"v_mfma_f32_16x16x32_bf16 v[60:63], a[104:107], a[100:103], v[60:63]\n"
"s_waitcnt vmcnt(20) lgkmcnt(0)\n"
"s_barrier\n"
"v_mfma_f32_16x16x32_bf16 v[32:35], a[32:35], a[0:3], v[32:35]\n"
"ds_read_b128 a[72:75], v7 offset:32768\n"
"s_add_u32 m0, s40, 40960\n"
"v_mfma_f32_16x16x32_bf16 v[64:67], a[36:39], a[0:3], v[64:67]\n"
"buffer_load_dwordx4 v1, s[24:27], s37 offen offset:0 lds\n"
"v_mfma_f32_16x16x32_bf16 v[96:99], a[0:3], a[40:43], v[96:99]\n"
"ds_read_b64_tr_b16 a[104:105], v8 offset:61440\n"
"v_mfma_f32_16x16x32_bf16 v[128:131], a[0:3], a[44:47], v[128:131]\n"
"v_mfma_f32_16x16x32_bf16 v[160:163], a[48:51], a[0:3], v[160:163]\n"
"ds_read_b64_tr_b16 a[106:107], v8 offset:61952\n"
"v_mfma_f32_16x16x32_bf16 v[192:195], a[52:55], a[0:3], v[192:195]\n"
"v_mfma_f32_16x16x32_bf16 v[224:227], a[56:59], a[0:3], v[224:227]\n"
"v_mfma_f32_16x16x32_bf16 a[144:147], a[60:63], a[0:3], a[144:147]\n"
"ds_read_b128 a[76:79], v7 offset:34816\n"
"v_mfma_f32_16x16x32_bf16 a[176:179], a[64:67], a[0:3], a[176:179]\n"
"v_mfma_f32_16x16x32_bf16 a[208:211], a[68:71], a[0:3], a[208:211]\n"
"ds_read_b64_tr_b16 a[108:109], v9 offset:61440\n"
"v_mfma_f32_16x16x32_bf16 a[212:215], a[68:71], a[4:7], a[212:215]\n"
"v_mfma_f32_16x16x32_bf16 a[252:255], a[32:35], v[24:27], a[252:255]\n"
"v_mfma_f32_16x16x32_bf16 a[180:183], a[64:67], a[4:7], a[180:183]\n"
"ds_read_b64_tr_b16 a[110:111], v9 offset:61952\n"
"v_mfma_f32_16x16x32_bf16 a[148:151], a[60:63], a[4:7], a[148:151]\n"
"v_mfma_f32_16x16x32_bf16 v[228:231], a[56:59], a[4:7], v[228:231]\n"
"v_mfma_f32_16x16x32_bf16 v[196:199], a[52:55], a[4:7], v[196:199]\n"
"ds_read_b64_tr_b16 a[112:113], v8 offset:62464\n"
"v_mfma_f32_16x16x32_bf16 v[164:167], a[48:51], a[4:7], v[164:167]\n"
"v_mfma_f32_16x16x32_bf16 v[132:135], a[4:7], a[44:47], v[132:135]\n"
"ds_read_b128 a[80:83], v7 offset:36864\n"
"buffer_load_dwordx4 v2, s[24:27], s37 offen offset:1024 lds\n"
"s_add_u32 s37, s37, 196608\n"
"v_mfma_f32_16x16x32_bf16 v[100:103], a[4:7], a[40:43], v[100:103]\n"
"v_mfma_f32_16x16x32_bf16 v[68:71], a[36:39], a[4:7], v[68:71]\n"
"ds_read_b64_tr_b16 a[114:115], v8 offset:62976\n"
"v_mfma_f32_16x16x32_bf16 v[36:39], a[32:35], a[4:7], v[36:39]\n"
"v_mov_b32_dpp v24, v24 row_shr:1 row_mask:0xf bank_mask:0xf bound_ctrl:1\n"
"v_mfma_f32_16x16x32_bf16 v[40:43], a[32:35], a[8:11], v[40:43]\n"
"v_mov_b32_dpp v25, v25 row_shr:1 row_mask:0xf bank_mask:0xf bound_ctrl:1\n"
"v_mfma_f32_16x16x32_bf16 v[72:75], a[36:39], a[8:11], v[72:75]\n"
"ds_read_b64_tr_b16 a[116:117], v9 offset:62464\n"
"v_mov_b32_dpp v26, v26 row_shr:1 row_mask:0xf bank_mask:0xf bound_ctrl:1\n"
"v_mfma_f32_16x16x32_bf16 v[104:107], a[8:11], a[40:43], v[104:107]\n"
"v_mov_b32_dpp v27, v27 row_shr:1 row_mask:0xf bank_mask:0xf bound_ctrl:1\n"
"v_mfma_f32_16x16x32_bf16 v[136:139], a[8:11], a[44:47], v[136:139]\n"
"ds_read_b128 a[84:87], v7 offset:38912\n"
"v_mfma_f32_16x16x32_bf16 v[168:171], a[48:51], a[8:11], v[168:171]\n"
"v_mfma_f32_16x16x32_bf16 a[252:255], a[36:39], v[24:27], a[252:255]\n"
"v_mfma_f32_16x16x32_bf16 v[200:203], a[52:55], a[8:11], v[200:203]\n"
"ds_read_b64_tr_b16 a[118:119], v9 offset:62976\n"
"v_mfma_f32_16x16x32_bf16 v[232:235], a[56:59], a[8:11], v[232:235]\n"
"v_mfma_f32_16x16x32_bf16 a[152:155], a[60:63], a[8:11], a[152:155]\n"
"v_mfma_f32_16x16x32_bf16 a[184:187], a[64:67], a[8:11], a[184:187]\n"
"ds_read_b64_tr_b16 a[120:121], v10 offset:61440\n"
"v_mfma_f32_16x16x32_bf16 a[216:219], a[68:71], a[8:11], a[216:219]\n"
"v_mfma_f32_16x16x32_bf16 a[220:223], a[68:71], a[12:15], a[220:223]\n"
"ds_read_b64_tr_b16 a[122:123], v10 offset:62208\n"
"v_mfma_f32_16x16x32_bf16 a[188:191], a[64:67], a[12:15], a[188:191]\n"
"buffer_load_dwordx4 v3, s[28:31], s38 offen offset:2048 lds\n"
"v_mfma_f32_16x16x32_bf16 a[156:159], a[60:63], a[12:15], a[156:159]\n"
"v_mfma_f32_16x16x32_bf16 v[236:239], a[56:59], a[12:15], v[236:239]\n"
"ds_read_b128 a[88:91], v7 offset:40960\n"
"v_mov_b32_dpp v24, v24 row_shr:1 row_mask:0xf bank_mask:0xf bound_ctrl:1\n"
"v_mfma_f32_16x16x32_bf16 v[204:207], a[52:55], a[12:15], v[204:207]\n"
"v_mov_b32_dpp v25, v25 row_shr:1 row_mask:0xf bank_mask:0xf bound_ctrl:1\n"
"v_mfma_f32_16x16x32_bf16 v[172:175], a[48:51], a[12:15], v[172:175]\n"
"ds_read_b64_tr_b16 a[124:125], v11 offset:61440\n"
"v_mov_b32_dpp v26, v26 row_shr:1 row_mask:0xf bank_mask:0xf bound_ctrl:1\n"
"v_mfma_f32_16x16x32_bf16 v[140:143], a[12:15], a[44:47], v[140:143]\n"
"v_mov_b32_dpp v27, v27 row_shr:1 row_mask:0xf bank_mask:0xf bound_ctrl:1\n"
"v_mfma_f32_16x16x32_bf16 v[108:111], a[12:15], a[40:43], v[108:111]\n"
"ds_read_b64_tr_b16 a[126:127], v11 offset:62208\n"
"v_mfma_f32_16x16x32_bf16 v[76:79], a[36:39], a[12:15], v[76:79]\n"
"v_mfma_f32_16x16x32_bf16 a[252:255], a[40:43], v[24:27], a[252:255]\n"
"v_mfma_f32_16x16x32_bf16 v[44:47], a[32:35], a[12:15], v[44:47]\n"
"v_mfma_f32_16x16x32_bf16 v[48:51], a[32:35], a[16:19], v[48:51]\n"
"ds_read_b128 a[92:95], v7 offset:43008\n"
"v_mfma_f32_16x16x32_bf16 v[80:83], a[36:39], a[16:19], v[80:83]\n"
"v_mfma_f32_16x16x32_bf16 v[112:115], a[16:19], a[40:43], v[112:115]\n"
"ds_read_b64_tr_b16 a[128:129], v12 offset:61440\n"
"v_mfma_f32_16x16x32_bf16 v[144:147], a[16:19], a[44:47], v[144:147]\n"
"v_mfma_f32_16x16x32_bf16 v[176:179], a[48:51], a[16:19], v[176:179]\n"
"ds_read_b64_tr_b16 a[130:131], v12 offset:62208\n"
"v_mfma_f32_16x16x32_bf16 v[208:211], a[52:55], a[16:19], v[208:211]\n"
"v_mfma_f32_16x16x32_bf16 v[240:243], a[56:59], a[16:19], v[240:243]\n"
"v_mfma_f32_16x16x32_bf16 a[160:163], a[60:63], a[16:19], a[160:163]\n"
"ds_read_b64_tr_b16 a[132:133], v13 offset:61440\n"
"v_mov_b32_dpp v24, v24 row_shr:1 row_mask:0xf bank_mask:0xf bound_ctrl:1\n"
"buffer_load_dwordx4 v4, s[28:31], s38 offen offset:3072 lds\n"
"v_mfma_f32_16x16x32_bf16 a[192:195], a[64:67], a[16:19], a[192:195]\n"
"v_mov_b32_dpp v25, v25 row_shr:1 row_mask:0xf bank_mask:0xf bound_ctrl:1\n"
"v_mfma_f32_16x16x32_bf16 a[224:227], a[68:71], a[16:19], a[224:227]\n"
"ds_read_b128 a[96:99], v7 offset:45056\n"
"v_mov_b32_dpp v26, v26 row_shr:1 row_mask:0xf bank_mask:0xf bound_ctrl:1\n"
"v_mfma_f32_16x16x32_bf16 a[228:231], a[68:71], a[20:23], a[228:231]\n"
"v_mov_b32_dpp v27, v27 row_shr:1 row_mask:0xf bank_mask:0xf bound_ctrl:1\n"
"v_mfma_f32_16x16x32_bf16 a[196:199], a[64:67], a[20:23], a[196:199]\n"
"ds_read_b64_tr_b16 a[134:135], v13 offset:62208\n"
"v_mfma_f32_16x16x32_bf16 a[164:167], a[60:63], a[20:23], a[164:167]\n"
"v_mfma_f32_16x16x32_bf16 a[252:255], a[44:47], v[24:27], a[252:255]\n"
"v_mfma_f32_16x16x32_bf16 v[244:247], a[56:59], a[20:23], v[244:247]\n"
"v_mfma_f32_16x16x32_bf16 v[212:215], a[52:55], a[20:23], v[212:215]\n"
"ds_read_b64_tr_b16 a[136:137], v14 offset:61440\n"
"v_mfma_f32_16x16x32_bf16 v[180:183], a[48:51], a[20:23], v[180:183]\n"
"v_mfma_f32_16x16x32_bf16 v[148:151], a[20:23], a[44:47], v[148:151]\n"
"ds_read_b128 a[100:103], v7 offset:47104\n"
"v_mfma_f32_16x16x32_bf16 v[116:119], a[20:23], a[40:43], v[116:119]\n"
"v_mfma_f32_16x16x32_bf16 v[84:87], a[36:39], a[20:23], v[84:87]\n"
"ds_read_b64_tr_b16 a[138:139], v14 offset:62208\n"
"v_mfma_f32_16x16x32_bf16 v[52:55], a[32:35], a[20:23], v[52:55]\n"
"v_mfma_f32_16x16x32_bf16 v[56:59], a[32:35], a[24:27], v[56:59]\n"
"v_mfma_f32_16x16x32_bf16 v[88:91], a[36:39], a[24:27], v[88:91]\n"
"ds_read_b64_tr_b16 a[140:141], v15 offset:61440\n"
"v_mov_b32_dpp v24, v24 row_shr:1 row_mask:0xf bank_mask:0xf bound_ctrl:1\n"
"s_add_u32 m0, s40, 45056\n"
"v_mfma_f32_16x16x32_bf16 v[120:123], a[24:27], a[40:43], v[120:123]\n"
"v_mov_b32_dpp v25, v25 row_shr:1 row_mask:0xf bank_mask:0xf bound_ctrl:1\n"
"buffer_load_dwordx4 v5, s[28:31], s38 offen offset:0 lds\n"
"s_add_u32 s38, s38, 229376\n"
"v_mfma_f32_16x16x32_bf16 v[152:155], a[24:27], a[44:47], v[152:155]\n"
"ds_read_b64_tr_b16 a[142:143], v15 offset:62208\n"
"v_mov_b32_dpp v26, v26 row_shr:1 row_mask:0xf bank_mask:0xf bound_ctrl:1\n"
"v_mfma_f32_16x16x32_bf16 v[184:187], a[48:51], a[24:27], v[184:187]\n"
"v_mov_b32_dpp v27, v27 row_shr:1 row_mask:0xf bank_mask:0xf bound_ctrl:1\n"
"v_mfma_f32_16x16x32_bf16 v[216:219], a[52:55], a[24:27], v[216:219]\n"
"v_mfma_f32_16x16x32_bf16 v[248:251], a[56:59], a[24:27], v[248:251]\n"
"v_mfma_f32_16x16x32_bf16 a[252:255], a[48:51], v[24:27], a[252:255]\n"
"v_mfma_f32_16x16x32_bf16 a[168:171], a[60:63], a[24:27], a[168:171]\n"
"v_mfma_f32_16x16x32_bf16 a[200:203], a[64:67], a[24:27], a[200:203]\n"
"v_mfma_f32_16x16x32_bf16 a[232:235], a[68:71], a[24:27], a[232:235]\n"
"v_mfma_f32_16x16x32_bf16 a[236:239], a[68:71], a[28:31], a[236:239]\n"
"v_mfma_f32_16x16x32_bf16 a[204:207], a[64:67], a[28:31], a[204:207]\n"
"v_mfma_f32_16x16x32_bf16 a[172:175], a[60:63], a[28:31], a[172:175]\n"
"v_mfma_f32_16x16x32_bf16 v[252:255], a[56:59], a[28:31], v[252:255]\n"
"v_mfma_f32_16x16x32_bf16 v[220:223], a[52:55], a[28:31], v[220:223]\n"
"v_mfma_f32_16x16x32_bf16 v[188:191], a[48:51], a[28:31], v[188:191]\n"
"v_mfma_f32_16x16x32_bf16 v[156:159], a[28:31], a[44:47], v[156:159]\n"
"v_mfma_f32_16x16x32_bf16 v[124:127], a[28:31], a[40:43], v[124:127]\n"
"v_mfma_f32_16x16x32_bf16 v[92:95], a[36:39], a[28:31], v[92:95]\n"
"v_mfma_f32_16x16x32_bf16 v[60:63], a[32:35], a[28:31], v[60:63]\n"
"s_waitcnt vmcnt(10) lgkmcnt(0)\n"
"s_barrier\n"
"v_mfma_f32_16x16x32_bf16 v[32:35], a[104:107], a[72:75], v[32:35]\n"
"ds_read_b128 a[0:3], v6 offset:0\n"
"s_add_u32 m0, s39, 32768\n"
"v_mfma_f32_16x16x32_bf16 v[64:67], a[108:111], a[72:75], v[64:67]\n"
"v_mfma_f32_16x16x32_bf16 a[252:255], a[120:123], v[24:27], a[252:255]\n"
"buffer_load_dwordx4 v20, s[44:47], s36 offen offset:0 lds\n"
"s_add_u32 s36, s36, 15360\n"
"v_mfma_f32_16x16x32_bf16 v[96:99], a[72:75], a[112:115], v[96:99]\n"
"ds_read_b64_tr_b16 a[32:33], v8 offset:0\n"
"v_mov_b32_dpp v24, v24 row_shl:1 row_mask:0xf bank_mask:0xf bound_ctrl:1\n"
"v_mfma_f32_16x16x32_bf16 v[128:131], a[72:75], a[116:119], v[128:131]\n"
"v_mov_b32_dpp v25, v25 row_shl:1 row_mask:0xf bank_mask:0xf bound_ctrl:1\n"
"v_mfma_f32_16x16x32_bf16 v[160:163], a[120:123], a[72:75], v[160:163]\n"
"ds_read_b64_tr_b16 a[34:35], v8 offset:512\n"
"v_mov_b32_dpp v26, v26 row_shl:1 row_mask:0xf bank_mask:0xf bound_ctrl:1\n"
"v_mfma_f32_16x16x32_bf16 v[192:195], a[124:127], a[72:75], v[192:195]\n"
"v_mov_b32_dpp v27, v27 row_shl:1 row_mask:0xf bank_mask:0xf bound_ctrl:1\n"
"v_mfma_f32_16x16x32_bf16 v[224:227], a[128:131], a[72:75], v[224:227]\n"
"buffer_load_dwordx4 v21, s[44:47], s36 offen offset:1024 lds\n"
"s_add_u32 s36, s36, 15360\n"
"v_mfma_f32_16x16x32_bf16 a[144:147], a[132:135], a[72:75], a[144:147]\n"
"ds_read_b128 a[4:7], v6 offset:2048\n"
"v_mfma_f32_16x16x32_bf16 a[252:255], a[116:119], v[24:27], a[252:255]\n"
"v_mfma_f32_16x16x32_bf16 a[176:179], a[136:139], a[72:75], a[176:179]\n"
"v_mov_b32_dpp v24, v24 row_shl:1 row_mask:0xf bank_mask:0xf bound_ctrl:1\n"
"v_mfma_f32_16x16x32_bf16 a[208:211], a[140:143], a[72:75], a[208:211]\n"
"ds_read_b64_tr_b16 a[36:37], v9 offset:0\n"
"v_mov_b32_dpp v25, v25 row_shl:1 row_mask:0xf bank_mask:0xf bound_ctrl:1\n"
"v_mfma_f32_16x16x32_bf16 a[212:215], a[140:143], a[76:79], a[212:215]\n"
"v_mov_b32_dpp v26, v26 row_shl:1 row_mask:0xf bank_mask:0xf bound_ctrl:1\n"
"v_mfma_f32_16x16x32_bf16 a[180:183], a[136:139], a[76:79], a[180:183]\n"
"ds_read_b64_tr_b16 a[38:39], v9 offset:512\n"
"v_mov_b32_dpp v27, v27 row_shl:1 row_mask:0xf bank_mask:0xf bound_ctrl:1\n"
"v_mfma_f32_16x16x32_bf16 a[148:151], a[132:135], a[76:79], a[148:151]\n"
"buffer_load_dwordx4 v20, s[44:47], s36 offen offset:2048 lds\n"
"s_add_u32 s36, s36, 15360\n"
"v_mfma_f32_16x16x32_bf16 v[228:231], a[128:131], a[76:79], v[228:231]\n"
"v_mfma_f32_16x16x32_bf16 a[252:255], a[112:115], v[24:27], a[252:255]\n"
"v_mfma_f32_16x16x32_bf16 v[196:199], a[124:127], a[76:79], v[196:199]\n"
"ds_read_b64_tr_b16 a[40:41], v8 offset:1024\n"
"v_mov_b32_dpp v24, v24 row_shl:1 row_mask:0xf bank_mask:0xf bound_ctrl:1\n"
"v_mfma_f32_16x16x32_bf16 v[164:167], a[120:123], a[76:79], v[164:167]\n"
"v_mov_b32_dpp v25, v25 row_shl:1 row_mask:0xf bank_mask:0xf bound_ctrl:1\n"
"v_mfma_f32_16x16x32_bf16 v[132:135], a[76:79], a[116:119], v[132:135]\n"
"ds_read_b128 a[8:11], v6 offset:4096\n"
"v_mov_b32_dpp v26, v26 row_shl:1 row_mask:0xf bank_mask:0xf bound_ctrl:1\n"
"v_mfma_f32_16x16x32_bf16 v[100:103], a[76:79], a[112:115], v[100:103]\n"
"v_mov_b32_dpp v27, v27 row_shl:1 row_mask:0xf bank_mask:0xf bound_ctrl:1\n"
"v_mfma_f32_16x16x32_bf16 v[68:71], a[108:111], a[76:79], v[68:71]\n"
"ds_read_b64_tr_b16 a[42:43], v8 offset:1536\n"
"buffer_load_dwordx4 v21, s[44:47], s36 offen offset:3072 lds\n"
"s_add_u32 s36, s36, 19456\n"
"v_mfma_f32_16x16x32_bf16 v[36:39], a[104:107], a[76:79], v[36:39]\n"
"v_mfma_f32_16x16x32_bf16 a[252:255], a[108:111], v[24:27], a[252:255]\n"
"v_mfma_f32_16x16x32_bf16 v[40:43], a[104:107], a[80:83], v[40:43]\n"
"v_mov_b32_dpp v24, v24 row_shl:1 row_mask:0xf bank_mask:0xf bound_ctrl:1\n"
"v_mfma_f32_16x16x32_bf16 v[72:75], a[108:111], a[80:83], v[72:75]\n"
"ds_read_b64_tr_b16 a[44:45], v9 offset:1024\n"
"v_mov_b32_dpp v25, v25 row_shl:1 row_mask:0xf bank_mask:0xf bound_ctrl:1\n"
"v_mfma_f32_16x16x32_bf16 v[104:107], a[80:83], a[112:115], v[104:107]\n"
"v_mov_b32_dpp v26, v26 row_shl:1 row_mask:0xf bank_mask:0xf bound_ctrl:1\n"
"v_mfma_f32_16x16x32_bf16 v[136:139], a[80:83], a[116:119], v[136:139]\n"
"ds_read_b128 a[12:15], v6 offset:6144\n"
"v_mov_b32_dpp v27, v27 row_shl:1 row_mask:0xf bank_mask:0xf bound_ctrl:1\n"
"s_add_u32 m0, s39, 36864\n"
"v_mfma_f32_16x16x32_bf16 v[168:171], a[120:123], a[80:83], v[168:171]\n"
"buffer_load_dwordx4 v20, s[44:47], s36 offen offset:0 lds\n"
"s_add_u32 s36, s36, 15360\n"
"v_mfma_f32_16x16x32_bf16 v[200:203], a[124:127], a[80:83], v[200:203]\n"
"ds_read_b64_tr_b16 a[46:47], v9 offset:1536\n"
"v_mfma_f32_16x16x32_bf16 a[252:255], a[104:107], v[24:27], a[252:255]\n"
"v_mfma_f32_16x16x32_bf16 v[232:235], a[128:131], a[80:83], v[232:235]\n"
"v_mfma_f32_16x16x32_bf16 a[152:155], a[132:135], a[80:83], a[152:155]\n"
"v_mfma_f32_16x16x32_bf16 a[184:187], a[136:139], a[80:83], a[184:187]\n"
"ds_read_b64_tr_b16 a[48:49], v10 offset:0\n"
"v_mfma_f32_16x16x32_bf16 a[216:219], a[140:143], a[80:83], a[216:219]\n"
"v_mfma_f32_16x16x32_bf16 a[220:223], a[140:143], a[84:87], a[220:223]\n"
"ds_read_b64_tr_b16 a[50:51], v10 offset:768\n"
"buffer_load_dwordx4 v21, s[44:47], s36 offen offset:1024 lds\n"
"s_add_u32 s36, s36, 15360\n"
"v_mfma_f32_16x16x32_bf16 a[188:191], a[136:139], a[84:87], a[188:191]\n"
"v_mfma_f32_16x16x32_bf16 a[156:159], a[132:135], a[84:87], a[156:159]\n"
"v_mfma_f32_16x16x32_bf16 v[236:239], a[128:131], a[84:87], v[236:239]\n"
"ds_read_b128 a[16:19], v6 offset:8192\n"
"v_mfma_f32_16x16x32_bf16 v[204:207], a[124:127], a[84:87], v[204:207]\n"
"v_accvgpr_read_b32 v23, a252\n"
"v_mfma_f32_16x16x32_bf16 v[172:175], a[120:123], a[84:87], v[172:175]\n"
"ds_read_b64_tr_b16 a[52:53], v11 offset:0\n"
"v_cvt_pk_bf16_f32 v24, v23, v23\n"
"v_mfma_f32_16x16x32_bf16 v[140:143], a[84:87], a[116:119], v[140:143]\n"
"v_and_b32 v25, 0xffff0000, v24\n"
"buffer_load_dwordx4 v20, s[44:47], s36 offen offset:2048 lds\n"
"s_add_u32 s36, s36, 15360\n"
"v_mfma_f32_16x16x32_bf16 v[108:111], a[84:87], a[112:115], v[108:111]\n"
"ds_read_b64_tr_b16 a[54:55], v11 offset:768\n"
"v_sub_f32 v23, v23, v25\n"
"v_mfma_f32_16x16x32_bf16 v[76:79], a[108:111], a[84:87], v[76:79]\n"
"v_cvt_pk_bf16_f32 v25, v23, v23\n"
"v_mfma_f32_16x16x32_bf16 v[44:47], a[104:107], a[84:87], v[44:47]\n"
"buffer_store_dwordx2 v[24:25], v22, s[20:23], s3 offen offset:0 sc1\n"
"v_mfma_f32_16x16x32_bf16 v[48:51], a[104:107], a[88:91], v[48:51]\n"
"ds_read_b128 a[20:23], v6 offset:10240\n"
"v_accvgpr_read_b32 v23, a253\n"
"v_mfma_f32_16x16x32_bf16 v[80:83], a[108:111], a[88:91], v[80:83]\n"
"v_cvt_pk_bf16_f32 v26, v23, v23\n"
"v_mfma_f32_16x16x32_bf16 v[112:115], a[88:91], a[112:115], v[112:115]\n"
"ds_read_b64_tr_b16 a[56:57], v12 offset:0\n"
"v_and_b32 v27, 0xffff0000, v26\n"
"buffer_load_dwordx4 v21, s[44:47], s36 offen offset:3072 lds\n"
"s_sub_u32 s36, s36, 111488\n"
"v_mfma_f32_16x16x32_bf16 v[144:147], a[88:91], a[116:119], v[144:147]\n"
"v_sub_f32 v23, v23, v27\n"
"v_mfma_f32_16x16x32_bf16 v[176:179], a[120:123], a[88:91], v[176:179]\n"
"ds_read_b64_tr_b16 a[58:59], v12 offset:768\n"
"v_cvt_pk_bf16_f32 v27, v23, v23\n"
"v_mfma_f32_16x16x32_bf16 v[208:211], a[124:127], a[88:91], v[208:211]\n"
"buffer_store_dwordx2 v[26:27], v22, s[20:23], s3 offen offset:16 sc1\n"
"v_mfma_f32_16x16x32_bf16 v[240:243], a[128:131], a[88:91], v[240:243]\n"
"v_accvgpr_read_b32 v23, a254\n"
"v_mfma_f32_16x16x32_bf16 a[160:163], a[132:135], a[88:91], a[160:163]\n"
"ds_read_b64_tr_b16 a[60:61], v13 offset:0\n"
"v_cvt_pk_bf16_f32 v24, v23, v23\n"
"s_add_u32 m0, s40, 61440\n"
"v_mfma_f32_16x16x32_bf16 a[192:195], a[136:139], a[88:91], a[192:195]\n"
"v_and_b32 v25, 0xffff0000, v24\n"
"buffer_load_dwordx4 v1, s[24:27], s37 offen offset:0 lds\n"
"v_mfma_f32_16x16x32_bf16 a[224:227], a[140:143], a[88:91], a[224:227]\n"
"ds_read_b128 a[24:27], v6 offset:12288\n"
"v_sub_f32 v23, v23, v25\n"
"v_mfma_f32_16x16x32_bf16 a[228:231], a[140:143], a[92:95], a[228:231]\n"
"v_cvt_pk_bf16_f32 v25, v23, v23\n"
"v_mfma_f32_16x16x32_bf16 a[196:199], a[136:139], a[92:95], a[196:199]\n"
"ds_read_b64_tr_b16 a[62:63], v13 offset:768\n"
"buffer_store_dwordx2 v[24:25], v22, s[20:23], s3 offen offset:32 sc1\n"
"v_mfma_f32_16x16x32_bf16 a[164:167], a[132:135], a[92:95], a[164:167]\n"
"v_accvgpr_read_b32 v23, a255\n"
"v_mfma_f32_16x16x32_bf16 v[244:247], a[128:131], a[92:95], v[244:247]\n"
"v_cvt_pk_bf16_f32 v26, v23, v23\n"
"v_mfma_f32_16x16x32_bf16 v[212:215], a[124:127], a[92:95], v[212:215]\n"
"ds_read_b64_tr_b16 a[64:65], v14 offset:0\n"
"v_and_b32 v27, 0xffff0000, v26\n"
"buffer_load_dwordx4 v2, s[24:27], s37 offen offset:1024 lds\n"
"s_add_u32 s37, s37, 196608\n"
"v_mfma_f32_16x16x32_bf16 v[180:183], a[120:123], a[92:95], v[180:183]\n"
"v_sub_f32 v23, v23, v27\n"
"v_mfma_f32_16x16x32_bf16 v[148:151], a[92:95], a[116:119], v[148:151]\n"
"ds_read_b128 a[28:31], v6 offset:14336\n"
"v_cvt_pk_bf16_f32 v27, v23, v23\n"
"v_mfma_f32_16x16x32_bf16 v[116:119], a[92:95], a[112:115], v[116:119]\n"
"buffer_store_dwordx2 v[26:27], v22, s[20:23], s3 offen offset:48 sc1\n"
"v_mfma_f32_16x16x32_bf16 v[84:87], a[108:111], a[92:95], v[84:87]\n"
"ds_read_b64_tr_b16 a[66:67], v14 offset:768\n"
"v_mfma_f32_16x16x32_bf16 v[52:55], a[104:107], a[92:95], v[52:55]\n"
"v_mfma_f32_16x16x32_bf16 v[56:59], a[104:107], a[96:99], v[56:59]\n"
"buffer_load_dwordx4 v3, s[28:31], s38 offen offset:2048 lds\n"
"v_mfma_f32_16x16x32_bf16 v[88:91], a[108:111], a[96:99], v[88:91]\n"
"ds_read_b64_tr_b16 a[68:69], v15 offset:0\n"
"v_mfma_f32_16x16x32_bf16 v[120:123], a[96:99], a[112:115], v[120:123]\n"
"v_mfma_f32_16x16x32_bf16 v[152:155], a[96:99], a[116:119], v[152:155]\n"
"ds_read_b64_tr_b16 a[70:71], v15 offset:768\n"
"v_mfma_f32_16x16x32_bf16 v[184:187], a[120:123], a[96:99], v[184:187]\n"
"v_mfma_f32_16x16x32_bf16 v[216:219], a[124:127], a[96:99], v[216:219]\n"
"v_mfma_f32_16x16x32_bf16 v[248:251], a[128:131], a[96:99], v[248:251]\n"
"buffer_load_dwordx4 v4, s[28:31], s38 offen offset:3072 lds\n"
"v_mfma_f32_16x16x32_bf16 a[168:171], a[132:135], a[96:99], a[168:171]\n"
"v_mfma_f32_16x16x32_bf16 a[200:203], a[136:139], a[96:99], a[200:203]\n"
"v_mfma_f32_16x16x32_bf16 a[232:235], a[140:143], a[96:99], a[232:235]\n"
"v_mfma_f32_16x16x32_bf16 a[236:239], a[140:143], a[100:103], a[236:239]\n"
"v_mfma_f32_16x16x32_bf16 a[204:207], a[136:139], a[100:103], a[204:207]\n"
"s_sub_u32 s41, s41, 1\n"
"s_add_u32 m0, s40, 65536\n"
"v_mfma_f32_16x16x32_bf16 a[172:175], a[132:135], a[100:103], a[172:175]\n"
"buffer_load_dwordx4 v5, s[28:31], s38 offen offset:0 lds\n"
"s_add_u32 s38, s38, 229376\n"
"v_mfma_f32_16x16x32_bf16 v[252:255], a[128:131], a[100:103], v[252:255]\n"
"s_cmp_eq_u32 s2, s41\n"
"v_mfma_f32_16x16x32_bf16 v[220:223], a[124:127], a[100:103], v[220:223]\n"
"s_cselect_b32 s96, 6291456, 0\n"
"v_mfma_f32_16x16x32_bf16 v[188:191], a[120:123], a[100:103], v[188:191]\n"
"s_cselect_b32 s97, 7340032, 0\n"
"v_mfma_f32_16x16x32_bf16 v[156:159], a[100:103], a[116:119], v[156:159]\n"
"s_cselect_b32 s16, 2048, 0\n"
"v_mfma_f32_16x16x32_bf16 v[124:127], a[100:103], a[112:115], v[124:127]\n"
"s_sub_u32 s37, s37, s96\n"
"v_mfma_f32_16x16x32_bf16 v[92:95], a[108:111], a[100:103], v[92:95]\n"
"s_sub_u32 s38, s38, s97\n"
"v_mfma_f32_16x16x32_bf16 v[60:63], a[104:107], a[100:103], v[60:63]\n"
"s_sub_u32 s36, s36, s16\n"
"s_branch G1KV1_%=\n"
"SDKV3_%=:\n"
"s_waitcnt vmcnt(18) lgkmcnt(0)\n"
"s_barrier\n"
"v_mfma_f32_16x16x32_bf16 v[32:35], a[32:35], a[0:3], v[32:35]\n"
"ds_read_b128 a[72:75], v7 offset:0\n"
"v_and_b32 v23, 0xf0, v0\n"
"s_add_u32 m0, s40, 0\n"
"v_mfma_f32_16x16x32_bf16 v[64:67], a[36:39], a[0:3], v[64:67]\n"
"v_mov_b32 v25, 0x3f803f80\n"
"buffer_load_dwordx4 v1, s[24:27], s37 offen offset:0 lds\n"
"v_mfma_f32_16x16x32_bf16 v[96:99], a[0:3], a[40:43], v[96:99]\n"
"ds_read_b64_tr_b16 a[104:105], v8 offset:20480\n"
"s_cmp_le_u32 s41, 3\n"
"s_cselect_b32 s54, 7344128, 0\n"
"s_cselect_b32 s58, 2101248, 0\n"
"v_cmp_eq_u32 vcc, 0, v23\n"
"v_mfma_f32_16x16x32_bf16 v[128:131], a[0:3], a[44:47], v[128:131]\n"
"buffer_load_dword a240, v29, s[52:55], 0 offen\n"
"v_cndmask_b32 v24, 0, v25, vcc\n"
"v_mfma_f32_16x16x32_bf16 v[160:163], a[48:51], a[0:3], v[160:163]\n"
"ds_read_b64_tr_b16 a[106:107], v8 offset:20992\n"
"v_mov_b32 v25, v24\n"
"v_mfma_f32_16x16x32_bf16 v[192:195], a[52:55], a[0:3], v[192:195]\n"
"v_add_u32 v29, 2432, v29\n"
"v_mov_b32 v26, v24\n"
"v_mfma_f32_16x16x32_bf16 v[224:227], a[56:59], a[0:3], v[224:227]\n"
"v_mov_b32 v27, v24\n"
"v_mfma_f32_16x16x32_bf16 a[144:147], a[60:63], a[0:3], a[144:147]\n"
"ds_read_b128 a[76:79], v7 offset:2048\n"
"v_mfma_f32_16x16x32_bf16 a[176:179], a[64:67], a[0:3], a[176:179]\n"
"v_mfma_f32_16x16x32_bf16 a[208:211], a[68:71], a[0:3], a[208:211]\n"
"ds_read_b64_tr_b16 a[108:109], v9 offset:20480\n"
"v_mfma_f32_16x16x32_bf16 a[212:215], a[68:71], a[4:7], a[212:215]\n"
"v_mfma_f32_16x16x32_bf16 a[252:255], a[52:55], v[24:27], 0\n"
"v_mfma_f32_16x16x32_bf16 a[180:183], a[64:67], a[4:7], a[180:183]\n"
"ds_read_b64_tr_b16 a[110:111], v9 offset:20992\n"
"v_mfma_f32_16x16x32_bf16 a[148:151], a[60:63], a[4:7], a[148:151]\n"
"v_mfma_f32_16x16x32_bf16 v[228:231], a[56:59], a[4:7], v[228:231]\n"
"v_mfma_f32_16x16x32_bf16 v[196:199], a[52:55], a[4:7], v[196:199]\n"
"ds_read_b64_tr_b16 a[112:113], v8 offset:21504\n"
"v_mfma_f32_16x16x32_bf16 v[164:167], a[48:51], a[4:7], v[164:167]\n"
"v_mfma_f32_16x16x32_bf16 v[132:135], a[4:7], a[44:47], v[132:135]\n"
"ds_read_b128 a[80:83], v7 offset:4096\n"
"buffer_load_dwordx4 v2, s[24:27], s37 offen offset:1024 lds\n"
"s_add_u32 s37, s37, 196608\n"
"v_mfma_f32_16x16x32_bf16 v[100:103], a[4:7], a[40:43], v[100:103]\n"
"v_mfma_f32_16x16x32_bf16 v[68:71], a[36:39], a[4:7], v[68:71]\n"
"ds_read_b64_tr_b16 a[114:115], v8 offset:22016\n"
"v_mfma_f32_16x16x32_bf16 v[36:39], a[32:35], a[4:7], v[36:39]\n"
"v_mov_b32_dpp v24, v24 row_shr:1 row_mask:0xf bank_mask:0xf bound_ctrl:1\n"
"v_mfma_f32_16x16x32_bf16 v[40:43], a[32:35], a[8:11], v[40:43]\n"
"v_mov_b32_dpp v25, v25 row_shr:1 row_mask:0xf bank_mask:0xf bound_ctrl:1\n"
"v_mfma_f32_16x16x32_bf16 v[72:75], a[36:39], a[8:11], v[72:75]\n"
"ds_read_b64_tr_b16 a[116:117], v9 offset:21504\n"
"v_mov_b32_dpp v26, v26 row_shr:1 row_mask:0xf bank_mask:0xf bound_ctrl:1\n"
"v_mfma_f32_16x16x32_bf16 v[104:107], a[8:11], a[40:43], v[104:107]\n"
"v_mov_b32_dpp v27, v27 row_shr:1 row_mask:0xf bank_mask:0xf bound_ctrl:1\n"
"v_mfma_f32_16x16x32_bf16 v[136:139], a[8:11], a[44:47], v[136:139]\n"
"ds_read_b128 a[84:87], v7 offset:6144\n"
"v_mfma_f32_16x16x32_bf16 v[168:171], a[48:51], a[8:11], v[168:171]\n"
"v_mfma_f32_16x16x32_bf16 a[252:255], a[56:59], v[24:27], a[252:255]\n"
"v_mfma_f32_16x16x32_bf16 v[200:203], a[52:55], a[8:11], v[200:203]\n"
"ds_read_b64_tr_b16 a[118:119], v9 offset:22016\n"
"v_mfma_f32_16x16x32_bf16 v[232:235], a[56:59], a[8:11], v[232:235]\n"
"v_mfma_f32_16x16x32_bf16 a[152:155], a[60:63], a[8:11], a[152:155]\n"
"v_mfma_f32_16x16x32_bf16 a[184:187], a[64:67], a[8:11], a[184:187]\n"
"ds_read_b64_tr_b16 a[120:121], v10 offset:20480\n"
"v_mfma_f32_16x16x32_bf16 a[216:219], a[68:71], a[8:11], a[216:219]\n"
"v_mfma_f32_16x16x32_bf16 a[220:223], a[68:71], a[12:15], a[220:223]\n"
"ds_read_b64_tr_b16 a[122:123], v10 offset:21248\n"
"v_mfma_f32_16x16x32_bf16 a[188:191], a[64:67], a[12:15], a[188:191]\n"
"buffer_load_dwordx4 v3, s[28:31], s38 offen offset:2048 lds\n"
"v_mfma_f32_16x16x32_bf16 a[156:159], a[60:63], a[12:15], a[156:159]\n"
"v_mfma_f32_16x16x32_bf16 v[236:239], a[56:59], a[12:15], v[236:239]\n"
"ds_read_b128 a[88:91], v7 offset:8192\n"
"v_mov_b32_dpp v24, v24 row_shr:1 row_mask:0xf bank_mask:0xf bound_ctrl:1\n"
"v_mfma_f32_16x16x32_bf16 v[204:207], a[52:55], a[12:15], v[204:207]\n"
"v_mov_b32_dpp v25, v25 row_shr:1 row_mask:0xf bank_mask:0xf bound_ctrl:1\n"
"v_mfma_f32_16x16x32_bf16 v[172:175], a[48:51], a[12:15], v[172:175]\n"
"ds_read_b64_tr_b16 a[124:125], v11 offset:20480\n"
"v_mov_b32_dpp v26, v26 row_shr:1 row_mask:0xf bank_mask:0xf bound_ctrl:1\n"
"v_mfma_f32_16x16x32_bf16 v[140:143], a[12:15], a[44:47], v[140:143]\n"
"v_mov_b32_dpp v27, v27 row_shr:1 row_mask:0xf bank_mask:0xf bound_ctrl:1\n"
"v_mfma_f32_16x16x32_bf16 v[108:111], a[12:15], a[40:43], v[108:111]\n"
"ds_read_b64_tr_b16 a[126:127], v11 offset:21248\n"
"v_mfma_f32_16x16x32_bf16 v[76:79], a[36:39], a[12:15], v[76:79]\n"
"v_mfma_f32_16x16x32_bf16 a[252:255], a[60:63], v[24:27], a[252:255]\n"
"v_mfma_f32_16x16x32_bf16 v[44:47], a[32:35], a[12:15], v[44:47]\n"
"v_mfma_f32_16x16x32_bf16 v[48:51], a[32:35], a[16:19], v[48:51]\n"
"ds_read_b128 a[92:95], v7 offset:10240\n"
"buffer_load_dword a240, v30, s[56:59], 0 offen\n"
"v_mfma_f32_16x16x32_bf16 v[80:83], a[36:39], a[16:19], v[80:83]\n"
"v_mfma_f32_16x16x32_bf16 v[112:115], a[16:19], a[40:43], v[112:115]\n"
"ds_read_b64_tr_b16 a[128:129], v12 offset:20480\n"
"v_add_u32 v30, 704, v30\n"
"v_mfma_f32_16x16x32_bf16 v[144:147], a[16:19], a[44:47], v[144:147]\n"
"v_mfma_f32_16x16x32_bf16 v[176:179], a[48:51], a[16:19], v[176:179]\n"
"ds_read_b64_tr_b16 a[130:131], v12 offset:21248\n"
"v_mfma_f32_16x16x32_bf16 v[208:211], a[52:55], a[16:19], v[208:211]\n"
"v_mfma_f32_16x16x32_bf16 v[240:243], a[56:59], a[16:19], v[240:243]\n"
"v_mfma_f32_16x16x32_bf16 a[160:163], a[60:63], a[16:19], a[160:163]\n"
"ds_read_b64_tr_b16 a[132:133], v13 offset:20480\n"
"v_mov_b32_dpp v24, v24 row_shr:1 row_mask:0xf bank_mask:0xf bound_ctrl:1\n"
"buffer_load_dwordx4 v4, s[28:31], s38 offen offset:3072 lds\n"
"v_mfma_f32_16x16x32_bf16 a[192:195], a[64:67], a[16:19], a[192:195]\n"
"v_mov_b32_dpp v25, v25 row_shr:1 row_mask:0xf bank_mask:0xf bound_ctrl:1\n"
"v_mfma_f32_16x16x32_bf16 a[224:227], a[68:71], a[16:19], a[224:227]\n"
"ds_read_b128 a[96:99], v7 offset:12288\n"
"v_mov_b32_dpp v26, v26 row_shr:1 row_mask:0xf bank_mask:0xf bound_ctrl:1\n"
"v_mfma_f32_16x16x32_bf16 a[228:231], a[68:71], a[20:23], a[228:231]\n"
"v_mov_b32_dpp v27, v27 row_shr:1 row_mask:0xf bank_mask:0xf bound_ctrl:1\n"
"v_mfma_f32_16x16x32_bf16 a[196:199], a[64:67], a[20:23], a[196:199]\n"
"ds_read_b64_tr_b16 a[134:135], v13 offset:21248\n"
"v_mfma_f32_16x16x32_bf16 a[164:167], a[60:63], a[20:23], a[164:167]\n"
"v_mfma_f32_16x16x32_bf16 a[252:255], a[64:67], v[24:27], a[252:255]\n"
"v_mfma_f32_16x16x32_bf16 v[244:247], a[56:59], a[20:23], v[244:247]\n"
"v_mfma_f32_16x16x32_bf16 v[212:215], a[52:55], a[20:23], v[212:215]\n"
"ds_read_b64_tr_b16 a[136:137], v14 offset:20480\n"
"v_mfma_f32_16x16x32_bf16 v[180:183], a[48:51], a[20:23], v[180:183]\n"
"v_mfma_f32_16x16x32_bf16 v[148:151], a[20:23], a[44:47], v[148:151]\n"
"ds_read_b128 a[100:103], v7 offset:14336\n"
"v_mfma_f32_16x16x32_bf16 v[116:119], a[20:23], a[40:43], v[116:119]\n"
"v_mfma_f32_16x16x32_bf16 v[84:87], a[36:39], a[20:23], v[84:87]\n"
"ds_read_b64_tr_b16 a[138:139], v14 offset:21248\n"
"v_mfma_f32_16x16x32_bf16 v[52:55], a[32:35], a[20:23], v[52:55]\n"
"v_mfma_f32_16x16x32_bf16 v[56:59], a[32:35], a[24:27], v[56:59]\n"
"v_mfma_f32_16x16x32_bf16 v[88:91], a[36:39], a[24:27], v[88:91]\n"
"ds_read_b64_tr_b16 a[140:141], v15 offset:20480\n"
"v_mov_b32_dpp v24, v24 row_shr:1 row_mask:0xf bank_mask:0xf bound_ctrl:1\n"
"s_add_u32 m0, s40, 4096\n"
"v_mfma_f32_16x16x32_bf16 v[120:123], a[24:27], a[40:43], v[120:123]\n"
"v_mov_b32_dpp v25, v25 row_shr:1 row_mask:0xf bank_mask:0xf bound_ctrl:1\n"
"buffer_load_dwordx4 v5, s[28:31], s38 offen offset:0 lds\n"
"s_add_u32 s38, s38, 229376\n"
"v_mfma_f32_16x16x32_bf16 v[152:155], a[24:27], a[44:47], v[152:155]\n"
"ds_read_b64_tr_b16 a[142:143], v15 offset:21248\n"
"v_mov_b32_dpp v26, v26 row_shr:1 row_mask:0xf bank_mask:0xf bound_ctrl:1\n"
"v_mfma_f32_16x16x32_bf16 v[184:187], a[48:51], a[24:27], v[184:187]\n"
"v_mov_b32_dpp v27, v27 row_shr:1 row_mask:0xf bank_mask:0xf bound_ctrl:1\n"
"v_mfma_f32_16x16x32_bf16 v[216:219], a[52:55], a[24:27], v[216:219]\n"
"v_mfma_f32_16x16x32_bf16 v[248:251], a[56:59], a[24:27], v[248:251]\n"
"v_mfma_f32_16x16x32_bf16 a[252:255], a[68:71], v[24:27], a[252:255]\n"
"v_mfma_f32_16x16x32_bf16 a[168:171], a[60:63], a[24:27], a[168:171]\n"
"v_mfma_f32_16x16x32_bf16 a[200:203], a[64:67], a[24:27], a[200:203]\n"
"v_mfma_f32_16x16x32_bf16 a[232:235], a[68:71], a[24:27], a[232:235]\n"
"v_mfma_f32_16x16x32_bf16 a[236:239], a[68:71], a[28:31], a[236:239]\n"
"v_mfma_f32_16x16x32_bf16 a[204:207], a[64:67], a[28:31], a[204:207]\n"
"v_mfma_f32_16x16x32_bf16 a[172:175], a[60:63], a[28:31], a[172:175]\n"
"v_mfma_f32_16x16x32_bf16 v[252:255], a[56:59], a[28:31], v[252:255]\n"
"v_mfma_f32_16x16x32_bf16 v[220:223], a[52:55], a[28:31], v[220:223]\n"
"v_mfma_f32_16x16x32_bf16 v[188:191], a[48:51], a[28:31], v[188:191]\n"
"v_mfma_f32_16x16x32_bf16 v[156:159], a[28:31], a[44:47], v[156:159]\n"
"v_mfma_f32_16x16x32_bf16 v[124:127], a[28:31], a[40:43], v[124:127]\n"
"v_mfma_f32_16x16x32_bf16 v[92:95], a[36:39], a[28:31], v[92:95]\n"
"v_mfma_f32_16x16x32_bf16 v[60:63], a[32:35], a[28:31], v[60:63]\n"
"s_waitcnt vmcnt(12) lgkmcnt(0)\n"
"s_barrier\n"
"v_mfma_f32_16x16x32_bf16 v[32:35], a[104:107], a[72:75], v[32:35]\n"
"ds_read_b128 a[0:3], v6 offset:32768\n"
"s_add_u32 m0, s39, 0\n"
"v_mfma_f32_16x16x32_bf16 v[64:67], a[108:111], a[72:75], v[64:67]\n"
"buffer_load_dwordx4 v20, s[44:47], s36 offen offset:0 lds\n"
"s_add_u32 s36, s36, 15360\n"
"v_mfma_f32_16x16x32_bf16 v[96:99], a[72:75], a[112:115], v[96:99]\n"
"ds_read_b64_tr_b16 a[32:33], v8 offset:40960\n"
"v_mfma_f32_16x16x32_bf16 v[128:131], a[72:75], a[116:119], v[128:131]\n"
"v_mfma_f32_16x16x32_bf16 v[160:163], a[120:123], a[72:75], v[160:163]\n"
"ds_read_b64_tr_b16 a[34:35], v8 offset:41472\n"
"v_mfma_f32_16x16x32_bf16 v[192:195], a[124:127], a[72:75], v[192:195]\n"
"v_mfma_f32_16x16x32_bf16 v[224:227], a[128:131], a[72:75], v[224:227]\n"
"buffer_load_dwordx4 v21, s[44:47], s36 offen offset:1024 lds\n"
"s_add_u32 s36, s36, 15360\n"
"v_mfma_f32_16x16x32_bf16 a[144:147], a[132:135], a[72:75], a[144:147]\n"
"ds_read_b128 a[4:7], v6 offset:34816\n"
"v_mfma_f32_16x16x32_bf16 a[176:179], a[136:139], a[72:75], a[176:179]\n"
"v_mfma_f32_16x16x32_bf16 a[208:211], a[140:143], a[72:75], a[208:211]\n"
"ds_read_b64_tr_b16 a[36:37], v9 offset:40960\n"
"v_mfma_f32_16x16x32_bf16 a[212:215], a[140:143], a[76:79], a[212:215]\n"
"v_mfma_f32_16x16x32_bf16 a[252:255], a[140:143], v[24:27], a[252:255]\n"
"v_mfma_f32_16x16x32_bf16 a[180:183], a[136:139], a[76:79], a[180:183]\n"
"ds_read_b64_tr_b16 a[38:39], v9 offset:41472\n"
"v_mfma_f32_16x16x32_bf16 a[148:151], a[132:135], a[76:79], a[148:151]\n"
"buffer_load_dwordx4 v20, s[44:47], s36 offen offset:2048 lds\n"
"s_add_u32 s36, s36, 15360\n"
"v_mfma_f32_16x16x32_bf16 v[228:231], a[128:131], a[76:79], v[228:231]\n"
"v_mfma_f32_16x16x32_bf16 v[196:199], a[124:127], a[76:79], v[196:199]\n"
"ds_read_b64_tr_b16 a[40:41], v8 offset:41984\n"
"v_mfma_f32_16x16x32_bf16 v[164:167], a[120:123], a[76:79], v[164:167]\n"
"v_mfma_f32_16x16x32_bf16 v[132:135], a[76:79], a[116:119], v[132:135]\n"
"ds_read_b128 a[8:11], v6 offset:36864\n"
"v_mfma_f32_16x16x32_bf16 v[100:103], a[76:79], a[112:115], v[100:103]\n"
"v_mfma_f32_16x16x32_bf16 v[68:71], a[108:111], a[76:79], v[68:71]\n"
"ds_read_b64_tr_b16 a[42:43], v8 offset:42496\n"
"buffer_load_dwordx4 v21, s[44:47], s36 offen offset:3072 lds\n"
"s_add_u32 s36, s36, 19456\n"
"v_mfma_f32_16x16x32_bf16 v[36:39], a[104:107], a[76:79], v[36:39]\n"
"v_mov_b32_dpp v24, v24 row_shl:1 row_mask:0xf bank_mask:0xf bound_ctrl:1\n"
"v_mfma_f32_16x16x32_bf16 v[40:43], a[104:107], a[80:83], v[40:43]\n"
"v_mov_b32_dpp v25, v25 row_shl:1 row_mask:0xf bank_mask:0xf bound_ctrl:1\n"
"v_mfma_f32_16x16x32_bf16 v[72:75], a[108:111], a[80:83], v[72:75]\n"
"ds_read_b64_tr_b16 a[44:45], v9 offset:41984\n"
"v_mov_b32_dpp v26, v26 row_shl:1 row_mask:0xf bank_mask:0xf bound_ctrl:1\n"
"v_mfma_f32_16x16x32_bf16 v[104:107], a[80:83], a[112:115], v[104:107]\n"
"v_mov_b32_dpp v27, v27 row_shl:1 row_mask:0xf bank_mask:0xf bound_ctrl:1\n"
"v_mfma_f32_16x16x32_bf16 v[136:139], a[80:83], a[116:119], v[136:139]\n"
"ds_read_b128 a[12:15], v6 offset:38912\n"
"s_add_u32 m0, s39, 4096\n"
"v_mfma_f32_16x16x32_bf16 v[168:171], a[120:123], a[80:83], v[168:171]\n"
"v_mfma_f32_16x16x32_bf16 a[252:255], a[136:139], v[24:27], a[252:255]\n"
"buffer_load_dwordx4 v20, s[44:47], s36 offen offset:0 lds\n"
"s_add_u32 s36, s36, 15360\n"
"v_mfma_f32_16x16x32_bf16 v[200:203], a[124:127], a[80:83], v[200:203]\n"
"ds_read_b64_tr_b16 a[46:47], v9 offset:42496\n"
"v_mfma_f32_16x16x32_bf16 v[232:235], a[128:131], a[80:83], v[232:235]\n"
"v_mfma_f32_16x16x32_bf16 a[152:155], a[132:135], a[80:83], a[152:155]\n"
"v_mfma_f32_16x16x32_bf16 a[184:187], a[136:139], a[80:83], a[184:187]\n"
"ds_read_b64_tr_b16 a[48:49], v10 offset:40960\n"
"v_mfma_f32_16x16x32_bf16 a[216:219], a[140:143], a[80:83], a[216:219]\n"
"v_mfma_f32_16x16x32_bf16 a[220:223], a[140:143], a[84:87], a[220:223]\n"
"ds_read_b64_tr_b16 a[50:51], v10 offset:41728\n"
"buffer_load_dwordx4 v21, s[44:47], s36 offen offset:1024 lds\n"
"s_add_u32 s36, s36, 15360\n"
"v_mfma_f32_16x16x32_bf16 a[188:191], a[136:139], a[84:87], a[188:191]\n"
"v_mfma_f32_16x16x32_bf16 a[156:159], a[132:135], a[84:87], a[156:159]\n"
"v_mfma_f32_16x16x32_bf16 v[236:239], a[128:131], a[84:87], v[236:239]\n"
"ds_read_b128 a[16:19], v6 offset:40960\n"
"v_mov_b32_dpp v24, v24 row_shl:1 row_mask:0xf bank_mask:0xf bound_ctrl:1\n"
"v_mfma_f32_16x16x32_bf16 v[204:207], a[124:127], a[84:87], v[204:207]\n"
"v_mov_b32_dpp v25, v25 row_shl:1 row_mask:0xf bank_mask:0xf bound_ctrl:1\n"
"v_mfma_f32_16x16x32_bf16 v[172:175], a[120:123], a[84:87], v[172:175]\n"
"ds_read_b64_tr_b16 a[52:53], v11 offset:40960\n"
"v_mov_b32_dpp v26, v26 row_shl:1 row_mask:0xf bank_mask:0xf bound_ctrl:1\n"
"v_mfma_f32_16x16x32_bf16 v[140:143], a[84:87], a[116:119], v[140:143]\n"
"v_mov_b32_dpp v27, v27 row_shl:1 row_mask:0xf bank_mask:0xf bound_ctrl:1\n"
"buffer_load_dwordx4 v20, s[44:47], s36 offen offset:2048 lds\n"
"s_add_u32 s36, s36, 15360\n"
"v_mfma_f32_16x16x32_bf16 v[108:111], a[84:87], a[112:115], v[108:111]\n"
"ds_read_b64_tr_b16 a[54:55], v11 offset:41728\n"
"v_mfma_f32_16x16x32_bf16 v[76:79], a[108:111], a[84:87], v[76:79]\n"
"v_mfma_f32_16x16x32_bf16 a[252:255], a[132:135], v[24:27], a[252:255]\n"
"v_mfma_f32_16x16x32_bf16 v[44:47], a[104:107], a[84:87], v[44:47]\n"
"v_mfma_f32_16x16x32_bf16 v[48:51], a[104:107], a[88:91], v[48:51]\n"
"ds_read_b128 a[20:23], v6 offset:43008\n"
"v_mfma_f32_16x16x32_bf16 v[80:83], a[108:111], a[88:91], v[80:83]\n"
"v_mfma_f32_16x16x32_bf16 v[112:115], a[88:91], a[112:115], v[112:115]\n"
"ds_read_b64_tr_b16 a[56:57], v12 offset:40960\n"
"buffer_load_dwordx4 v21, s[44:47], s36 offen offset:3072 lds\n"
"s_sub_u32 s36, s36, 111488\n"
"v_mfma_f32_16x16x32_bf16 v[144:147], a[88:91], a[116:119], v[144:147]\n"
"v_mfma_f32_16x16x32_bf16 v[176:179], a[120:123], a[88:91], v[176:179]\n"
"ds_read_b64_tr_b16 a[58:59], v12 offset:41728\n"
"v_mfma_f32_16x16x32_bf16 v[208:211], a[124:127], a[88:91], v[208:211]\n"
"v_mfma_f32_16x16x32_bf16 v[240:243], a[128:131], a[88:91], v[240:243]\n"
"v_mfma_f32_16x16x32_bf16 a[160:163], a[132:135], a[88:91], a[160:163]\n"
"ds_read_b64_tr_b16 a[60:61], v13 offset:40960\n"
"v_mov_b32_dpp v24, v24 row_shl:1 row_mask:0xf bank_mask:0xf bound_ctrl:1\n"
"s_add_u32 m0, s40, 20480\n"
"v_mfma_f32_16x16x32_bf16 a[192:195], a[136:139], a[88:91], a[192:195]\n"
"v_mov_b32_dpp v25, v25 row_shl:1 row_mask:0xf bank_mask:0xf bound_ctrl:1\n"
"buffer_load_dwordx4 v1, s[24:27], s37 offen offset:0 lds\n"
"v_mfma_f32_16x16x32_bf16 a[224:227], a[140:143], a[88:91], a[224:227]\n"
"ds_read_b128 a[24:27], v6 offset:45056\n"
"v_mov_b32_dpp v26, v26 row_shl:1 row_mask:0xf bank_mask:0xf bound_ctrl:1\n"
"v_mfma_f32_16x16x32_bf16 a[228:231], a[140:143], a[92:95], a[228:231]\n"
"v_mov_b32_dpp v27, v27 row_shl:1 row_mask:0xf bank_mask:0xf bound_ctrl:1\n"
"v_mfma_f32_16x16x32_bf16 a[196:199], a[136:139], a[92:95], a[196:199]\n"
"ds_read_b64_tr_b16 a[62:63], v13 offset:41728\n"
"v_mfma_f32_16x16x32_bf16 a[164:167], a[132:135], a[92:95], a[164:167]\n"
"v_mfma_f32_16x16x32_bf16 a[252:255], a[128:131], v[24:27], a[252:255]\n"
"v_mfma_f32_16x16x32_bf16 v[244:247], a[128:131], a[92:95], v[244:247]\n"
"v_mfma_f32_16x16x32_bf16 v[212:215], a[124:127], a[92:95], v[212:215]\n"
"ds_read_b64_tr_b16 a[64:65], v14 offset:40960\n"
"buffer_load_dwordx4 v2, s[24:27], s37 offen offset:1024 lds\n"
"s_add_u32 s37, s37, 196608\n"
"v_mfma_f32_16x16x32_bf16 v[180:183], a[120:123], a[92:95], v[180:183]\n"
"v_mfma_f32_16x16x32_bf16 v[148:151], a[92:95], a[116:119], v[148:151]\n"
"ds_read_b128 a[28:31], v6 offset:47104\n"
"v_mfma_f32_16x16x32_bf16 v[116:119], a[92:95], a[112:115], v[116:119]\n"
"v_mfma_f32_16x16x32_bf16 v[84:87], a[108:111], a[92:95], v[84:87]\n"
"ds_read_b64_tr_b16 a[66:67], v14 offset:41728\n"
"v_mfma_f32_16x16x32_bf16 v[52:55], a[104:107], a[92:95], v[52:55]\n"
"v_mfma_f32_16x16x32_bf16 v[56:59], a[104:107], a[96:99], v[56:59]\n"
"buffer_load_dwordx4 v3, s[28:31], s38 offen offset:2048 lds\n"
"v_mfma_f32_16x16x32_bf16 v[88:91], a[108:111], a[96:99], v[88:91]\n"
"ds_read_b64_tr_b16 a[68:69], v15 offset:40960\n"
"v_mov_b32_dpp v24, v24 row_shl:1 row_mask:0xf bank_mask:0xf bound_ctrl:1\n"
"v_mfma_f32_16x16x32_bf16 v[120:123], a[96:99], a[112:115], v[120:123]\n"
"v_mov_b32_dpp v25, v25 row_shl:1 row_mask:0xf bank_mask:0xf bound_ctrl:1\n"
"v_mfma_f32_16x16x32_bf16 v[152:155], a[96:99], a[116:119], v[152:155]\n"
"ds_read_b64_tr_b16 a[70:71], v15 offset:41728\n"
"v_mov_b32_dpp v26, v26 row_shl:1 row_mask:0xf bank_mask:0xf bound_ctrl:1\n"
"v_mfma_f32_16x16x32_bf16 v[184:187], a[120:123], a[96:99], v[184:187]\n"
"v_mov_b32_dpp v27, v27 row_shl:1 row_mask:0xf bank_mask:0xf bound_ctrl:1\n"
"v_mfma_f32_16x16x32_bf16 v[216:219], a[124:127], a[96:99], v[216:219]\n"
"v_mfma_f32_16x16x32_bf16 v[248:251], a[128:131], a[96:99], v[248:251]\n"
"v_mfma_f32_16x16x32_bf16 a[252:255], a[124:127], v[24:27], a[252:255]\n"
"buffer_load_dwordx4 v4, s[28:31], s38 offen offset:3072 lds\n"
"v_mfma_f32_16x16x32_bf16 a[168:171], a[132:135], a[96:99], a[168:171]\n"
"v_mfma_f32_16x16x32_bf16 a[200:203], a[136:139], a[96:99], a[200:203]\n"
"v_and_b32 v23, 0xf0, v0\n"
"v_mfma_f32_16x16x32_bf16 a[232:235], a[140:143], a[96:99], a[232:235]\n"
"v_lshrrev_b32 v22, 8, v0\n"
"v_mfma_f32_16x16x32_bf16 a[236:239], a[140:143], a[100:103], a[236:239]\n"
"v_add_lshl_u32 v22, v22, v23, 6\n"
"v_mfma_f32_16x16x32_bf16 a[204:207], a[136:139], a[100:103], a[204:207]\n"
"v_cmp_lt_u32 vcc, 0x4f, v23\n"
"s_add_u32 m0, s40, 24576\n"
"v_mfma_f32_16x16x32_bf16 a[172:175], a[132:135], a[100:103], a[172:175]\n"
"v_bfrev_b32 v23, 1\n"
"buffer_load_dwordx4 v5, s[28:31], s38 offen offset:0 lds\n"
"s_add_u32 s38, s38, 229376\n"
"v_mfma_f32_16x16x32_bf16 v[252:255], a[128:131], a[100:103], v[252:255]\n"
"s_nop 0\n"
"v_mfma_f32_16x16x32_bf16 v[220:223], a[124:127], a[100:103], v[220:223]\n"
"v_cndmask_b32 v22, v22, v23, vcc\n"
"v_mfma_f32_16x16x32_bf16 v[188:191], a[120:123], a[100:103], v[188:191]\n"
"v_mfma_f32_16x16x32_bf16 v[156:159], a[100:103], a[116:119], v[156:159]\n"
"v_mfma_f32_16x16x32_bf16 v[124:127], a[100:103], a[112:115], v[124:127]\n"
"v_mfma_f32_16x16x32_bf16 v[92:95], a[108:111], a[100:103], v[92:95]\n"
"v_mfma_f32_16x16x32_bf16 v[60:63], a[104:107], a[100:103], v[60:63]\n"
"s_waitcnt vmcnt(20) lgkmcnt(0)\n"
"s_barrier\n"
"v_mfma_f32_16x16x32_bf16 v[32:35], a[32:35], a[0:3], v[32:35]\n"
"ds_read_b128 a[72:75], v7 offset:32768\n"
"s_add_u32 m0, s40, 40960\n"
"v_mfma_f32_16x16x32_bf16 v[64:67], a[36:39], a[0:3], v[64:67]\n"
"buffer_load_dwordx4 v1, s[24:27], s37 offen offset:0 lds\n"
"v_mfma_f32_16x16x32_bf16 v[96:99], a[0:3], a[40:43], v[96:99]\n"
"ds_read_b64_tr_b16 a[104:105], v8 offset:61440\n"
"v_mfma_f32_16x16x32_bf16 v[128:131], a[0:3], a[44:47], v[128:131]\n"
"v_mfma_f32_16x16x32_bf16 v[160:163], a[48:51], a[0:3], v[160:163]\n"
"ds_read_b64_tr_b16 a[106:107], v8 offset:61952\n"
"v_mfma_f32_16x16x32_bf16 v[192:195], a[52:55], a[0:3], v[192:195]\n"
"v_mfma_f32_16x16x32_bf16 v[224:227], a[56:59], a[0:3], v[224:227]\n"
"v_mfma_f32_16x16x32_bf16 a[144:147], a[60:63], a[0:3], a[144:147]\n"
"ds_read_b128 a[76:79], v7 offset:34816\n"
"v_mfma_f32_16x16x32_bf16 a[176:179], a[64:67], a[0:3], a[176:179]\n"
"v_mfma_f32_16x16x32_bf16 a[208:211], a[68:71], a[0:3], a[208:211]\n"
"ds_read_b64_tr_b16 a[108:109], v9 offset:61440\n"
"v_mfma_f32_16x16x32_bf16 a[212:215], a[68:71], a[4:7], a[212:215]\n"
"v_mfma_f32_16x16x32_bf16 a[252:255], a[52:55], v[24:27], a[252:255]\n"
"v_mfma_f32_16x16x32_bf16 a[180:183], a[64:67], a[4:7], a[180:183]\n"
"ds_read_b64_tr_b16 a[110:111], v9 offset:61952\n"
"v_mfma_f32_16x16x32_bf16 a[148:151], a[60:63], a[4:7], a[148:151]\n"
"v_mfma_f32_16x16x32_bf16 v[228:231], a[56:59], a[4:7], v[228:231]\n"
"v_mfma_f32_16x16x32_bf16 v[196:199], a[52:55], a[4:7], v[196:199]\n"
"ds_read_b64_tr_b16 a[112:113], v8 offset:62464\n"
"v_mfma_f32_16x16x32_bf16 v[164:167], a[48:51], a[4:7], v[164:167]\n"
"v_mfma_f32_16x16x32_bf16 v[132:135], a[4:7], a[44:47], v[132:135]\n"
"ds_read_b128 a[80:83], v7 offset:36864\n"
"buffer_load_dwordx4 v2, s[24:27], s37 offen offset:1024 lds\n"
"s_add_u32 s37, s37, 196608\n"
"v_mfma_f32_16x16x32_bf16 v[100:103], a[4:7], a[40:43], v[100:103]\n"
"v_mfma_f32_16x16x32_bf16 v[68:71], a[36:39], a[4:7], v[68:71]\n"
"ds_read_b64_tr_b16 a[114:115], v8 offset:62976\n"
"v_mfma_f32_16x16x32_bf16 v[36:39], a[32:35], a[4:7], v[36:39]\n"
"v_mov_b32_dpp v24, v24 row_shr:1 row_mask:0xf bank_mask:0xf bound_ctrl:1\n"
"v_mfma_f32_16x16x32_bf16 v[40:43], a[32:35], a[8:11], v[40:43]\n"
"v_mov_b32_dpp v25, v25 row_shr:1 row_mask:0xf bank_mask:0xf bound_ctrl:1\n"
"v_mfma_f32_16x16x32_bf16 v[72:75], a[36:39], a[8:11], v[72:75]\n"
"ds_read_b64_tr_b16 a[116:117], v9 offset:62464\n"
"v_mov_b32_dpp v26, v26 row_shr:1 row_mask:0xf bank_mask:0xf bound_ctrl:1\n"
"v_mfma_f32_16x16x32_bf16 v[104:107], a[8:11], a[40:43], v[104:107]\n"
"v_mov_b32_dpp v27, v27 row_shr:1 row_mask:0xf bank_mask:0xf bound_ctrl:1\n"
"v_mfma_f32_16x16x32_bf16 v[136:139], a[8:11], a[44:47], v[136:139]\n"
"ds_read_b128 a[84:87], v7 offset:38912\n"
"v_mfma_f32_16x16x32_bf16 v[168:171], a[48:51], a[8:11], v[168:171]\n"
"v_mfma_f32_16x16x32_bf16 a[252:255], a[56:59], v[24:27], a[252:255]\n"
"v_mfma_f32_16x16x32_bf16 v[200:203], a[52:55], a[8:11], v[200:203]\n"
"ds_read_b64_tr_b16 a[118:119], v9 offset:62976\n"
"v_mfma_f32_16x16x32_bf16 v[232:235], a[56:59], a[8:11], v[232:235]\n"
"v_mfma_f32_16x16x32_bf16 a[152:155], a[60:63], a[8:11], a[152:155]\n"
"v_mfma_f32_16x16x32_bf16 a[184:187], a[64:67], a[8:11], a[184:187]\n"
"ds_read_b64_tr_b16 a[120:121], v10 offset:61440\n"
"v_mfma_f32_16x16x32_bf16 a[216:219], a[68:71], a[8:11], a[216:219]\n"
"v_mfma_f32_16x16x32_bf16 a[220:223], a[68:71], a[12:15], a[220:223]\n"
"ds_read_b64_tr_b16 a[122:123], v10 offset:62208\n"
"v_mfma_f32_16x16x32_bf16 a[188:191], a[64:67], a[12:15], a[188:191]\n"
"buffer_load_dwordx4 v3, s[28:31], s38 offen offset:2048 lds\n"
"v_mfma_f32_16x16x32_bf16 a[156:159], a[60:63], a[12:15], a[156:159]\n"
"v_mfma_f32_16x16x32_bf16 v[236:239], a[56:59], a[12:15], v[236:239]\n"
"ds_read_b128 a[88:91], v7 offset:40960\n"
"v_mov_b32_dpp v24, v24 row_shr:1 row_mask:0xf bank_mask:0xf bound_ctrl:1\n"
"v_mfma_f32_16x16x32_bf16 v[204:207], a[52:55], a[12:15], v[204:207]\n"
"v_mov_b32_dpp v25, v25 row_shr:1 row_mask:0xf bank_mask:0xf bound_ctrl:1\n"
"v_mfma_f32_16x16x32_bf16 v[172:175], a[48:51], a[12:15], v[172:175]\n"
"ds_read_b64_tr_b16 a[124:125], v11 offset:61440\n"
"v_mov_b32_dpp v26, v26 row_shr:1 row_mask:0xf bank_mask:0xf bound_ctrl:1\n"
"v_mfma_f32_16x16x32_bf16 v[140:143], a[12:15], a[44:47], v[140:143]\n"
"v_mov_b32_dpp v27, v27 row_shr:1 row_mask:0xf bank_mask:0xf bound_ctrl:1\n"
"v_mfma_f32_16x16x32_bf16 v[108:111], a[12:15], a[40:43], v[108:111]\n"
"ds_read_b64_tr_b16 a[126:127], v11 offset:62208\n"
"v_mfma_f32_16x16x32_bf16 v[76:79], a[36:39], a[12:15], v[76:79]\n"
"v_mfma_f32_16x16x32_bf16 a[252:255], a[60:63], v[24:27], a[252:255]\n"
"v_mfma_f32_16x16x32_bf16 v[44:47], a[32:35], a[12:15], v[44:47]\n"
"v_mfma_f32_16x16x32_bf16 v[48:51], a[32:35], a[16:19], v[48:51]\n"
"ds_read_b128 a[92:95], v7 offset:43008\n"
"v_mfma_f32_16x16x32_bf16 v[80:83], a[36:39], a[16:19], v[80:83]\n"
"v_mfma_f32_16x16x32_bf16 v[112:115], a[16:19], a[40:43], v[112:115]\n"
"ds_read_b64_tr_b16 a[128:129], v12 offset:61440\n"
"v_mfma_f32_16x16x32_bf16 v[144:147], a[16:19], a[44:47], v[144:147]\n"
"v_mfma_f32_16x16x32_bf16 v[176:179], a[48:51], a[16:19], v[176:179]\n"
"ds_read_b64_tr_b16 a[130:131], v12 offset:62208\n"
"v_mfma_f32_16x16x32_bf16 v[208:211], a[52:55], a[16:19], v[208:211]\n"
"v_mfma_f32_16x16x32_bf16 v[240:243], a[56:59], a[16:19], v[240:243]\n"
"v_mfma_f32_16x16x32_bf16 a[160:163], a[60:63], a[16:19], a[160:163]\n"
"ds_read_b64_tr_b16 a[132:133], v13 offset:61440\n"
"v_mov_b32_dpp v24, v24 row_shr:1 row_mask:0xf bank_mask:0xf bound_ctrl:1\n"
"buffer_load_dwordx4 v4, s[28:31], s38 offen offset:3072 lds\n"
"v_mfma_f32_16x16x32_bf16 a[192:195], a[64:67], a[16:19], a[192:195]\n"
"v_mov_b32_dpp v25, v25 row_shr:1 row_mask:0xf bank_mask:0xf bound_ctrl:1\n"
"v_mfma_f32_16x16x32_bf16 a[224:227], a[68:71], a[16:19], a[224:227]\n"
"ds_read_b128 a[96:99], v7 offset:45056\n"
"v_mov_b32_dpp v26, v26 row_shr:1 row_mask:0xf bank_mask:0xf bound_ctrl:1\n"
"v_mfma_f32_16x16x32_bf16 a[228:231], a[68:71], a[20:23], a[228:231]\n"
"v_mov_b32_dpp v27, v27 row_shr:1 row_mask:0xf bank_mask:0xf bound_ctrl:1\n"
"v_mfma_f32_16x16x32_bf16 a[196:199], a[64:67], a[20:23], a[196:199]\n"
"ds_read_b64_tr_b16 a[134:135], v13 offset:62208\n"
"v_mfma_f32_16x16x32_bf16 a[164:167], a[60:63], a[20:23], a[164:167]\n"
"v_mfma_f32_16x16x32_bf16 a[252:255], a[64:67], v[24:27], a[252:255]\n"
"v_mfma_f32_16x16x32_bf16 v[244:247], a[56:59], a[20:23], v[244:247]\n"
"v_mfma_f32_16x16x32_bf16 v[212:215], a[52:55], a[20:23], v[212:215]\n"
"ds_read_b64_tr_b16 a[136:137], v14 offset:61440\n"
"v_mfma_f32_16x16x32_bf16 v[180:183], a[48:51], a[20:23], v[180:183]\n"
"v_mfma_f32_16x16x32_bf16 v[148:151], a[20:23], a[44:47], v[148:151]\n"
"ds_read_b128 a[100:103], v7 offset:47104\n"
"v_mfma_f32_16x16x32_bf16 v[116:119], a[20:23], a[40:43], v[116:119]\n"
"v_mfma_f32_16x16x32_bf16 v[84:87], a[36:39], a[20:23], v[84:87]\n"
"ds_read_b64_tr_b16 a[138:139], v14 offset:62208\n"
"v_mfma_f32_16x16x32_bf16 v[52:55], a[32:35], a[20:23], v[52:55]\n"
"v_mfma_f32_16x16x32_bf16 v[56:59], a[32:35], a[24:27], v[56:59]\n"
"v_mfma_f32_16x16x32_bf16 v[88:91], a[36:39], a[24:27], v[88:91]\n"
"ds_read_b64_tr_b16 a[140:141], v15 offset:61440\n"
"v_mov_b32_dpp v24, v24 row_shr:1 row_mask:0xf bank_mask:0xf bound_ctrl:1\n"
"s_add_u32 m0, s40, 45056\n"
"v_mfma_f32_16x16x32_bf16 v[120:123], a[24:27], a[40:43], v[120:123]\n"
"v_mov_b32_dpp v25, v25 row_shr:1 row_mask:0xf bank_mask:0xf bound_ctrl:1\n"
"buffer_load_dwordx4 v5, s[28:31], s38 offen offset:0 lds\n"
"s_add_u32 s38, s38, 229376\n"
"v_mfma_f32_16x16x32_bf16 v[152:155], a[24:27], a[44:47], v[152:155]\n"
"ds_read_b64_tr_b16 a[142:143], v15 offset:62208\n"
"v_mov_b32_dpp v26, v26 row_shr:1 row_mask:0xf bank_mask:0xf bound_ctrl:1\n"
"v_mfma_f32_16x16x32_bf16 v[184:187], a[48:51], a[24:27], v[184:187]\n"
"v_mov_b32_dpp v27, v27 row_shr:1 row_mask:0xf bank_mask:0xf bound_ctrl:1\n"
"v_mfma_f32_16x16x32_bf16 v[216:219], a[52:55], a[24:27], v[216:219]\n"
"v_mfma_f32_16x16x32_bf16 v[248:251], a[56:59], a[24:27], v[248:251]\n"
"v_mfma_f32_16x16x32_bf16 a[252:255], a[68:71], v[24:27], a[252:255]\n"
"v_mfma_f32_16x16x32_bf16 a[168:171], a[60:63], a[24:27], a[168:171]\n"
"v_mfma_f32_16x16x32_bf16 a[200:203], a[64:67], a[24:27], a[200:203]\n"
"v_mfma_f32_16x16x32_bf16 a[232:235], a[68:71], a[24:27], a[232:235]\n"
"v_mfma_f32_16x16x32_bf16 a[236:239], a[68:71], a[28:31], a[236:239]\n"
"v_mfma_f32_16x16x32_bf16 a[204:207], a[64:67], a[28:31], a[204:207]\n"
"v_mfma_f32_16x16x32_bf16 a[172:175], a[60:63], a[28:31], a[172:175]\n"
"v_mfma_f32_16x16x32_bf16 v[252:255], a[56:59], a[28:31], v[252:255]\n"
"v_mfma_f32_16x16x32_bf16 v[220:223], a[52:55], a[28:31], v[220:223]\n"
"v_mfma_f32_16x16x32_bf16 v[188:191], a[48:51], a[28:31], v[188:191]\n"
"v_mfma_f32_16x16x32_bf16 v[156:159], a[28:31], a[44:47], v[156:159]\n"
"v_mfma_f32_16x16x32_bf16 v[124:127], a[28:31], a[40:43], v[124:127]\n"
"v_mfma_f32_16x16x32_bf16 v[92:95], a[36:39], a[28:31], v[92:95]\n"
"v_mfma_f32_16x16x32_bf16 v[60:63], a[32:35], a[28:31], v[60:63]\n"
"s_waitcnt vmcnt(10) lgkmcnt(0)\n"
"s_barrier\n"
"v_mfma_f32_16x16x32_bf16 v[32:35], a[104:107], a[72:75], v[32:35]\n"
"ds_read_b128 a[0:3], v6 offset:0\n"
"s_add_u32 m0, s39, 32768\n"
"v_mfma_f32_16x16x32_bf16 v[64:67], a[108:111], a[72:75], v[64:67]\n"
"v_mfma_f32_16x16x32_bf16 a[252:255], a[140:143], v[24:27], a[252:255]\n"
"buffer_load_dwordx4 v20, s[44:47], s36 offen offset:0 lds\n"
"s_add_u32 s36, s36, 15360\n"
"v_mfma_f32_16x16x32_bf16 v[96:99], a[72:75], a[112:115], v[96:99]\n"
"ds_read_b64_tr_b16 a[32:33], v8 offset:0\n"
"v_mov_b32_dpp v24, v24 row_shl:1 row_mask:0xf bank_mask:0xf bound_ctrl:1\n"
"v_mfma_f32_16x16x32_bf16 v[128:131], a[72:75], a[116:119], v[128:131]\n"
"v_mov_b32_dpp v25, v25 row_shl:1 row_mask:0xf bank_mask:0xf bound_ctrl:1\n"
"v_mfma_f32_16x16x32_bf16 v[160:163], a[120:123], a[72:75], v[160:163]\n"
"ds_read_b64_tr_b16 a[34:35], v8 offset:512\n"
"v_mov_b32_dpp v26, v26 row_shl:1 row_mask:0xf bank_mask:0xf bound_ctrl:1\n"
"v_mfma_f32_16x16x32_bf16 v[192:195], a[124:127], a[72:75], v[192:195]\n"
"v_mov_b32_dpp v27, v27 row_shl:1 row_mask:0xf bank_mask:0xf bound_ctrl:1\n"
"v_mfma_f32_16x16x32_bf16 v[224:227], a[128:131], a[72:75], v[224:227]\n"
"buffer_load_dwordx4 v21, s[44:47], s36 offen offset:1024 lds\n"
"s_add_u32 s36, s36, 15360\n"
"v_mfma_f32_16x16x32_bf16 a[144:147], a[132:135], a[72:75], a[144:147]\n"
"ds_read_b128 a[4:7], v6 offset:2048\n"
"v_mfma_f32_16x16x32_bf16 a[252:255], a[136:139], v[24:27], a[252:255]\n"
"v_mfma_f32_16x16x32_bf16 a[176:179], a[136:139], a[72:75], a[176:179]\n"
"v_mov_b32_dpp v24, v24 row_shl:1 row_mask:0xf bank_mask:0xf bound_ctrl:1\n"
"v_mfma_f32_16x16x32_bf16 a[208:211], a[140:143], a[72:75], a[208:211]\n"
"ds_read_b64_tr_b16 a[36:37], v9 offset:0\n"
"v_mov_b32_dpp v25, v25 row_shl:1 row_mask:0xf bank_mask:0xf bound_ctrl:1\n"
"v_mfma_f32_16x16x32_bf16 a[212:215], a[140:143], a[76:79], a[212:215]\n"
"v_mov_b32_dpp v26, v26 row_shl:1 row_mask:0xf bank_mask:0xf bound_ctrl:1\n"
"v_mfma_f32_16x16x32_bf16 a[180:183], a[136:139], a[76:79], a[180:183]\n"
"ds_read_b64_tr_b16 a[38:39], v9 offset:512\n"
"v_mov_b32_dpp v27, v27 row_shl:1 row_mask:0xf bank_mask:0xf bound_ctrl:1\n"
"v_mfma_f32_16x16x32_bf16 a[148:151], a[132:135], a[76:79], a[148:151]\n"
"buffer_load_dwordx4 v20, s[44:47], s36 offen offset:2048 lds\n"
"s_add_u32 s36, s36, 15360\n"
"v_mfma_f32_16x16x32_bf16 v[228:231], a[128:131], a[76:79], v[228:231]\n"
"v_mfma_f32_16x16x32_bf16 a[252:255], a[132:135], v[24:27], a[252:255]\n"
"v_mfma_f32_16x16x32_bf16 v[196:199], a[124:127], a[76:79], v[196:199]\n"
"ds_read_b64_tr_b16 a[40:41], v8 offset:1024\n"
"v_mov_b32_dpp v24, v24 row_shl:1 row_mask:0xf bank_mask:0xf bound_ctrl:1\n"
"v_mfma_f32_16x16x32_bf16 v[164:167], a[120:123], a[76:79], v[164:167]\n"
"v_mov_b32_dpp v25, v25 row_shl:1 row_mask:0xf bank_mask:0xf bound_ctrl:1\n"
"v_mfma_f32_16x16x32_bf16 v[132:135], a[76:79], a[116:119], v[132:135]\n"
"ds_read_b128 a[8:11], v6 offset:4096\n"
"v_mov_b32_dpp v26, v26 row_shl:1 row_mask:0xf bank_mask:0xf bound_ctrl:1\n"
"v_mfma_f32_16x16x32_bf16 v[100:103], a[76:79], a[112:115], v[100:103]\n"
"v_mov_b32_dpp v27, v27 row_shl:1 row_mask:0xf bank_mask:0xf bound_ctrl:1\n"
"v_mfma_f32_16x16x32_bf16 v[68:71], a[108:111], a[76:79], v[68:71]\n"
"ds_read_b64_tr_b16 a[42:43], v8 offset:1536\n"
"buffer_load_dwordx4 v21, s[44:47], s36 offen offset:3072 lds\n"
"s_add_u32 s36, s36, 19456\n"
"v_mfma_f32_16x16x32_bf16 v[36:39], a[104:107], a[76:79], v[36:39]\n"
"v_mfma_f32_16x16x32_bf16 a[252:255], a[128:131], v[24:27], a[252:255]\n"
"v_mfma_f32_16x16x32_bf16 v[40:43], a[104:107], a[80:83], v[40:43]\n"
"v_mov_b32_dpp v24, v24 row_shl:1 row_mask:0xf bank_mask:0xf bound_ctrl:1\n"
"v_mfma_f32_16x16x32_bf16 v[72:75], a[108:111], a[80:83], v[72:75]\n"
"ds_read_b64_tr_b16 a[44:45], v9 offset:1024\n"
"v_mov_b32_dpp v25, v25 row_shl:1 row_mask:0xf bank_mask:0xf bound_ctrl:1\n"
"v_mfma_f32_16x16x32_bf16 v[104:107], a[80:83], a[112:115], v[104:107]\n"
"v_mov_b32_dpp v26, v26 row_shl:1 row_mask:0xf bank_mask:0xf bound_ctrl:1\n"
"v_mfma_f32_16x16x32_bf16 v[136:139], a[80:83], a[116:119], v[136:139]\n"
"ds_read_b128 a[12:15], v6 offset:6144\n"
"v_mov_b32_dpp v27, v27 row_shl:1 row_mask:0xf bank_mask:0xf bound_ctrl:1\n"
"s_add_u32 m0, s39, 36864\n"
"v_mfma_f32_16x16x32_bf16 v[168:171], a[120:123], a[80:83], v[168:171]\n"
"buffer_load_dwordx4 v20, s[44:47], s36 offen offset:0 lds\n"
"s_add_u32 s36, s36, 15360\n"
"v_mfma_f32_16x16x32_bf16 v[200:203], a[124:127], a[80:83], v[200:203]\n"
"ds_read_b64_tr_b16 a[46:47], v9 offset:1536\n"
"v_mfma_f32_16x16x32_bf16 a[252:255], a[124:127], v[24:27], a[252:255]\n"
"v_mfma_f32_16x16x32_bf16 v[232:235], a[128:131], a[80:83], v[232:235]\n"
"v_mfma_f32_16x16x32_bf16 a[152:155], a[132:135], a[80:83], a[152:155]\n"
"v_mfma_f32_16x16x32_bf16 a[184:187], a[136:139], a[80:83], a[184:187]\n"
"ds_read_b64_tr_b16 a[48:49], v10 offset:0\n"
"v_mfma_f32_16x16x32_bf16 a[216:219], a[140:143], a[80:83], a[216:219]\n"
"v_mfma_f32_16x16x32_bf16 a[220:223], a[140:143], a[84:87], a[220:223]\n"
"ds_read_b64_tr_b16 a[50:51], v10 offset:768\n"
"buffer_load_dwordx4 v21, s[44:47], s36 offen offset:1024 lds\n"
"s_add_u32 s36, s36, 15360\n"
"v_mfma_f32_16x16x32_bf16 a[188:191], a[136:139], a[84:87], a[188:191]\n"
"v_mfma_f32_16x16x32_bf16 a[156:159], a[132:135], a[84:87], a[156:159]\n"
"v_mfma_f32_16x16x32_bf16 v[236:239], a[128:131], a[84:87], v[236:239]\n"
"ds_read_b128 a[16:19], v6 offset:8192\n"
"v_mfma_f32_16x16x32_bf16 v[204:207], a[124:127], a[84:87], v[204:207]\n"
"v_accvgpr_read_b32 v23, a252\n"
"v_mfma_f32_16x16x32_bf16 v[172:175], a[120:123], a[84:87], v[172:175]\n"
"ds_read_b64_tr_b16 a[52:53], v11 offset:0\n"
"v_cvt_pk_bf16_f32 v24, v23, v23\n"
"v_mfma_f32_16x16x32_bf16 v[140:143], a[84:87], a[116:119], v[140:143]\n"
"v_and_b32 v25, 0xffff0000, v24\n"
"buffer_load_dwordx4 v20, s[44:47], s36 offen offset:2048 lds\n"
"s_add_u32 s36, s36, 15360\n"
"v_mfma_f32_16x16x32_bf16 v[108:111], a[84:87], a[112:115], v[108:111]\n"
"ds_read_b64_tr_b16 a[54:55], v11 offset:768\n"
"v_sub_f32 v23, v23, v25\n"
"v_mfma_f32_16x16x32_bf16 v[76:79], a[108:111], a[84:87], v[76:79]\n"
"v_cvt_pk_bf16_f32 v25, v23, v23\n"
"v_mfma_f32_16x16x32_bf16 v[44:47], a[104:107], a[84:87], v[44:47]\n"
"buffer_store_dwordx2 v[24:25], v22, s[20:23], s3 offen offset:0 sc1\n"
"v_mfma_f32_16x16x32_bf16 v[48:51], a[104:107], a[88:91], v[48:51]\n"
"ds_read_b128 a[20:23], v6 offset:10240\n"
"v_accvgpr_read_b32 v23, a253\n"
"v_mfma_f32_16x16x32_bf16 v[80:83], a[108:111], a[88:91], v[80:83]\n"
"v_cvt_pk_bf16_f32 v26, v23, v23\n"
"v_mfma_f32_16x16x32_bf16 v[112:115], a[88:91], a[112:115], v[112:115]\n"
"ds_read_b64_tr_b16 a[56:57], v12 offset:0\n"
"v_and_b32 v27, 0xffff0000, v26\n"
"buffer_load_dwordx4 v21, s[44:47], s36 offen offset:3072 lds\n"
"s_sub_u32 s36, s36, 111488\n"
"v_mfma_f32_16x16x32_bf16 v[144:147], a[88:91], a[116:119], v[144:147]\n"
"v_sub_f32 v23, v23, v27\n"
"v_mfma_f32_16x16x32_bf16 v[176:179], a[120:123], a[88:91], v[176:179]\n"
"ds_read_b64_tr_b16 a[58:59], v12 offset:768\n"
"v_cvt_pk_bf16_f32 v27, v23, v23\n"
"v_mfma_f32_16x16x32_bf16 v[208:211], a[124:127], a[88:91], v[208:211]\n"
"buffer_store_dwordx2 v[26:27], v22, s[20:23], s3 offen offset:16 sc1\n"
"v_mfma_f32_16x16x32_bf16 v[240:243], a[128:131], a[88:91], v[240:243]\n"
"v_accvgpr_read_b32 v23, a254\n"
"v_mfma_f32_16x16x32_bf16 a[160:163], a[132:135], a[88:91], a[160:163]\n"
"ds_read_b64_tr_b16 a[60:61], v13 offset:0\n"
"v_cvt_pk_bf16_f32 v24, v23, v23\n"
"s_add_u32 m0, s40, 61440\n"
"v_mfma_f32_16x16x32_bf16 a[192:195], a[136:139], a[88:91], a[192:195]\n"
"v_and_b32 v25, 0xffff0000, v24\n"
"buffer_load_dwordx4 v1, s[24:27], s37 offen offset:0 lds\n"
"v_mfma_f32_16x16x32_bf16 a[224:227], a[140:143], a[88:91], a[224:227]\n"
"ds_read_b128 a[24:27], v6 offset:12288\n"
"v_sub_f32 v23, v23, v25\n"
"v_mfma_f32_16x16x32_bf16 a[228:231], a[140:143], a[92:95], a[228:231]\n"
"v_cvt_pk_bf16_f32 v25, v23, v23\n"
"v_mfma_f32_16x16x32_bf16 a[196:199], a[136:139], a[92:95], a[196:199]\n"
"ds_read_b64_tr_b16 a[62:63], v13 offset:768\n"
"buffer_store_dwordx2 v[24:25], v22, s[20:23], s3 offen offset:32 sc1\n"
"v_mfma_f32_16x16x32_bf16 a[164:167], a[132:135], a[92:95], a[164:167]\n"
"v_accvgpr_read_b32 v23, a255\n"
"v_mfma_f32_16x16x32_bf16 v[244:247], a[128:131], a[92:95], v[244:247]\n"
"v_cvt_pk_bf16_f32 v26, v23, v23\n"
"v_mfma_f32_16x16x32_bf16 v[212:215], a[124:127], a[92:95], v[212:215]\n"
"ds_read_b64_tr_b16 a[64:65], v14 offset:0\n"
"v_and_b32 v27, 0xffff0000, v26\n"
"buffer_load_dwordx4 v2, s[24:27], s37 offen offset:1024 lds\n"
"s_add_u32 s37, s37, 196608\n"
"v_mfma_f32_16x16x32_bf16 v[180:183], a[120:123], a[92:95], v[180:183]\n"
"v_sub_f32 v23, v23, v27\n"
"v_mfma_f32_16x16x32_bf16 v[148:151], a[92:95], a[116:119], v[148:151]\n"
"ds_read_b128 a[28:31], v6 offset:14336\n"
"v_cvt_pk_bf16_f32 v27, v23, v23\n"
"v_mfma_f32_16x16x32_bf16 v[116:119], a[92:95], a[112:115], v[116:119]\n"
"buffer_store_dwordx2 v[26:27], v22, s[20:23], s3 offen offset:48 sc1\n"
"v_mfma_f32_16x16x32_bf16 v[84:87], a[108:111], a[92:95], v[84:87]\n"
"ds_read_b64_tr_b16 a[66:67], v14 offset:768\n"
"v_mfma_f32_16x16x32_bf16 v[52:55], a[104:107], a[92:95], v[52:55]\n"
"v_mfma_f32_16x16x32_bf16 v[56:59], a[104:107], a[96:99], v[56:59]\n"
"buffer_load_dwordx4 v3, s[28:31], s38 offen offset:2048 lds\n"
"v_mfma_f32_16x16x32_bf16 v[88:91], a[108:111], a[96:99], v[88:91]\n"
"ds_read_b64_tr_b16 a[68:69], v15 offset:0\n"
"v_mfma_f32_16x16x32_bf16 v[120:123], a[96:99], a[112:115], v[120:123]\n"
"v_mfma_f32_16x16x32_bf16 v[152:155], a[96:99], a[116:119], v[152:155]\n"
"ds_read_b64_tr_b16 a[70:71], v15 offset:768\n"
"v_mfma_f32_16x16x32_bf16 v[184:187], a[120:123], a[96:99], v[184:187]\n"
"v_mfma_f32_16x16x32_bf16 v[216:219], a[124:127], a[96:99], v[216:219]\n"
"v_mfma_f32_16x16x32_bf16 v[248:251], a[128:131], a[96:99], v[248:251]\n"
"buffer_load_dwordx4 v4, s[28:31], s38 offen offset:3072 lds\n"
"v_mfma_f32_16x16x32_bf16 a[168:171], a[132:135], a[96:99], a[168:171]\n"
"v_mfma_f32_16x16x32_bf16 a[200:203], a[136:139], a[96:99], a[200:203]\n"
"v_mfma_f32_16x16x32_bf16 a[232:235], a[140:143], a[96:99], a[232:235]\n"
"v_mfma_f32_16x16x32_bf16 a[236:239], a[140:143], a[100:103], a[236:239]\n"
"v_mfma_f32_16x16x32_bf16 a[204:207], a[136:139], a[100:103], a[204:207]\n"
"s_sub_u32 s41, s41, 1\n"
"s_add_u32 m0, s40, 65536\n"
"v_mfma_f32_16x16x32_bf16 a[172:175], a[132:135], a[100:103], a[172:175]\n"
"buffer_load_dwordx4 v5, s[28:31], s38 offen offset:0 lds\n"
"s_add_u32 s38, s38, 229376\n"
"v_mfma_f32_16x16x32_bf16 v[252:255], a[128:131], a[100:103], v[252:255]\n"
"s_cmp_eq_u32 s2, s41\n"
"v_mfma_f32_16x16x32_bf16 v[220:223], a[124:127], a[100:103], v[220:223]\n"
"s_cselect_b32 s96, 6291456, 0\n"
"v_mfma_f32_16x16x32_bf16 v[188:191], a[120:123], a[100:103], v[188:191]\n"
"s_cselect_b32 s97, 7340032, 0\n"
"v_mfma_f32_16x16x32_bf16 v[156:159], a[100:103], a[116:119], v[156:159]\n"
"s_cselect_b32 s16, 2048, 0\n"
"v_mfma_f32_16x16x32_bf16 v[124:127], a[100:103], a[112:115], v[124:127]\n"
"s_sub_u32 s37, s37, s96\n"
"v_mfma_f32_16x16x32_bf16 v[92:95], a[108:111], a[100:103], v[92:95]\n"
"s_sub_u32 s38, s38, s97\n"
"v_mfma_f32_16x16x32_bf16 v[60:63], a[104:107], a[100:103], v[60:63]\n"
"s_sub_u32 s36, s36, s16\n"
"s_branch G1KV1_%=\n"
"SPKV5_%=:\n"
"s_mov_b32 s94, 1048576\n"
"LPKV7_%=:\n"
"buffer_load_dwordx4 v[24:27], v0, s[20:23], s65 offen sc1\n"
"s_waitcnt vmcnt(0)\n"
"v_cmp_ne_u64 vcc, s[90:91], v[24:25]\n"
"v_cmp_ne_u64_e64 s[96:97], s[90:91], v[26:27]\n"
"s_nop 0\n"
"s_or_b64 vcc, vcc, s[96:97]\n"
"s_cbranch_vccz BKKV6_%=\n"
"s_sub_u32 s94, s94, 1\n"
"s_cbranch_scc0 LPKV7_%=\n"
"s_branch BKKV6_%=\n"
"SPKV8_%=:\n"
"s_mov_b32 s94, 1048576\n"
"LPKV10_%=:\n"
"buffer_load_dwordx4 v[20:23], v0, s[20:23], s5 offen sc1\n"
"s_waitcnt vmcnt(0)\n"
"v_cmp_ne_u64 vcc, s[90:91], v[20:21]\n"
"v_cmp_ne_u64_e64 s[96:97], s[90:91], v[22:23]\n"
"s_nop 0\n"
"s_or_b64 vcc, vcc, s[96:97]\n"
"s_cbranch_vccz BKKV9_%=\n"
"s_sub_u32 s94, s94, 1\n"
"s_cbranch_scc0 LPKV10_%=\n"
"s_branch BKKV9_%=\n"
"SPKV11_%=:\n"
"s_mov_b32 s94, 1048576\n"
"LPKV13_%=:\n"
"buffer_load_dwordx2 v[250:251], v9, s[20:23], s75 offen sc1\n"
"s_waitcnt vmcnt(0)\n"
"v_cmp_ne_u64 vcc, s[90:91], v[250:251]\n"
"s_cbranch_vccz BKKV12_%=\n"
"s_sub_u32 s94, s94, 1\n"
"s_cbranch_scc0 LPKV13_%=\n"
"s_branch BKKV12_%=\n"
"SPKV14_%=:\n"
"s_mov_b32 s94, 1048576\n"
"LPKV16_%=:\n"
"buffer_load_dwordx2 v[250:251], v8, s[20:23], s88 offen sc1\n"
"s_waitcnt vmcnt(0)\n"
"v_cmp_ne_u64 vcc, s[90:91], v[250:251]\n"
"s_cbranch_vccz BKKV15_%=\n"
"s_sub_u32 s94, s94, 1\n"
"s_cbranch_scc0 LPKV16_%=\n"
"s_branch BKKV15_%=\n"
"LXKV17_%=:\n"
"s_mov_b32 s94, 1048576\n"
"LLKV19_%=:\n"
"buffer_load_dwordx2 v[14:15], v9, s[20:23], s69 offen sc1\n"
"s_waitcnt vmcnt(0)\n"
"v_cmp_ne_u64 vcc, s[90:91], v[14:15]\n"
"s_cbranch_vccz LOKV20_%=\n"
"s_sub_u32 s94, s94, 1\n"
"s_cbranch_scc0 LLKV19_%=\n"
"LOKV20_%=:\n"
"s_sub_u32 s72, s72, 114688\n"
"s_add_u32 m0, s76, 0\n"
"s_nop 0\n"
"buffer_load_dwordx4 v0, s[20:23], s72 offen offset:0 sc1 lds\n"
"buffer_load_dwordx4 v0, s[20:23], s72 offen offset:1024 sc1 lds\n"
"buffer_load_dwordx4 v0, s[20:23], s72 offen offset:2048 sc1 lds\n"
"buffer_load_dwordx4 v0, s[20:23], s72 offen offset:3072 sc1 lds\n"
"s_add_u32 s72, s72, 16384\n"
"s_add_u32 m0, s76, 16384\n"
"s_nop 0\n"
"buffer_load_dwordx4 v0, s[20:23], s72 offen offset:0 sc1 lds\n"
"buffer_load_dwordx4 v0, s[20:23], s72 offen offset:1024 sc1 lds\n"
"buffer_load_dwordx4 v0, s[20:23], s72 offen offset:2048 sc1 lds\n"
"buffer_load_dwordx4 v0, s[20:23], s72 offen offset:3072 sc1 lds\n"
"s_add_u32 s72, s72, 65536\n"
"s_add_u32 m0, s76, 32768\n"
"s_nop 0\n"
"buffer_load_dwordx4 v0, s[20:23], s72 offen offset:0 sc1 lds\n"
"buffer_load_dwordx4 v0, s[20:23], s72 offen offset:1024 sc1 lds\n"
"buffer_load_dwordx4 v0, s[20:23], s72 offen offset:2048 sc1 lds\n"
"buffer_load_dwordx4 v0, s[20:23], s72 offen offset:3072 sc1 lds\n"
"s_add_u32 s72, s72, 16384\n"
"s_add_u32 m0, s76, 49152\n"
"s_nop 0\n"
"buffer_load_dwordx4 v0, s[20:23], s72 offen offset:0 sc1 lds\n"
"buffer_load_dwordx4 v0, s[20:23], s72 offen offset:1024 sc1 lds\n"
"buffer_load_dwordx4 v0, s[20:23], s72 offen offset:2048 sc1 lds\n"
"buffer_load_dwordx4 v0, s[20:23], s72 offen offset:3072 sc1 lds\n"
"s_add_u32 s72, s72, 16384\n"
"s_waitcnt vmcnt(0)\n"
"s_branch LBKV18_%=\n"
"SMKV21_%=:\n"
"buffer_load_dword a240, v16, s[56:59], s0 offen offset:0\n"
"s_nop 15\n"
"s_nop 7\n"
"v_cvt_pk_bf16_f32 v160, v160, v161\n"
"v_cvt_pk_bf16_f32 v161, v162, v163\n"
"v_cvt_pk_bf16_f32 v162, v164, v165\n"
"v_cvt_pk_bf16_f32 v163, v166, v167\n"
"buffer_store_dwordx4 v[160:163], v0, s[20:23], s85 offen offset:0 sc1\n"
"v_cvt_pk_bf16_f32 v164, v168, v169\n"
"v_cvt_pk_bf16_f32 v165, v170, v171\n"
"v_cvt_pk_bf16_f32 v166, v172, v173\n"
"v_cvt_pk_bf16_f32 v167, v174, v175\n"
"buffer_store_dwordx4 v[164:167], v0, s[20:23], s85 offen offset:1024 sc1\n"
"v_cvt_pk_bf16_f32 v168, v176, v177\n"
"v_cvt_pk_bf16_f32 v169, v178, v179\n"
"v_cvt_pk_bf16_f32 v170, v180, v181\n"
"v_cvt_pk_bf16_f32 v171, v182, v183\n"
"buffer_store_dwordx4 v[168:171], v0, s[20:23], s85 offen offset:2048 sc1\n"
"v_cvt_pk_bf16_f32 v172, v184, v185\n"
"v_cvt_pk_bf16_f32 v173, v186, v187\n"
"v_cvt_pk_bf16_f32 v174, v188, v189\n"
"v_cvt_pk_bf16_f32 v175, v190, v191\n"
"buffer_store_dwordx4 v[172:175], v0, s[20:23], s85 offen offset:3072 sc1\n"
"v_mov_b32 v8, 0\n"
"s_mov_b32 s3, 0\n"
"s_mov_b32 s10, 0\n"
"s_waitcnt vmcnt(0)\n"
"s_mov_b64 exec, 1\n"
"buffer_store_dwordx2 v[2:3], off, s[20:23], s87 sc1\n"
"s_mov_b64 exec, -1\n"
"s_mov_b32 s94, 1048576\n"
"SLKV23_%=:\n"
"buffer_load_dwordx2 v[250:251], v10, s[20:23], s84 offen sc1\n"
"buffer_load_dwordx2 v[14:15], v8, s[20:23], s88 offen sc1\n"
"s_waitcnt vmcnt(0)\n"
"v_cmp_ne_u64 vcc, s[90:91], v[250:251]\n"
"s_cbranch_vccz SOKV24_%=\n"
"v_cmp_ne_u64 vcc, s[90:91], v[14:15]\n"
"s_cmp_lg_u32 s3, 0\n"
"s_cbranch_scc1 SKKV25_%=\n"
"s_cbranch_vccnz SKKV25_%=\n"
"buffer_load_dword a240, v17, s[20:23], s86 offen offset:0 sc1\n"
"s_mov_b32 s3, 1\n"
"SKKV25_%=:\n"
"s_sub_u32 s94, s94, 1\n"
"s_cbranch_scc0 SLKV23_%=\n"
"SOKV24_%=:\n"
"s_add_u32 m0, s76, 0\n"
"s_nop 0\n"
"buffer_load_dwordx4 v0, s[20:23], s72 offen offset:0 sc1 lds\n"
"buffer_load_dwordx4 v0, s[20:23], s72 offen offset:1024 sc1 lds\n"
"s_add_u32 s72, s72, 16384\n"
"s_add_u32 m0, s76, 16384\n"
"s_nop 0\n"
"buffer_load_dwordx4 v0, s[20:23], s72 offen offset:0 sc1 lds\n"
"buffer_load_dwordx4 v0, s[20:23], s72 offen offset:1024 sc1 lds\n"
"s_add_u32 s72, s72, 16384\n"
"s_add_u32 m0, s76, 32768\n"
"s_nop 0\n"
"buffer_load_dwordx4 v0, s[20:23], s72 offen offset:0 sc1 lds\n"
"buffer_load_dwordx4 v0, s[20:23], s72 offen offset:1024 sc1 lds\n"
"s_add_u32 s72, s72, 16384\n"
"s_add_u32 m0, s76, 49152\n"
"s_nop 0\n"
"buffer_load_dwordx4 v0, s[20:23], s72 offen offset:0 sc1 lds\n"
"buffer_load_dwordx4 v0, s[20:23], s72 offen offset:1024 sc1 lds\n"
"s_add_u32 s72, s72, 16384\n"
"s_waitcnt vmcnt(0)\n"
"s_branch SBKV22_%=\n"
"END_%=:\n"
  : "+{v[0:15]}"(va),"+{v[16:31]}"(vb),"+{v[32:35]}"(vc),"+{s[18:19]}"(tb),"+{s[20:35]}"(ka),"+{s[36:43]}"(sb),"+{s[44:59]}"(kb),"+{s[60:63]}"(kc) : : "v36","v37","v38","v39","v40","v41","v42","v43","v44","v45","v46","v47","v48","v49","v50","v51","v52","v53","v54","v55","v56","v57","v58","v59","v60","v61","v62","v63","v64","v65","v66","v67","v68","v69","v70","v71","v72","v73","v74","v75","v76","v77","v78","v79","v80","v81","v82","v83","v84","v85","v86","v87","v88","v89","v90","v91","v92","v93","v94","v95","v96","v97","v98","v99","v100","v101","v102","v103","v104","v105","v106","v107","v108","v109","v110","v111","v112","v113","v114","v115","v116","v117","v118","v119","v120","v121","v122","v123","v124","v125","v126","v127","v128","v129","v130","v131","v132","v133","v134","v135","v136","v137","v138","v139","v140","v141","v142","v143","v144","v145","v146","v147","v148","v149","v150","v151","v152","v153","v154","v155","v156","v157","v158","v159","v160","v161","v162","v163","v164","v165","v166","v167","v168","v169","v170","v171","v172","v173","v174","v175","v176","v177","v178","v179","v180","v181","v182","v183","v184","v185","v186","v187","v188","v189","v190","v191","v192","v193","v194","v195","v196","v197","v198","v199","v200","v201","v202","v203","v204","v205","v206","v207","v208","v209","v210","v211","v212","v213","v214","v215","v216","v217","v218","v219","v220","v221","v222","v223","v224","v225","v226","v227","v228","v229","v230","v231","v232","v233","v234","v235","v236","v237","v238","v239","v240","v241","v242","v243","v244","v245","v246","v247","v248","v249","v250","v251","v252","v253","v254","v255","a0","a1","a2","a3","a4","a5","a6","a7","a8","a9","a10","a11","a12","a13","a14","a15","a16","a17","a18","a19","a20","a21","a22","a23","a24","a25","a26","a27","a28","a29","a30","a31","a32","a33","a34","a35","a36","a37","a38","a39","a40","a41","a42","a43","a44","a45","a46","a47","a48","a49","a50","a51","a52","a53","a54","a55","a56","a57","a58","a59","a60","a61","a62","a63","a64","a65","a66","a67","a68","a69","a70","a71","a72","a73","a74","a75","a76","a77","a78","a79","a80","a81","a82","a83","a84","a85","a86","a87","a88","a89","a90","a91","a92","a93","a94","a95","a96","a97","a98","a99","a100","a101","a102","a103","a104","a105","a106","a107","a108","a109","a110","a111","a112","a113","a114","a115","a116","a117","a118","a119","a120","a121","a122","a123","a124","a125","a126","a127","a128","a129","a130","a131","a132","a133","a134","a135","a136","a137","a138","a139","a140","a141","a142","a143","a144","a145","a146","a147","a148","a149","a150","a151","a152","a153","a154","a155","a156","a157","a158","a159","a160","a161","a162","a163","a164","a165","a166","a167","a168","a169","a170","a171","a172","a173","a174","a175","a176","a177","a178","a179","a180","a181","a182","a183","a184","a185","a186","a187","a188","a189","a190","a191","a192","a193","a194","a195","a196","a197","a198","a199","a200","a201","a202","a203","a204","a205","a206","a207","a208","a209","a210","a211","a212","a213","a214","a215","a216","a217","a218","a219","a220","a221","a222","a223","a224","a225","a226","a227","a228","a229","a230","a231","a232","a233","a234","a235","a236","a237","a238","a239","a240","a241","a242","a243","a244","a245","a246","a247","a248","a249","a250","a251","a252","a253","a254","a255","s0","s1","s2","s3","s4","s5","s6","s7","s8","s9","s10","s11","s12","s13","s14","s15","s16","s64","s65","s66","s67","s68","s69","s70","s71","s72","s73","s74","s75","s76","s77","s78","s79","s80","s81","s82","s83","s84","s85","s86","s87","s88","s89","s90","s91","s92","s93","s94","s95","s96","s97","s98","s99","vcc","scc","memory");
}
extern "C" void kernel_launch(void* const* d_in, const int* in_sizes, int n_in, void* d_out, int out_size, void* d_ws, size_t ws_size, hipStream_t stream){
  const void* q[9]={d_ws,d_in[1],d_in[5],d_in[6],d_in[0],d_out,d_in[7],d_in[3],d_in[2]};
  const u32 n[9]={WSZ,1024u*6144u+4096u,1024u*7168u+4096u,1024u*7168u+4096u,2048u*2048u,2048u*2048u,3584u*2048u+4096u,1024u*2048u+4096u,65536u};
  P p; for(int i=0;i<9;i++){ p.d[i].p=q[i]; p.d[i].n=n[i]; p.d[i].f=0x00020000u; }
  (void)hipFuncSetAttribute((const void*)fused,hipFuncAttributeMaxDynamicSharedMemorySize,163840);
  fused<<<256,256,163840,stream>>>(p);
}
```
